```python
import torch

def _softmax(x, axis=-1):
    return torch.softmax(x, dim=axis)
def _gelu(x):
    return torch.nn.functional.gelu(x, approximate="tanh")
def _silu(x):
    return torch.nn.functional.silu(x)
def _sigmoid(x):
    return torch.sigmoid(x)
def _layernorm(x, axis=-1, eps=1e-5):
    m = x.mean(dim=axis, keepdim=True)
    v = x.var(dim=axis, keepdim=True, unbiased=False)
    return (x - m) / torch.sqrt(v + eps)
def _scatter_add(tgt, idx, src, axis=0):
    return tgt.clone().index_add_(axis, idx, src)
def _segment_sum(src, seg, n):
    out = torch.zeros((n,) + src.shape[1:], dtype=src.dtype)
    return out.index_add_(0, seg, src)
def _pad(x, pw):
    flat = tuple(v for pair in reversed(pw) for v in pair)
    return torch.nn.functional.pad(x, flat)

kernel_name = 'standalone__deep_cross_xdeepfm__recsys'

def setup_inputs():

    _w = (lambda a: (lambda t: t.to(torch.bfloat16) if t.is_floating_point() else t)(torch.as_tensor(a)))
    B, F, D, L = 1024, 26, 32, 3
    V = min(100000, 100000)
    Din = F * D
    return {
        "idx": _w((lambda lo, hi, *s: torch.randint(lo, hi, s))(0, V, B, F).to(torch.int64)),
        "table": _w((lambda *s: torch.randn(*s, dtype=torch.bfloat16) * 1.6)(V, D)),
        "ws_cin": _w((lambda *s: torch.randn(*s, dtype=torch.bfloat16) * 0.1)(L, F, F, F) * (1.0 / F)),
        "w_cin_out": _w((lambda *s: torch.randn(*s, dtype=torch.bfloat16) * 0.1)(L * F) * ((L * F) ** -0.5)),
        "w_dnn1": _w((lambda *s: torch.randn(*s, dtype=torch.bfloat16) * 1.6)(Din, Din) * (Din ** -0.5)),
        "b_dnn1": _w((lambda *s: torch.randn(*s, dtype=torch.bfloat16) * 12.8)(Din) * 0.1),
        "w_dnn2": _w((lambda *s: torch.randn(*s, dtype=torch.bfloat16) * 0.05)(Din) * (Din ** -0.5)),
        "b_out": _w((lambda *s: torch.randn(*s, dtype=torch.bfloat16) * 1.6)(1)),
    }

def reference(idx, table, ws_cin, w_cin_out, w_dnn1, b_dnn1, w_dnn2, b_out):
    x0 = table[idx]
    B = x0.shape[0]
    L = ws_cin.shape[0]
    xk = x0
    pooled = []
    for l in range(L):
        xk = torch.einsum('bid,bjd,hij->bhd', xk, x0, ws_cin[l])
        pooled.append(xk.sum(axis=-1))
    p_plus = torch.stack(pooled, axis=1).reshape(B, -1)
    logit_cin = p_plus @ w_cin_out
    h = x0.reshape(B, -1)
    h = _gelu(h @ w_dnn1 + b_dnn1)
    logit_dnn = h @ w_dnn2
    return logit_cin + logit_dnn + b_out
```

```cpp
#include <hip/hip_runtime.h>
#include <hip/hip_bf16.h>
#include <stdint.h>

static constexpr int F = 26, D = 32, DIN = 832;

__device__ __forceinline__ float bf2f(uint16_t v) { return __uint_as_float(((uint32_t)v) << 16); }

__global__ void __launch_bounds__(256) naive_kernel(const int* __restrict__ idx, const uint16_t* __restrict__ table,
    const uint16_t* __restrict__ wc, const uint16_t* __restrict__ wco, const uint16_t* __restrict__ w1,
    const uint16_t* __restrict__ b1, const uint16_t* __restrict__ w2, const uint16_t* __restrict__ bo,
    __hip_bfloat16* __restrict__ out, int V) {
  __shared__ float x0[DIN];
  __shared__ float xa[DIN];
  __shared__ float xb[DIN];
  __shared__ float red[256];
  int b = blockIdx.x, t = threadIdx.x;
  for (int e = t; e < DIN; e += 256) {
    int f = e / D, d = e % D;
    int r = idx[b * F + f];
    float v = bf2f(table[(size_t)r * D + d]);
    x0[e] = v; xa[e] = v;
  }
  __syncthreads();
  float acc = 0.f;
  float* cur = xa; float* nxt = xb;
  for (int l = 0; l < 3; ++l) {
    for (int e = t; e < DIN; e += 256) {
      int h = e / D, d = e % D;
      float s = 0.f;
      for (int i = 0; i < F; ++i) {
        float xi = cur[i * D + d];
        float u = 0.f;
        for (int j = 0; j < F; ++j) u += bf2f(wc[((l * F + h) * F + i) * F + j]) * x0[j * D + d];
        s += xi * u;
      }
      nxt[e] = s;
      acc += s * bf2f(wco[l * F + h]);
    }
    __syncthreads();
    float* tmp = cur; cur = nxt; nxt = tmp;
  }
  for (int n = t; n < DIN; n += 256) {
    float s = bf2f(b1[n]);
    for (int k = 0; k < DIN; ++k) s += x0[k] * bf2f(w1[(size_t)k * DIN + n]);
    float g = 0.5f * s * (1.f + tanhf(0.7978845608028654f * (s + 0.044715f * s * s * s)));
    acc += g * bf2f(w2[n]);
  }
  red[t] = acc;
  __syncthreads();
  for (int s = 128; s > 0; s >>= 1) { if (t < s) red[t] += red[t + s]; __syncthreads(); }
  if (t == 0) out[b] = __float2bfloat16(red[0] + bf2f(bo[0]));
}

extern "C" void kernel_launch(void* const* d_in, const int* in_sizes, int n_in,
                              void* d_out, int out_size, void* d_ws, size_t ws_size,
                              hipStream_t stream) {
  const int* idx = (const int*)d_in[0];
  int V = in_sizes[1] / D;
  int B = in_sizes[0] / F;
  naive_kernel<<<B, 256, 0, stream>>>(idx, (const uint16_t*)d_in[1], (const uint16_t*)d_in[2], (const uint16_t*)d_in[3],
    (const uint16_t*)d_in[4], (const uint16_t*)d_in[5], (const uint16_t*)d_in[6], (const uint16_t*)d_in[7],
    (__hip_bfloat16*)d_out, V);
}
```

```cpp
#include <hip/hip_runtime.h>
#include <hip/hip_bf16.h>
#include <stdint.h>

#define P_ADELAY 8
#define P_BDELAY 8
#define P_WW 10
#define P_PQ 16
#define P_XI 10
#define P_XDELAY 12
#define P_CG 4
#define P_XW 12
namespace xdfm {

typedef unsigned int u32;
typedef unsigned long long u64;
typedef u32 u32x2 __attribute__((ext_vector_type(2)));
typedef u32 u32x4 __attribute__((ext_vector_type(4)));
typedef short s16x4 __attribute__((ext_vector_type(4)));
typedef short s16x8 __attribute__((ext_vector_type(8)));
typedef float f32x2 __attribute__((ext_vector_type(2)));
typedef float f32x4 __attribute__((ext_vector_type(4)));
typedef float f32x16 __attribute__((ext_vector_type(16)));
typedef __bf16 bf16x2 __attribute__((ext_vector_type(2)));
typedef __bf16 bf16x8 __attribute__((ext_vector_type(8)));
#define GAS __attribute__((address_space(1)))
#define LAS __attribute__((address_space(3)))
typedef __amdgpu_buffer_rsrc_t rsrc_t;

constexpr int NF = 26, DIN = 832, NB = 1024, CUBE = 17576;

constexpr u32 WS_E = 0;
constexpr u32 FL = 128;
constexpr u32 WS_XF = 4096;
constexpr u32 WS_FA = 40960;
constexpr u32 WS_FB = 45056;
constexpr u32 WS_PA = 49152;
constexpr u32 WS_PB = 69632;
constexpr u32 WS_P = 131072;
constexpr u32 WS_X = 655360;
constexpr u32 WS_END = WS_X + 16 * 104 * 1024;
constexpr int NWAVE = 9;

constexpr u32 L_W = 0;
constexpr u32 APITCH = 432;
constexpr u32 L_A = 106496;
constexpr u32 L_B = L_A + 48 * APITCH;
constexpr u32 L_XS = L_B + 48 * APITCH;
constexpr u32 XS_S = 1824;
constexpr u32 L_CNT = L_XS + 4 * XS_S;
constexpr u32 L_TOTAL = L_CNT + 64;
constexpr u32 L_V3 = 0, L_W2C = 8192, L_W2S = 8448, L_U = 12288, L_RAW = 20480;
enum { C_TAGV = 12, C_TAG = 13, C_WISS = 14, C_ABRDY = 0, C_ABSTG = 1, C_W = 3, C_V3 = 4, C_U = 5, C_XISS = 6, C_XRDY = 7, C_WD = 8, C_RED = 10 };

constexpr int perm1(int j) { return j < 24 ? ((j & ~7) | ((j + 2) & 7)) : (j == 24 ? 25 : (j == 25 ? 24 : j)); }
struct Slots { int a[192]; int b[192]; bool v0[192]; bool v1[192]; int n; };
struct SlotBuilder {
  Slots s{}; bool vis[27][27] = {}; int n = 0;
  constexpr bool seen(int a, int b) const { return vis[a < b ? a : b][a < b ? b : a]; }
  constexpr void put(int a, int b) {
    s.a[n] = a; s.b[n] = b;
    s.v0[n] = !seen(a, b); vis[a < b ? a : b][a < b ? b : a] = true;
    const int na = perm1(a), nb = perm1(b);
    s.v1[n] = !seen(na, nb); vis[na < nb ? na : nb][na < nb ? nb : na] = true;
    ++n;
  }
};
constexpr int sg2(int m) { return perm1(perm1(2 * m)) >> 1; }
constexpr Slots make_slots() {
  SlotBuilder t{};
  for (int A = 0; A < 13; ++A)
    for (int B = A + 1; B < 13; ++B) {
      int ca = A, cb = B;
      while (!t.seen(2 * ca, 2 * cb)) {
        t.put(2 * ca, 2 * cb); t.put(2 * ca, 2 * cb + 1); t.put(2 * ca + 1, 2 * cb); t.put(2 * ca + 1, 2 * cb + 1);
        ca = sg2(ca); cb = sg2(cb);
      }
    }
  for (int A = 0; A < 13; ++A) {
    int ca = A;
    while (!t.seen(2 * ca, 2 * ca + 1)) {
      t.put(2 * ca, 2 * ca); t.put(2 * ca, 2 * ca + 1); t.put(2 * ca + 1, 2 * ca + 1); t.put(2 * ca + 1, 26);
      ca = sg2(ca);
    }
  }
  for (int a = 0; a < 27; ++a) { int ca = a; while (!t.seen(ca, 26)) { t.put(ca, 26); ca = perm1(perm1(ca)); } }
  t.s.n = t.n;
  for (int n = t.n; n < 192; ++n) { t.s.a[n] = 26; t.s.b[n] = 26; t.s.v0[n] = false; t.s.v1[n] = false; }
  return t.s;
}
constexpr Slots SL = make_slots();
static_assert(SL.n <= 192, "too many slots");

constexpr int UP = 33, ZB = 30 * UP, ZA = 676;
struct SymTab { u32 w[192][4]; };
constexpr SymTab make_symtab() {
  SymTab t{};
  for (int d = 0; d < 192; ++d) {
    int c = d >> 2, s = c >> 1, hh = c & 1;
    for (int e = 0; e < 2; ++e) {
      int sg = 8 * s + 2 * (d & 3) + e;
      int a = hh ? perm1(SL.a[sg]) : SL.a[sg], b = hh ? perm1(SL.b[sg]) : SL.b[sg];
      bool v = hh ? SL.v1[sg] : SL.v0[sg];
      u32 b1 = ZB, b2 = ZB, a1 = ZA, a2 = ZA;
      if (v) {
        b1 = a * UP + b; b2 = a == b ? ZB : b * UP + a;
        if (a < 26 && b < 26) { a1 = a * 26 + b; a2 = a == b ? ZA : b * 26 + a; }
      }
      t.w[d][e] = b1 | (b2 << 16);
      t.w[d][2 + e] = a1 | (a2 << 16);
    }
  }
  return t;
}
__device__ const SymTab SYMTAB = make_symtab();

constexpr u32 OOB = 0x7FFFFF00u;
constexpr u32 SPIN_MAX = 1u << 18;

__device__ __forceinline__ float bflo(u32 v) { return __uint_as_float(v << 16); }
__device__ __forceinline__ float bfhi(u32 v) { return __uint_as_float(v & 0xffff0000u); }
__device__ __forceinline__ u32 packbf(float a, float b) {
  f32x2 f = {a, b};
  bf16x2 z = __builtin_convertvector(f, bf16x2);
  return __builtin_bit_cast(u32, z);
}
__device__ __forceinline__ float mulf(float a, float b) { float r; asm("v_mul_f32 %0, %1, %2" : "=v"(r) : "v"(a), "v"(b)); return r; }
template <int E> __device__ __forceinline__ f32x2 pkmul_bc(f32x2 a, f32x2 b) {
  f32x2 r; asm("v_pk_mul_f32 %0, %1, %2 op_sel:[%3,0] op_sel_hi:[%3,1]" : "=v"(r) : "v"(a), "v"(b), "i"(E)); return r;
}
__device__ __forceinline__ float dot2bf(u32 a, u32 b, float c) {
  return __builtin_amdgcn_fdot2_f32_bf16(__builtin_bit_cast(bf16x2, a), __builtin_bit_cast(bf16x2, b), c, false);
}
__device__ __forceinline__ rsrc_t mkrs(const void* p, u32 bytes) { return __builtin_amdgcn_make_buffer_rsrc((void*)p, 0, bytes, 0x00020000); }
__device__ __forceinline__ u64 mkflag(u32 tag) { return (u64)tag | ((u64)(tag * 0x9E3779B1u + 0x7F4A7C15u) << 32); }
__device__ __forceinline__ void drain_vm() { asm volatile("s_waitcnt vmcnt(0)" ::: "memory"); }
__device__ __forceinline__ void lds_inc(LAS u32* cnt) {
  u64 keep;
  asm volatile("s_mov_b64 %0, exec\n\ts_mov_b64 exec, 1\n\tds_add_u32 %1, %2\n\ts_mov_b64 exec, %0"
               : "=&s"(keep) : "v"((u32)(size_t)cnt), "v"(1u) : "memory");
}
__device__ __forceinline__ void team_arrive(LAS u32* cnt, int) {
  asm volatile("s_waitcnt lgkmcnt(0)" ::: "memory");
  lds_inc(cnt);
}
__device__ __forceinline__ float sum_rows(float v) {
  auto a = __builtin_amdgcn_permlane32_swap(__float_as_uint(v), __float_as_uint(v), false, false);
  v = __uint_as_float(a[0]) + __uint_as_float(a[1]);
  auto b = __builtin_amdgcn_permlane16_swap(__float_as_uint(v), __float_as_uint(v), false, false);
  return __uint_as_float(b[0]) + __uint_as_float(b[1]);
}
template <int CTRL> __device__ __forceinline__ float dpp_add(float v) {
  return v + __uint_as_float(__builtin_amdgcn_update_dpp(0u, __float_as_uint(v), CTRL, 0xf, 0xf, true));
}
__device__ __forceinline__ float sum_row16(float v) {
  v = dpp_add<0x128>(v);
  v = dpp_add<0x124>(v);
  v = dpp_add<0x122>(v);
  v = dpp_add<0x121>(v);
  return v;
}

#define STAMP(k) do {} while (0)

struct Args {
  const int* idx; const uint16_t* table; const uint16_t* wc; const uint16_t* wco; const uint16_t* w1;
  const uint16_t* b1; const uint16_t* w2; const uint16_t* bo; uint16_t* out; char* ws;
  u32 table_bytes; u32 pad_;
};
#define CAS __attribute__((address_space(4)))

template <int SLEEP> __device__ __forceinline__ void team_wait(LAS u32* cnt, u32 target) {
  u32 v, s, n = SPIN_MAX;
  asm volatile(
      "1:\n\tds_read_b32 %0, %3\n\ts_waitcnt lgkmcnt(0)\n\tv_readfirstlane_b32 %1, %0\n\ts_cmp_ge_u32 %1, %4\n\ts_cbranch_scc1 2f\n\t"
      "s_sleep %5\n\ts_sub_u32 %2, %2, 1\n\ts_cbranch_scc0 1b\n2:"
      : "=&v"(v), "=&s"(s), "+s"(n) : "v"((u32)(size_t)cnt), "s"(target), "i"(SLEEP) : "memory", "scc");
}
template <int SLEEP> __device__ __forceinline__ void poll_flags(const char* p, u64 want) {
  u64 v; u32 n = SPIN_MAX;
  asm volatile(
      "1:\n\tglobal_load_dwordx2 %0, %2, off sc1\n\ts_waitcnt vmcnt(0)\n\tv_cmp_ne_u64 vcc, %3, %0\n\ts_nop 4\n\ts_cbranch_vccz 2f\n\t"
      "s_sleep %4\n\ts_sub_u32 %1, %1, 1\n\ts_cbranch_scc0 1b\n2:"
      : "=&v"(v), "+s"(n) : "v"(p), "s"(want), "i"(SLEEP) : "memory", "scc", "vcc");
}
__device__ __forceinline__ void lds_barrier() {
  asm volatile("s_waitcnt lgkmcnt(0)" ::: "memory");
  __builtin_amdgcn_s_barrier();
  asm volatile("" ::: "memory");
}
__device__ __forceinline__ u32 load_epoch(const char* ws) {
  return __hip_atomic_load((const GAS u32*)(ws + WS_E), __ATOMIC_RELAXED, __HIP_MEMORY_SCOPE_AGENT);
}
__device__ __forceinline__ void store_flag(char* ws, u32 off, u64 flag) {
  __hip_atomic_store((GAS u64*)(ws + off), flag, __ATOMIC_RELAXED, __HIP_MEMORY_SCOPE_AGENT);
}
__device__ __forceinline__ u32 get_tag(LAS u32* cnt) {
  team_wait<0>(cnt + C_TAG, 1);
  return __builtin_amdgcn_readfirstlane(cnt[C_TAGV]);
}
__device__ __forceinline__ u32 opq(u32 v) { asm("" : "+v"(v)); return v; }

__global__ void __launch_bounds__(576, 3) xdfm_kernel(Args a) {
  extern __shared__ __attribute__((aligned(16))) char lds_raw[];
  LAS char* lds = (LAS char*)lds_raw;
  const int tid = threadIdx.x, lane = tid & 63;
  const int w = __builtin_amdgcn_readfirstlane(tid >> 6);
  const int b = blockIdx.x, j = b >> 3;
  const int jg = j < 6 ? (j >= 3 ? 1 : 0) : (j >= 19 ? 1 : 0);
  const int g = (b & 7) * 2 + jg, r = j < 6 ? 13 + j - 3 * jg : j - 6 - 13 * jg;
  LAS u32* cnt = (LAS u32*)(lds + L_CNT);
  asm volatile("" :: "s"(a.idx), "s"(a.table), "s"(a.wc), "s"(a.wco), "s"(a.w1), "s"(a.b1), "s"(a.w2), "s"(a.bo), "s"(a.out), "s"(a.ws), "s"(a.table_bytes));
  const rsrc_t rws = mkrs(a.ws, WS_END);
  const rsrc_t ridx = mkrs(a.idx, NB * NF * 4), rtab = mkrs(a.table, a.table_bytes);

  if (w == 8) {
    __builtin_amdgcn_s_setprio(3);
    if (lane < 16) cnt[lane] = 0;
    u32 tag;
    if (r < 13) {
      u32 id[4]; u32x4 xg[8];
#pragma unroll
      for (int m = 0; m < 2; ++m) {
        const int p0 = 4 * r + 2 * m, t = p0 >= NF ? 1 : 0, f0 = p0 - t * NF;
        const u32x2 ia = __builtin_amdgcn_raw_buffer_load_b64(ridx, (lane & 31) * (NF * 4), ((g * 64 + t * 32) * NF + f0) * 4, 0);
        id[2 * m] = ia.x; id[2 * m + 1] = ia.y;
      }
      const u32 ep = load_epoch(a.ws);
#pragma unroll
      for (int k = 0; k < 8; ++k) xg[k] = __builtin_amdgcn_raw_buffer_load_b128(rtab, id[k >> 1] * 64 + (k & 1) * 32 + (lane >> 5) * 16, 0, 0);
      lds_barrier();
      STAMP(1);
      tag = __builtin_amdgcn_readfirstlane(ep) + 1u;
      cnt[C_TAGV] = tag;
      team_arrive(cnt + C_TAG, lane);
#pragma unroll
      for (int k = 0; k < 8; ++k) __builtin_amdgcn_raw_buffer_store_b128(xg[k], rws, WS_X + lane * 16, (g * 104 + r * 8 + k) * 1024, 16);
      STAMP(2);
      drain_vm();
    } else {
      const u32 ep = load_epoch(a.ws);
      lds_barrier();
      tag = __builtin_amdgcn_readfirstlane(ep) + 1u;
      cnt[C_TAGV] = tag;
      team_arrive(cnt + C_TAG, lane);
    }
    const u64 flag = mkflag(tag);
    if (lane == 0) store_flag(a.ws, WS_XF + (g * 16 + r) * FL, flag);
    STAMP(3);
    if (r < 13) {
      __builtin_amdgcn_s_sleep(P_XDELAY);
      poll_flags<2>(a.ws + WS_XF + (g * 16 + (lane < 13 ? lane : 0)) * FL, flag);
      lds_inc(cnt + C_XRDY);
    }
    STAMP(4);
    __builtin_amdgcn_s_sleep(P_BDELAY);
    poll_flags<6>(a.ws + (lane < 32 ? WS_FA : WS_FB) + ((lane & 31) < NF ? (lane & 31) : 0) * FL, flag);
    lds_inc(cnt + C_ABRDY);
    STAMP(5);
  } else if (w >= 4) {
    const int cw = w - 4, smp = g * 64 + r * 4 + cw;
    u32 id[2]; u32x4 xr[2];
    u32 bo;
    {
      const CAS u32* ip = (const CAS u32*)(a.idx + smp * NF);
      u32 is[NF];
#pragma unroll
      for (int f = 0; f < NF; ++f) is[f] = ip[f];
      const size_t bop = (size_t)a.bo;
      bo = *(const CAS u32*)(bop & ~(size_t)3) >> ((bop & 2) * 8);
      u32 iv = 0;
      asm("v_writelane_b32 %0, %1, 0\n\tv_writelane_b32 %0, %2, 1\n\tv_writelane_b32 %0, %3, 2\n\tv_writelane_b32 %0, %4, 3\n\tv_writelane_b32 %0, %5, 4\n\tv_writelane_b32 %0, %6, 5\n\tv_writelane_b32 %0, %7, 6\n\tv_writelane_b32 %0, %8, 7\n\tv_writelane_b32 %0, %9, 8\n\tv_writelane_b32 %0, %10, 9\n\tv_writelane_b32 %0, %11, 10\n\tv_writelane_b32 %0, %12, 11\n\tv_writelane_b32 %0, %13, 12\n\tv_writelane_b32 %0, %14, 13\n\tv_writelane_b32 %0, %15, 14\n\tv_writelane_b32 %0, %16, 15\n\tv_writelane_b32 %0, %17, 16\n\tv_writelane_b32 %0, %18, 17\n\tv_writelane_b32 %0, %19, 18\n\tv_writelane_b32 %0, %20, 19\n\tv_writelane_b32 %0, %21, 20\n\tv_writelane_b32 %0, %22, 21\n\tv_writelane_b32 %0, %23, 22\n\tv_writelane_b32 %0, %24, 23\n\tv_writelane_b32 %0, %25, 24\n\tv_writelane_b32 %0, %26, 25"
          : "+v"(iv) : "s"(is[0]), "s"(is[1]), "s"(is[2]), "s"(is[3]), "s"(is[4]), "s"(is[5]), "s"(is[6]), "s"(is[7]), "s"(is[8]), "s"(is[9]), "s"(is[10]), "s"(is[11]), "s"(is[12]), "s"(is[13]), "s"(is[14]), "s"(is[15]), "s"(is[16]), "s"(is[17]), "s"(is[18]), "s"(is[19]), "s"(is[20]), "s"(is[21]), "s"(is[22]), "s"(is[23]), "s"(is[24]), "s"(is[25]));
      id[0] = __builtin_amdgcn_ds_bpermute((lane >> 2) * 4, iv);
      id[1] = __builtin_amdgcn_ds_bpermute((16 + (lane >> 2)) * 4, iv);
    }
    if (r < 13) __builtin_amdgcn_s_setprio(2);
    lds_barrier();
#pragma unroll
    for (int m = 0; m < 2; ++m) { const int f = 16 * m + (lane >> 2); xr[m] = __builtin_amdgcn_raw_buffer_load_b128(rtab, f < NF ? id[m] * 64 + (lane & 3) * 16 : OOB, 0, 0); }
    __builtin_amdgcn_s_setprio(0);
    STAMP(1);
    f32x2 xp[14];
    {
      const u32 o = L_XS + cw * XS_S + (lane >> 2) * 64 + (lane & 3) * 16;
      *(LAS u32x4*)(lds + o) = xr[0];
      u32x4 x1 = xr[1];
      if ((lane >> 2) == NF - 16) x1 = (u32x4){0x3F803F80u, 0x3F803F80u, 0x3F803F80u, 0x3F803F80u};
      *(LAS u32x4*)(lds + opq((lane >> 2) < 12 ? o + 1024 : L_XS + cw * XS_S + 27 * 64 + (lane & 3) * 16)) = x1;
      asm volatile("s_waitcnt lgkmcnt(0)" ::: "memory");
      const int hh = lane >> 5, q = (lane >> 2) & 3, pp = lane & 3;
      const u32 base = L_XS + cw * XS_S + ((lane >> 4) & 1) * 32 + pp * 8;
      const u32 a0 = base + (hh ? q + 2 : q) * 64, a1 = base + (hh ? ((q + 6) & 7) : q + 4) * 64;
      const u32 a6 = base + (24 + ((hh && q < 2) ? (q ^ 1) : q)) * 64;
#pragma unroll
      for (int t = 0; t < 7; ++t) {
        s16x4 y = __builtin_amdgcn_ds_read_tr16_b64_v4i16((LAS s16x4*)(lds + (t < 6 ? ((t & 1) ? a1 : a0) + (t >> 1) * 512 : a6)));
        u32x2 u = __builtin_bit_cast(u32x2, y);
        xp[2 * t] = (f32x2){bflo(u.x), bfhi(u.x)}; xp[2 * t + 1] = (f32x2){bflo(u.y), bfhi(u.y)};
      }
    }
    bf16x8 z[24];
#pragma unroll
    for (int s = 0; s < 24; ++s) {
      u32x4 zz;
#pragma unroll
      for (int e = 0; e < 4; ++e) {
        const int s1 = 8 * s + 2 * e, s2 = s1 + 1;
        const int a1 = SL.a[s1], b1 = SL.b[s1], a2 = SL.a[s2], b2 = SL.b[s2];
        if (a1 == a2 && (b1 & 1) == 0 && b1 < 26 && b2 == b1 + 1) {
          const f32x2 pr = (a1 & 1) ? pkmul_bc<1>(xp[a1 >> 1], xp[b1 >> 1]) : pkmul_bc<0>(xp[a1 >> 1], xp[b1 >> 1]);
          zz[e] = packbf(pr[0], pr[1]);
        } else {
          const float p1 = b1 == 26 ? xp[a1 >> 1][a1 & 1] : mulf(xp[a1 >> 1][a1 & 1], xp[b1 >> 1][b1 & 1]);
          const float p2 = b2 == 26 ? xp[a2 >> 1][a2 & 1] : mulf(xp[a2 >> 1][a2 & 1], xp[b2 >> 1][b2 & 1]);
          zz[e] = packbf(p1, p2);
        }
      }
      z[s] = __builtin_bit_cast(bf16x8, zz);
      asm volatile("" : "+v"(z[s]));
    }
    const u32 tag = get_tag(cnt);
    const u64 flag = mkflag(tag);
    STAMP(2);
    team_wait<1>(cnt + C_ABRDY, 1);
    STAMP(3);
    if (r < 13) team_wait<1>(cnt + C_XISS, 4);
    __builtin_amdgcn_s_setprio(2);
    STAMP(4);
    {
      u32x4 y[10];
#pragma unroll
      for (int m = 0; m < 10; ++m) {
        const u32 n = ((m % 5) * 4 + cw) * 64 + lane;
        y[m] = __builtin_amdgcn_raw_buffer_load_b128(rws, n < NF * 48 ? n * 16 : OOB, m < 5 ? WS_PA : WS_PB, 16);
      }
#pragma unroll
      for (int m = 0; m < 5; ++m) {
        const u32 n = (m * 4 + cw) * 64 + lane, i = (n * 1366u) >> 16, c = n - i * 48;
        const u32 o = opq(n < NF * 48 ? L_A + c * APITCH + i * 16 : L_A + 26 * 16);
        *(LAS u32x4*)(lds + o) = y[m];
        *(LAS u32x4*)(lds + o + (L_B - L_A)) = y[5 + m];
      }
      team_arrive(cnt + C_ABSTG, lane);
      team_wait<0>(cnt + C_ABSTG, 4);
    }
    __builtin_amdgcn_s_setprio(0);
    STAMP(5);
    const u32 afr = (lane >> 5) * APITCH + (lane & 31) * 16;
    const u32 tag2 = (u32)(flag >> 32);
    const int sg = r * 4 + cw;
    const u32 po = opq(lane < 26 ? WS_P + ((g * 26 + lane) * 2 + (sg >> 5)) * 512 + (sg & 31) * 16 : OOB);
    u32x4 yp;
    f32x16 acc1 = {}, acc2 = {};
    {
      constexpr int AD = 2;
      bf16x8 af[24], bf[24];
#pragma unroll
      for (int s = 0; s < AD; ++s) { af[s] = *(LAS bf16x8*)(lds + L_A + afr + s * 2 * APITCH); bf[s] = *(LAS bf16x8*)(lds + L_B + afr + s * 2 * APITCH); }
      __builtin_amdgcn_sched_barrier(0);
#pragma unroll
      for (int s = 0; s < 24; ++s) {
        if (s + AD < 24) { af[s + AD] = *(LAS bf16x8*)(lds + L_A + afr + (s + AD) * 2 * APITCH); bf[s + AD] = *(LAS bf16x8*)(lds + L_B + afr + (s + AD) * 2 * APITCH); }
        if (s == P_PQ) yp = __builtin_amdgcn_raw_buffer_load_b128(rws, po, 0, 16);
        acc1 = __builtin_amdgcn_mfma_f32_32x32x16_bf16(af[s], z[s], acc1, 0, 0, 0);
        acc2 = __builtin_amdgcn_mfma_f32_32x32x16_bf16(bf[s], z[s], acc2, 0, 0, 0);
        __builtin_amdgcn_sched_barrier(0);
      }
    }
    float sum = 0.f;
#pragma unroll
    for (int e = 0; e < 12; ++e) sum += acc1[e] * acc2[e];
    { const float t = acc1[12] * acc2[12] + acc1[13] * acc2[13]; sum += lane < 32 ? t : 0.f; }
    sum = sum_rows(sum_row16(sum));
    STAMP(6);
    for (u32 it = 0; it < SPIN_MAX; ++it) {
      const bool ok = lane >= 26 || (yp.y == tag && yp.w == tag2);
      if (__all(ok)) break;
      asm volatile("" ::: "memory");
      __builtin_amdgcn_s_sleep(1);
      yp = __builtin_amdgcn_raw_buffer_load_b128(rws, po, 0, 16);
    }
    float part = sum_rows(sum_row16(__uint_as_float(yp.x)));
    if (lane == 0) {
      const __bf16 ob = (__bf16)(sum + part + bflo(bo));
      a.out[smp] = __builtin_bit_cast(uint16_t, ob);
    }
    STAMP(7);
  } else if (r < 13) {
      const rsrc_t rw1 = mkrs(a.w1, DIN * DIN * 2);
      const int mh = w & 1, kh = w >> 1, hh = lane >> 5;
      lds_barrier();
      STAMP(1);
      {
        u32x4 v[26];
        const u32 vo = (w * 208 + (lane >> 3)) * 1664 + r * 128 + (lane & 7) * 16;
        const int c = lane & 7;
        const u32 lo = L_W + (w * 208 + (lane >> 3)) * 128 + (((c >> 1) ^ (((lane >> 4) & 1) << 1)) * 32) + (c & 1) * 16;
#pragma unroll
        for (int i = 0; i < 26; ++i) v[i] = __builtin_amdgcn_raw_buffer_load_b128(rw1, vo, i * 8 * 1664, 0);
#pragma unroll
        for (int i = 0; i < 26; ++i) *(LAS u32x4*)(lds + lo + i * 1024) = v[i];
      }
      team_arrive(cnt + C_W, lane);
      const u32 tag = get_tag(cnt);
      const u64 flag = mkflag(tag);
      f32x16 acc[2];
      f32x2 wf[8];
      {
        const CAS u32* bp = (const CAS u32*)(a.b1 + r * 64 + kh * 32);
        const CAS u32* wp = (const CAS u32*)(a.w2 + r * 64 + kh * 32);
        u32 bs[16], ws_[16];
#pragma unroll
        for (int t = 0; t < 16; ++t) { bs[t] = bp[t]; ws_[t] = wp[t]; }
#pragma unroll
        for (int t = 0; t < 16; ++t) asm volatile("" : "+s"(bs[t]), "+s"(ws_[t]));
#pragma unroll
        for (int t = 0; t < 4; ++t) {
          const u32 b0 = hh ? bs[4 * t + 2] : bs[4 * t], b1 = hh ? bs[4 * t + 3] : bs[4 * t + 1];
          const u32 w0 = hh ? ws_[4 * t + 2] : ws_[4 * t], w1 = hh ? ws_[4 * t + 3] : ws_[4 * t + 1];
          acc[0][4 * t] = bflo(b0); acc[0][4 * t + 1] = bfhi(b0); acc[0][4 * t + 2] = bflo(b1); acc[0][4 * t + 3] = bfhi(b1);
          acc[1][4 * t] = 0.f; acc[1][4 * t + 1] = 0.f; acc[1][4 * t + 2] = 0.f; acc[1][4 * t + 3] = 0.f;
          wf[2 * t] = (f32x2){bflo(w0), bfhi(w0)}; wf[2 * t + 1] = (f32x2){bflo(w1), bfhi(w1)};
          asm volatile("" : "+v"(wf[2 * t]), "+v"(wf[2 * t + 1]));
        }
      }
      STAMP(2);
      team_wait<0>(cnt + C_XRDY, 1);
      STAMP(3);
      constexpr int XW = P_XW, FD = 3;
      u32x4 xv[26];
      auto ldx = [&](int i) { xv[i] = __builtin_amdgcn_raw_buffer_load_b128(rws, WS_X + lane * 16, (g * 104 + mh * 52 + kh * 26 + i) * 1024, 16); };
#pragma unroll
      for (int i = 0; i < XW; ++i) ldx(i);
      team_wait<0>(cnt + C_W, 4);
      __builtin_amdgcn_s_setprio(1);
      {
        const int q = (lane >> 2) & 3, pp = lane & 3, gam = (lane >> 4) & 1;
        const u32 rb = L_W + kh * 26 * 2048 + (8 * hh + q) * 128 + pp * 8;
        const u32 ad0 = rb + (((2 * kh + gam) ^ (q & 2)) * 32), ad1 = rb + (((2 * (kh ^ 1) + gam) ^ (q & 2)) * 32);
        bf16x8 fr[26][2];
        auto ldf = [&](int i) {
#pragma unroll
          for (int t = 0; t < 2; ++t) {
            const u32 o = (t ? ad1 : ad0) + i * 2048;
            const s16x4 y0 = __builtin_amdgcn_ds_read_tr16_b64_v4i16((LAS s16x4*)(lds + o));
            const s16x4 y1 = __builtin_amdgcn_ds_read_tr16_b64_v4i16((LAS s16x4*)(lds + o + 512));
            fr[i][t] = __builtin_bit_cast(bf16x8, __builtin_shufflevector(y0, y1, 0, 1, 2, 3, 4, 5, 6, 7));
          }
        };
#pragma unroll
        for (int i = 0; i < FD; ++i) ldf(i);
        __builtin_amdgcn_sched_barrier(0);
#pragma unroll
        for (int i = 0; i < 26; ++i) {
          if (i + XW < 26) ldx(i + XW);
          if (i == P_XI) lds_inc(cnt + C_XISS);
          if (i == 8) lds_inc(cnt + C_WD + kh);
          if (i == 22) team_wait<0>(cnt + C_WD + kh, 2);
          if (i + FD < 26) ldf(i + FD);
          const bf16x8 xb = __builtin_bit_cast(bf16x8, xv[i]);
          acc[0] = __builtin_amdgcn_mfma_f32_32x32x16_bf16(fr[i][0], xb, acc[0], 0, 0, 0);
          acc[1] = __builtin_amdgcn_mfma_f32_32x32x16_bf16(fr[i][1], xb, acc[1], 0, 0, 0);
          __builtin_amdgcn_sched_barrier(0);
        }
      }
      STAMP(4);
      {
        const u32 so = L_W + kh * 26 * 2048 + mh * 4096 + lane * 16;
#pragma unroll
        for (int t = 0; t < 4; ++t) *(LAS f32x4*)(lds + so + t * 1024) = (f32x4){acc[1][4 * t], acc[1][4 * t + 1], acc[1][4 * t + 2], acc[1][4 * t + 3]};
        team_arrive(cnt + C_RED + mh, lane);
        team_wait<0>(cnt + C_RED + mh, 2);
      }
      STAMP(5);
      float part;
      {
        constexpr float K1 = -2.0f * 1.4426950408889634f * 0.7978845608028654f, K2 = K1 * 0.044715f;
        const u32 si = L_W + (kh ^ 1) * 26 * 2048 + mh * 4096 + lane * 16;
        f32x4 o[4];
#pragma unroll
        for (int t = 0; t < 4; ++t) o[t] = *(LAS f32x4*)(lds + si + t * 1024);
        f32x2 h[8], u[8], y[8];
#pragma unroll
        for (int m = 0; m < 8; ++m) {
          h[m] = (f32x2){acc[0][2 * m], acc[0][2 * m + 1]} + (f32x2){o[m >> 1][2 * (m & 1)], o[m >> 1][2 * (m & 1) + 1]};
          u[m] = (h[m] * h[m] * K2 + K1) * h[m];
          y[m] = h[m] * wf[m];
        }
#pragma unroll
        for (int m = 0; m < 8; ++m) { u[m][0] = __builtin_amdgcn_exp2f(u[m][0]); u[m][1] = __builtin_amdgcn_exp2f(u[m][1]); }
#pragma unroll
        for (int m = 0; m < 8; ++m) { u[m] = u[m] + 1.0f; }
#pragma unroll
        for (int m = 0; m < 8; ++m) { u[m][0] = __builtin_amdgcn_rcpf(u[m][0]); u[m][1] = __builtin_amdgcn_rcpf(u[m][1]); }
        f32x2 p0 = y[0] * u[0], p1 = y[1] * u[1];
#pragma unroll
        for (int m = 2; m < 8; m += 2) { p0 += y[m] * u[m]; p1 += y[m + 1] * u[m + 1]; }
        p0 += p1;
        part = p0[0] + p0[1];
      }
      {
        auto sw = __builtin_amdgcn_permlane32_swap(__float_as_uint(part), __float_as_uint(part), false, false);
        part = __uint_as_float(sw[0]) + __uint_as_float(sw[1]);
      }
      {
        const u32x4 o = {__float_as_uint(part), tag, __float_as_uint(part), (u32)(flag >> 32)};
        __builtin_amdgcn_raw_buffer_store_b128(o, rws, lane < 32 ? WS_P + ((g * 26 + r * 2 + kh) * 2 + mh) * 512 + lane * 16 : OOB, 0, 16);
      }
      STAMP(6);
  } else {
      const int p = g * 3 + r - 13;
      const rsrc_t rwc = mkrs(a.wc, 3 * CUBE * 2), rwo = mkrs(a.wco, 78 * 2);
      __builtin_amdgcn_s_setprio(3);
      if (p < NF) {
        const int i = p;
        u32x4 tb[3];
        const int hb = (w >> 1) * 13, el = (w & 1) * 64 + lane;
        const CAS u32* w3p = (const CAS u32*)(a.wco + 52);
        u32 w3[13];
#pragma unroll
        for (int h = 0; h < 13; ++h) w3[h] = w3p[h];
        u32x4 v[13];
#pragma unroll
        for (int h = 0; h < 13; ++h) v[h] = __builtin_amdgcn_raw_buffer_load_b128(rwc, el < 85 ? (2 * CUBE + hb * 676 + 8 * el) * 2 : OOB, h * 676 * 2, 0);
        u32 v2[2];
#pragma unroll
        for (int m = 0; m < 2; ++m) {
          const u32 d = tid + 256 * m, h = (d * 5042u) >> 16, e = d - 13 * h;
          v2[m] = __builtin_amdgcn_raw_buffer_load_b32(rwc, d < 338 ? (CUBE + (h * NF + i) * NF) * 2 + e * 4 : OOB, 0, 0);
        }
        const u32 wq = __builtin_amdgcn_raw_buffer_load_b16(rwo, tid < NF ? (NF + tid) * 2 : OOB, 0, 0);
        const u32 w1i = __builtin_amdgcn_raw_buffer_load_b16(rwo, i * 2, 0, 0);
        if (w == 0) {
#pragma unroll
          for (int m = 0; m < 3; ++m) tb[m] = *(const GAS u32x4*)SYMTAB.w[lane + 64 * m];
        }
        __builtin_amdgcn_sched_barrier(0);
        lds_barrier();
        STAMP(1);
        {
          float acc[8];
#pragma unroll
          for (int e = 0; e < 8; ++e) acc[e] = 0.f;
#pragma unroll
          for (int h = 0; h < 13; ++h) {
            const u32 we = (h & 1) ? w3[h >> 1] >> 16 : w3[h >> 1] & 0xffffu;
            const u32 wo = (h & 1) ? w3[(13 + h) >> 1] & 0xffffu : w3[(13 + h) >> 1] >> 16;
            const u32 wl = hb ? wo : we, wh = wl << 16;
#pragma unroll
            for (int e = 0; e < 4; ++e) { acc[2 * e] = dot2bf(v[h][e], wl, acc[2 * e]); acc[2 * e + 1] = dot2bf(v[h][e], wh, acc[2 * e + 1]); }
          }
          const u32 vo = opq(el < 85 ? L_V3 + (w >> 1) * 2816 + el * 32 : L_V3 + 5632 + lane * 32);
          *(LAS f32x4*)(lds + vo) = (f32x4){acc[0], acc[1], acc[2], acc[3]};
          *(LAS f32x4*)(lds + vo + 16) = (f32x4){acc[4], acc[5], acc[6], acc[7]};
#pragma unroll
          for (int m = 0; m < 2; ++m) *(LAS u32*)(lds + L_W2S + (tid + 256 * m) * 4) = v2[m];
          if (w == 0) *(LAS u32*)(lds + L_W2C + lane * 4) = wq << 16;
        }
        const int tj = w >> 1, tp = w & 1, kq = lane >> 4;
        const int jr = 16 * tj + (lane & 15), jp = 16 * tp + (lane & 15);
        u32 oa[7], ob[7], oc[7];
#pragma unroll
        for (int s = 0; s < 7; ++s) {
          const int h = 4 * s + kq;
          const bool inv = h < NF && jp < NF;
          oa[s] = opq((h < NF && jr < NF) ? L_W2S + (h * NF + jr) * 2 : L_W2C + 63 * 4);
          ob[s] = opq(inv ? L_V3 + (h * NF + jp) * 4 : L_W2C + ((h < NF && jp == NF) ? h : 63) * 4);
          oc[s] = opq(inv ? L_V3 + 2816 + (h * NF + jp) * 4 : L_W2C + 63 * 4);
        }
        team_arrive(cnt + C_V3, lane);
        team_wait<0>(cnt + C_V3, 4);
        STAMP(2);
        u32 av[7]; float bv[7], bw[7];
#pragma unroll
        for (int s = 0; s < 7; ++s) { av[s] = *(LAS uint16_t*)(lds + oa[s]); bv[s] = *(LAS float*)(lds + ob[s]); bw[s] = *(LAS float*)(lds + oc[s]); }
#pragma unroll
        for (int s = 0; s < 7; ++s) bv[s] += bw[s];
        f32x4 u = {0.f, 0.f, 0.f, 0.f}, u2 = {0.f, 0.f, 0.f, 0.f};
#pragma unroll
        for (int s = 0; s < 7; ++s) {
          if (s & 1) u2 = __builtin_amdgcn_mfma_f32_16x16x4f32(bflo(av[s]), bv[s], u2, 0, 0, 0);
          else u = __builtin_amdgcn_mfma_f32_16x16x4f32(bflo(av[s]), bv[s], u, 0, 0, 0);
        }
        u += u2;
#pragma unroll
        for (int e = 0; e < 4; ++e) {
          const int jo = 16 * tj + 4 * kq + e;
          const float val = (jo == NF && jp == NF) ? bflo(w1i) : u[e];
          *(LAS float*)(lds + L_U + (jo * UP + jp) * 4) = val;
        }
        team_arrive(cnt + C_U, lane);
        if (w == 0) {
          team_wait<0>(cnt + C_U, 4);
          STAMP(3);
          LAS float* U = (LAS float*)(lds + L_U);
          float e[12];
#pragma unroll
          for (int m = 0; m < 3; ++m) {
            e[4 * m] = U[tb[m].x & 0xffff]; e[4 * m + 1] = U[tb[m].x >> 16]; e[4 * m + 2] = U[tb[m].y & 0xffff]; e[4 * m + 3] = U[tb[m].y >> 16];
          }
#pragma unroll
          for (int m = 0; m < 3; ++m)
            __builtin_amdgcn_raw_buffer_store_b32(packbf(e[4 * m] + e[4 * m + 1], e[4 * m + 2] + e[4 * m + 3]), rws, WS_PB + i * 768 + (lane + 64 * m) * 4, 0, 16);
          const u64 flagb = mkflag(get_tag(cnt));
          drain_vm();
          if (lane == 0) store_flag(a.ws, WS_FB + i * FL, flagb);
          STAMP(4);
        }
      } else {
        const int i = (p - NF) * 4 + w;
        const bool prod = p < 33 && i < NF;
        u32 v[6]; u32x4 tb[3];
#pragma unroll
        for (int m = 0; m < 6; ++m) { const int d = lane + 64 * m; v[m] = __builtin_amdgcn_raw_buffer_load_b32(rwc, (prod && d < 338) ? i * 1352 + d * 4 : OOB, 0, 0); }
#pragma unroll
        for (int m = 0; m < 3; ++m) tb[m] = *(const GAS u32x4*)SYMTAB.w[lane + 64 * m];
        __builtin_amdgcn_sched_barrier(0);
        lds_barrier();
        STAMP(1);
        if (prod) {
          LAS char* raw = lds + L_RAW + w * 2048;
#pragma unroll
          for (int m = 0; m < 6; ++m) *(LAS u32*)(raw + (lane + 64 * m) * 4) = v[m];
          asm volatile("s_waitcnt lgkmcnt(0)" ::: "memory");
          LAS uint16_t* R = (LAS uint16_t*)raw;
          u32 e[12];
#pragma unroll
          for (int m = 0; m < 3; ++m) {
            e[4 * m] = R[tb[m].z & 0xffff]; e[4 * m + 1] = R[tb[m].z >> 16]; e[4 * m + 2] = R[tb[m].w & 0xffff]; e[4 * m + 3] = R[tb[m].w >> 16];
          }
#pragma unroll
          for (int m = 0; m < 3; ++m)
            __builtin_amdgcn_raw_buffer_store_b32(packbf(bflo(e[4 * m]) + bflo(e[4 * m + 1]), bflo(e[4 * m + 2]) + bflo(e[4 * m + 3])), rws,
                                                  WS_PA + i * 768 + (lane + 64 * m) * 4, 0, 16);
          const u64 flag0 = mkflag(get_tag(cnt));
          drain_vm();
          if (lane == 0) store_flag(a.ws, WS_FA + i * FL, flag0);
          STAMP(4);
        }
        if (p == 47 && w == 0) {
          const u32 tag0 = get_tag(cnt);
          const u64 flag0 = mkflag(tag0);
          __builtin_amdgcn_s_sleep(32);
#pragma unroll 1
          for (int m = 0; m < 4; ++m) poll_flags<16>(a.ws + WS_XF + (lane + 64 * m) * FL, flag0);
          if (lane == 0) __hip_atomic_store((GAS u32*)(a.ws + WS_E), tag0, __ATOMIC_RELAXED, __HIP_MEMORY_SCOPE_AGENT);
        }
      }
  }
}

}

extern "C" void kernel_launch(void* const* d_in, const int* in_sizes, int n_in, void* d_out, int out_size,
                              void* d_ws, size_t ws_size, hipStream_t stream) {
  using namespace xdfm;
  Args a;
  a.idx = (const int*)d_in[0]; a.table = (const uint16_t*)d_in[1]; a.wc = (const uint16_t*)d_in[2];
  a.wco = (const uint16_t*)d_in[3]; a.w1 = (const uint16_t*)d_in[4]; a.b1 = (const uint16_t*)d_in[5];
  a.w2 = (const uint16_t*)d_in[6]; a.bo = (const uint16_t*)d_in[7]; a.out = (uint16_t*)d_out; a.ws = (char*)d_ws;
  a.table_bytes = (u32)in_sizes[1] * 2u; a.pad_ = 0;
  (void)hipFuncSetAttribute((const void*)xdfm_kernel, hipFuncAttributeMaxDynamicSharedMemorySize, L_TOTAL);
  xdfm_kernel<<<256, NWAVE * 64, L_TOTAL, stream>>>(a);
}
```

```cpp
#include <hip/hip_runtime.h>
#include <hip/hip_bf16.h>
#include <stdint.h>
#include <stdio.h>
#define STAMPS 1

#define P_ADELAY 8
#define P_BDELAY 8
#define P_WW 10
#define P_PQ 16
#define P_XI 10
#define P_XDELAY 12
#define P_CG 4
#define P_XW 12
namespace xdfm {

typedef unsigned int u32;
typedef unsigned long long u64;
typedef u32 u32x2 __attribute__((ext_vector_type(2)));
typedef u32 u32x4 __attribute__((ext_vector_type(4)));
typedef short s16x4 __attribute__((ext_vector_type(4)));
typedef short s16x8 __attribute__((ext_vector_type(8)));
typedef float f32x2 __attribute__((ext_vector_type(2)));
typedef float f32x4 __attribute__((ext_vector_type(4)));
typedef float f32x16 __attribute__((ext_vector_type(16)));
typedef __bf16 bf16x2 __attribute__((ext_vector_type(2)));
typedef __bf16 bf16x8 __attribute__((ext_vector_type(8)));
#define GAS __attribute__((address_space(1)))
#define LAS __attribute__((address_space(3)))
typedef __amdgpu_buffer_rsrc_t rsrc_t;

constexpr int NF = 26, DIN = 832, NB = 1024, CUBE = 17576;

constexpr u32 WS_E = 0;
constexpr u32 FL = 128;
constexpr u32 WS_XF = 4096;
constexpr u32 WS_FA = 40960;
constexpr u32 WS_FB = 45056;
constexpr u32 WS_PA = 49152;
constexpr u32 WS_PB = 69632;
constexpr u32 WS_P = 131072;
constexpr u32 WS_X = 655360;
constexpr u32 WS_END = WS_X + 16 * 104 * 1024;
constexpr int NWAVE = 9;
constexpr u32 WS_STAMP = 4u << 20;
constexpr int NSTAMP = 12;

constexpr u32 L_W = 0;
constexpr u32 APITCH = 432;
constexpr u32 L_A = 106496;
constexpr u32 L_B = L_A + 48 * APITCH;
constexpr u32 L_XS = L_B + 48 * APITCH;
constexpr u32 XS_S = 1824;
constexpr u32 L_CNT = L_XS + 4 * XS_S;
constexpr u32 L_TOTAL = L_CNT + 64;
constexpr u32 L_V3 = 0, L_W2C = 8192, L_W2S = 8448, L_U = 12288, L_RAW = 20480;
enum { C_TAGV = 12, C_TAG = 13, C_WISS = 14, C_ABRDY = 0, C_ABSTG = 1, C_W = 3, C_V3 = 4, C_U = 5, C_XISS = 6, C_XRDY = 7, C_WD = 8, C_RED = 10 };

constexpr int perm1(int j) { return j < 24 ? ((j & ~7) | ((j + 2) & 7)) : (j == 24 ? 25 : (j == 25 ? 24 : j)); }
struct Slots { int a[192]; int b[192]; bool v0[192]; bool v1[192]; int n; };
struct SlotBuilder {
  Slots s{}; bool vis[27][27] = {}; int n = 0;
  constexpr bool seen(int a, int b) const { return vis[a < b ? a : b][a < b ? b : a]; }
  constexpr void put(int a, int b) {
    s.a[n] = a; s.b[n] = b;
    s.v0[n] = !seen(a, b); vis[a < b ? a : b][a < b ? b : a] = true;
    const int na = perm1(a), nb = perm1(b);
    s.v1[n] = !seen(na, nb); vis[na < nb ? na : nb][na < nb ? nb : na] = true;
    ++n;
  }
};
constexpr int sg2(int m) { return perm1(perm1(2 * m)) >> 1; }
constexpr Slots make_slots() {
  SlotBuilder t{};
  for (int A = 0; A < 13; ++A)
    for (int B = A + 1; B < 13; ++B) {
      int ca = A, cb = B;
      while (!t.seen(2 * ca, 2 * cb)) {
        t.put(2 * ca, 2 * cb); t.put(2 * ca, 2 * cb + 1); t.put(2 * ca + 1, 2 * cb); t.put(2 * ca + 1, 2 * cb + 1);
        ca = sg2(ca); cb = sg2(cb);
      }
    }
  for (int A = 0; A < 13; ++A) {
    int ca = A;
    while (!t.seen(2 * ca, 2 * ca + 1)) {
      t.put(2 * ca, 2 * ca); t.put(2 * ca, 2 * ca + 1); t.put(2 * ca + 1, 2 * ca + 1); t.put(2 * ca + 1, 26);
      ca = sg2(ca);
    }
  }
  for (int a = 0; a < 27; ++a) { int ca = a; while (!t.seen(ca, 26)) { t.put(ca, 26); ca = perm1(perm1(ca)); } }
  t.s.n = t.n;
  for (int n = t.n; n < 192; ++n) { t.s.a[n] = 26; t.s.b[n] = 26; t.s.v0[n] = false; t.s.v1[n] = false; }
  return t.s;
}
constexpr Slots SL = make_slots();
static_assert(SL.n <= 192, "too many slots");

constexpr int UP = 33, ZB = 30 * UP, ZA = 676;
struct SymTab { u32 w[192][4]; };
constexpr SymTab make_symtab() {
  SymTab t{};
  for (int d = 0; d < 192; ++d) {
    int c = d >> 2, s = c >> 1, hh = c & 1;
    for (int e = 0; e < 2; ++e) {
      int sg = 8 * s + 2 * (d & 3) + e;
      int a = hh ? perm1(SL.a[sg]) : SL.a[sg], b = hh ? perm1(SL.b[sg]) : SL.b[sg];
      bool v = hh ? SL.v1[sg] : SL.v0[sg];
      u32 b1 = ZB, b2 = ZB, a1 = ZA, a2 = ZA;
      if (v) {
        b1 = a * UP + b; b2 = a == b ? ZB : b * UP + a;
        if (a < 26 && b < 26) { a1 = a * 26 + b; a2 = a == b ? ZA : b * 26 + a; }
      }
      t.w[d][e] = b1 | (b2 << 16);
      t.w[d][2 + e] = a1 | (a2 << 16);
    }
  }
  return t;
}
__device__ const SymTab SYMTAB = make_symtab();

constexpr u32 OOB = 0x7FFFFF00u;
constexpr u32 SPIN_MAX = 1u << 18;

__device__ __forceinline__ float bflo(u32 v) { return __uint_as_float(v << 16); }
__device__ __forceinline__ float bfhi(u32 v) { return __uint_as_float(v & 0xffff0000u); }
__device__ __forceinline__ u32 packbf(float a, float b) {
  f32x2 f = {a, b};
  bf16x2 z = __builtin_convertvector(f, bf16x2);
  return __builtin_bit_cast(u32, z);
}
__device__ __forceinline__ float mulf(float a, float b) { float r; asm("v_mul_f32 %0, %1, %2" : "=v"(r) : "v"(a), "v"(b)); return r; }
template <int E> __device__ __forceinline__ f32x2 pkmul_bc(f32x2 a, f32x2 b) {
  f32x2 r; asm("v_pk_mul_f32 %0, %1, %2 op_sel:[%3,0] op_sel_hi:[%3,1]" : "=v"(r) : "v"(a), "v"(b), "i"(E)); return r;
}
__device__ __forceinline__ float dot2bf(u32 a, u32 b, float c) {
  return __builtin_amdgcn_fdot2_f32_bf16(__builtin_bit_cast(bf16x2, a), __builtin_bit_cast(bf16x2, b), c, false);
}
__device__ __forceinline__ rsrc_t mkrs(const void* p, u32 bytes) { return __builtin_amdgcn_make_buffer_rsrc((void*)p, 0, bytes, 0x00020000); }
__device__ __forceinline__ u64 mkflag(u32 tag) { return (u64)tag | ((u64)(tag * 0x9E3779B1u + 0x7F4A7C15u) << 32); }
__device__ __forceinline__ void drain_vm() { asm volatile("s_waitcnt vmcnt(0)" ::: "memory"); }
__device__ __forceinline__ void lds_inc(LAS u32* cnt) {
  u64 keep;
  asm volatile("s_mov_b64 %0, exec\n\ts_mov_b64 exec, 1\n\tds_add_u32 %1, %2\n\ts_mov_b64 exec, %0"
               : "=&s"(keep) : "v"((u32)(size_t)cnt), "v"(1u) : "memory");
}
__device__ __forceinline__ void team_arrive(LAS u32* cnt, int) {
  asm volatile("s_waitcnt lgkmcnt(0)" ::: "memory");
  lds_inc(cnt);
}
__device__ __forceinline__ float sum_rows(float v) {
  auto a = __builtin_amdgcn_permlane32_swap(__float_as_uint(v), __float_as_uint(v), false, false);
  v = __uint_as_float(a[0]) + __uint_as_float(a[1]);
  auto b = __builtin_amdgcn_permlane16_swap(__float_as_uint(v), __float_as_uint(v), false, false);
  return __uint_as_float(b[0]) + __uint_as_float(b[1]);
}
template <int CTRL> __device__ __forceinline__ float dpp_add(float v) {
  return v + __uint_as_float(__builtin_amdgcn_update_dpp(0u, __float_as_uint(v), CTRL, 0xf, 0xf, true));
}
__device__ __forceinline__ float sum_row16(float v) {
  v = dpp_add<0x128>(v);
  v = dpp_add<0x124>(v);
  v = dpp_add<0x122>(v);
  v = dpp_add<0x121>(v);
  return v;
}

#define STAMP(k) do { __builtin_amdgcn_sched_barrier(0); asm volatile("" ::: "memory"); stv[k] = __builtin_amdgcn_s_memtime() - t00; asm volatile("s_waitcnt lgkmcnt(0)" ::: "memory"); __builtin_amdgcn_sched_barrier(0); } while (0)

struct Args {
  const int* idx; const uint16_t* table; const uint16_t* wc; const uint16_t* wco; const uint16_t* w1;
  const uint16_t* b1; const uint16_t* w2; const uint16_t* bo; uint16_t* out; char* ws;
  u32 table_bytes; u32 pad_;
};
#define CAS __attribute__((address_space(4)))

template <int SLEEP> __device__ __forceinline__ void team_wait(LAS u32* cnt, u32 target) {
  u32 v, s, n = SPIN_MAX;
  asm volatile(
      "1:\n\tds_read_b32 %0, %3\n\ts_waitcnt lgkmcnt(0)\n\tv_readfirstlane_b32 %1, %0\n\ts_cmp_ge_u32 %1, %4\n\ts_cbranch_scc1 2f\n\t"
      "s_sleep %5\n\ts_sub_u32 %2, %2, 1\n\ts_cbranch_scc0 1b\n2:"
      : "=&v"(v), "=&s"(s), "+s"(n) : "v"((u32)(size_t)cnt), "s"(target), "i"(SLEEP) : "memory", "scc");
}
template <int SLEEP> __device__ __forceinline__ void poll_flags(const char* p, u64 want) {
  u64 v; u32 n = SPIN_MAX;
  asm volatile(
      "1:\n\tglobal_load_dwordx2 %0, %2, off sc1\n\ts_waitcnt vmcnt(0)\n\tv_cmp_ne_u64 vcc, %3, %0\n\ts_nop 4\n\ts_cbranch_vccz 2f\n\t"
      "s_sleep %4\n\ts_sub_u32 %1, %1, 1\n\ts_cbranch_scc0 1b\n2:"
      : "=&v"(v), "+s"(n) : "v"(p), "s"(want), "i"(SLEEP) : "memory", "scc", "vcc");
}
__device__ __forceinline__ void lds_barrier() {
  asm volatile("s_waitcnt lgkmcnt(0)" ::: "memory");
  __builtin_amdgcn_s_barrier();
  asm volatile("" ::: "memory");
}
__device__ __forceinline__ u32 load_epoch(const char* ws) {
  return __hip_atomic_load((const GAS u32*)(ws + WS_E), __ATOMIC_RELAXED, __HIP_MEMORY_SCOPE_AGENT);
}
__device__ __forceinline__ void store_flag(char* ws, u32 off, u64 flag) {
  __hip_atomic_store((GAS u64*)(ws + off), flag, __ATOMIC_RELAXED, __HIP_MEMORY_SCOPE_AGENT);
}
__device__ __forceinline__ u32 get_tag(LAS u32* cnt) {
  team_wait<0>(cnt + C_TAG, 1);
  return __builtin_amdgcn_readfirstlane(cnt[C_TAGV]);
}
__device__ __forceinline__ u32 opq(u32 v) { asm("" : "+v"(v)); return v; }

__global__ void __launch_bounds__(576, 3) xdfm_kernel(Args a) {
  extern __shared__ __attribute__((aligned(16))) char lds_raw[];
  LAS char* lds = (LAS char*)lds_raw;
  const int tid = threadIdx.x, lane = tid & 63;
  const int w = __builtin_amdgcn_readfirstlane(tid >> 6);
  const int b = blockIdx.x, j = b >> 3;
  const int jg = j < 6 ? (j >= 3 ? 1 : 0) : (j >= 19 ? 1 : 0);
  const int g = (b & 7) * 2 + jg, r = j < 6 ? 13 + j - 3 * jg : j - 6 - 13 * jg;
  LAS u32* cnt = (LAS u32*)(lds + L_CNT);
  asm volatile("" :: "s"(a.idx), "s"(a.table), "s"(a.wc), "s"(a.wco), "s"(a.w1), "s"(a.b1), "s"(a.w2), "s"(a.bo), "s"(a.out), "s"(a.ws), "s"(a.table_bytes));
  u64 stv[NSTAMP];
#pragma unroll
  for (int k = 0; k < NSTAMP; ++k) stv[k] = 0;
  stv[0] = __builtin_amdgcn_s_memrealtime();
  const u64 t00 = __builtin_amdgcn_s_memtime();
  const rsrc_t rws = mkrs(a.ws, WS_END);
  const rsrc_t ridx = mkrs(a.idx, NB * NF * 4), rtab = mkrs(a.table, a.table_bytes);

  if (w == 8) {
    __builtin_amdgcn_s_setprio(3);
    if (lane < 16) cnt[lane] = 0;
    u32 tag;
    if (r < 13) {
      u32 id[4]; u32x4 xg[8];
#pragma unroll
      for (int m = 0; m < 2; ++m) {
        const int p0 = 4 * r + 2 * m, t = p0 >= NF ? 1 : 0, f0 = p0 - t * NF;
        const u32x2 ia = __builtin_amdgcn_raw_buffer_load_b64(ridx, (lane & 31) * (NF * 4), ((g * 64 + t * 32) * NF + f0) * 4, 0);
        id[2 * m] = ia.x; id[2 * m + 1] = ia.y;
      }
      const u32 ep = load_epoch(a.ws);
#pragma unroll
      for (int k = 0; k < 8; ++k) xg[k] = __builtin_amdgcn_raw_buffer_load_b128(rtab, id[k >> 1] * 64 + (k & 1) * 32 + (lane >> 5) * 16, 0, 0);
      lds_barrier();
      STAMP(1);
      tag = __builtin_amdgcn_readfirstlane(ep) + 1u;
      cnt[C_TAGV] = tag;
      team_arrive(cnt + C_TAG, lane);
#pragma unroll
      for (int k = 0; k < 8; ++k) __builtin_amdgcn_raw_buffer_store_b128(xg[k], rws, WS_X + lane * 16, (g * 104 + r * 8 + k) * 1024, 16);
      STAMP(2);
      drain_vm();
    } else {
      const u32 ep = load_epoch(a.ws);
      lds_barrier();
      tag = __builtin_amdgcn_readfirstlane(ep) + 1u;
      cnt[C_TAGV] = tag;
      team_arrive(cnt + C_TAG, lane);
    }
    const u64 flag = mkflag(tag);
    if (lane == 0) store_flag(a.ws, WS_XF + (g * 16 + r) * FL, flag);
    STAMP(3);
    if (r < 13) {
      __builtin_amdgcn_s_sleep(P_XDELAY);
      poll_flags<2>(a.ws + WS_XF + (g * 16 + (lane < 13 ? lane : 0)) * FL, flag);
      lds_inc(cnt + C_XRDY);
    }
    STAMP(4);
    __builtin_amdgcn_s_sleep(P_BDELAY);
    poll_flags<6>(a.ws + (lane < 32 ? WS_FA : WS_FB) + ((lane & 31) < NF ? (lane & 31) : 0) * FL, flag);
    lds_inc(cnt + C_ABRDY);
    STAMP(5);
  } else if (w >= 4) {
    const int cw = w - 4, smp = g * 64 + r * 4 + cw;
    u32 id[2]; u32x4 xr[2];
    u32 bo;
    {
      const CAS u32* ip = (const CAS u32*)(a.idx + smp * NF);
      u32 is[NF];
#pragma unroll
      for (int f = 0; f < NF; ++f) is[f] = ip[f];
      const size_t bop = (size_t)a.bo;
      bo = *(const CAS u32*)(bop & ~(size_t)3) >> ((bop & 2) * 8);
      u32 iv = 0;
      asm("v_writelane_b32 %0, %1, 0\n\tv_writelane_b32 %0, %2, 1\n\tv_writelane_b32 %0, %3, 2\n\tv_writelane_b32 %0, %4, 3\n\tv_writelane_b32 %0, %5, 4\n\tv_writelane_b32 %0, %6, 5\n\tv_writelane_b32 %0, %7, 6\n\tv_writelane_b32 %0, %8, 7\n\tv_writelane_b32 %0, %9, 8\n\tv_writelane_b32 %0, %10, 9\n\tv_writelane_b32 %0, %11, 10\n\tv_writelane_b32 %0, %12, 11\n\tv_writelane_b32 %0, %13, 12\n\tv_writelane_b32 %0, %14, 13\n\tv_writelane_b32 %0, %15, 14\n\tv_writelane_b32 %0, %16, 15\n\tv_writelane_b32 %0, %17, 16\n\tv_writelane_b32 %0, %18, 17\n\tv_writelane_b32 %0, %19, 18\n\tv_writelane_b32 %0, %20, 19\n\tv_writelane_b32 %0, %21, 20\n\tv_writelane_b32 %0, %22, 21\n\tv_writelane_b32 %0, %23, 22\n\tv_writelane_b32 %0, %24, 23\n\tv_writelane_b32 %0, %25, 24\n\tv_writelane_b32 %0, %26, 25"
          : "+v"(iv) : "s"(is[0]), "s"(is[1]), "s"(is[2]), "s"(is[3]), "s"(is[4]), "s"(is[5]), "s"(is[6]), "s"(is[7]), "s"(is[8]), "s"(is[9]), "s"(is[10]), "s"(is[11]), "s"(is[12]), "s"(is[13]), "s"(is[14]), "s"(is[15]), "s"(is[16]), "s"(is[17]), "s"(is[18]), "s"(is[19]), "s"(is[20]), "s"(is[21]), "s"(is[22]), "s"(is[23]), "s"(is[24]), "s"(is[25]));
      id[0] = __builtin_amdgcn_ds_bpermute((lane >> 2) * 4, iv);
      id[1] = __builtin_amdgcn_ds_bpermute((16 + (lane >> 2)) * 4, iv);
    }
    if (r < 13) __builtin_amdgcn_s_setprio(2);
    lds_barrier();
#pragma unroll
    for (int m = 0; m < 2; ++m) { const int f = 16 * m + (lane >> 2); xr[m] = __builtin_amdgcn_raw_buffer_load_b128(rtab, f < NF ? id[m] * 64 + (lane & 3) * 16 : OOB, 0, 0); }
    __builtin_amdgcn_s_setprio(0);
    STAMP(1);
    f32x2 xp[14];
    {
      const u32 o = L_XS + cw * XS_S + (lane >> 2) * 64 + (lane & 3) * 16;
      *(LAS u32x4*)(lds + o) = xr[0];
      u32x4 x1 = xr[1];
      if ((lane >> 2) == NF - 16) x1 = (u32x4){0x3F803F80u, 0x3F803F80u, 0x3F803F80u, 0x3F803F80u};
      *(LAS u32x4*)(lds + opq((lane >> 2) < 12 ? o + 1024 : L_XS + cw * XS_S + 27 * 64 + (lane & 3) * 16)) = x1;
      asm volatile("s_waitcnt lgkmcnt(0)" ::: "memory");
      const int hh = lane >> 5, q = (lane >> 2) & 3, pp = lane & 3;
      const u32 base = L_XS + cw * XS_S + ((lane >> 4) & 1) * 32 + pp * 8;
      const u32 a0 = base + (hh ? q + 2 : q) * 64, a1 = base + (hh ? ((q + 6) & 7) : q + 4) * 64;
      const u32 a6 = base + (24 + ((hh && q < 2) ? (q ^ 1) : q)) * 64;
#pragma unroll
      for (int t = 0; t < 7; ++t) {
        s16x4 y = __builtin_amdgcn_ds_read_tr16_b64_v4i16((LAS s16x4*)(lds + (t < 6 ? ((t & 1) ? a1 : a0) + (t >> 1) * 512 : a6)));
        u32x2 u = __builtin_bit_cast(u32x2, y);
        xp[2 * t] = (f32x2){bflo(u.x), bfhi(u.x)}; xp[2 * t + 1] = (f32x2){bflo(u.y), bfhi(u.y)};
      }
    }
    bf16x8 z[24];
#pragma unroll
    for (int s = 0; s < 24; ++s) {
      u32x4 zz;
#pragma unroll
      for (int e = 0; e < 4; ++e) {
        const int s1 = 8 * s + 2 * e, s2 = s1 + 1;
        const int a1 = SL.a[s1], b1 = SL.b[s1], a2 = SL.a[s2], b2 = SL.b[s2];
        if (a1 == a2 && (b1 & 1) == 0 && b1 < 26 && b2 == b1 + 1) {
          const f32x2 pr = (a1 & 1) ? pkmul_bc<1>(xp[a1 >> 1], xp[b1 >> 1]) : pkmul_bc<0>(xp[a1 >> 1], xp[b1 >> 1]);
          zz[e] = packbf(pr[0], pr[1]);
        } else {
          const float p1 = b1 == 26 ? xp[a1 >> 1][a1 & 1] : mulf(xp[a1 >> 1][a1 & 1], xp[b1 >> 1][b1 & 1]);
          const float p2 = b2 == 26 ? xp[a2 >> 1][a2 & 1] : mulf(xp[a2 >> 1][a2 & 1], xp[b2 >> 1][b2 & 1]);
          zz[e] = packbf(p1, p2);
        }
      }
      z[s] = __builtin_bit_cast(bf16x8, zz);
      asm volatile("" : "+v"(z[s]));
    }
    const u32 tag = get_tag(cnt);
    const u64 flag = mkflag(tag);
    STAMP(2);
    team_wait<1>(cnt + C_ABRDY, 1);
    STAMP(3);
    if (r < 13) team_wait<1>(cnt + C_XISS, 4);
    __builtin_amdgcn_s_setprio(2);
    STAMP(4);
    {
      u32x4 y[10];
#pragma unroll
      for (int m = 0; m < 10; ++m) {
        const u32 n = ((m % 5) * 4 + cw) * 64 + lane;
        y[m] = __builtin_amdgcn_raw_buffer_load_b128(rws, n < NF * 48 ? n * 16 : OOB, m < 5 ? WS_PA : WS_PB, 16);
      }
#pragma unroll
      for (int m = 0; m < 5; ++m) {
        const u32 n = (m * 4 + cw) * 64 + lane, i = (n * 1366u) >> 16, c = n - i * 48;
        const u32 o = opq(n < NF * 48 ? L_A + c * APITCH + i * 16 : L_A + 26 * 16);
        *(LAS u32x4*)(lds + o) = y[m];
        *(LAS u32x4*)(lds + o + (L_B - L_A)) = y[5 + m];
      }
      team_arrive(cnt + C_ABSTG, lane);
      team_wait<0>(cnt + C_ABSTG, 4);
    }
    __builtin_amdgcn_s_setprio(0);
    STAMP(5);
    const u32 afr = (lane >> 5) * APITCH + (lane & 31) * 16;
    const u32 tag2 = (u32)(flag >> 32);
    const int sg = r * 4 + cw;
    const u32 po = opq(lane < 26 ? WS_P + ((g * 26 + lane) * 2 + (sg >> 5)) * 512 + (sg & 31) * 16 : OOB);
    u32x4 yp;
    f32x16 acc1 = {}, acc2 = {};
    {
      constexpr int AD = 2;
      bf16x8 af[24], bf[24];
#pragma unroll
      for (int s = 0; s < AD; ++s) { af[s] = *(LAS bf16x8*)(lds + L_A + afr + s * 2 * APITCH); bf[s] = *(LAS bf16x8*)(lds + L_B + afr + s * 2 * APITCH); }
      __builtin_amdgcn_sched_barrier(0);
#pragma unroll
      for (int s = 0; s < 24; ++s) {
        if (s + AD < 24) { af[s + AD] = *(LAS bf16x8*)(lds + L_A + afr + (s + AD) * 2 * APITCH); bf[s + AD] = *(LAS bf16x8*)(lds + L_B + afr + (s + AD) * 2 * APITCH); }
        if (s == P_PQ) yp = __builtin_amdgcn_raw_buffer_load_b128(rws, po, 0, 16);
        acc1 = __builtin_amdgcn_mfma_f32_32x32x16_bf16(af[s], z[s], acc1, 0, 0, 0);
        acc2 = __builtin_amdgcn_mfma_f32_32x32x16_bf16(bf[s], z[s], acc2, 0, 0, 0);
        __builtin_amdgcn_sched_barrier(0);
      }
    }
    float sum = 0.f;
#pragma unroll
    for (int e = 0; e < 12; ++e) sum += acc1[e] * acc2[e];
    { const float t = acc1[12] * acc2[12] + acc1[13] * acc2[13]; sum += lane < 32 ? t : 0.f; }
    sum = sum_rows(sum_row16(sum));
    STAMP(6);
    for (u32 it = 0; it < SPIN_MAX; ++it) {
      const bool ok = lane >= 26 || (yp.y == tag && yp.w == tag2);
      if (__all(ok)) break;
      asm volatile("" ::: "memory");
      __builtin_amdgcn_s_sleep(1);
      yp = __builtin_amdgcn_raw_buffer_load_b128(rws, po, 0, 16);
    }
    float part = sum_rows(sum_row16(__uint_as_float(yp.x)));
    if (lane == 0) {
      const __bf16 ob = (__bf16)(sum + part + bflo(bo));
      a.out[smp] = __builtin_bit_cast(uint16_t, ob);
    }
    STAMP(7);
  } else if (r < 13) {
      const rsrc_t rw1 = mkrs(a.w1, DIN * DIN * 2);
      const int mh = w & 1, kh = w >> 1, hh = lane >> 5;
      lds_barrier();
      STAMP(1);
      {
        u32x4 v[26];
        const u32 vo = (w * 208 + (lane >> 3)) * 1664 + r * 128 + (lane & 7) * 16;
        const int c = lane & 7;
        const u32 lo = L_W + (w * 208 + (lane >> 3)) * 128 + (((c >> 1) ^ (((lane >> 4) & 1) << 1)) * 32) + (c & 1) * 16;
#pragma unroll
        for (int i = 0; i < 26; ++i) v[i] = __builtin_amdgcn_raw_buffer_load_b128(rw1, vo, i * 8 * 1664, 0);
#pragma unroll
        for (int i = 0; i < 26; ++i) *(LAS u32x4*)(lds + lo + i * 1024) = v[i];
      }
      team_arrive(cnt + C_W, lane);
      const u32 tag = get_tag(cnt);
      const u64 flag = mkflag(tag);
      f32x16 acc[2];
      f32x2 wf[8];
      {
        const CAS u32* bp = (const CAS u32*)(a.b1 + r * 64 + kh * 32);
        const CAS u32* wp = (const CAS u32*)(a.w2 + r * 64 + kh * 32);
        u32 bs[16], ws_[16];
#pragma unroll
        for (int t = 0; t < 16; ++t) { bs[t] = bp[t]; ws_[t] = wp[t]; }
#pragma unroll
        for (int t = 0; t < 16; ++t) asm volatile("" : "+s"(bs[t]), "+s"(ws_[t]));
#pragma unroll
        for (int t = 0; t < 4; ++t) {
          const u32 b0 = hh ? bs[4 * t + 2] : bs[4 * t], b1 = hh ? bs[4 * t + 3] : bs[4 * t + 1];
          const u32 w0 = hh ? ws_[4 * t + 2] : ws_[4 * t], w1 = hh ? ws_[4 * t + 3] : ws_[4 * t + 1];
          acc[0][4 * t] = bflo(b0); acc[0][4 * t + 1] = bfhi(b0); acc[0][4 * t + 2] = bflo(b1); acc[0][4 * t + 3] = bfhi(b1);
          acc[1][4 * t] = 0.f; acc[1][4 * t + 1] = 0.f; acc[1][4 * t + 2] = 0.f; acc[1][4 * t + 3] = 0.f;
          wf[2 * t] = (f32x2){bflo(w0), bfhi(w0)}; wf[2 * t + 1] = (f32x2){bflo(w1), bfhi(w1)};
          asm volatile("" : "+v"(wf[2 * t]), "+v"(wf[2 * t + 1]));
        }
      }
      STAMP(2);
      team_wait<0>(cnt + C_XRDY, 1);
      STAMP(3);
      constexpr int XW = P_XW, FD = 3;
      u32x4 xv[26];
      auto ldx = [&](int i) { xv[i] = __builtin_amdgcn_raw_buffer_load_b128(rws, WS_X + lane * 16, (g * 104 + mh * 52 + kh * 26 + i) * 1024, 16); };
#pragma unroll
      for (int i = 0; i < XW; ++i) ldx(i);
      team_wait<0>(cnt + C_W, 4);
      __builtin_amdgcn_s_setprio(1);
      {
        const int q = (lane >> 2) & 3, pp = lane & 3, gam = (lane >> 4) & 1;
        const u32 rb = L_W + kh * 26 * 2048 + (8 * hh + q) * 128 + pp * 8;
        const u32 ad0 = rb + (((2 * kh + gam) ^ (q & 2)) * 32), ad1 = rb + (((2 * (kh ^ 1) + gam) ^ (q & 2)) * 32);
        bf16x8 fr[26][2];
        auto ldf = [&](int i) {
#pragma unroll
          for (int t = 0; t < 2; ++t) {
            const u32 o = (t ? ad1 : ad0) + i * 2048;
            const s16x4 y0 = __builtin_amdgcn_ds_read_tr16_b64_v4i16((LAS s16x4*)(lds + o));
            const s16x4 y1 = __builtin_amdgcn_ds_read_tr16_b64_v4i16((LAS s16x4*)(lds + o + 512));
            fr[i][t] = __builtin_bit_cast(bf16x8, __builtin_shufflevector(y0, y1, 0, 1, 2, 3, 4, 5, 6, 7));
          }
        };
#pragma unroll
        for (int i = 0; i < FD; ++i) ldf(i);
        __builtin_amdgcn_sched_barrier(0);
#pragma unroll
        for (int i = 0; i < 26; ++i) {
          if (i + XW < 26) ldx(i + XW);
          if (i == P_XI) lds_inc(cnt + C_XISS);
          if (i == 8) lds_inc(cnt + C_WD + kh);
          if (i == 22) team_wait<0>(cnt + C_WD + kh, 2);
          if (i + FD < 26) ldf(i + FD);
          const bf16x8 xb = __builtin_bit_cast(bf16x8, xv[i]);
          acc[0] = __builtin_amdgcn_mfma_f32_32x32x16_bf16(fr[i][0], xb, acc[0], 0, 0, 0);
          acc[1] = __builtin_amdgcn_mfma_f32_32x32x16_bf16(fr[i][1], xb, acc[1], 0, 0, 0);
          __builtin_amdgcn_sched_barrier(0);
        }
      }
      STAMP(4);
      {
        const u32 so = L_W + kh * 26 * 2048 + mh * 4096 + lane * 16;
#pragma unroll
        for (int t = 0; t < 4; ++t) *(LAS f32x4*)(lds + so + t * 1024) = (f32x4){acc[1][4 * t], acc[1][4 * t + 1], acc[1][4 * t + 2], acc[1][4 * t + 3]};
        team_arrive(cnt + C_RED + mh, lane);
        team_wait<0>(cnt + C_RED + mh, 2);
      }
      STAMP(5);
      float part;
      {
        constexpr float K1 = -2.0f * 1.4426950408889634f * 0.7978845608028654f, K2 = K1 * 0.044715f;
        const u32 si = L_W + (kh ^ 1) * 26 * 2048 + mh * 4096 + lane * 16;
        f32x4 o[4];
#pragma unroll
        for (int t = 0; t < 4; ++t) o[t] = *(LAS f32x4*)(lds + si + t * 1024);
        f32x2 h[8], u[8], y[8];
#pragma unroll
        for (int m = 0; m < 8; ++m) {
          h[m] = (f32x2){acc[0][2 * m], acc[0][2 * m + 1]} + (f32x2){o[m >> 1][2 * (m & 1)], o[m >> 1][2 * (m & 1) + 1]};
          u[m] = (h[m] * h[m] * K2 + K1) * h[m];
          y[m] = h[m] * wf[m];
        }
#pragma unroll
        for (int m = 0; m < 8; ++m) { u[m][0] = __builtin_amdgcn_exp2f(u[m][0]); u[m][1] = __builtin_amdgcn_exp2f(u[m][1]); }
#pragma unroll
        for (int m = 0; m < 8; ++m) { u[m] = u[m] + 1.0f; }
#pragma unroll
        for (int m = 0; m < 8; ++m) { u[m][0] = __builtin_amdgcn_rcpf(u[m][0]); u[m][1] = __builtin_amdgcn_rcpf(u[m][1]); }
        f32x2 p0 = y[0] * u[0], p1 = y[1] * u[1];
#pragma unroll
        for (int m = 2; m < 8; m += 2) { p0 += y[m] * u[m]; p1 += y[m + 1] * u[m + 1]; }
        p0 += p1;
        part = p0[0] + p0[1];
      }
      {
        auto sw = __builtin_amdgcn_permlane32_swap(__float_as_uint(part), __float_as_uint(part), false, false);
        part = __uint_as_float(sw[0]) + __uint_as_float(sw[1]);
      }
      {
        const u32x4 o = {__float_as_uint(part), tag, __float_as_uint(part), (u32)(flag >> 32)};
        __builtin_amdgcn_raw_buffer_store_b128(o, rws, lane < 32 ? WS_P + ((g * 26 + r * 2 + kh) * 2 + mh) * 512 + lane * 16 : OOB, 0, 16);
      }
      STAMP(6);
  } else {
      const int p = g * 3 + r - 13;
      const rsrc_t rwc = mkrs(a.wc, 3 * CUBE * 2), rwo = mkrs(a.wco, 78 * 2);
      __builtin_amdgcn_s_setprio(3);
      if (p < NF) {
        const int i = p;
        u32x4 tb[3];
        const int hb = (w >> 1) * 13, el = (w & 1) * 64 + lane;
        const CAS u32* w3p = (const CAS u32*)(a.wco + 52);
        u32 w3[13];
#pragma unroll
        for (int h = 0; h < 13; ++h) w3[h] = w3p[h];
        u32x4 v[13];
#pragma unroll
        for (int h = 0; h < 13; ++h) v[h] = __builtin_amdgcn_raw_buffer_load_b128(rwc, el < 85 ? (2 * CUBE + hb * 676 + 8 * el) * 2 : OOB, h * 676 * 2, 0);
        u32 v2[2];
#pragma unroll
        for (int m = 0; m < 2; ++m) {
          const u32 d = tid + 256 * m, h = (d * 5042u) >> 16, e = d - 13 * h;
          v2[m] = __builtin_amdgcn_raw_buffer_load_b32(rwc, d < 338 ? (CUBE + (h * NF + i) * NF) * 2 + e * 4 : OOB, 0, 0);
        }
        const u32 wq = __builtin_amdgcn_raw_buffer_load_b16(rwo, tid < NF ? (NF + tid) * 2 : OOB, 0, 0);
        const u32 w1i = __builtin_amdgcn_raw_buffer_load_b16(rwo, i * 2, 0, 0);
        if (w == 0) {
#pragma unroll
          for (int m = 0; m < 3; ++m) tb[m] = *(const GAS u32x4*)SYMTAB.w[lane + 64 * m];
        }
        __builtin_amdgcn_sched_barrier(0);
        lds_barrier();
        STAMP(1);
        {
          float acc[8];
#pragma unroll
          for (int e = 0; e < 8; ++e) acc[e] = 0.f;
#pragma unroll
          for (int h = 0; h < 13; ++h) {
            const u32 we = (h & 1) ? w3[h >> 1] >> 16 : w3[h >> 1] & 0xffffu;
            const u32 wo = (h & 1) ? w3[(13 + h) >> 1] & 0xffffu : w3[(13 + h) >> 1] >> 16;
            const u32 wl = hb ? wo : we, wh = wl << 16;
#pragma unroll
            for (int e = 0; e < 4; ++e) { acc[2 * e] = dot2bf(v[h][e], wl, acc[2 * e]); acc[2 * e + 1] = dot2bf(v[h][e], wh, acc[2 * e + 1]); }
          }
          const u32 vo = opq(el < 85 ? L_V3 + (w >> 1) * 2816 + el * 32 : L_V3 + 5632 + lane * 32);
          *(LAS f32x4*)(lds + vo) = (f32x4){acc[0], acc[1], acc[2], acc[3]};
          *(LAS f32x4*)(lds + vo + 16) = (f32x4){acc[4], acc[5], acc[6], acc[7]};
#pragma unroll
          for (int m = 0; m < 2; ++m) *(LAS u32*)(lds + L_W2S + (tid + 256 * m) * 4) = v2[m];
          if (w == 0) *(LAS u32*)(lds + L_W2C + lane * 4) = wq << 16;
        }
        const int tj = w >> 1, tp = w & 1, kq = lane >> 4;
        const int jr = 16 * tj + (lane & 15), jp = 16 * tp + (lane & 15);
        u32 oa[7], ob[7], oc[7];
#pragma unroll
        for (int s = 0; s < 7; ++s) {
          const int h = 4 * s + kq;
          const bool inv = h < NF && jp < NF;
          oa[s] = opq((h < NF && jr < NF) ? L_W2S + (h * NF + jr) * 2 : L_W2C + 63 * 4);
          ob[s] = opq(inv ? L_V3 + (h * NF + jp) * 4 : L_W2C + ((h < NF && jp == NF) ? h : 63) * 4);
          oc[s] = opq(inv ? L_V3 + 2816 + (h * NF + jp) * 4 : L_W2C + 63 * 4);
        }
        team_arrive(cnt + C_V3, lane);
        team_wait<0>(cnt + C_V3, 4);
        STAMP(2);
        u32 av[7]; float bv[7], bw[7];
#pragma unroll
        for (int s = 0; s < 7; ++s) { av[s] = *(LAS uint16_t*)(lds + oa[s]); bv[s] = *(LAS float*)(lds + ob[s]); bw[s] = *(LAS float*)(lds + oc[s]); }
#pragma unroll
        for (int s = 0; s < 7; ++s) bv[s] += bw[s];
        f32x4 u = {0.f, 0.f, 0.f, 0.f}, u2 = {0.f, 0.f, 0.f, 0.f};
#pragma unroll
        for (int s = 0; s < 7; ++s) {
          if (s & 1) u2 = __builtin_amdgcn_mfma_f32_16x16x4f32(bflo(av[s]), bv[s], u2, 0, 0, 0);
          else u = __builtin_amdgcn_mfma_f32_16x16x4f32(bflo(av[s]), bv[s], u, 0, 0, 0);
        }
        u += u2;
#pragma unroll
        for (int e = 0; e < 4; ++e) {
          const int jo = 16 * tj + 4 * kq + e;
          const float val = (jo == NF && jp == NF) ? bflo(w1i) : u[e];
          *(LAS float*)(lds + L_U + (jo * UP + jp) * 4) = val;
        }
        team_arrive(cnt + C_U, lane);
        if (w == 0) {
          team_wait<0>(cnt + C_U, 4);
          STAMP(3);
          LAS float* U = (LAS float*)(lds + L_U);
          float e[12];
#pragma unroll
          for (int m = 0; m < 3; ++m) {
            e[4 * m] = U[tb[m].x & 0xffff]; e[4 * m + 1] = U[tb[m].x >> 16]; e[4 * m + 2] = U[tb[m].y & 0xffff]; e[4 * m + 3] = U[tb[m].y >> 16];
          }
#pragma unroll
          for (int m = 0; m < 3; ++m)
            __builtin_amdgcn_raw_buffer_store_b32(packbf(e[4 * m] + e[4 * m + 1], e[4 * m + 2] + e[4 * m + 3]), rws, WS_PB + i * 768 + (lane + 64 * m) * 4, 0, 16);
          const u64 flagb = mkflag(get_tag(cnt));
          drain_vm();
          if (lane == 0) store_flag(a.ws, WS_FB + i * FL, flagb);
          STAMP(4);
        }
      } else {
        const int i = (p - NF) * 4 + w;
        const bool prod = p < 33 && i < NF;
        u32 v[6]; u32x4 tb[3];
#pragma unroll
        for (int m = 0; m < 6; ++m) { const int d = lane + 64 * m; v[m] = __builtin_amdgcn_raw_buffer_load_b32(rwc, (prod && d < 338) ? i * 1352 + d * 4 : OOB, 0, 0); }
#pragma unroll
        for (int m = 0; m < 3; ++m) tb[m] = *(const GAS u32x4*)SYMTAB.w[lane + 64 * m];
        __builtin_amdgcn_sched_barrier(0);
        lds_barrier();
        STAMP(1);
        if (prod) {
          LAS char* raw = lds + L_RAW + w * 2048;
#pragma unroll
          for (int m = 0; m < 6; ++m) *(LAS u32*)(raw + (lane + 64 * m) * 4) = v[m];
          asm volatile("s_waitcnt lgkmcnt(0)" ::: "memory");
          LAS uint16_t* R = (LAS uint16_t*)raw;
          u32 e[12];
#pragma unroll
          for (int m = 0; m < 3; ++m) {
            e[4 * m] = R[tb[m].z & 0xffff]; e[4 * m + 1] = R[tb[m].z >> 16]; e[4 * m + 2] = R[tb[m].w & 0xffff]; e[4 * m + 3] = R[tb[m].w >> 16];
          }
#pragma unroll
          for (int m = 0; m < 3; ++m)
            __builtin_amdgcn_raw_buffer_store_b32(packbf(bflo(e[4 * m]) + bflo(e[4 * m + 1]), bflo(e[4 * m + 2]) + bflo(e[4 * m + 3])), rws,
                                                  WS_PA + i * 768 + (lane + 64 * m) * 4, 0, 16);
          const u64 flag0 = mkflag(get_tag(cnt));
          drain_vm();
          if (lane == 0) store_flag(a.ws, WS_FA + i * FL, flag0);
          STAMP(4);
        }
        if (p == 47 && w == 0) {
          const u32 tag0 = get_tag(cnt);
          const u64 flag0 = mkflag(tag0);
          __builtin_amdgcn_s_sleep(32);
#pragma unroll 1
          for (int m = 0; m < 4; ++m) poll_flags<16>(a.ws + WS_XF + (lane + 64 * m) * FL, flag0);
          if (lane == 0) __hip_atomic_store((GAS u32*)(a.ws + WS_E), tag0, __ATOMIC_RELAXED, __HIP_MEMORY_SCOPE_AGENT);
        }
      }
  }
  stv[NSTAMP - 1] = __builtin_amdgcn_s_memrealtime();
  if (lane == 0 && (b == 9 || b == 50 || b == 83 || b == 250 || b == 2))
    printf("b%d w%d r%d rt0 %llu dur_rt %llu | %llu %llu %llu %llu %llu %llu %llu %llu\n", b, w, r, stv[0] % 100000, stv[NSTAMP - 1] - stv[0],
           stv[1], stv[2], stv[3], stv[4], stv[5], stv[6], stv[7], stv[8]);
}

}

extern "C" void kernel_launch(void* const* d_in, const int* in_sizes, int n_in, void* d_out, int out_size,
                              void* d_ws, size_t ws_size, hipStream_t stream) {
  using namespace xdfm;
  Args a;
  a.idx = (const int*)d_in[0]; a.table = (const uint16_t*)d_in[1]; a.wc = (const uint16_t*)d_in[2];
  a.wco = (const uint16_t*)d_in[3]; a.w1 = (const uint16_t*)d_in[4]; a.b1 = (const uint16_t*)d_in[5];
  a.w2 = (const uint16_t*)d_in[6]; a.bo = (const uint16_t*)d_in[7]; a.out = (uint16_t*)d_out; a.ws = (char*)d_ws;
  a.table_bytes = (u32)in_sizes[1] * 2u; a.pad_ = 0;
  (void)hipFuncSetAttribute((const void*)xdfm_kernel, hipFuncAttributeMaxDynamicSharedMemorySize, L_TOTAL);
  xdfm_kernel<<<256, NWAVE * 64, L_TOTAL, stream>>>(a);
}
```

```cpp
#include <hip/hip_runtime.h>
#include <hip/hip_bf16.h>
#include <stdint.h>

#define P_ADELAY 8
#define P_BDELAY 8
#define P_WW 10
#define P_PQ 16
#define P_XI 10
#define P_XDELAY 12
#define P_CG 4
#define P_XW 12
namespace xdfm {

typedef unsigned int u32;
typedef unsigned long long u64;
typedef u32 u32x2 __attribute__((ext_vector_type(2)));
typedef u32 u32x4 __attribute__((ext_vector_type(4)));
typedef short s16x4 __attribute__((ext_vector_type(4)));
typedef short s16x8 __attribute__((ext_vector_type(8)));
typedef float f32x2 __attribute__((ext_vector_type(2)));
typedef float f32x4 __attribute__((ext_vector_type(4)));
typedef float f32x16 __attribute__((ext_vector_type(16)));
typedef __bf16 bf16x2 __attribute__((ext_vector_type(2)));
typedef __bf16 bf16x8 __attribute__((ext_vector_type(8)));
#define GAS __attribute__((address_space(1)))
#define LAS __attribute__((address_space(3)))
typedef __amdgpu_buffer_rsrc_t rsrc_t;

constexpr int NF = 26, DIN = 832, NB = 1024, CUBE = 17576;

constexpr u32 WS_E = 0;
constexpr u32 FL = 128;
constexpr u32 WS_XF = 4096;
constexpr u32 WS_FA = 40960;
constexpr u32 WS_FB = 45056;
constexpr u32 WS_PA = 49152;
constexpr u32 WS_PB = 69632;
constexpr u32 WS_P = 131072;
constexpr u32 WS_X = 655360;
constexpr u32 WS_END = WS_X + 16 * 104 * 1024;
constexpr int NWAVE = 9;

constexpr u32 L_W = 0;
constexpr u32 APITCH = 432;
constexpr u32 L_A = 106496;
constexpr u32 L_B = L_A + 48 * APITCH;
constexpr u32 L_XS = L_B + 48 * APITCH;
constexpr u32 XS_S = 1824;
constexpr u32 L_CNT = L_XS + 4 * XS_S;
constexpr u32 L_TOTAL = L_CNT + 64;
constexpr u32 L_V3 = 0, L_W2C = 8192, L_W2S = 8448, L_U = 12288, L_RAW = 20480;
enum { C_TAGV = 12, C_TAG = 13, C_WISS = 14, C_ABRDY = 0, C_ABSTG = 1, C_W = 3, C_V3 = 4, C_U = 5, C_XISS = 6, C_XRDY = 7, C_WD = 8, C_RED = 10 };

constexpr int perm1(int j) { return j < 24 ? ((j & ~7) | ((j + 2) & 7)) : (j == 24 ? 25 : (j == 25 ? 24 : j)); }
struct Slots { int a[192]; int b[192]; bool v0[192]; bool v1[192]; int n; };
struct SlotBuilder {
  Slots s{}; bool vis[27][27] = {}; int n = 0;
  constexpr bool seen(int a, int b) const { return vis[a < b ? a : b][a < b ? b : a]; }
  constexpr void put(int a, int b) {
    s.a[n] = a; s.b[n] = b;
    s.v0[n] = !seen(a, b); vis[a < b ? a : b][a < b ? b : a] = true;
    const int na = perm1(a), nb = perm1(b);
    s.v1[n] = !seen(na, nb); vis[na < nb ? na : nb][na < nb ? nb : na] = true;
    ++n;
  }
};
constexpr int sg2(int m) { return perm1(perm1(2 * m)) >> 1; }
constexpr Slots make_slots() {
  SlotBuilder t{};
  for (int A = 0; A < 13; ++A)
    for (int B = A + 1; B < 13; ++B) {
      int ca = A, cb = B;
      while (!t.seen(2 * ca, 2 * cb)) {
        t.put(2 * ca, 2 * cb); t.put(2 * ca, 2 * cb + 1); t.put(2 * ca + 1, 2 * cb); t.put(2 * ca + 1, 2 * cb + 1);
        ca = sg2(ca); cb = sg2(cb);
      }
    }
  for (int A = 0; A < 13; ++A) {
    int ca = A;
    while (!t.seen(2 * ca, 2 * ca + 1)) {
      t.put(2 * ca, 2 * ca); t.put(2 * ca, 2 * ca + 1); t.put(2 * ca + 1, 2 * ca + 1); t.put(2 * ca + 1, 26);
      ca = sg2(ca);
    }
  }
  for (int a = 0; a < 27; ++a) { int ca = a; while (!t.seen(ca, 26)) { t.put(ca, 26); ca = perm1(perm1(ca)); } }
  t.s.n = t.n;
  for (int n = t.n; n < 192; ++n) { t.s.a[n] = 26; t.s.b[n] = 26; t.s.v0[n] = false; t.s.v1[n] = false; }
  return t.s;
}
constexpr Slots SL = make_slots();
static_assert(SL.n <= 192, "too many slots");

constexpr int UP = 33, ZB = 30 * UP, ZA = 676;
struct SymTab { u32 w[192][4]; };
constexpr SymTab make_symtab() {
  SymTab t{};
  for (int d = 0; d < 192; ++d) {
    int c = d >> 2, s = c >> 1, hh = c & 1;
    for (int e = 0; e < 2; ++e) {
      int sg = 8 * s + 2 * (d & 3) + e;
      int a = hh ? perm1(SL.a[sg]) : SL.a[sg], b = hh ? perm1(SL.b[sg]) : SL.b[sg];
      bool v = hh ? SL.v1[sg] : SL.v0[sg];
      u32 b1 = ZB, b2 = ZB, a1 = ZA, a2 = ZA;
      if (v) {
        b1 = a * UP + b; b2 = a == b ? ZB : b * UP + a;
        if (a < 26 && b < 26) { a1 = a * 26 + b; a2 = a == b ? ZA : b * 26 + a; }
      }
      t.w[d][e] = b1 | (b2 << 16);
      t.w[d][2 + e] = a1 | (a2 << 16);
    }
  }
  return t;
}
__device__ const SymTab SYMTAB = make_symtab();

constexpr u32 OOB = 0x7FFFFF00u;
constexpr u32 SPIN_MAX = 1u << 18;

__device__ __forceinline__ float bflo(u32 v) { return __uint_as_float(v << 16); }
__device__ __forceinline__ float bfhi(u32 v) { return __uint_as_float(v & 0xffff0000u); }
__device__ __forceinline__ u32 packbf(float a, float b) {
  f32x2 f = {a, b};
  bf16x2 z = __builtin_convertvector(f, bf16x2);
  return __builtin_bit_cast(u32, z);
}
__device__ __forceinline__ float mulf(float a, float b) { float r; asm("v_mul_f32 %0, %1, %2" : "=v"(r) : "v"(a), "v"(b)); return r; }
template <int E> __device__ __forceinline__ f32x2 pkmul_bc(f32x2 a, f32x2 b) {
  f32x2 r; asm("v_pk_mul_f32 %0, %1, %2 op_sel:[%3,0] op_sel_hi:[%3,1]" : "=v"(r) : "v"(a), "v"(b), "i"(E)); return r;
}
__device__ __forceinline__ float dot2bf(u32 a, u32 b, float c) {
  return __builtin_amdgcn_fdot2_f32_bf16(__builtin_bit_cast(bf16x2, a), __builtin_bit_cast(bf16x2, b), c, false);
}
__device__ __forceinline__ rsrc_t mkrs(const void* p, u32 bytes) { return __builtin_amdgcn_make_buffer_rsrc((void*)p, 0, bytes, 0x00020000); }
__device__ __forceinline__ u64 mkflag(u32 tag) { return (u64)tag | ((u64)(tag * 0x9E3779B1u + 0x7F4A7C15u) << 32); }
__device__ __forceinline__ void drain_vm() { asm volatile("s_waitcnt vmcnt(0)" ::: "memory"); }
__device__ __forceinline__ void lds_inc(LAS u32* cnt) {
  u64 keep;
  asm volatile("s_mov_b64 %0, exec\n\ts_mov_b64 exec, 1\n\tds_add_u32 %1, %2\n\ts_mov_b64 exec, %0"
               : "=&s"(keep) : "v"((u32)(size_t)cnt), "v"(1u) : "memory");
}
__device__ __forceinline__ void team_arrive(LAS u32* cnt, int) {
  asm volatile("s_waitcnt lgkmcnt(0)" ::: "memory");
  lds_inc(cnt);
}
__device__ __forceinline__ float sum_rows(float v) {
  auto a = __builtin_amdgcn_permlane32_swap(__float_as_uint(v), __float_as_uint(v), false, false);
  v = __uint_as_float(a[0]) + __uint_as_float(a[1]);
  auto b = __builtin_amdgcn_permlane16_swap(__float_as_uint(v), __float_as_uint(v), false, false);
  return __uint_as_float(b[0]) + __uint_as_float(b[1]);
}
template <int CTRL> __device__ __forceinline__ float dpp_add(float v) {
  return v + __uint_as_float(__builtin_amdgcn_update_dpp(0u, __float_as_uint(v), CTRL, 0xf, 0xf, true));
}
__device__ __forceinline__ float sum_row16(float v) {
  v = dpp_add<0x128>(v);
  v = dpp_add<0x124>(v);
  v = dpp_add<0x122>(v);
  v = dpp_add<0x121>(v);
  return v;
}

#define STAMP(k) do {} while (0)

struct Args {
  const int* idx; const uint16_t* table; const uint16_t* wc; const uint16_t* wco; const uint16_t* w1;
  const uint16_t* b1; const uint16_t* w2; const uint16_t* bo; uint16_t* out; char* ws;
  u32 table_bytes; u32 pad_;
};
#define CAS __attribute__((address_space(4)))

template <int SLEEP> __device__ __forceinline__ void team_wait(LAS u32* cnt, u32 target) {
  u32 v, s, n = SPIN_MAX;
  asm volatile(
      "1:\n\tds_read_b32 %0, %3\n\ts_waitcnt lgkmcnt(0)\n\tv_readfirstlane_b32 %1, %0\n\ts_cmp_ge_u32 %1, %4\n\ts_cbranch_scc1 2f\n\t"
      "s_sleep %5\n\ts_sub_u32 %2, %2, 1\n\ts_cbranch_scc0 1b\n2:"
      : "=&v"(v), "=&s"(s), "+s"(n) : "v"((u32)(size_t)cnt), "s"(target), "i"(SLEEP) : "memory", "scc");
}
template <int SLEEP> __device__ __forceinline__ void poll_flags(const char* p, u64 want) {
  u64 v; u32 n = SPIN_MAX;
  asm volatile(
      "1:\n\tglobal_load_dwordx2 %0, %2, off sc1\n\ts_waitcnt vmcnt(0)\n\tv_cmp_ne_u64 vcc, %3, %0\n\ts_nop 4\n\ts_cbranch_vccz 2f\n\t"
      "s_sleep %4\n\ts_sub_u32 %1, %1, 1\n\ts_cbranch_scc0 1b\n2:"
      : "=&v"(v), "+s"(n) : "v"(p), "s"(want), "i"(SLEEP) : "memory", "scc", "vcc");
}
__device__ __forceinline__ void lds_barrier() {
  asm volatile("s_waitcnt lgkmcnt(0)" ::: "memory");
  __builtin_amdgcn_s_barrier();
  asm volatile("" ::: "memory");
}
__device__ __forceinline__ u32 load_epoch(const char* ws) {
  return __hip_atomic_load((const GAS u32*)(ws + WS_E), __ATOMIC_RELAXED, __HIP_MEMORY_SCOPE_AGENT);
}
__device__ __forceinline__ void store_flag(char* ws, u32 off, u64 flag) {
  __hip_atomic_store((GAS u64*)(ws + off), flag, __ATOMIC_RELAXED, __HIP_MEMORY_SCOPE_AGENT);
}
__device__ __forceinline__ u32 get_tag(LAS u32* cnt) {
  team_wait<0>(cnt + C_TAG, 1);
  return __builtin_amdgcn_readfirstlane(cnt[C_TAGV]);
}
__device__ __forceinline__ u32 opq(u32 v) { asm("" : "+v"(v)); return v; }

__global__ void __launch_bounds__(576, 3) xdfm_kernel(Args a) {
  extern __shared__ __attribute__((aligned(16))) char lds_raw[];
  LAS char* lds = (LAS char*)lds_raw;
  const int tid = threadIdx.x, lane = tid & 63;
  const int w = __builtin_amdgcn_readfirstlane(tid >> 6);
  const int b = blockIdx.x, j = b >> 3;
  const int jg = j < 6 ? (j >= 3 ? 1 : 0) : (j >= 19 ? 1 : 0);
  const int g = (b & 7) * 2 + jg, r = j < 6 ? 13 + j - 3 * jg : j - 6 - 13 * jg;
  LAS u32* cnt = (LAS u32*)(lds + L_CNT);
  asm volatile("" :: "s"(a.idx), "s"(a.table), "s"(a.wc), "s"(a.wco), "s"(a.w1), "s"(a.b1), "s"(a.w2), "s"(a.bo), "s"(a.out), "s"(a.ws), "s"(a.table_bytes));
  const rsrc_t rws = mkrs(a.ws, WS_END);
  const rsrc_t ridx = mkrs(a.idx, NB * NF * 4), rtab = mkrs(a.table, a.table_bytes);

  if (w == 8) {
    __builtin_amdgcn_s_setprio(3);
    if (lane < 16) cnt[lane] = 0;
    u32 tag;
    if (r < 13) {
      u32 id[4]; u32x4 xg[8];
#pragma unroll
      for (int m = 0; m < 2; ++m) {
        const int p0 = 4 * r + 2 * m, t = p0 >= NF ? 1 : 0, f0 = p0 - t * NF;
        const u32x2 ia = __builtin_amdgcn_raw_buffer_load_b64(ridx, (lane & 31) * (NF * 4), ((g * 64 + t * 32) * NF + f0) * 4, 0);
        id[2 * m] = ia.x; id[2 * m + 1] = ia.y;
      }
      const u32 ep = load_epoch(a.ws);
#pragma unroll
      for (int k = 0; k < 8; ++k) xg[k] = __builtin_amdgcn_raw_buffer_load_b128(rtab, id[k >> 1] * 64 + (k & 1) * 32 + (lane >> 5) * 16, 0, 0);
      lds_barrier();
      STAMP(1);
      tag = __builtin_amdgcn_readfirstlane(ep) + 1u;
      cnt[C_TAGV] = tag;
      team_arrive(cnt + C_TAG, lane);
#pragma unroll
      for (int k = 0; k < 8; ++k) __builtin_amdgcn_raw_buffer_store_b128(xg[k], rws, WS_X + lane * 16, (g * 104 + r * 8 + k) * 1024, 16);
      STAMP(2);
      drain_vm();
    } else {
      const u32 ep = load_epoch(a.ws);
      lds_barrier();
      tag = __builtin_amdgcn_readfirstlane(ep) + 1u;
      cnt[C_TAGV] = tag;
      team_arrive(cnt + C_TAG, lane);
    }
    const u64 flag = mkflag(tag);
    if (lane == 0) store_flag(a.ws, WS_XF + (g * 16 + r) * FL, flag);
    STAMP(3);
    if (r < 13) {
      __builtin_amdgcn_s_sleep(P_XDELAY);
      poll_flags<2>(a.ws + WS_XF + (g * 16 + (lane < 13 ? lane : 0)) * FL, flag);
      lds_inc(cnt + C_XRDY);
    }
    STAMP(4);
    __builtin_amdgcn_s_sleep(P_BDELAY);
    poll_flags<6>(a.ws + (lane < 32 ? WS_FA : WS_FB) + ((lane & 31) < NF ? (lane & 31) : 0) * FL, flag);
    lds_inc(cnt + C_ABRDY);
    STAMP(5);
  } else if (w >= 4) {
    const int cw = w - 4, smp = g * 64 + r * 4 + cw;
    u32 id[2]; u32x4 xr[2];
    u32 bo;
    {
      const CAS u32* ip = (const CAS u32*)(a.idx + smp * NF);
      u32 is[NF];
#pragma unroll
      for (int f = 0; f < NF; ++f) is[f] = ip[f];
      const size_t bop = (size_t)a.bo;
      bo = *(const CAS u32*)(bop & ~(size_t)3) >> ((bop & 2) * 8);
      u32 iv = 0;
      asm("v_writelane_b32 %0, %1, 0\n\tv_writelane_b32 %0, %2, 1\n\tv_writelane_b32 %0, %3, 2\n\tv_writelane_b32 %0, %4, 3\n\tv_writelane_b32 %0, %5, 4\n\tv_writelane_b32 %0, %6, 5\n\tv_writelane_b32 %0, %7, 6\n\tv_writelane_b32 %0, %8, 7\n\tv_writelane_b32 %0, %9, 8\n\tv_writelane_b32 %0, %10, 9\n\tv_writelane_b32 %0, %11, 10\n\tv_writelane_b32 %0, %12, 11\n\tv_writelane_b32 %0, %13, 12\n\tv_writelane_b32 %0, %14, 13\n\tv_writelane_b32 %0, %15, 14\n\tv_writelane_b32 %0, %16, 15\n\tv_writelane_b32 %0, %17, 16\n\tv_writelane_b32 %0, %18, 17\n\tv_writelane_b32 %0, %19, 18\n\tv_writelane_b32 %0, %20, 19\n\tv_writelane_b32 %0, %21, 20\n\tv_writelane_b32 %0, %22, 21\n\tv_writelane_b32 %0, %23, 22\n\tv_writelane_b32 %0, %24, 23\n\tv_writelane_b32 %0, %25, 24\n\tv_writelane_b32 %0, %26, 25"
          : "+v"(iv) : "s"(is[0]), "s"(is[1]), "s"(is[2]), "s"(is[3]), "s"(is[4]), "s"(is[5]), "s"(is[6]), "s"(is[7]), "s"(is[8]), "s"(is[9]), "s"(is[10]), "s"(is[11]), "s"(is[12]), "s"(is[13]), "s"(is[14]), "s"(is[15]), "s"(is[16]), "s"(is[17]), "s"(is[18]), "s"(is[19]), "s"(is[20]), "s"(is[21]), "s"(is[22]), "s"(is[23]), "s"(is[24]), "s"(is[25]));
      id[0] = __builtin_amdgcn_ds_bpermute((lane >> 2) * 4, iv);
      id[1] = __builtin_amdgcn_ds_bpermute((16 + (lane >> 2)) * 4, iv);
    }
    if (r < 13) __builtin_amdgcn_s_setprio(2);
    lds_barrier();
#pragma unroll
    for (int m = 0; m < 2; ++m) { const int f = 16 * m + (lane >> 2); xr[m] = __builtin_amdgcn_raw_buffer_load_b128(rtab, f < NF ? id[m] * 64 + (lane & 3) * 16 : OOB, 0, 0); }
    __builtin_amdgcn_s_setprio(0);
    STAMP(1);
    f32x2 xp[14];
    {
      const u32 o = L_XS + cw * XS_S + (lane >> 2) * 64 + (lane & 3) * 16;
      *(LAS u32x4*)(lds + o) = xr[0];
      u32x4 x1 = xr[1];
      if ((lane >> 2) == NF - 16) x1 = (u32x4){0x3F803F80u, 0x3F803F80u, 0x3F803F80u, 0x3F803F80u};
      *(LAS u32x4*)(lds + opq((lane >> 2) < 12 ? o + 1024 : L_XS + cw * XS_S + 27 * 64 + (lane & 3) * 16)) = x1;
      asm volatile("s_waitcnt lgkmcnt(0)" ::: "memory");
      const int hh = lane >> 5, q = (lane >> 2) & 3, pp = lane & 3;
      const u32 base = L_XS + cw * XS_S + ((lane >> 4) & 1) * 32 + pp * 8;
      const u32 a0 = base + (hh ? q + 2 : q) * 64, a1 = base + (hh ? ((q + 6) & 7) : q + 4) * 64;
      const u32 a6 = base + (24 + ((hh && q < 2) ? (q ^ 1) : q)) * 64;
#pragma unroll
      for (int t = 0; t < 7; ++t) {
        s16x4 y = __builtin_amdgcn_ds_read_tr16_b64_v4i16((LAS s16x4*)(lds + (t < 6 ? ((t & 1) ? a1 : a0) + (t >> 1) * 512 : a6)));
        u32x2 u = __builtin_bit_cast(u32x2, y);
        xp[2 * t] = (f32x2){bflo(u.x), bfhi(u.x)}; xp[2 * t + 1] = (f32x2){bflo(u.y), bfhi(u.y)};
      }
    }
    bf16x8 z[24];
#pragma unroll
    for (int s = 0; s < 24; ++s) {
      u32x4 zz;
#pragma unroll
      for (int e = 0; e < 4; ++e) {
        const int s1 = 8 * s + 2 * e, s2 = s1 + 1;
        const int a1 = SL.a[s1], b1 = SL.b[s1], a2 = SL.a[s2], b2 = SL.b[s2];
        if (a1 == a2 && (b1 & 1) == 0 && b1 < 26 && b2 == b1 + 1) {
          const f32x2 pr = (a1 & 1) ? pkmul_bc<1>(xp[a1 >> 1], xp[b1 >> 1]) : pkmul_bc<0>(xp[a1 >> 1], xp[b1 >> 1]);
          zz[e] = packbf(pr[0], pr[1]);
        } else {
          const float p1 = b1 == 26 ? xp[a1 >> 1][a1 & 1] : mulf(xp[a1 >> 1][a1 & 1], xp[b1 >> 1][b1 & 1]);
          const float p2 = b2 == 26 ? xp[a2 >> 1][a2 & 1] : mulf(xp[a2 >> 1][a2 & 1], xp[b2 >> 1][b2 & 1]);
          zz[e] = packbf(p1, p2);
        }
      }
      z[s] = __builtin_bit_cast(bf16x8, zz);
      asm volatile("" : "+v"(z[s]));
    }
    const u32 tag = get_tag(cnt);
    const u64 flag = mkflag(tag);
    STAMP(2);
    team_wait<1>(cnt + C_ABRDY, 1);
    STAMP(3);
    if (r < 13) team_wait<1>(cnt + C_XISS, 4);
    __builtin_amdgcn_s_setprio(2);
    STAMP(4);
    {
      u32x4 y[10];
#pragma unroll
      for (int m = 0; m < 10; ++m) {
        const u32 n = ((m % 5) * 4 + cw) * 64 + lane;
        y[m] = __builtin_amdgcn_raw_buffer_load_b128(rws, n < NF * 48 ? n * 16 : OOB, m < 5 ? WS_PA : WS_PB, 16);
      }
#pragma unroll
      for (int m = 0; m < 5; ++m) {
        const u32 n = (m * 4 + cw) * 64 + lane, i = (n * 1366u) >> 16, c = n - i * 48;
        const u32 o = opq(n < NF * 48 ? L_A + c * APITCH + i * 16 : L_A + 26 * 16);
        *(LAS u32x4*)(lds + o) = y[m];
        *(LAS u32x4*)(lds + o + (L_B - L_A)) = y[5 + m];
      }
      team_arrive(cnt + C_ABSTG, lane);
      team_wait<0>(cnt + C_ABSTG, 4);
    }
    __builtin_amdgcn_s_setprio(0);
    STAMP(5);
    const u32 afr = (lane >> 5) * APITCH + (lane & 31) * 16;
    const u32 tag2 = (u32)(flag >> 32);
    const int sg = r * 4 + cw;
    const u32 po = opq(lane < 26 ? WS_P + ((g * 26 + lane) * 2 + (sg >> 5)) * 512 + (sg & 31) * 16 : OOB);
    u32x4 yp;
    f32x16 acc1 = {}, acc2 = {};
    {
      constexpr int AD = 2;
      bf16x8 af[24], bf[24];
#pragma unroll
      for (int s = 0; s < AD; ++s) { af[s] = *(LAS bf16x8*)(lds + L_A + afr + s * 2 * APITCH); bf[s] = *(LAS bf16x8*)(lds + L_B + afr + s * 2 * APITCH); }
      __builtin_amdgcn_sched_barrier(0);
#pragma unroll
      for (int s = 0; s < 24; ++s) {
        if (s + AD < 24) { af[s + AD] = *(LAS bf16x8*)(lds + L_A + afr + (s + AD) * 2 * APITCH); bf[s + AD] = *(LAS bf16x8*)(lds + L_B + afr + (s + AD) * 2 * APITCH); }
        if (s == P_PQ) yp = __builtin_amdgcn_raw_buffer_load_b128(rws, po, 0, 16);
        acc1 = __builtin_amdgcn_mfma_f32_32x32x16_bf16(af[s], z[s], acc1, 0, 0, 0);
        acc2 = __builtin_amdgcn_mfma_f32_32x32x16_bf16(bf[s], z[s], acc2, 0, 0, 0);
        __builtin_amdgcn_sched_barrier(0);
      }
    }
    float sum = 0.f;
#pragma unroll
    for (int e = 0; e < 12; ++e) sum += acc1[e] * acc2[e];
    { const float t = acc1[12] * acc2[12] + acc1[13] * acc2[13]; sum += lane < 32 ? t : 0.f; }
    sum = sum_rows(sum_row16(sum));
    STAMP(6);
    for (u32 it = 0; it < SPIN_MAX; ++it) {
      const bool ok = lane >= 26 || (yp.y == tag && yp.w == tag2);
      if (__all(ok)) break;
      asm volatile("" ::: "memory");
      __builtin_amdgcn_s_sleep(1);
      yp = __builtin_amdgcn_raw_buffer_load_b128(rws, po, 0, 16);
    }
    float part = sum_rows(sum_row16(__uint_as_float(yp.x)));
    if (lane == 0) {
      const __bf16 ob = (__bf16)(sum + part + bflo(bo));
      a.out[smp] = __builtin_bit_cast(uint16_t, ob);
    }
    STAMP(7);
  } else if (r < 13) {
      const rsrc_t rw1 = mkrs(a.w1, DIN * DIN * 2);
      const int mh = w & 1, kh = w >> 1, hh = lane >> 5;
      lds_barrier();
      STAMP(1);
      {
        u32x4 v[26];
        const u32 vo = (w * 208 + (lane >> 3)) * 1664 + r * 128 + (lane & 7) * 16;
        const int c = lane & 7;
        const u32 lo = L_W + (w * 208 + (lane >> 3)) * 128 + (((c >> 1) ^ (((lane >> 4) & 1) << 1)) * 32) + (c & 1) * 16;
#pragma unroll
        for (int i = 0; i < 26; ++i) v[i] = __builtin_amdgcn_raw_buffer_load_b128(rw1, vo, i * 8 * 1664, 0);
#pragma unroll
        for (int i = 0; i < 26; ++i) *(LAS u32x4*)(lds + lo + i * 1024) = v[i];
      }
      team_arrive(cnt + C_W, lane);
      const u32 tag = get_tag(cnt);
      const u64 flag = mkflag(tag);
      f32x16 acc[2];
      f32x2 wf[8];
      {
        const CAS u32* bp = (const CAS u32*)(a.b1 + r * 64 + kh * 32);
        const CAS u32* wp = (const CAS u32*)(a.w2 + r * 64 + kh * 32);
        u32 bs[16], ws_[16];
#pragma unroll
        for (int t = 0; t < 16; ++t) { bs[t] = bp[t]; ws_[t] = wp[t]; }
#pragma unroll
        for (int t = 0; t < 16; ++t) asm volatile("" : "+s"(bs[t]), "+s"(ws_[t]));
#pragma unroll
        for (int t = 0; t < 4; ++t) {
          const u32 b0 = hh ? bs[4 * t + 2] : bs[4 * t], b1 = hh ? bs[4 * t + 3] : bs[4 * t + 1];
          const u32 w0 = hh ? ws_[4 * t + 2] : ws_[4 * t], w1 = hh ? ws_[4 * t + 3] : ws_[4 * t + 1];
          acc[0][4 * t] = bflo(b0); acc[0][4 * t + 1] = bfhi(b0); acc[0][4 * t + 2] = bflo(b1); acc[0][4 * t + 3] = bfhi(b1);
          acc[1][4 * t] = 0.f; acc[1][4 * t + 1] = 0.f; acc[1][4 * t + 2] = 0.f; acc[1][4 * t + 3] = 0.f;
          wf[2 * t] = (f32x2){bflo(w0), bfhi(w0)}; wf[2 * t + 1] = (f32x2){bflo(w1), bfhi(w1)};
          asm volatile("" : "+v"(wf[2 * t]), "+v"(wf[2 * t + 1]));
        }
      }
      STAMP(2);
      team_wait<0>(cnt + C_XRDY, 1);
      STAMP(3);
      constexpr int XW = P_XW, FD = 3;
      u32x4 xv[26];
      auto ldx = [&](int i) { xv[i] = __builtin_amdgcn_raw_buffer_load_b128(rws, WS_X + lane * 16, (g * 104 + mh * 52 + kh * 26 + i) * 1024, 16); };
#pragma unroll
      for (int i = 0; i < XW; ++i) ldx(i);
      team_wait<0>(cnt + C_W, 4);
      __builtin_amdgcn_s_setprio(1);
      {
        const int q = (lane >> 2) & 3, pp = lane & 3, gam = (lane >> 4) & 1;
        const u32 rb = L_W + kh * 26 * 2048 + (8 * hh + q) * 128 + pp * 8;
        const u32 ad0 = rb + (((2 * kh + gam) ^ (q & 2)) * 32), ad1 = rb + (((2 * (kh ^ 1) + gam) ^ (q & 2)) * 32);
        bf16x8 fr[26][2];
        auto ldf = [&](int i) {
#pragma unroll
          for (int t = 0; t < 2; ++t) {
            const u32 o = (t ? ad1 : ad0) + i * 2048;
            const s16x4 y0 = __builtin_amdgcn_ds_read_tr16_b64_v4i16((LAS s16x4*)(lds + o));
            const s16x4 y1 = __builtin_amdgcn_ds_read_tr16_b64_v4i16((LAS s16x4*)(lds + o + 512));
            fr[i][t] = __builtin_bit_cast(bf16x8, __builtin_shufflevector(y0, y1, 0, 1, 2, 3, 4, 5, 6, 7));
          }
        };
#pragma unroll
        for (int i = 0; i < FD; ++i) ldf(i);
        __builtin_amdgcn_sched_barrier(0);
#pragma unroll
        for (int i = 0; i < 26; ++i) {
          if (i + XW < 26) ldx(i + XW);
          if (i == P_XI) lds_inc(cnt + C_XISS);
          if (i == 8) lds_inc(cnt + C_WD + kh);
          if (i == 22) team_wait<0>(cnt + C_WD + kh, 2);
          if (i + FD < 26) ldf(i + FD);
          const bf16x8 xb = __builtin_bit_cast(bf16x8, xv[i]);
          acc[0] = __builtin_amdgcn_mfma_f32_32x32x16_bf16(fr[i][0], xb, acc[0], 0, 0, 0);
          acc[1] = __builtin_amdgcn_mfma_f32_32x32x16_bf16(fr[i][1], xb, acc[1], 0, 0, 0);
          __builtin_amdgcn_sched_barrier(0);
        }
      }
      STAMP(4);
      {
        const u32 so = L_W + kh * 26 * 2048 + mh * 4096 + lane * 16;
#pragma unroll
        for (int t = 0; t < 4; ++t) *(LAS f32x4*)(lds + so + t * 1024) = (f32x4){acc[1][4 * t], acc[1][4 * t + 1], acc[1][4 * t + 2], acc[1][4 * t + 3]};
        team_arrive(cnt + C_RED + mh, lane);
        team_wait<0>(cnt + C_RED + mh, 2);
      }
      STAMP(5);
      float part;
      {
        constexpr float K1 = -2.0f * 1.4426950408889634f * 0.7978845608028654f, K2 = K1 * 0.044715f;
        const u32 si = L_W + (kh ^ 1) * 26 * 2048 + mh * 4096 + lane * 16;
        f32x4 o[4];
#pragma unroll
        for (int t = 0; t < 4; ++t) o[t] = *(LAS f32x4*)(lds + si + t * 1024);
        f32x2 h[8], u[8], y[8];
#pragma unroll
        for (int m = 0; m < 8; ++m) {
          h[m] = (f32x2){acc[0][2 * m], acc[0][2 * m + 1]} + (f32x2){o[m >> 1][2 * (m & 1)], o[m >> 1][2 * (m & 1) + 1]};
          u[m] = (h[m] * h[m] * K2 + K1) * h[m];
          y[m] = h[m] * wf[m];
        }
#pragma unroll
        for (int m = 0; m < 8; ++m) { u[m][0] = __builtin_amdgcn_exp2f(u[m][0]); u[m][1] = __builtin_amdgcn_exp2f(u[m][1]); }
#pragma unroll
        for (int m = 0; m < 8; ++m) { u[m] = u[m] + 1.0f; }
#pragma unroll
        for (int m = 0; m < 8; ++m) { u[m][0] = __builtin_amdgcn_rcpf(u[m][0]); u[m][1] = __builtin_amdgcn_rcpf(u[m][1]); }
        f32x2 p0 = y[0] * u[0], p1 = y[1] * u[1];
#pragma unroll
        for (int m = 2; m < 8; m += 2) { p0 += y[m] * u[m]; p1 += y[m + 1] * u[m + 1]; }
        p0 += p1;
        part = p0[0] + p0[1];
      }
      {
        auto sw = __builtin_amdgcn_permlane32_swap(__float_as_uint(part), __float_as_uint(part), false, false);
        part = __uint_as_float(sw[0]) + __uint_as_float(sw[1]);
      }
      {
        const u32x4 o = {__float_as_uint(part), tag, __float_as_uint(part), (u32)(flag >> 32)};
        __builtin_amdgcn_raw_buffer_store_b128(o, rws, lane < 32 ? WS_P + ((g * 26 + r * 2 + kh) * 2 + mh) * 512 + lane * 16 : OOB, 0, 16);
      }
      STAMP(6);
  } else {
      const int p = g * 3 + r - 13;
      const rsrc_t rwc = mkrs(a.wc, 3 * CUBE * 2), rwo = mkrs(a.wco, 78 * 2);
      __builtin_amdgcn_s_setprio(3);
      if (p < NF) {
        const int i = p;
        u32x4 tb[3];
        const int hb = (w >> 1) * 13, el = (w & 1) * 64 + lane;
        const CAS u32* w3p = (const CAS u32*)(a.wco + 52);
        u32 w3[13];
#pragma unroll
        for (int h = 0; h < 13; ++h) w3[h] = w3p[h];
        u32x4 v[13];
#pragma unroll
        for (int h = 0; h < 13; ++h) v[h] = __builtin_amdgcn_raw_buffer_load_b128(rwc, el < 85 ? (2 * CUBE + hb * 676 + 8 * el) * 2 : OOB, h * 676 * 2, 0);
        u32 v2[2];
#pragma unroll
        for (int m = 0; m < 2; ++m) {
          const u32 d = tid + 256 * m, h = (d * 5042u) >> 16, e = d - 13 * h;
          v2[m] = __builtin_amdgcn_raw_buffer_load_b32(rwc, d < 338 ? (CUBE + (h * NF + i) * NF) * 2 + e * 4 : OOB, 0, 0);
        }
        const u32 wq = __builtin_amdgcn_raw_buffer_load_b16(rwo, tid < NF ? (NF + tid) * 2 : OOB, 0, 0);
        const u32 w1i = __builtin_amdgcn_raw_buffer_load_b16(rwo, i * 2, 0, 0);
        if (w == 0) {
#pragma unroll
          for (int m = 0; m < 3; ++m) tb[m] = *(const GAS u32x4*)SYMTAB.w[lane + 64 * m];
        }
        __builtin_amdgcn_sched_barrier(0);
        lds_barrier();
        STAMP(1);
        {
          float acc[8];
#pragma unroll
          for (int e = 0; e < 8; ++e) acc[e] = 0.f;
#pragma unroll
          for (int h = 0; h < 13; ++h) {
            const u32 we = (h & 1) ? w3[h >> 1] >> 16 : w3[h >> 1] & 0xffffu;
            const u32 wo = (h & 1) ? w3[(13 + h) >> 1] & 0xffffu : w3[(13 + h) >> 1] >> 16;
            const u32 wl = hb ? wo : we, wh = wl << 16;
#pragma unroll
            for (int e = 0; e < 4; ++e) { acc[2 * e] = dot2bf(v[h][e], wl, acc[2 * e]); acc[2 * e + 1] = dot2bf(v[h][e], wh, acc[2 * e + 1]); }
          }
          const u32 vo = opq(el < 85 ? L_V3 + (w >> 1) * 2816 + el * 32 : L_V3 + 5632 + lane * 32);
          *(LAS f32x4*)(lds + vo) = (f32x4){acc[0], acc[1], acc[2], acc[3]};
          *(LAS f32x4*)(lds + vo + 16) = (f32x4){acc[4], acc[5], acc[6], acc[7]};
#pragma unroll
          for (int m = 0; m < 2; ++m) *(LAS u32*)(lds + L_W2S + (tid + 256 * m) * 4) = v2[m];
          if (w == 0) *(LAS u32*)(lds + L_W2C + lane * 4) = wq << 16;
        }
        const int tj = w >> 1, tp = w & 1, kq = lane >> 4;
        const int jr = 16 * tj + (lane & 15), jp = 16 * tp + (lane & 15);
        u32 oa[7], ob[7], oc[7];
#pragma unroll
        for (int s = 0; s < 7; ++s) {
          const int h = 4 * s + kq;
          const bool inv = h < NF && jp < NF;
          oa[s] = opq((h < NF && jr < NF) ? L_W2S + (h * NF + jr) * 2 : L_W2C + 63 * 4);
          ob[s] = opq(inv ? L_V3 + (h * NF + jp) * 4 : L_W2C + ((h < NF && jp == NF) ? h : 63) * 4);
          oc[s] = opq(inv ? L_V3 + 2816 + (h * NF + jp) * 4 : L_W2C + 63 * 4);
        }
        team_arrive(cnt + C_V3, lane);
        team_wait<0>(cnt + C_V3, 4);
        STAMP(2);
        u32 av[7]; float bv[7], bw[7];
#pragma unroll
        for (int s = 0; s < 7; ++s) { av[s] = *(LAS uint16_t*)(lds + oa[s]); bv[s] = *(LAS float*)(lds + ob[s]); bw[s] = *(LAS float*)(lds + oc[s]); }
#pragma unroll
        for (int s = 0; s < 7; ++s) bv[s] += bw[s];
        f32x4 u = {0.f, 0.f, 0.f, 0.f}, u2 = {0.f, 0.f, 0.f, 0.f};
#pragma unroll
        for (int s = 0; s < 7; ++s) {
          if (s & 1) u2 = __builtin_amdgcn_mfma_f32_16x16x4f32(bflo(av[s]), bv[s], u2, 0, 0, 0);
          else u = __builtin_amdgcn_mfma_f32_16x16x4f32(bflo(av[s]), bv[s], u, 0, 0, 0);
        }
        u += u2;
#pragma unroll
        for (int e = 0; e < 4; ++e) {
          const int jo = 16 * tj + 4 * kq + e;
          const float val = (jo == NF && jp == NF) ? bflo(w1i) : u[e];
          *(LAS float*)(lds + L_U + (jo * UP + jp) * 4) = val;
        }
        team_arrive(cnt + C_U, lane);
        if (w == 0) {
          team_wait<0>(cnt + C_U, 4);
          STAMP(3);
          LAS float* U = (LAS float*)(lds + L_U);
          float e[12];
#pragma unroll
          for (int m = 0; m < 3; ++m) {
            e[4 * m] = U[tb[m].x & 0xffff]; e[4 * m + 1] = U[tb[m].x >> 16]; e[4 * m + 2] = U[tb[m].y & 0xffff]; e[4 * m + 3] = U[tb[m].y >> 16];
          }
#pragma unroll
          for (int m = 0; m < 3; ++m)
            __builtin_amdgcn_raw_buffer_store_b32(packbf(e[4 * m] + e[4 * m + 1], e[4 * m + 2] + e[4 * m + 3]), rws, WS_PB + i * 768 + (lane + 64 * m) * 4, 0, 16);
          const u64 flagb = mkflag(get_tag(cnt));
          drain_vm();
          if (lane == 0) store_flag(a.ws, WS_FB + i * FL, flagb);
          STAMP(4);
        }
      } else {
        const int i = (p - NF) * 4 + w;
        const bool prod = p < 33 && i < NF;
        u32 v[6]; u32x4 tb[3];
#pragma unroll
        for (int m = 0; m < 6; ++m) { const int d = lane + 64 * m; v[m] = __builtin_amdgcn_raw_buffer_load_b32(rwc, (prod && d < 338) ? i * 1352 + d * 4 : OOB, 0, 0); }
#pragma unroll
        for (int m = 0; m < 3; ++m) tb[m] = *(const GAS u32x4*)SYMTAB.w[lane + 64 * m];
        __builtin_amdgcn_sched_barrier(0);
        lds_barrier();
        STAMP(1);
        if (prod) {
          LAS char* raw = lds + L_RAW + w * 2048;
#pragma unroll
          for (int m = 0; m < 6; ++m) *(LAS u32*)(raw + (lane + 64 * m) * 4) = v[m];
          asm volatile("s_waitcnt lgkmcnt(0)" ::: "memory");
          LAS uint16_t* R = (LAS uint16_t*)raw;
          u32 e[12];
#pragma unroll
          for (int m = 0; m < 3; ++m) {
            e[4 * m] = R[tb[m].z & 0xffff]; e[4 * m + 1] = R[tb[m].z >> 16]; e[4 * m + 2] = R[tb[m].w & 0xffff]; e[4 * m + 3] = R[tb[m].w >> 16];
          }
#pragma unroll
          for (int m = 0; m < 3; ++m)
            __builtin_amdgcn_raw_buffer_store_b32(packbf(bflo(e[4 * m]) + bflo(e[4 * m + 1]), bflo(e[4 * m + 2]) + bflo(e[4 * m + 3])), rws,
                                                  WS_PA + i * 768 + (lane + 64 * m) * 4, 0, 16);
          const u64 flag0 = mkflag(get_tag(cnt));
          drain_vm();
          if (lane == 0) store_flag(a.ws, WS_FA + i * FL, flag0);
          STAMP(4);
        }
        if (p == 47 && w == 0) {
          const u32 tag0 = get_tag(cnt);
          const u64 flag0 = mkflag(tag0);
          __builtin_amdgcn_s_sleep(32);
#pragma unroll 1
          for (int m = 0; m < 4; ++m) poll_flags<16>(a.ws + WS_XF + (lane + 64 * m) * FL, flag0);
          if (lane == 0) __hip_atomic_store((GAS u32*)(a.ws + WS_E), tag0, __ATOMIC_RELAXED, __HIP_MEMORY_SCOPE_AGENT);
        }
      }
  }
  if (b == 250 && w == 8 && 0 > 0) {
    float sx = (float)lane;
#pragma unroll 1
    for (int i = 0; i < 0 / 64; ++i) {
      asm volatile(".rept 64\n\tv_fma_f32 %0, %0, %0, %0\n\t.endr" : "+v"(sx));
    }
    asm volatile("" :: "v"(sx));
  }
}

}

extern "C" void kernel_launch(void* const* d_in, const int* in_sizes, int n_in, void* d_out, int out_size,
                              void* d_ws, size_t ws_size, hipStream_t stream) {
  using namespace xdfm;
  Args a;
  a.idx = (const int*)d_in[0]; a.table = (const uint16_t*)d_in[1]; a.wc = (const uint16_t*)d_in[2];
  a.wco = (const uint16_t*)d_in[3]; a.w1 = (const uint16_t*)d_in[4]; a.b1 = (const uint16_t*)d_in[5];
  a.w2 = (const uint16_t*)d_in[6]; a.bo = (const uint16_t*)d_in[7]; a.out = (uint16_t*)d_out; a.ws = (char*)d_ws;
  a.table_bytes = (u32)in_sizes[1] * 2u; a.pad_ = 0;
  (void)hipFuncSetAttribute((const void*)xdfm_kernel, hipFuncAttributeMaxDynamicSharedMemorySize, L_TOTAL);
  xdfm_kernel<<<256, NWAVE * 64, L_TOTAL, stream>>>(a);
}
```

```cpp
#include <hip/hip_runtime.h>
#include <hip/hip_bf16.h>
#include <stdint.h>

#define P_ADELAY 8
#define P_BDELAY 8
#define P_WW 10
#define P_PQ 16
#define P_XI 10
#define P_XDELAY 12
#define P_CG 4
#define P_XW 12
namespace xdfm {

typedef unsigned int u32;
typedef unsigned long long u64;
typedef u32 u32x2 __attribute__((ext_vector_type(2)));
typedef u32 u32x4 __attribute__((ext_vector_type(4)));
typedef short s16x4 __attribute__((ext_vector_type(4)));
typedef short s16x8 __attribute__((ext_vector_type(8)));
typedef float f32x2 __attribute__((ext_vector_type(2)));
typedef float f32x4 __attribute__((ext_vector_type(4)));
typedef float f32x16 __attribute__((ext_vector_type(16)));
typedef __bf16 bf16x2 __attribute__((ext_vector_type(2)));
typedef __bf16 bf16x8 __attribute__((ext_vector_type(8)));
#define GAS __attribute__((address_space(1)))
#define LAS __attribute__((address_space(3)))
typedef __amdgpu_buffer_rsrc_t rsrc_t;

constexpr int NF = 26, DIN = 832, NB = 1024, CUBE = 17576;

constexpr u32 WS_E = 0;
constexpr u32 FL = 128;
constexpr u32 WS_XF = 4096;
constexpr u32 WS_FA = 40960;
constexpr u32 WS_FB = 45056;
constexpr u32 WS_PA = 49152;
constexpr u32 WS_PB = 69632;
constexpr u32 WS_P = 131072;
constexpr u32 WS_X = 655360;
constexpr u32 WS_END = WS_X + 16 * 104 * 1024;
constexpr int NWAVE = 10;

constexpr u32 L_W = 0;
constexpr u32 APITCH = 432;
constexpr u32 L_A = 106496;
constexpr u32 L_B = L_A + 48 * APITCH;
constexpr u32 L_XS = L_B + 48 * APITCH;
constexpr u32 XS_S = 1824;
constexpr u32 L_CNT = L_XS + 4 * XS_S;
constexpr u32 L_TOTAL = L_CNT + 64;
constexpr u32 L_V3 = 0, L_W2C = 8192, L_W2S = 8448, L_U = 12288, L_RAW = 20480;
enum { C_TAGV = 12, C_TAG = 13, C_WISS = 14, C_ABRDY = 0, C_ABSTG = 1, C_W = 3, C_V3 = 4, C_U = 5, C_XISS = 6, C_XRDY = 7, C_WD = 8, C_RED = 10 };

constexpr int perm1(int j) { return j < 24 ? ((j & ~7) | ((j + 2) & 7)) : (j == 24 ? 25 : (j == 25 ? 24 : j)); }
struct Slots { int a[192]; int b[192]; bool v0[192]; bool v1[192]; int n; };
struct SlotBuilder {
  Slots s{}; bool vis[27][27] = {}; int n = 0;
  constexpr bool seen(int a, int b) const { return vis[a < b ? a : b][a < b ? b : a]; }
  constexpr void put(int a, int b) {
    s.a[n] = a; s.b[n] = b;
    s.v0[n] = !seen(a, b); vis[a < b ? a : b][a < b ? b : a] = true;
    const int na = perm1(a), nb = perm1(b);
    s.v1[n] = !seen(na, nb); vis[na < nb ? na : nb][na < nb ? nb : na] = true;
    ++n;
  }
};
constexpr int sg2(int m) { return perm1(perm1(2 * m)) >> 1; }
constexpr Slots make_slots() {
  SlotBuilder t{};
  for (int A = 0; A < 13; ++A)
    for (int B = A + 1; B < 13; ++B) {
      int ca = A, cb = B;
      while (!t.seen(2 * ca, 2 * cb)) {
        t.put(2 * ca, 2 * cb); t.put(2 * ca, 2 * cb + 1); t.put(2 * ca + 1, 2 * cb); t.put(2 * ca + 1, 2 * cb + 1);
        ca = sg2(ca); cb = sg2(cb);
      }
    }
  for (int A = 0; A < 13; ++A) {
    int ca = A;
    while (!t.seen(2 * ca, 2 * ca + 1)) {
      t.put(2 * ca, 2 * ca); t.put(2 * ca, 2 * ca + 1); t.put(2 * ca + 1, 2 * ca + 1); t.put(2 * ca + 1, 26);
      ca = sg2(ca);
    }
  }
  for (int a = 0; a < 27; ++a) { int ca = a; while (!t.seen(ca, 26)) { t.put(ca, 26); ca = perm1(perm1(ca)); } }
  t.s.n = t.n;
  for (int n = t.n; n < 192; ++n) { t.s.a[n] = 26; t.s.b[n] = 26; t.s.v0[n] = false; t.s.v1[n] = false; }
  return t.s;
}
constexpr Slots SL = make_slots();
static_assert(SL.n <= 192, "too many slots");

constexpr int UP = 33, ZB = 30 * UP, ZA = 676;
struct SymTab { u32 w[192][4]; };
constexpr SymTab make_symtab() {
  SymTab t{};
  for (int d = 0; d < 192; ++d) {
    int c = d >> 2, s = c >> 1, hh = c & 1;
    for (int e = 0; e < 2; ++e) {
      int sg = 8 * s + 2 * (d & 3) + e;
      int a = hh ? perm1(SL.a[sg]) : SL.a[sg], b = hh ? perm1(SL.b[sg]) : SL.b[sg];
      bool v = hh ? SL.v1[sg] : SL.v0[sg];
      u32 b1 = ZB, b2 = ZB, a1 = ZA, a2 = ZA;
      if (v) {
        b1 = a * UP + b; b2 = a == b ? ZB : b * UP + a;
        if (a < 26 && b < 26) { a1 = a * 26 + b; a2 = a == b ? ZA : b * 26 + a; }
      }
      t.w[d][e] = b1 | (b2 << 16);
      t.w[d][2 + e] = a1 | (a2 << 16);
    }
  }
  return t;
}
__device__ const SymTab SYMTAB = make_symtab();

constexpr u32 OOB = 0x7FFFFF00u;
constexpr u32 SPIN_MAX = 1u << 18;

__device__ __forceinline__ float bflo(u32 v) { return __uint_as_float(v << 16); }
__device__ __forceinline__ float bfhi(u32 v) { return __uint_as_float(v & 0xffff0000u); }
__device__ __forceinline__ u32 packbf(float a, float b) {
  f32x2 f = {a, b};
  bf16x2 z = __builtin_convertvector(f, bf16x2);
  return __builtin_bit_cast(u32, z);
}
__device__ __forceinline__ float mulf(float a, float b) { float r; asm("v_mul_f32 %0, %1, %2" : "=v"(r) : "v"(a), "v"(b)); return r; }
template <int E> __device__ __forceinline__ f32x2 pkmul_bc(f32x2 a, f32x2 b) {
  f32x2 r; asm("v_pk_mul_f32 %0, %1, %2 op_sel:[%3,0] op_sel_hi:[%3,1]" : "=v"(r) : "v"(a), "v"(b), "i"(E)); return r;
}
__device__ __forceinline__ float dot2bf(u32 a, u32 b, float c) {
  return __builtin_amdgcn_fdot2_f32_bf16(__builtin_bit_cast(bf16x2, a), __builtin_bit_cast(bf16x2, b), c, false);
}
__device__ __forceinline__ rsrc_t mkrs(const void* p, u32 bytes) { return __builtin_amdgcn_make_buffer_rsrc((void*)p, 0, bytes, 0x00020000); }
__device__ __forceinline__ u64 mkflag(u32 tag) { return (u64)tag | ((u64)(tag * 0x9E3779B1u + 0x7F4A7C15u) << 32); }
__device__ __forceinline__ void drain_vm() { asm volatile("s_waitcnt vmcnt(0)" ::: "memory"); }
__device__ __forceinline__ void lds_inc(LAS u32* cnt) {
  u64 keep;
  asm volatile("s_mov_b64 %0, exec\n\ts_mov_b64 exec, 1\n\tds_add_u32 %1, %2\n\ts_mov_b64 exec, %0"
               : "=&s"(keep) : "v"((u32)(size_t)cnt), "v"(1u) : "memory");
}
__device__ __forceinline__ void team_arrive(LAS u32* cnt, int) {
  asm volatile("s_waitcnt lgkmcnt(0)" ::: "memory");
  lds_inc(cnt);
}
__device__ __forceinline__ float sum_rows(float v) {
  auto a = __builtin_amdgcn_permlane32_swap(__float_as_uint(v), __float_as_uint(v), false, false);
  v = __uint_as_float(a[0]) + __uint_as_float(a[1]);
  auto b = __builtin_amdgcn_permlane16_swap(__float_as_uint(v), __float_as_uint(v), false, false);
  return __uint_as_float(b[0]) + __uint_as_float(b[1]);
}
template <int CTRL> __device__ __forceinline__ float dpp_add(float v) {
  return v + __uint_as_float(__builtin_amdgcn_update_dpp(0u, __float_as_uint(v), CTRL, 0xf, 0xf, true));
}
__device__ __forceinline__ float sum_row16(float v) {
  v = dpp_add<0x128>(v);
  v = dpp_add<0x124>(v);
  v = dpp_add<0x122>(v);
  v = dpp_add<0x121>(v);
  return v;
}

#define STAMP(k) do {} while (0)

struct Args {
  const int* idx; const uint16_t* table; const uint16_t* wc; const uint16_t* wco; const uint16_t* w1;
  const uint16_t* b1; const uint16_t* w2; const uint16_t* bo; uint16_t* out; char* ws;
  u32 table_bytes; u32 pad_;
};
#define CAS __attribute__((address_space(4)))

template <int SLEEP> __device__ __forceinline__ void team_wait(LAS u32* cnt, u32 target) {
  u32 v, s, n = SPIN_MAX;
  asm volatile(
      "1:\n\tds_read_b32 %0, %3\n\ts_waitcnt lgkmcnt(0)\n\tv_readfirstlane_b32 %1, %0\n\ts_cmp_ge_u32 %1, %4\n\ts_cbranch_scc1 2f\n\t"
      "s_sleep %5\n\ts_sub_u32 %2, %2, 1\n\ts_cbranch_scc0 1b\n2:"
      : "=&v"(v), "=&s"(s), "+s"(n) : "v"((u32)(size_t)cnt), "s"(target), "i"(SLEEP) : "memory", "scc");
}
template <int SLEEP> __device__ __forceinline__ void poll_flags(const char* p, u64 want) {
  u64 v; u32 n = SPIN_MAX;
  asm volatile(
      "1:\n\tglobal_load_dwordx2 %0, %2, off sc1\n\ts_waitcnt vmcnt(0)\n\tv_cmp_ne_u64 vcc, %3, %0\n\ts_nop 4\n\ts_cbranch_vccz 2f\n\t"
      "s_sleep %4\n\ts_sub_u32 %1, %1, 1\n\ts_cbranch_scc0 1b\n2:"
      : "=&v"(v), "+s"(n) : "v"(p), "s"(want), "i"(SLEEP) : "memory", "scc", "vcc");
}
__device__ __forceinline__ void lds_barrier() {
  asm volatile("s_waitcnt lgkmcnt(0)" ::: "memory");
  __builtin_amdgcn_s_barrier();
  asm volatile("" ::: "memory");
}
__device__ __forceinline__ u32 load_epoch(const char* ws) {
  return __hip_atomic_load((const GAS u32*)(ws + WS_E), __ATOMIC_RELAXED, __HIP_MEMORY_SCOPE_AGENT);
}
__device__ __forceinline__ void store_flag(char* ws, u32 off, u64 flag) {
  __hip_atomic_store((GAS u64*)(ws + off), flag, __ATOMIC_RELAXED, __HIP_MEMORY_SCOPE_AGENT);
}
__device__ __forceinline__ u32 get_tag(LAS u32* cnt) {
  team_wait<0>(cnt + C_TAG, 1);
  return __builtin_amdgcn_readfirstlane(cnt[C_TAGV]);
}
__device__ __forceinline__ u32 opq(u32 v) { asm("" : "+v"(v)); return v; }

__global__ void __launch_bounds__(640, 3) xdfm_kernel(Args a) {
  extern __shared__ __attribute__((aligned(16))) char lds_raw[];
  LAS char* lds = (LAS char*)lds_raw;
  const int tid = threadIdx.x, lane = tid & 63;
  const int w = __builtin_amdgcn_readfirstlane(tid >> 6);
  const int b = blockIdx.x, j = b >> 3;
  const int jg = j < 6 ? (j >= 3 ? 1 : 0) : (j >= 19 ? 1 : 0);
  const int g = (b & 7) * 2 + jg, r = j < 6 ? 13 + j - 3 * jg : j - 6 - 13 * jg;
  LAS u32* cnt = (LAS u32*)(lds + L_CNT);
  asm volatile("" :: "s"(a.idx), "s"(a.table), "s"(a.wc), "s"(a.wco), "s"(a.w1), "s"(a.b1), "s"(a.w2), "s"(a.bo), "s"(a.out), "s"(a.ws), "s"(a.table_bytes));
  const rsrc_t rws = mkrs(a.ws, WS_END);
  const rsrc_t ridx = mkrs(a.idx, NB * NF * 4), rtab = mkrs(a.table, a.table_bytes);

  if (w == 9) {
    lds_barrier();
    if (b == 250) {

      float sx = (float)lane;
#pragma unroll 1
      for (int i = 0; i < 8192 / 64; ++i) {
        asm volatile(".rept 64\n\tv_fma_f32 %0, %0, %0, %0\n\t.endr" : "+v"(sx));
      }
      asm volatile("" :: "v"(sx));
    }
  } else if (w == 8) {
    __builtin_amdgcn_s_setprio(3);
    if (lane < 16) cnt[lane] = 0;
    u32 tag;
    if (r < 13) {
      u32 id[4]; u32x4 xg[8];
#pragma unroll
      for (int m = 0; m < 2; ++m) {
        const int p0 = 4 * r + 2 * m, t = p0 >= NF ? 1 : 0, f0 = p0 - t * NF;
        const u32x2 ia = __builtin_amdgcn_raw_buffer_load_b64(ridx, (lane & 31) * (NF * 4), ((g * 64 + t * 32) * NF + f0) * 4, 0);
        id[2 * m] = ia.x; id[2 * m + 1] = ia.y;
      }
      const u32 ep = load_epoch(a.ws);
#pragma unroll
      for (int k = 0; k < 8; ++k) xg[k] = __builtin_amdgcn_raw_buffer_load_b128(rtab, id[k >> 1] * 64 + (k & 1) * 32 + (lane >> 5) * 16, 0, 0);
      lds_barrier();
      STAMP(1);
      tag = __builtin_amdgcn_readfirstlane(ep) + 1u;
      cnt[C_TAGV] = tag;
      team_arrive(cnt + C_TAG, lane);
#pragma unroll
      for (int k = 0; k < 8; ++k) __builtin_amdgcn_raw_buffer_store_b128(xg[k], rws, WS_X + lane * 16, (g * 104 + r * 8 + k) * 1024, 16);
      STAMP(2);
      drain_vm();
    } else {
      const u32 ep = load_epoch(a.ws);
      lds_barrier();
      tag = __builtin_amdgcn_readfirstlane(ep) + 1u;
      cnt[C_TAGV] = tag;
      team_arrive(cnt + C_TAG, lane);
    }
    const u64 flag = mkflag(tag);
    if (lane == 0) store_flag(a.ws, WS_XF + (g * 16 + r) * FL, flag);
    STAMP(3);
    if (r < 13) {
      __builtin_amdgcn_s_sleep(P_XDELAY);
      poll_flags<2>(a.ws + WS_XF + (g * 16 + (lane < 13 ? lane : 0)) * FL, flag);
      lds_inc(cnt + C_XRDY);
    }
    STAMP(4);
    __builtin_amdgcn_s_sleep(P_BDELAY);
    poll_flags<6>(a.ws + (lane < 32 ? WS_FA : WS_FB) + ((lane & 31) < NF ? (lane & 31) : 0) * FL, flag);
    lds_inc(cnt + C_ABRDY);
    STAMP(5);
  } else if (w >= 4) {
    const int cw = w - 4, smp = g * 64 + r * 4 + cw;
    u32 id[2]; u32x4 xr[2];
    u32 bo;
    {
      const CAS u32* ip = (const CAS u32*)(a.idx + smp * NF);
      u32 is[NF];
#pragma unroll
      for (int f = 0; f < NF; ++f) is[f] = ip[f];
      const size_t bop = (size_t)a.bo;
      bo = *(const CAS u32*)(bop & ~(size_t)3) >> ((bop & 2) * 8);
      u32 iv = 0;
      asm("v_writelane_b32 %0, %1, 0\n\tv_writelane_b32 %0, %2, 1\n\tv_writelane_b32 %0, %3, 2\n\tv_writelane_b32 %0, %4, 3\n\tv_writelane_b32 %0, %5, 4\n\tv_writelane_b32 %0, %6, 5\n\tv_writelane_b32 %0, %7, 6\n\tv_writelane_b32 %0, %8, 7\n\tv_writelane_b32 %0, %9, 8\n\tv_writelane_b32 %0, %10, 9\n\tv_writelane_b32 %0, %11, 10\n\tv_writelane_b32 %0, %12, 11\n\tv_writelane_b32 %0, %13, 12\n\tv_writelane_b32 %0, %14, 13\n\tv_writelane_b32 %0, %15, 14\n\tv_writelane_b32 %0, %16, 15\n\tv_writelane_b32 %0, %17, 16\n\tv_writelane_b32 %0, %18, 17\n\tv_writelane_b32 %0, %19, 18\n\tv_writelane_b32 %0, %20, 19\n\tv_writelane_b32 %0, %21, 20\n\tv_writelane_b32 %0, %22, 21\n\tv_writelane_b32 %0, %23, 22\n\tv_writelane_b32 %0, %24, 23\n\tv_writelane_b32 %0, %25, 24\n\tv_writelane_b32 %0, %26, 25"
          : "+v"(iv) : "s"(is[0]), "s"(is[1]), "s"(is[2]), "s"(is[3]), "s"(is[4]), "s"(is[5]), "s"(is[6]), "s"(is[7]), "s"(is[8]), "s"(is[9]), "s"(is[10]), "s"(is[11]), "s"(is[12]), "s"(is[13]), "s"(is[14]), "s"(is[15]), "s"(is[16]), "s"(is[17]), "s"(is[18]), "s"(is[19]), "s"(is[20]), "s"(is[21]), "s"(is[22]), "s"(is[23]), "s"(is[24]), "s"(is[25]));
      id[0] = __builtin_amdgcn_ds_bpermute((lane >> 2) * 4, iv);
      id[1] = __builtin_amdgcn_ds_bpermute((16 + (lane >> 2)) * 4, iv);
    }
    if (r < 13) __builtin_amdgcn_s_setprio(2);
    lds_barrier();
#pragma unroll
    for (int m = 0; m < 2; ++m) { const int f = 16 * m + (lane >> 2); xr[m] = __builtin_amdgcn_raw_buffer_load_b128(rtab, f < NF ? id[m] * 64 + (lane & 3) * 16 : OOB, 0, 0); }
    __builtin_amdgcn_s_setprio(0);
    STAMP(1);
    f32x2 xp[14];
    {
      const u32 o = L_XS + cw * XS_S + (lane >> 2) * 64 + (lane & 3) * 16;
      *(LAS u32x4*)(lds + o) = xr[0];
      u32x4 x1 = xr[1];
      if ((lane >> 2) == NF - 16) x1 = (u32x4){0x3F803F80u, 0x3F803F80u, 0x3F803F80u, 0x3F803F80u};
      *(LAS u32x4*)(lds + opq((lane >> 2) < 12 ? o + 1024 : L_XS + cw * XS_S + 27 * 64 + (lane & 3) * 16)) = x1;
      asm volatile("s_waitcnt lgkmcnt(0)" ::: "memory");
      const int hh = lane >> 5, q = (lane >> 2) & 3, pp = lane & 3;
      const u32 base = L_XS + cw * XS_S + ((lane >> 4) & 1) * 32 + pp * 8;
      const u32 a0 = base + (hh ? q + 2 : q) * 64, a1 = base + (hh ? ((q + 6) & 7) : q + 4) * 64;
      const u32 a6 = base + (24 + ((hh && q < 2) ? (q ^ 1) : q)) * 64;
#pragma unroll
      for (int t = 0; t < 7; ++t) {
        s16x4 y = __builtin_amdgcn_ds_read_tr16_b64_v4i16((LAS s16x4*)(lds + (t < 6 ? ((t & 1) ? a1 : a0) + (t >> 1) * 512 : a6)));
        u32x2 u = __builtin_bit_cast(u32x2, y);
        xp[2 * t] = (f32x2){bflo(u.x), bfhi(u.x)}; xp[2 * t + 1] = (f32x2){bflo(u.y), bfhi(u.y)};
      }
    }
    bf16x8 z[24];
#pragma unroll
    for (int s = 0; s < 24; ++s) {
      u32x4 zz;
#pragma unroll
      for (int e = 0; e < 4; ++e) {
        const int s1 = 8 * s + 2 * e, s2 = s1 + 1;
        const int a1 = SL.a[s1], b1 = SL.b[s1], a2 = SL.a[s2], b2 = SL.b[s2];
        if (a1 == a2 && (b1 & 1) == 0 && b1 < 26 && b2 == b1 + 1) {
          const f32x2 pr = (a1 & 1) ? pkmul_bc<1>(xp[a1 >> 1], xp[b1 >> 1]) : pkmul_bc<0>(xp[a1 >> 1], xp[b1 >> 1]);
          zz[e] = packbf(pr[0], pr[1]);
        } else {
          const float p1 = b1 == 26 ? xp[a1 >> 1][a1 & 1] : mulf(xp[a1 >> 1][a1 & 1], xp[b1 >> 1][b1 & 1]);
          const float p2 = b2 == 26 ? xp[a2 >> 1][a2 & 1] : mulf(xp[a2 >> 1][a2 & 1], xp[b2 >> 1][b2 & 1]);
          zz[e] = packbf(p1, p2);
        }
      }
      z[s] = __builtin_bit_cast(bf16x8, zz);
      asm volatile("" : "+v"(z[s]));
    }
    const u32 tag = get_tag(cnt);
    const u64 flag = mkflag(tag);
    STAMP(2);
    team_wait<1>(cnt + C_ABRDY, 1);
    STAMP(3);
    if (r < 13) team_wait<1>(cnt + C_XISS, 4);
    __builtin_amdgcn_s_setprio(2);
    STAMP(4);
    {
      u32x4 y[10];
#pragma unroll
      for (int m = 0; m < 10; ++m) {
        const u32 n = ((m % 5) * 4 + cw) * 64 + lane;
        y[m] = __builtin_amdgcn_raw_buffer_load_b128(rws, n < NF * 48 ? n * 16 : OOB, m < 5 ? WS_PA : WS_PB, 16);
      }
#pragma unroll
      for (int m = 0; m < 5; ++m) {
        const u32 n = (m * 4 + cw) * 64 + lane, i = (n * 1366u) >> 16, c = n - i * 48;
        const u32 o = opq(n < NF * 48 ? L_A + c * APITCH + i * 16 : L_A + 26 * 16);
        *(LAS u32x4*)(lds + o) = y[m];
        *(LAS u32x4*)(lds + o + (L_B - L_A)) = y[5 + m];
      }
      team_arrive(cnt + C_ABSTG, lane);
      team_wait<0>(cnt + C_ABSTG, 4);
    }
    __builtin_amdgcn_s_setprio(0);
    STAMP(5);
    const u32 afr = (lane >> 5) * APITCH + (lane & 31) * 16;
    const u32 tag2 = (u32)(flag >> 32);
    const int sg = r * 4 + cw;
    const u32 po = opq(lane < 26 ? WS_P + ((g * 26 + lane) * 2 + (sg >> 5)) * 512 + (sg & 31) * 16 : OOB);
    u32x4 yp;
    f32x16 acc1 = {}, acc2 = {};
    {
      constexpr int AD = 2;
      bf16x8 af[24], bf[24];
#pragma unroll
      for (int s = 0; s < AD; ++s) { af[s] = *(LAS bf16x8*)(lds + L_A + afr + s * 2 * APITCH); bf[s] = *(LAS bf16x8*)(lds + L_B + afr + s * 2 * APITCH); }
      __builtin_amdgcn_sched_barrier(0);
#pragma unroll
      for (int s = 0; s < 24; ++s) {
        if (s + AD < 24) { af[s + AD] = *(LAS bf16x8*)(lds + L_A + afr + (s + AD) * 2 * APITCH); bf[s + AD] = *(LAS bf16x8*)(lds + L_B + afr + (s + AD) * 2 * APITCH); }
        if (s == P_PQ) yp = __builtin_amdgcn_raw_buffer_load_b128(rws, po, 0, 16);
        acc1 = __builtin_amdgcn_mfma_f32_32x32x16_bf16(af[s], z[s], acc1, 0, 0, 0);
        acc2 = __builtin_amdgcn_mfma_f32_32x32x16_bf16(bf[s], z[s], acc2, 0, 0, 0);
        __builtin_amdgcn_sched_barrier(0);
      }
    }
    float sum = 0.f;
#pragma unroll
    for (int e = 0; e < 12; ++e) sum += acc1[e] * acc2[e];
    { const float t = acc1[12] * acc2[12] + acc1[13] * acc2[13]; sum += lane < 32 ? t : 0.f; }
    sum = sum_rows(sum_row16(sum));
    STAMP(6);
    for (u32 it = 0; it < SPIN_MAX; ++it) {
      const bool ok = lane >= 26 || (yp.y == tag && yp.w == tag2);
      if (__all(ok)) break;
      asm volatile("" ::: "memory");
      __builtin_amdgcn_s_sleep(1);
      yp = __builtin_amdgcn_raw_buffer_load_b128(rws, po, 0, 16);
    }
    float part = sum_rows(sum_row16(__uint_as_float(yp.x)));
    if (lane == 0) {
      const __bf16 ob = (__bf16)(sum + part + bflo(bo));
      a.out[smp] = __builtin_bit_cast(uint16_t, ob);
    }
    STAMP(7);
  } else if (r < 13) {
      const rsrc_t rw1 = mkrs(a.w1, DIN * DIN * 2);
      const int mh = w & 1, kh = w >> 1, hh = lane >> 5;
      lds_barrier();
      STAMP(1);
      {
        u32x4 v[26];
        const u32 vo = (w * 208 + (lane >> 3)) * 1664 + r * 128 + (lane & 7) * 16;
        const int c = lane & 7;
        const u32 lo = L_W + (w * 208 + (lane >> 3)) * 128 + (((c >> 1) ^ (((lane >> 4) & 1) << 1)) * 32) + (c & 1) * 16;
#pragma unroll
        for (int i = 0; i < 26; ++i) v[i] = __builtin_amdgcn_raw_buffer_load_b128(rw1, vo, i * 8 * 1664, 0);
#pragma unroll
        for (int i = 0; i < 26; ++i) *(LAS u32x4*)(lds + lo + i * 1024) = v[i];
      }
      team_arrive(cnt + C_W, lane);
      const u32 tag = get_tag(cnt);
      const u64 flag = mkflag(tag);
      f32x16 acc[2];
      f32x2 wf[8];
      {
        const CAS u32* bp = (const CAS u32*)(a.b1 + r * 64 + kh * 32);
        const CAS u32* wp = (const CAS u32*)(a.w2 + r * 64 + kh * 32);
        u32 bs[16], ws_[16];
#pragma unroll
        for (int t = 0; t < 16; ++t) { bs[t] = bp[t]; ws_[t] = wp[t]; }
#pragma unroll
        for (int t = 0; t < 16; ++t) asm volatile("" : "+s"(bs[t]), "+s"(ws_[t]));
#pragma unroll
        for (int t = 0; t < 4; ++t) {
          const u32 b0 = hh ? bs[4 * t + 2] : bs[4 * t], b1 = hh ? bs[4 * t + 3] : bs[4 * t + 1];
          const u32 w0 = hh ? ws_[4 * t + 2] : ws_[4 * t], w1 = hh ? ws_[4 * t + 3] : ws_[4 * t + 1];
          acc[0][4 * t] = bflo(b0); acc[0][4 * t + 1] = bfhi(b0); acc[0][4 * t + 2] = bflo(b1); acc[0][4 * t + 3] = bfhi(b1);
          acc[1][4 * t] = 0.f; acc[1][4 * t + 1] = 0.f; acc[1][4 * t + 2] = 0.f; acc[1][4 * t + 3] = 0.f;
          wf[2 * t] = (f32x2){bflo(w0), bfhi(w0)}; wf[2 * t + 1] = (f32x2){bflo(w1), bfhi(w1)};
          asm volatile("" : "+v"(wf[2 * t]), "+v"(wf[2 * t + 1]));
        }
      }
      STAMP(2);
      team_wait<0>(cnt + C_XRDY, 1);
      STAMP(3);
      constexpr int XW = P_XW, FD = 3;
      u32x4 xv[26];
      auto ldx = [&](int i) { xv[i] = __builtin_amdgcn_raw_buffer_load_b128(rws, WS_X + lane * 16, (g * 104 + mh * 52 + kh * 26 + i) * 1024, 16); };
#pragma unroll
      for (int i = 0; i < XW; ++i) ldx(i);
      team_wait<0>(cnt + C_W, 4);
      __builtin_amdgcn_s_setprio(1);
      {
        const int q = (lane >> 2) & 3, pp = lane & 3, gam = (lane >> 4) & 1;
        const u32 rb = L_W + kh * 26 * 2048 + (8 * hh + q) * 128 + pp * 8;
        const u32 ad0 = rb + (((2 * kh + gam) ^ (q & 2)) * 32), ad1 = rb + (((2 * (kh ^ 1) + gam) ^ (q & 2)) * 32);
        bf16x8 fr[26][2];
        auto ldf = [&](int i) {
#pragma unroll
          for (int t = 0; t < 2; ++t) {
            const u32 o = (t ? ad1 : ad0) + i * 2048;
            const s16x4 y0 = __builtin_amdgcn_ds_read_tr16_b64_v4i16((LAS s16x4*)(lds + o));
            const s16x4 y1 = __builtin_amdgcn_ds_read_tr16_b64_v4i16((LAS s16x4*)(lds + o + 512));
            fr[i][t] = __builtin_bit_cast(bf16x8, __builtin_shufflevector(y0, y1, 0, 1, 2, 3, 4, 5, 6, 7));
          }
        };
#pragma unroll
        for (int i = 0; i < FD; ++i) ldf(i);
        __builtin_amdgcn_sched_barrier(0);
#pragma unroll
        for (int i = 0; i < 26; ++i) {
          if (i + XW < 26) ldx(i + XW);
          if (i == P_XI) lds_inc(cnt + C_XISS);
          if (i == 8) lds_inc(cnt + C_WD + kh);
          if (i == 22) team_wait<0>(cnt + C_WD + kh, 2);
          if (i + FD < 26) ldf(i + FD);
          const bf16x8 xb = __builtin_bit_cast(bf16x8, xv[i]);
          acc[0] = __builtin_amdgcn_mfma_f32_32x32x16_bf16(fr[i][0], xb, acc[0], 0, 0, 0);
          acc[1] = __builtin_amdgcn_mfma_f32_32x32x16_bf16(fr[i][1], xb, acc[1], 0, 0, 0);
          __builtin_amdgcn_sched_barrier(0);
        }
      }
      STAMP(4);
      {
        const u32 so = L_W + kh * 26 * 2048 + mh * 4096 + lane * 16;
#pragma unroll
        for (int t = 0; t < 4; ++t) *(LAS f32x4*)(lds + so + t * 1024) = (f32x4){acc[1][4 * t], acc[1][4 * t + 1], acc[1][4 * t + 2], acc[1][4 * t + 3]};
        team_arrive(cnt + C_RED + mh, lane);
        team_wait<0>(cnt + C_RED + mh, 2);
      }
      STAMP(5);
      float part;
      {
        constexpr float K1 = -2.0f * 1.4426950408889634f * 0.7978845608028654f, K2 = K1 * 0.044715f;
        const u32 si = L_W + (kh ^ 1) * 26 * 2048 + mh * 4096 + lane * 16;
        f32x4 o[4];
#pragma unroll
        for (int t = 0; t < 4; ++t) o[t] = *(LAS f32x4*)(lds + si + t * 1024);
        f32x2 h[8], u[8], y[8];
#pragma unroll
        for (int m = 0; m < 8; ++m) {
          h[m] = (f32x2){acc[0][2 * m], acc[0][2 * m + 1]} + (f32x2){o[m >> 1][2 * (m & 1)], o[m >> 1][2 * (m & 1) + 1]};
          u[m] = (h[m] * h[m] * K2 + K1) * h[m];
          y[m] = h[m] * wf[m];
        }
#pragma unroll
        for (int m = 0; m < 8; ++m) { u[m][0] = __builtin_amdgcn_exp2f(u[m][0]); u[m][1] = __builtin_amdgcn_exp2f(u[m][1]); }
#pragma unroll
        for (int m = 0; m < 8; ++m) { u[m] = u[m] + 1.0f; }
#pragma unroll
        for (int m = 0; m < 8; ++m) { u[m][0] = __builtin_amdgcn_rcpf(u[m][0]); u[m][1] = __builtin_amdgcn_rcpf(u[m][1]); }
        f32x2 p0 = y[0] * u[0], p1 = y[1] * u[1];
#pragma unroll
        for (int m = 2; m < 8; m += 2) { p0 += y[m] * u[m]; p1 += y[m + 1] * u[m + 1]; }
        p0 += p1;
        part = p0[0] + p0[1];
      }
      {
        auto sw = __builtin_amdgcn_permlane32_swap(__float_as_uint(part), __float_as_uint(part), false, false);
        part = __uint_as_float(sw[0]) + __uint_as_float(sw[1]);
      }
      {
        const u32x4 o = {__float_as_uint(part), tag, __float_as_uint(part), (u32)(flag >> 32)};
        __builtin_amdgcn_raw_buffer_store_b128(o, rws, lane < 32 ? WS_P + ((g * 26 + r * 2 + kh) * 2 + mh) * 512 + lane * 16 : OOB, 0, 16);
      }
      STAMP(6);
  } else {
      const int p = g * 3 + r - 13;
      const rsrc_t rwc = mkrs(a.wc, 3 * CUBE * 2), rwo = mkrs(a.wco, 78 * 2);
      __builtin_amdgcn_s_setprio(3);
      if (p < NF) {
        const int i = p;
        u32x4 tb[3];
        const int hb = (w >> 1) * 13, el = (w & 1) * 64 + lane;
        const CAS u32* w3p = (const CAS u32*)(a.wco + 52);
        u32 w3[13];
#pragma unroll
        for (int h = 0; h < 13; ++h) w3[h] = w3p[h];
        u32x4 v[13];
#pragma unroll
        for (int h = 0; h < 13; ++h) v[h] = __builtin_amdgcn_raw_buffer_load_b128(rwc, el < 85 ? (2 * CUBE + hb * 676 + 8 * el) * 2 : OOB, h * 676 * 2, 0);
        u32 v2[2];
#pragma unroll
        for (int m = 0; m < 2; ++m) {
          const u32 d = tid + 256 * m, h = (d * 5042u) >> 16, e = d - 13 * h;
          v2[m] = __builtin_amdgcn_raw_buffer_load_b32(rwc, d < 338 ? (CUBE + (h * NF + i) * NF) * 2 + e * 4 : OOB, 0, 0);
        }
        const u32 wq = __builtin_amdgcn_raw_buffer_load_b16(rwo, tid < NF ? (NF + tid) * 2 : OOB, 0, 0);
        const u32 w1i = __builtin_amdgcn_raw_buffer_load_b16(rwo, i * 2, 0, 0);
        if (w == 0) {
#pragma unroll
          for (int m = 0; m < 3; ++m) tb[m] = *(const GAS u32x4*)SYMTAB.w[lane + 64 * m];
        }
        __builtin_amdgcn_sched_barrier(0);
        lds_barrier();
        STAMP(1);
        {
          float acc[8];
#pragma unroll
          for (int e = 0; e < 8; ++e) acc[e] = 0.f;
#pragma unroll
          for (int h = 0; h < 13; ++h) {
            const u32 we = (h & 1) ? w3[h >> 1] >> 16 : w3[h >> 1] & 0xffffu;
            const u32 wo = (h & 1) ? w3[(13 + h) >> 1] & 0xffffu : w3[(13 + h) >> 1] >> 16;
            const u32 wl = hb ? wo : we, wh = wl << 16;
#pragma unroll
            for (int e = 0; e < 4; ++e) { acc[2 * e] = dot2bf(v[h][e], wl, acc[2 * e]); acc[2 * e + 1] = dot2bf(v[h][e], wh, acc[2 * e + 1]); }
          }
          const u32 vo = opq(el < 85 ? L_V3 + (w >> 1) * 2816 + el * 32 : L_V3 + 5632 + lane * 32);
          *(LAS f32x4*)(lds + vo) = (f32x4){acc[0], acc[1], acc[2], acc[3]};
          *(LAS f32x4*)(lds + vo + 16) = (f32x4){acc[4], acc[5], acc[6], acc[7]};
#pragma unroll
          for (int m = 0; m < 2; ++m) *(LAS u32*)(lds + L_W2S + (tid + 256 * m) * 4) = v2[m];
          if (w == 0) *(LAS u32*)(lds + L_W2C + lane * 4) = wq << 16;
        }
        const int tj = w >> 1, tp = w & 1, kq = lane >> 4;
        const int jr = 16 * tj + (lane & 15), jp = 16 * tp + (lane & 15);
        u32 oa[7], ob[7], oc[7];
#pragma unroll
        for (int s = 0; s < 7; ++s) {
          const int h = 4 * s + kq;
          const bool inv = h < NF && jp < NF;
          oa[s] = opq((h < NF && jr < NF) ? L_W2S + (h * NF + jr) * 2 : L_W2C + 63 * 4);
          ob[s] = opq(inv ? L_V3 + (h * NF + jp) * 4 : L_W2C + ((h < NF && jp == NF) ? h : 63) * 4);
          oc[s] = opq(inv ? L_V3 + 2816 + (h * NF + jp) * 4 : L_W2C + 63 * 4);
        }
        team_arrive(cnt + C_V3, lane);
        team_wait<0>(cnt + C_V3, 4);
        STAMP(2);
        u32 av[7]; float bv[7], bw[7];
#pragma unroll
        for (int s = 0; s < 7; ++s) { av[s] = *(LAS uint16_t*)(lds + oa[s]); bv[s] = *(LAS float*)(lds + ob[s]); bw[s] = *(LAS float*)(lds + oc[s]); }
#pragma unroll
        for (int s = 0; s < 7; ++s) bv[s] += bw[s];
        f32x4 u = {0.f, 0.f, 0.f, 0.f}, u2 = {0.f, 0.f, 0.f, 0.f};
#pragma unroll
        for (int s = 0; s < 7; ++s) {
          if (s & 1) u2 = __builtin_amdgcn_mfma_f32_16x16x4f32(bflo(av[s]), bv[s], u2, 0, 0, 0);
          else u = __builtin_amdgcn_mfma_f32_16x16x4f32(bflo(av[s]), bv[s], u, 0, 0, 0);
        }
        u += u2;
#pragma unroll
        for (int e = 0; e < 4; ++e) {
          const int jo = 16 * tj + 4 * kq + e;
          const float val = (jo == NF && jp == NF) ? bflo(w1i) : u[e];
          *(LAS float*)(lds + L_U + (jo * UP + jp) * 4) = val;
        }
        team_arrive(cnt + C_U, lane);
        if (w == 0) {
          team_wait<0>(cnt + C_U, 4);
          STAMP(3);
          LAS float* U = (LAS float*)(lds + L_U);
          float e[12];
#pragma unroll
          for (int m = 0; m < 3; ++m) {
            e[4 * m] = U[tb[m].x & 0xffff]; e[4 * m + 1] = U[tb[m].x >> 16]; e[4 * m + 2] = U[tb[m].y & 0xffff]; e[4 * m + 3] = U[tb[m].y >> 16];
          }
#pragma unroll
          for (int m = 0; m < 3; ++m)
            __builtin_amdgcn_raw_buffer_store_b32(packbf(e[4 * m] + e[4 * m + 1], e[4 * m + 2] + e[4 * m + 3]), rws, WS_PB + i * 768 + (lane + 64 * m) * 4, 0, 16);
          const u64 flagb = mkflag(get_tag(cnt));
          drain_vm();
          if (lane == 0) store_flag(a.ws, WS_FB + i * FL, flagb);
          STAMP(4);
        }
      } else {
        const int i = (p - NF) * 4 + w;
        const bool prod = p < 33 && i < NF;
        u32 v[6]; u32x4 tb[3];
#pragma unroll
        for (int m = 0; m < 6; ++m) { const int d = lane + 64 * m; v[m] = __builtin_amdgcn_raw_buffer_load_b32(rwc, (prod && d < 338) ? i * 1352 + d * 4 : OOB, 0, 0); }
#pragma unroll
        for (int m = 0; m < 3; ++m) tb[m] = *(const GAS u32x4*)SYMTAB.w[lane + 64 * m];
        __builtin_amdgcn_sched_barrier(0);
        lds_barrier();
        STAMP(1);
        if (prod) {
          LAS char* raw = lds + L_RAW + w * 2048;
#pragma unroll
          for (int m = 0; m < 6; ++m) *(LAS u32*)(raw + (lane + 64 * m) * 4) = v[m];
          asm volatile("s_waitcnt lgkmcnt(0)" ::: "memory");
          LAS uint16_t* R = (LAS uint16_t*)raw;
          u32 e[12];
#pragma unroll
          for (int m = 0; m < 3; ++m) {
            e[4 * m] = R[tb[m].z & 0xffff]; e[4 * m + 1] = R[tb[m].z >> 16]; e[4 * m + 2] = R[tb[m].w & 0xffff]; e[4 * m + 3] = R[tb[m].w >> 16];
          }
#pragma unroll
          for (int m = 0; m < 3; ++m)
            __builtin_amdgcn_raw_buffer_store_b32(packbf(bflo(e[4 * m]) + bflo(e[4 * m + 1]), bflo(e[4 * m + 2]) + bflo(e[4 * m + 3])), rws,
                                                  WS_PA + i * 768 + (lane + 64 * m) * 4, 0, 16);
          const u64 flag0 = mkflag(get_tag(cnt));
          drain_vm();
          if (lane == 0) store_flag(a.ws, WS_FA + i * FL, flag0);
          STAMP(4);
        }
        if (p == 47 && w == 0) {
          const u32 tag0 = get_tag(cnt);
          const u64 flag0 = mkflag(tag0);
          __builtin_amdgcn_s_sleep(32);
#pragma unroll 1
          for (int m = 0; m < 4; ++m) poll_flags<16>(a.ws + WS_XF + (lane + 64 * m) * FL, flag0);
          if (lane == 0) __hip_atomic_store((GAS u32*)(a.ws + WS_E), tag0, __ATOMIC_RELAXED, __HIP_MEMORY_SCOPE_AGENT);
        }
      }
  }
}

}

extern "C" void kernel_launch(void* const* d_in, const int* in_sizes, int n_in, void* d_out, int out_size,
                              void* d_ws, size_t ws_size, hipStream_t stream) {
  using namespace xdfm;
  Args a;
  a.idx = (const int*)d_in[0]; a.table = (const uint16_t*)d_in[1]; a.wc = (const uint16_t*)d_in[2];
  a.wco = (const uint16_t*)d_in[3]; a.w1 = (const uint16_t*)d_in[4]; a.b1 = (const uint16_t*)d_in[5];
  a.w2 = (const uint16_t*)d_in[6]; a.bo = (const uint16_t*)d_in[7]; a.out = (uint16_t*)d_out; a.ws = (char*)d_ws;
  a.table_bytes = (u32)in_sizes[1] * 2u; a.pad_ = 0;
  (void)hipFuncSetAttribute((const void*)xdfm_kernel, hipFuncAttributeMaxDynamicSharedMemorySize, L_TOTAL);
  xdfm_kernel<<<256, NWAVE * 64, L_TOTAL, stream>>>(a);
}
```

```cpp
#include <hip/hip_runtime.h>
#include <hip/hip_bf16.h>
#include <stdint.h>

#define P_ADELAY 8
#define P_BDELAY 8
#define P_WW 10
#define P_PQ 16
#define P_XI 10
#define P_XDELAY 12
#define P_CG 4
#define P_XW 12
namespace xdfm {

typedef unsigned int u32;
typedef unsigned long long u64;
typedef u32 u32x2 __attribute__((ext_vector_type(2)));
typedef u32 u32x4 __attribute__((ext_vector_type(4)));
typedef short s16x4 __attribute__((ext_vector_type(4)));
typedef short s16x8 __attribute__((ext_vector_type(8)));
typedef float f32x2 __attribute__((ext_vector_type(2)));
typedef float f32x4 __attribute__((ext_vector_type(4)));
typedef float f32x16 __attribute__((ext_vector_type(16)));
typedef __bf16 bf16x2 __attribute__((ext_vector_type(2)));
typedef __bf16 bf16x8 __attribute__((ext_vector_type(8)));
#define GAS __attribute__((address_space(1)))
#define LAS __attribute__((address_space(3)))
typedef __amdgpu_buffer_rsrc_t rsrc_t;

constexpr int NF = 26, DIN = 832, NB = 1024, CUBE = 17576;

constexpr u32 WS_E = 0;
constexpr u32 FL = 128;
constexpr u32 WS_XF = 4096;
constexpr u32 WS_FA = 40960;
constexpr u32 WS_FB = 45056;
constexpr u32 WS_PA = 49152;
constexpr u32 WS_PB = 69632;
constexpr u32 WS_P = 131072;
constexpr u32 WS_X = 655360;
constexpr u32 WS_END = WS_X + 16 * 104 * 1024;
constexpr int NWAVE = 10;

constexpr u32 L_W = 0;
constexpr u32 APITCH = 432;
constexpr u32 L_A = 106496;
constexpr u32 L_B = L_A + 48 * APITCH;
constexpr u32 L_XS = L_B + 48 * APITCH;
constexpr u32 XS_S = 1824;
constexpr u32 L_CNT = L_XS + 4 * XS_S;
constexpr u32 L_TOTAL = L_CNT + 64;
constexpr u32 L_V3 = 0, L_W2C = 8192, L_W2S = 8448, L_U = 12288, L_RAW = 20480;
enum { C_TAGV = 12, C_TAG = 13, C_WISS = 14, C_ABRDY = 0, C_ABSTG = 1, C_W = 3, C_V3 = 4, C_U = 5, C_XISS = 6, C_XRDY = 7, C_WD = 8, C_RED = 10 };

constexpr int perm1(int j) { return j < 24 ? ((j & ~7) | ((j + 2) & 7)) : (j == 24 ? 25 : (j == 25 ? 24 : j)); }
struct Slots { int a[192]; int b[192]; bool v0[192]; bool v1[192]; int n; };
struct SlotBuilder {
  Slots s{}; bool vis[27][27] = {}; int n = 0;
  constexpr bool seen(int a, int b) const { return vis[a < b ? a : b][a < b ? b : a]; }
  constexpr void put(int a, int b) {
    s.a[n] = a; s.b[n] = b;
    s.v0[n] = !seen(a, b); vis[a < b ? a : b][a < b ? b : a] = true;
    const int na = perm1(a), nb = perm1(b);
    s.v1[n] = !seen(na, nb); vis[na < nb ? na : nb][na < nb ? nb : na] = true;
    ++n;
  }
};
constexpr int sg2(int m) { return perm1(perm1(2 * m)) >> 1; }
constexpr Slots make_slots() {
  SlotBuilder t{};
  for (int A = 0; A < 13; ++A)
    for (int B = A + 1; B < 13; ++B) {
      int ca = A, cb = B;
      while (!t.seen(2 * ca, 2 * cb)) {
        t.put(2 * ca, 2 * cb); t.put(2 * ca, 2 * cb + 1); t.put(2 * ca + 1, 2 * cb); t.put(2 * ca + 1, 2 * cb + 1);
        ca = sg2(ca); cb = sg2(cb);
      }
    }
  for (int A = 0; A < 13; ++A) {
    int ca = A;
    while (!t.seen(2 * ca, 2 * ca + 1)) {
      t.put(2 * ca, 2 * ca); t.put(2 * ca, 2 * ca + 1); t.put(2 * ca + 1, 2 * ca + 1); t.put(2 * ca + 1, 26);
      ca = sg2(ca);
    }
  }
  for (int a = 0; a < 27; ++a) { int ca = a; while (!t.seen(ca, 26)) { t.put(ca, 26); ca = perm1(perm1(ca)); } }
  t.s.n = t.n;
  for (int n = t.n; n < 192; ++n) { t.s.a[n] = 26; t.s.b[n] = 26; t.s.v0[n] = false; t.s.v1[n] = false; }
  return t.s;
}
constexpr Slots SL = make_slots();
static_assert(SL.n <= 192, "too many slots");

constexpr int UP = 33, ZB = 30 * UP, ZA = 676;
struct SymTab { u32 w[192][4]; };
constexpr SymTab make_symtab() {
  SymTab t{};
  for (int d = 0; d < 192; ++d) {
    int c = d >> 2, s = c >> 1, hh = c & 1;
    for (int e = 0; e < 2; ++e) {
      int sg = 8 * s + 2 * (d & 3) + e;
      int a = hh ? perm1(SL.a[sg]) : SL.a[sg], b = hh ? perm1(SL.b[sg]) : SL.b[sg];
      bool v = hh ? SL.v1[sg] : SL.v0[sg];
      u32 b1 = ZB, b2 = ZB, a1 = ZA, a2 = ZA;
      if (v) {
        b1 = a * UP + b; b2 = a == b ? ZB : b * UP + a;
        if (a < 26 && b < 26) { a1 = a * 26 + b; a2 = a == b ? ZA : b * 26 + a; }
      }
      t.w[d][e] = b1 | (b2 << 16);
      t.w[d][2 + e] = a1 | (a2 << 16);
    }
  }
  return t;
}
__device__ const SymTab SYMTAB = make_symtab();

constexpr u32 OOB = 0x7FFFFF00u;
constexpr u32 SPIN_MAX = 1u << 18;

__device__ __forceinline__ float bflo(u32 v) { return __uint_as_float(v << 16); }
__device__ __forceinline__ float bfhi(u32 v) { return __uint_as_float(v & 0xffff0000u); }
__device__ __forceinline__ u32 packbf(float a, float b) {
  f32x2 f = {a, b};
  bf16x2 z = __builtin_convertvector(f, bf16x2);
  return __builtin_bit_cast(u32, z);
}
__device__ __forceinline__ float mulf(float a, float b) { float r; asm("v_mul_f32 %0, %1, %2" : "=v"(r) : "v"(a), "v"(b)); return r; }
template <int E> __device__ __forceinline__ f32x2 pkmul_bc(f32x2 a, f32x2 b) {
  f32x2 r; asm("v_pk_mul_f32 %0, %1, %2 op_sel:[%3,0] op_sel_hi:[%3,1]" : "=v"(r) : "v"(a), "v"(b), "i"(E)); return r;
}
__device__ __forceinline__ float dot2bf(u32 a, u32 b, float c) {
  return __builtin_amdgcn_fdot2_f32_bf16(__builtin_bit_cast(bf16x2, a), __builtin_bit_cast(bf16x2, b), c, false);
}
__device__ __forceinline__ rsrc_t mkrs(const void* p, u32 bytes) { return __builtin_amdgcn_make_buffer_rsrc((void*)p, 0, bytes, 0x00020000); }
__device__ __forceinline__ u64 mkflag(u32 tag) { return (u64)tag | ((u64)(tag * 0x9E3779B1u + 0x7F4A7C15u) << 32); }
__device__ __forceinline__ void drain_vm() { asm volatile("s_waitcnt vmcnt(0)" ::: "memory"); }
__device__ __forceinline__ void lds_inc(LAS u32* cnt) {
  u64 keep;
  asm volatile("s_mov_b64 %0, exec\n\ts_mov_b64 exec, 1\n\tds_add_u32 %1, %2\n\ts_mov_b64 exec, %0"
               : "=&s"(keep) : "v"((u32)(size_t)cnt), "v"(1u) : "memory");
}
__device__ __forceinline__ void team_arrive(LAS u32* cnt, int) {
  asm volatile("s_waitcnt lgkmcnt(0)" ::: "memory");
  lds_inc(cnt);
}
__device__ __forceinline__ float sum_rows(float v) {
  auto a = __builtin_amdgcn_permlane32_swap(__float_as_uint(v), __float_as_uint(v), false, false);
  v = __uint_as_float(a[0]) + __uint_as_float(a[1]);
  auto b = __builtin_amdgcn_permlane16_swap(__float_as_uint(v), __float_as_uint(v), false, false);
  return __uint_as_float(b[0]) + __uint_as_float(b[1]);
}
template <int CTRL> __device__ __forceinline__ float dpp_add(float v) {
  return v + __uint_as_float(__builtin_amdgcn_update_dpp(0u, __float_as_uint(v), CTRL, 0xf, 0xf, true));
}
__device__ __forceinline__ float sum_row16(float v) {
  v = dpp_add<0x128>(v);
  v = dpp_add<0x124>(v);
  v = dpp_add<0x122>(v);
  v = dpp_add<0x121>(v);
  return v;
}

#define STAMP(k) do {} while (0)

struct Args {
  const int* idx; const uint16_t* table; const uint16_t* wc; const uint16_t* wco; const uint16_t* w1;
  const uint16_t* b1; const uint16_t* w2; const uint16_t* bo; uint16_t* out; char* ws;
  u32 table_bytes; u32 pad_;
};
#define CAS __attribute__((address_space(4)))

template <int SLEEP> __device__ __forceinline__ void team_wait(LAS u32* cnt, u32 target) {
  u32 v, s, n = SPIN_MAX;
  asm volatile(
      "1:\n\tds_read_b32 %0, %3\n\ts_waitcnt lgkmcnt(0)\n\tv_readfirstlane_b32 %1, %0\n\ts_cmp_ge_u32 %1, %4\n\ts_cbranch_scc1 2f\n\t"
      "s_sleep %5\n\ts_sub_u32 %2, %2, 1\n\ts_cbranch_scc0 1b\n2:"
      : "=&v"(v), "=&s"(s), "+s"(n) : "v"((u32)(size_t)cnt), "s"(target), "i"(SLEEP) : "memory", "scc");
}
template <int SLEEP> __device__ __forceinline__ void poll_flags(const char* p, u64 want) {
  u64 v; u32 n = SPIN_MAX;
  asm volatile(
      "1:\n\tglobal_load_dwordx2 %0, %2, off sc1\n\ts_waitcnt vmcnt(0)\n\tv_cmp_ne_u64 vcc, %3, %0\n\ts_nop 4\n\ts_cbranch_vccz 2f\n\t"
      "s_sleep %4\n\ts_sub_u32 %1, %1, 1\n\ts_cbranch_scc0 1b\n2:"
      : "=&v"(v), "+s"(n) : "v"(p), "s"(want), "i"(SLEEP) : "memory", "scc", "vcc");
}
__device__ __forceinline__ void lds_barrier() {
  asm volatile("s_waitcnt lgkmcnt(0)" ::: "memory");
  __builtin_amdgcn_s_barrier();
  asm volatile("" ::: "memory");
}
__device__ __forceinline__ u32 load_epoch(const char* ws) {
  return __hip_atomic_load((const GAS u32*)(ws + WS_E), __ATOMIC_RELAXED, __HIP_MEMORY_SCOPE_AGENT);
}
__device__ __forceinline__ void store_flag(char* ws, u32 off, u64 flag) {
  __hip_atomic_store((GAS u64*)(ws + off), flag, __ATOMIC_RELAXED, __HIP_MEMORY_SCOPE_AGENT);
}
__device__ __forceinline__ u32 get_tag(LAS u32* cnt) {
  team_wait<0>(cnt + C_TAG, 1);
  return __builtin_amdgcn_readfirstlane(cnt[C_TAGV]);
}
__device__ __forceinline__ u32 opq(u32 v) { asm("" : "+v"(v)); return v; }

__global__ void __launch_bounds__(640, 3) xdfm_kernel(Args a) {
  extern __shared__ __attribute__((aligned(16))) char lds_raw[];
  LAS char* lds = (LAS char*)lds_raw;
  const int tid = threadIdx.x, lane = tid & 63;
  const int w = __builtin_amdgcn_readfirstlane(tid >> 6);
  const int b = blockIdx.x, j = b >> 3;
  const int jg = j < 6 ? (j >= 3 ? 1 : 0) : (j >= 19 ? 1 : 0);
  const int g = (b & 7) * 2 + jg, r = j < 6 ? 13 + j - 3 * jg : j - 6 - 13 * jg;
  LAS u32* cnt = (LAS u32*)(lds + L_CNT);
  asm volatile("" :: "s"(a.idx), "s"(a.table), "s"(a.wc), "s"(a.wco), "s"(a.w1), "s"(a.b1), "s"(a.w2), "s"(a.bo), "s"(a.out), "s"(a.ws), "s"(a.table_bytes));
  const rsrc_t rws = mkrs(a.ws, WS_END);
  const rsrc_t ridx = mkrs(a.idx, NB * NF * 4), rtab = mkrs(a.table, a.table_bytes);

  if (w == 9) {
    lds_barrier();
    if (b == 250) {
      const rsrc_t rp = mkrs(a.w1, 1384448);
      u32 dep = 0;
#pragma unroll 1
      for (u32 i = 0; i < 256; ++i) {
        const u32 v = __builtin_amdgcn_raw_buffer_load_b32(rp, ((i * 4736u) % 1384448u) + dep, 0, 16);
        asm volatile("v_and_b32 %0, 0, %1" : "=v"(dep) : "v"(v));
      }
      asm volatile("" :: "v"(dep));
    }
  } else if (w == 8) {
    __builtin_amdgcn_s_setprio(3);
    if (lane < 16) cnt[lane] = 0;
    u32 tag;
    if (r < 13) {
      u32 id[4]; u32x4 xg[8];
#pragma unroll
      for (int m = 0; m < 2; ++m) {
        const int p0 = 4 * r + 2 * m, t = p0 >= NF ? 1 : 0, f0 = p0 - t * NF;
        const u32x2 ia = __builtin_amdgcn_raw_buffer_load_b64(ridx, (lane & 31) * (NF * 4), ((g * 64 + t * 32) * NF + f0) * 4, 0);
        id[2 * m] = ia.x; id[2 * m + 1] = ia.y;
      }
      const u32 ep = load_epoch(a.ws);
#pragma unroll
      for (int k = 0; k < 8; ++k) xg[k] = __builtin_amdgcn_raw_buffer_load_b128(rtab, id[k >> 1] * 64 + (k & 1) * 32 + (lane >> 5) * 16, 0, 0);
      lds_barrier();
      STAMP(1);
      tag = __builtin_amdgcn_readfirstlane(ep) + 1u;
      cnt[C_TAGV] = tag;
      team_arrive(cnt + C_TAG, lane);
#pragma unroll
      for (int k = 0; k < 8; ++k) __builtin_amdgcn_raw_buffer_store_b128(xg[k], rws, WS_X + lane * 16, (g * 104 + r * 8 + k) * 1024, 16);
      STAMP(2);
      drain_vm();
    } else {
      const u32 ep = load_epoch(a.ws);
      lds_barrier();
      tag = __builtin_amdgcn_readfirstlane(ep) + 1u;
      cnt[C_TAGV] = tag;
      team_arrive(cnt + C_TAG, lane);
    }
    const u64 flag = mkflag(tag);
    if (lane == 0) store_flag(a.ws, WS_XF + (g * 16 + r) * FL, flag);
    STAMP(3);
    if (r < 13) {
      __builtin_amdgcn_s_sleep(P_XDELAY);
      poll_flags<2>(a.ws + WS_XF + (g * 16 + (lane < 13 ? lane : 0)) * FL, flag);
      lds_inc(cnt + C_XRDY);
    }
    STAMP(4);
    __builtin_amdgcn_s_sleep(P_BDELAY);
    poll_flags<6>(a.ws + (lane < 32 ? WS_FA : WS_FB) + ((lane & 31) < NF ? (lane & 31) : 0) * FL, flag);
    lds_inc(cnt + C_ABRDY);
    STAMP(5);
  } else if (w >= 4) {
    const int cw = w - 4, smp = g * 64 + r * 4 + cw;
    u32 id[2]; u32x4 xr[2];
    u32 bo;
    {
      const CAS u32* ip = (const CAS u32*)(a.idx + smp * NF);
      u32 is[NF];
#pragma unroll
      for (int f = 0; f < NF; ++f) is[f] = ip[f];
      const size_t bop = (size_t)a.bo;
      bo = *(const CAS u32*)(bop & ~(size_t)3) >> ((bop & 2) * 8);
      u32 iv = 0;
      asm("v_writelane_b32 %0, %1, 0\n\tv_writelane_b32 %0, %2, 1\n\tv_writelane_b32 %0, %3, 2\n\tv_writelane_b32 %0, %4, 3\n\tv_writelane_b32 %0, %5, 4\n\tv_writelane_b32 %0, %6, 5\n\tv_writelane_b32 %0, %7, 6\n\tv_writelane_b32 %0, %8, 7\n\tv_writelane_b32 %0, %9, 8\n\tv_writelane_b32 %0, %10, 9\n\tv_writelane_b32 %0, %11, 10\n\tv_writelane_b32 %0, %12, 11\n\tv_writelane_b32 %0, %13, 12\n\tv_writelane_b32 %0, %14, 13\n\tv_writelane_b32 %0, %15, 14\n\tv_writelane_b32 %0, %16, 15\n\tv_writelane_b32 %0, %17, 16\n\tv_writelane_b32 %0, %18, 17\n\tv_writelane_b32 %0, %19, 18\n\tv_writelane_b32 %0, %20, 19\n\tv_writelane_b32 %0, %21, 20\n\tv_writelane_b32 %0, %22, 21\n\tv_writelane_b32 %0, %23, 22\n\tv_writelane_b32 %0, %24, 23\n\tv_writelane_b32 %0, %25, 24\n\tv_writelane_b32 %0, %26, 25"
          : "+v"(iv) : "s"(is[0]), "s"(is[1]), "s"(is[2]), "s"(is[3]), "s"(is[4]), "s"(is[5]), "s"(is[6]), "s"(is[7]), "s"(is[8]), "s"(is[9]), "s"(is[10]), "s"(is[11]), "s"(is[12]), "s"(is[13]), "s"(is[14]), "s"(is[15]), "s"(is[16]), "s"(is[17]), "s"(is[18]), "s"(is[19]), "s"(is[20]), "s"(is[21]), "s"(is[22]), "s"(is[23]), "s"(is[24]), "s"(is[25]));
      id[0] = __builtin_amdgcn_ds_bpermute((lane >> 2) * 4, iv);
      id[1] = __builtin_amdgcn_ds_bpermute((16 + (lane >> 2)) * 4, iv);
    }
    if (r < 13) __builtin_amdgcn_s_setprio(2);
    lds_barrier();
#pragma unroll
    for (int m = 0; m < 2; ++m) { const int f = 16 * m + (lane >> 2); xr[m] = __builtin_amdgcn_raw_buffer_load_b128(rtab, f < NF ? id[m] * 64 + (lane & 3) * 16 : OOB, 0, 0); }
    __builtin_amdgcn_s_setprio(0);
    STAMP(1);
    f32x2 xp[14];
    {
      const u32 o = L_XS + cw * XS_S + (lane >> 2) * 64 + (lane & 3) * 16;
      *(LAS u32x4*)(lds + o) = xr[0];
      u32x4 x1 = xr[1];
      if ((lane >> 2) == NF - 16) x1 = (u32x4){0x3F803F80u, 0x3F803F80u, 0x3F803F80u, 0x3F803F80u};
      *(LAS u32x4*)(lds + opq((lane >> 2) < 12 ? o + 1024 : L_XS + cw * XS_S + 27 * 64 + (lane & 3) * 16)) = x1;
      asm volatile("s_waitcnt lgkmcnt(0)" ::: "memory");
      const int hh = lane >> 5, q = (lane >> 2) & 3, pp = lane & 3;
      const u32 base = L_XS + cw * XS_S + ((lane >> 4) & 1) * 32 + pp * 8;
      const u32 a0 = base + (hh ? q + 2 : q) * 64, a1 = base + (hh ? ((q + 6) & 7) : q + 4) * 64;
      const u32 a6 = base + (24 + ((hh && q < 2) ? (q ^ 1) : q)) * 64;
#pragma unroll
      for (int t = 0; t < 7; ++t) {
        s16x4 y = __builtin_amdgcn_ds_read_tr16_b64_v4i16((LAS s16x4*)(lds + (t < 6 ? ((t & 1) ? a1 : a0) + (t >> 1) * 512 : a6)));
        u32x2 u = __builtin_bit_cast(u32x2, y);
        xp[2 * t] = (f32x2){bflo(u.x), bfhi(u.x)}; xp[2 * t + 1] = (f32x2){bflo(u.y), bfhi(u.y)};
      }
    }
    bf16x8 z[24];
#pragma unroll
    for (int s = 0; s < 24; ++s) {
      u32x4 zz;
#pragma unroll
      for (int e = 0; e < 4; ++e) {
        const int s1 = 8 * s + 2 * e, s2 = s1 + 1;
        const int a1 = SL.a[s1], b1 = SL.b[s1], a2 = SL.a[s2], b2 = SL.b[s2];
        if (a1 == a2 && (b1 & 1) == 0 && b1 < 26 && b2 == b1 + 1) {
          const f32x2 pr = (a1 & 1) ? pkmul_bc<1>(xp[a1 >> 1], xp[b1 >> 1]) : pkmul_bc<0>(xp[a1 >> 1], xp[b1 >> 1]);
          zz[e] = packbf(pr[0], pr[1]);
        } else {
          const float p1 = b1 == 26 ? xp[a1 >> 1][a1 & 1] : mulf(xp[a1 >> 1][a1 & 1], xp[b1 >> 1][b1 & 1]);
          const float p2 = b2 == 26 ? xp[a2 >> 1][a2 & 1] : mulf(xp[a2 >> 1][a2 & 1], xp[b2 >> 1][b2 & 1]);
          zz[e] = packbf(p1, p2);
        }
      }
      z[s] = __builtin_bit_cast(bf16x8, zz);
      asm volatile("" : "+v"(z[s]));
    }
    const u32 tag = get_tag(cnt);
    const u64 flag = mkflag(tag);
    STAMP(2);
    team_wait<1>(cnt + C_ABRDY, 1);
    STAMP(3);
    if (r < 13) team_wait<1>(cnt + C_XISS, 4);
    __builtin_amdgcn_s_setprio(2);
    STAMP(4);
    {
      u32x4 y[10];
#pragma unroll
      for (int m = 0; m < 10; ++m) {
        const u32 n = ((m % 5) * 4 + cw) * 64 + lane;
        y[m] = __builtin_amdgcn_raw_buffer_load_b128(rws, n < NF * 48 ? n * 16 : OOB, m < 5 ? WS_PA : WS_PB, 16);
      }
#pragma unroll
      for (int m = 0; m < 5; ++m) {
        const u32 n = (m * 4 + cw) * 64 + lane, i = (n * 1366u) >> 16, c = n - i * 48;
        const u32 o = opq(n < NF * 48 ? L_A + c * APITCH + i * 16 : L_A + 26 * 16);
        *(LAS u32x4*)(lds + o) = y[m];
        *(LAS u32x4*)(lds + o + (L_B - L_A)) = y[5 + m];
      }
      team_arrive(cnt + C_ABSTG, lane);
      team_wait<0>(cnt + C_ABSTG, 4);
    }
    __builtin_amdgcn_s_setprio(0);
    STAMP(5);
    const u32 afr = (lane >> 5) * APITCH + (lane & 31) * 16;
    const u32 tag2 = (u32)(flag >> 32);
    const int sg = r * 4 + cw;
    const u32 po = opq(lane < 26 ? WS_P + ((g * 26 + lane) * 2 + (sg >> 5)) * 512 + (sg & 31) * 16 : OOB);
    u32x4 yp;
    f32x16 acc1 = {}, acc2 = {};
    {
      constexpr int AD = 2;
      bf16x8 af[24], bf[24];
#pragma unroll
      for (int s = 0; s < AD; ++s) { af[s] = *(LAS bf16x8*)(lds + L_A + afr + s * 2 * APITCH); bf[s] = *(LAS bf16x8*)(lds + L_B + afr + s * 2 * APITCH); }
      __builtin_amdgcn_sched_barrier(0);
#pragma unroll
      for (int s = 0; s < 24; ++s) {
        if (s + AD < 24) { af[s + AD] = *(LAS bf16x8*)(lds + L_A + afr + (s + AD) * 2 * APITCH); bf[s + AD] = *(LAS bf16x8*)(lds + L_B + afr + (s + AD) * 2 * APITCH); }
        if (s == P_PQ) yp = __builtin_amdgcn_raw_buffer_load_b128(rws, po, 0, 16);
        acc1 = __builtin_amdgcn_mfma_f32_32x32x16_bf16(af[s], z[s], acc1, 0, 0, 0);
        acc2 = __builtin_amdgcn_mfma_f32_32x32x16_bf16(bf[s], z[s], acc2, 0, 0, 0);
        __builtin_amdgcn_sched_barrier(0);
      }
    }
    float sum = 0.f;
#pragma unroll
    for (int e = 0; e < 12; ++e) sum += acc1[e] * acc2[e];
    { const float t = acc1[12] * acc2[12] + acc1[13] * acc2[13]; sum += lane < 32 ? t : 0.f; }
    sum = sum_rows(sum_row16(sum));
    STAMP(6);
    for (u32 it = 0; it < SPIN_MAX; ++it) {
      const bool ok = lane >= 26 || (yp.y == tag && yp.w == tag2);
      if (__all(ok)) break;
      asm volatile("" ::: "memory");
      __builtin_amdgcn_s_sleep(1);
      yp = __builtin_amdgcn_raw_buffer_load_b128(rws, po, 0, 16);
    }
    float part = sum_rows(sum_row16(__uint_as_float(yp.x)));
    if (lane == 0) {
      const __bf16 ob = (__bf16)(sum + part + bflo(bo));
      a.out[smp] = __builtin_bit_cast(uint16_t, ob);
    }
    STAMP(7);
  } else if (r < 13) {
      const rsrc_t rw1 = mkrs(a.w1, DIN * DIN * 2);
      const int mh = w & 1, kh = w >> 1, hh = lane >> 5;
      lds_barrier();
      STAMP(1);
      {
        u32x4 v[26];
        const u32 vo = (w * 208 + (lane >> 3)) * 1664 + r * 128 + (lane & 7) * 16;
        const int c = lane & 7;
        const u32 lo = L_W + (w * 208 + (lane >> 3)) * 128 + (((c >> 1) ^ (((lane >> 4) & 1) << 1)) * 32) + (c & 1) * 16;
#pragma unroll
        for (int i = 0; i < 26; ++i) v[i] = __builtin_amdgcn_raw_buffer_load_b128(rw1, vo, i * 8 * 1664, 0);
#pragma unroll
        for (int i = 0; i < 26; ++i) *(LAS u32x4*)(lds + lo + i * 1024) = v[i];
      }
      team_arrive(cnt + C_W, lane);
      const u32 tag = get_tag(cnt);
      const u64 flag = mkflag(tag);
      f32x16 acc[2];
      f32x2 wf[8];
      {
        const CAS u32* bp = (const CAS u32*)(a.b1 + r * 64 + kh * 32);
        const CAS u32* wp = (const CAS u32*)(a.w2 + r * 64 + kh * 32);
        u32 bs[16], ws_[16];
#pragma unroll
        for (int t = 0; t < 16; ++t) { bs[t] = bp[t]; ws_[t] = wp[t]; }
#pragma unroll
        for (int t = 0; t < 16; ++t) asm volatile("" : "+s"(bs[t]), "+s"(ws_[t]));
#pragma unroll
        for (int t = 0; t < 4; ++t) {
          const u32 b0 = hh ? bs[4 * t + 2] : bs[4 * t], b1 = hh ? bs[4 * t + 3] : bs[4 * t + 1];
          const u32 w0 = hh ? ws_[4 * t + 2] : ws_[4 * t], w1 = hh ? ws_[4 * t + 3] : ws_[4 * t + 1];
          acc[0][4 * t] = bflo(b0); acc[0][4 * t + 1] = bfhi(b0); acc[0][4 * t + 2] = bflo(b1); acc[0][4 * t + 3] = bfhi(b1);
          acc[1][4 * t] = 0.f; acc[1][4 * t + 1] = 0.f; acc[1][4 * t + 2] = 0.f; acc[1][4 * t + 3] = 0.f;
          wf[2 * t] = (f32x2){bflo(w0), bfhi(w0)}; wf[2 * t + 1] = (f32x2){bflo(w1), bfhi(w1)};
          asm volatile("" : "+v"(wf[2 * t]), "+v"(wf[2 * t + 1]));
        }
      }
      STAMP(2);
      team_wait<0>(cnt + C_XRDY, 1);
      STAMP(3);
      constexpr int XW = P_XW, FD = 3;
      u32x4 xv[26];
      auto ldx = [&](int i) { xv[i] = __builtin_amdgcn_raw_buffer_load_b128(rws, WS_X + lane * 16, (g * 104 + mh * 52 + kh * 26 + i) * 1024, 16); };
#pragma unroll
      for (int i = 0; i < XW; ++i) ldx(i);
      team_wait<0>(cnt + C_W, 4);
      __builtin_amdgcn_s_setprio(1);
      {
        const int q = (lane >> 2) & 3, pp = lane & 3, gam = (lane >> 4) & 1;
        const u32 rb = L_W + kh * 26 * 2048 + (8 * hh + q) * 128 + pp * 8;
        const u32 ad0 = rb + (((2 * kh + gam) ^ (q & 2)) * 32), ad1 = rb + (((2 * (kh ^ 1) + gam) ^ (q & 2)) * 32);
        bf16x8 fr[26][2];
        auto ldf = [&](int i) {
#pragma unroll
          for (int t = 0; t < 2; ++t) {
            const u32 o = (t ? ad1 : ad0) + i * 2048;
            const s16x4 y0 = __builtin_amdgcn_ds_read_tr16_b64_v4i16((LAS s16x4*)(lds + o));
            const s16x4 y1 = __builtin_amdgcn_ds_read_tr16_b64_v4i16((LAS s16x4*)(lds + o + 512));
            fr[i][t] = __builtin_bit_cast(bf16x8, __builtin_shufflevector(y0, y1, 0, 1, 2, 3, 4, 5, 6, 7));
          }
        };
#pragma unroll
        for (int i = 0; i < FD; ++i) ldf(i);
        __builtin_amdgcn_sched_barrier(0);
#pragma unroll
        for (int i = 0; i < 26; ++i) {
          if (i + XW < 26) ldx(i + XW);
          if (i == P_XI) lds_inc(cnt + C_XISS);
          if (i == 8) lds_inc(cnt + C_WD + kh);
          if (i == 22) team_wait<0>(cnt + C_WD + kh, 2);
          if (i + FD < 26) ldf(i + FD);
          const bf16x8 xb = __builtin_bit_cast(bf16x8, xv[i]);
          acc[0] = __builtin_amdgcn_mfma_f32_32x32x16_bf16(fr[i][0], xb, acc[0], 0, 0, 0);
          acc[1] = __builtin_amdgcn_mfma_f32_32x32x16_bf16(fr[i][1], xb, acc[1], 0, 0, 0);
          __builtin_amdgcn_sched_barrier(0);
        }
      }
      STAMP(4);
      {
        const u32 so = L_W + kh * 26 * 2048 + mh * 4096 + lane * 16;
#pragma unroll
        for (int t = 0; t < 4; ++t) *(LAS f32x4*)(lds + so + t * 1024) = (f32x4){acc[1][4 * t], acc[1][4 * t + 1], acc[1][4 * t + 2], acc[1][4 * t + 3]};
        team_arrive(cnt + C_RED + mh, lane);
        team_wait<0>(cnt + C_RED + mh, 2);
      }
      STAMP(5);
      float part;
      {
        constexpr float K1 = -2.0f * 1.4426950408889634f * 0.7978845608028654f, K2 = K1 * 0.044715f;
        const u32 si = L_W + (kh ^ 1) * 26 * 2048 + mh * 4096 + lane * 16;
        f32x4 o[4];
#pragma unroll
        for (int t = 0; t < 4; ++t) o[t] = *(LAS f32x4*)(lds + si + t * 1024);
        f32x2 h[8], u[8], y[8];
#pragma unroll
        for (int m = 0; m < 8; ++m) {
          h[m] = (f32x2){acc[0][2 * m], acc[0][2 * m + 1]} + (f32x2){o[m >> 1][2 * (m & 1)], o[m >> 1][2 * (m & 1) + 1]};
          u[m] = (h[m] * h[m] * K2 + K1) * h[m];
          y[m] = h[m] * wf[m];
        }
#pragma unroll
        for (int m = 0; m < 8; ++m) { u[m][0] = __builtin_amdgcn_exp2f(u[m][0]); u[m][1] = __builtin_amdgcn_exp2f(u[m][1]); }
#pragma unroll
        for (int m = 0; m < 8; ++m) { u[m] = u[m] + 1.0f; }
#pragma unroll
        for (int m = 0; m < 8; ++m) { u[m][0] = __builtin_amdgcn_rcpf(u[m][0]); u[m][1] = __builtin_amdgcn_rcpf(u[m][1]); }
        f32x2 p0 = y[0] * u[0], p1 = y[1] * u[1];
#pragma unroll
        for (int m = 2; m < 8; m += 2) { p0 += y[m] * u[m]; p1 += y[m + 1] * u[m + 1]; }
        p0 += p1;
        part = p0[0] + p0[1];
      }
      {
        auto sw = __builtin_amdgcn_permlane32_swap(__float_as_uint(part), __float_as_uint(part), false, false);
        part = __uint_as_float(sw[0]) + __uint_as_float(sw[1]);
      }
      {
        const u32x4 o = {__float_as_uint(part), tag, __float_as_uint(part), (u32)(flag >> 32)};
        __builtin_amdgcn_raw_buffer_store_b128(o, rws, lane < 32 ? WS_P + ((g * 26 + r * 2 + kh) * 2 + mh) * 512 + lane * 16 : OOB, 0, 16);
      }
      STAMP(6);
  } else {
      const int p = g * 3 + r - 13;
      const rsrc_t rwc = mkrs(a.wc, 3 * CUBE * 2), rwo = mkrs(a.wco, 78 * 2);
      __builtin_amdgcn_s_setprio(3);
      if (p < NF) {
        const int i = p;
        u32x4 tb[3];
        const int hb = (w >> 1) * 13, el = (w & 1) * 64 + lane;
        const CAS u32* w3p = (const CAS u32*)(a.wco + 52);
        u32 w3[13];
#pragma unroll
        for (int h = 0; h < 13; ++h) w3[h] = w3p[h];
        u32x4 v[13];
#pragma unroll
        for (int h = 0; h < 13; ++h) v[h] = __builtin_amdgcn_raw_buffer_load_b128(rwc, el < 85 ? (2 * CUBE + hb * 676 + 8 * el) * 2 : OOB, h * 676 * 2, 0);
        u32 v2[2];
#pragma unroll
        for (int m = 0; m < 2; ++m) {
          const u32 d = tid + 256 * m, h = (d * 5042u) >> 16, e = d - 13 * h;
          v2[m] = __builtin_amdgcn_raw_buffer_load_b32(rwc, d < 338 ? (CUBE + (h * NF + i) * NF) * 2 + e * 4 : OOB, 0, 0);
        }
        const u32 wq = __builtin_amdgcn_raw_buffer_load_b16(rwo, tid < NF ? (NF + tid) * 2 : OOB, 0, 0);
        const u32 w1i = __builtin_amdgcn_raw_buffer_load_b16(rwo, i * 2, 0, 0);
        if (w == 0) {
#pragma unroll
          for (int m = 0; m < 3; ++m) tb[m] = *(const GAS u32x4*)SYMTAB.w[lane + 64 * m];
        }
        __builtin_amdgcn_sched_barrier(0);
        lds_barrier();
        STAMP(1);
        {
          float acc[8];
#pragma unroll
          for (int e = 0; e < 8; ++e) acc[e] = 0.f;
#pragma unroll
          for (int h = 0; h < 13; ++h) {
            const u32 we = (h & 1) ? w3[h >> 1] >> 16 : w3[h >> 1] & 0xffffu;
            const u32 wo = (h & 1) ? w3[(13 + h) >> 1] & 0xffffu : w3[(13 + h) >> 1] >> 16;
            const u32 wl = hb ? wo : we, wh = wl << 16;
#pragma unroll
            for (int e = 0; e < 4; ++e) { acc[2 * e] = dot2bf(v[h][e], wl, acc[2 * e]); acc[2 * e + 1] = dot2bf(v[h][e], wh, acc[2 * e + 1]); }
          }
          const u32 vo = opq(el < 85 ? L_V3 + (w >> 1) * 2816 + el * 32 : L_V3 + 5632 + lane * 32);
          *(LAS f32x4*)(lds + vo) = (f32x4){acc[0], acc[1], acc[2], acc[3]};
          *(LAS f32x4*)(lds + vo + 16) = (f32x4){acc[4], acc[5], acc[6], acc[7]};
#pragma unroll
          for (int m = 0; m < 2; ++m) *(LAS u32*)(lds + L_W2S + (tid + 256 * m) * 4) = v2[m];
          if (w == 0) *(LAS u32*)(lds + L_W2C + lane * 4) = wq << 16;
        }
        const int tj = w >> 1, tp = w & 1, kq = lane >> 4;
        const int jr = 16 * tj + (lane & 15), jp = 16 * tp + (lane & 15);
        u32 oa[7], ob[7], oc[7];
#pragma unroll
        for (int s = 0; s < 7; ++s) {
          const int h = 4 * s + kq;
          const bool inv = h < NF && jp < NF;
          oa[s] = opq((h < NF && jr < NF) ? L_W2S + (h * NF + jr) * 2 : L_W2C + 63 * 4);
          ob[s] = opq(inv ? L_V3 + (h * NF + jp) * 4 : L_W2C + ((h < NF && jp == NF) ? h : 63) * 4);
          oc[s] = opq(inv ? L_V3 + 2816 + (h * NF + jp) * 4 : L_W2C + 63 * 4);
        }
        team_arrive(cnt + C_V3, lane);
        team_wait<0>(cnt + C_V3, 4);
        STAMP(2);
        u32 av[7]; float bv[7], bw[7];
#pragma unroll
        for (int s = 0; s < 7; ++s) { av[s] = *(LAS uint16_t*)(lds + oa[s]); bv[s] = *(LAS float*)(lds + ob[s]); bw[s] = *(LAS float*)(lds + oc[s]); }
#pragma unroll
        for (int s = 0; s < 7; ++s) bv[s] += bw[s];
        f32x4 u = {0.f, 0.f, 0.f, 0.f}, u2 = {0.f, 0.f, 0.f, 0.f};
#pragma unroll
        for (int s = 0; s < 7; ++s) {
          if (s & 1) u2 = __builtin_amdgcn_mfma_f32_16x16x4f32(bflo(av[s]), bv[s], u2, 0, 0, 0);
          else u = __builtin_amdgcn_mfma_f32_16x16x4f32(bflo(av[s]), bv[s], u, 0, 0, 0);
        }
        u += u2;
#pragma unroll
        for (int e = 0; e < 4; ++e) {
          const int jo = 16 * tj + 4 * kq + e;
          const float val = (jo == NF && jp == NF) ? bflo(w1i) : u[e];
          *(LAS float*)(lds + L_U + (jo * UP + jp) * 4) = val;
        }
        team_arrive(cnt + C_U, lane);
        if (w == 0) {
          team_wait<0>(cnt + C_U, 4);
          STAMP(3);
          LAS float* U = (LAS float*)(lds + L_U);
          float e[12];
#pragma unroll
          for (int m = 0; m < 3; ++m) {
            e[4 * m] = U[tb[m].x & 0xffff]; e[4 * m + 1] = U[tb[m].x >> 16]; e[4 * m + 2] = U[tb[m].y & 0xffff]; e[4 * m + 3] = U[tb[m].y >> 16];
          }
#pragma unroll
          for (int m = 0; m < 3; ++m)
            __builtin_amdgcn_raw_buffer_store_b32(packbf(e[4 * m] + e[4 * m + 1], e[4 * m + 2] + e[4 * m + 3]), rws, WS_PB + i * 768 + (lane + 64 * m) * 4, 0, 16);
          const u64 flagb = mkflag(get_tag(cnt));
          drain_vm();
          if (lane == 0) store_flag(a.ws, WS_FB + i * FL, flagb);
          STAMP(4);
        }
      } else {
        const int i = (p - NF) * 4 + w;
        const bool prod = p < 33 && i < NF;
        u32 v[6]; u32x4 tb[3];
#pragma unroll
        for (int m = 0; m < 6; ++m) { const int d = lane + 64 * m; v[m] = __builtin_amdgcn_raw_buffer_load_b32(rwc, (prod && d < 338) ? i * 1352 + d * 4 : OOB, 0, 0); }
#pragma unroll
        for (int m = 0; m < 3; ++m) tb[m] = *(const GAS u32x4*)SYMTAB.w[lane + 64 * m];
        __builtin_amdgcn_sched_barrier(0);
        lds_barrier();
        STAMP(1);
        if (prod) {
          LAS char* raw = lds + L_RAW + w * 2048;
#pragma unroll
          for (int m = 0; m < 6; ++m) *(LAS u32*)(raw + (lane + 64 * m) * 4) = v[m];
          asm volatile("s_waitcnt lgkmcnt(0)" ::: "memory");
          LAS uint16_t* R = (LAS uint16_t*)raw;
          u32 e[12];
#pragma unroll
          for (int m = 0; m < 3; ++m) {
            e[4 * m] = R[tb[m].z & 0xffff]; e[4 * m + 1] = R[tb[m].z >> 16]; e[4 * m + 2] = R[tb[m].w & 0xffff]; e[4 * m + 3] = R[tb[m].w >> 16];
          }
#pragma unroll
          for (int m = 0; m < 3; ++m)
            __builtin_amdgcn_raw_buffer_store_b32(packbf(bflo(e[4 * m]) + bflo(e[4 * m + 1]), bflo(e[4 * m + 2]) + bflo(e[4 * m + 3])), rws,
                                                  WS_PA + i * 768 + (lane + 64 * m) * 4, 0, 16);
          const u64 flag0 = mkflag(get_tag(cnt));
          drain_vm();
          if (lane == 0) store_flag(a.ws, WS_FA + i * FL, flag0);
          STAMP(4);
        }
        if (p == 47 && w == 0) {
          const u32 tag0 = get_tag(cnt);
          const u64 flag0 = mkflag(tag0);
          __builtin_amdgcn_s_sleep(32);
#pragma unroll 1
          for (int m = 0; m < 4; ++m) poll_flags<16>(a.ws + WS_XF + (lane + 64 * m) * FL, flag0);
          if (lane == 0) __hip_atomic_store((GAS u32*)(a.ws + WS_E), tag0, __ATOMIC_RELAXED, __HIP_MEMORY_SCOPE_AGENT);
        }
      }
  }
}

}

extern "C" void kernel_launch(void* const* d_in, const int* in_sizes, int n_in, void* d_out, int out_size,
                              void* d_ws, size_t ws_size, hipStream_t stream) {
  using namespace xdfm;
  Args a;
  a.idx = (const int*)d_in[0]; a.table = (const uint16_t*)d_in[1]; a.wc = (const uint16_t*)d_in[2];
  a.wco = (const uint16_t*)d_in[3]; a.w1 = (const uint16_t*)d_in[4]; a.b1 = (const uint16_t*)d_in[5];
  a.w2 = (const uint16_t*)d_in[6]; a.bo = (const uint16_t*)d_in[7]; a.out = (uint16_t*)d_out; a.ws = (char*)d_ws;
  a.table_bytes = (u32)in_sizes[1] * 2u; a.pad_ = 0;
  (void)hipFuncSetAttribute((const void*)xdfm_kernel, hipFuncAttributeMaxDynamicSharedMemorySize, L_TOTAL);
  xdfm_kernel<<<256, NWAVE * 64, L_TOTAL, stream>>>(a);
}
```

```cpp
#include <hip/hip_runtime.h>
#include <hip/hip_bf16.h>
#include <stdint.h>

#define P_ADELAY 8
#define P_BDELAY 8
#define P_WW 10
#define P_PQ 16
#define P_XI 10
#define P_XDELAY 12
#define P_CG 4
#define P_XW 12
namespace xdfm {

typedef unsigned int u32;
typedef unsigned long long u64;
typedef u32 u32x2 __attribute__((ext_vector_type(2)));
typedef u32 u32x4 __attribute__((ext_vector_type(4)));
typedef short s16x4 __attribute__((ext_vector_type(4)));
typedef short s16x8 __attribute__((ext_vector_type(8)));
typedef float f32x2 __attribute__((ext_vector_type(2)));
typedef float f32x4 __attribute__((ext_vector_type(4)));
typedef float f32x16 __attribute__((ext_vector_type(16)));
typedef __bf16 bf16x2 __attribute__((ext_vector_type(2)));
typedef __bf16 bf16x8 __attribute__((ext_vector_type(8)));
#define GAS __attribute__((address_space(1)))
#define LAS __attribute__((address_space(3)))
typedef __amdgpu_buffer_rsrc_t rsrc_t;

constexpr int NF = 26, DIN = 832, NB = 1024, CUBE = 17576;

constexpr u32 WS_E = 0;
constexpr u32 FL = 128;
constexpr u32 WS_XF = 4096;
constexpr u32 WS_FA = 40960;
constexpr u32 WS_FB = 45056;
constexpr u32 WS_PA = 49152;
constexpr u32 WS_PB = 69632;
constexpr u32 WS_P = 131072;
constexpr u32 WS_X = 655360;
constexpr u32 WS_END = WS_X + 16 * 104 * 1024;
constexpr int NWAVE = 10;

constexpr u32 L_W = 0;
constexpr u32 APITCH = 432;
constexpr u32 L_A = 106496;
constexpr u32 L_B = L_A + 48 * APITCH;
constexpr u32 L_XS = L_B + 48 * APITCH;
constexpr u32 XS_S = 1824;
constexpr u32 L_CNT = L_XS + 4 * XS_S;
constexpr u32 L_TOTAL = L_CNT + 64;
constexpr u32 L_V3 = 0, L_W2C = 8192, L_W2S = 8448, L_U = 12288, L_RAW = 20480;
enum { C_TAGV = 12, C_TAG = 13, C_WISS = 14, C_ABRDY = 0, C_ABSTG = 1, C_W = 3, C_V3 = 4, C_U = 5, C_XISS = 6, C_XRDY = 7, C_WD = 8, C_RED = 10 };

constexpr int perm1(int j) { return j < 24 ? ((j & ~7) | ((j + 2) & 7)) : (j == 24 ? 25 : (j == 25 ? 24 : j)); }
struct Slots { int a[192]; int b[192]; bool v0[192]; bool v1[192]; int n; };
struct SlotBuilder {
  Slots s{}; bool vis[27][27] = {}; int n = 0;
  constexpr bool seen(int a, int b) const { return vis[a < b ? a : b][a < b ? b : a]; }
  constexpr void put(int a, int b) {
    s.a[n] = a; s.b[n] = b;
    s.v0[n] = !seen(a, b); vis[a < b ? a : b][a < b ? b : a] = true;
    const int na = perm1(a), nb = perm1(b);
    s.v1[n] = !seen(na, nb); vis[na < nb ? na : nb][na < nb ? nb : na] = true;
    ++n;
  }
};
constexpr int sg2(int m) { return perm1(perm1(2 * m)) >> 1; }
constexpr Slots make_slots() {
  SlotBuilder t{};
  for (int A = 0; A < 13; ++A)
    for (int B = A + 1; B < 13; ++B) {
      int ca = A, cb = B;
      while (!t.seen(2 * ca, 2 * cb)) {
        t.put(2 * ca, 2 * cb); t.put(2 * ca, 2 * cb + 1); t.put(2 * ca + 1, 2 * cb); t.put(2 * ca + 1, 2 * cb + 1);
        ca = sg2(ca); cb = sg2(cb);
      }
    }
  for (int A = 0; A < 13; ++A) {
    int ca = A;
    while (!t.seen(2 * ca, 2 * ca + 1)) {
      t.put(2 * ca, 2 * ca); t.put(2 * ca, 2 * ca + 1); t.put(2 * ca + 1, 2 * ca + 1); t.put(2 * ca + 1, 26);
      ca = sg2(ca);
    }
  }
  for (int a = 0; a < 27; ++a) { int ca = a; while (!t.seen(ca, 26)) { t.put(ca, 26); ca = perm1(perm1(ca)); } }
  t.s.n = t.n;
  for (int n = t.n; n < 192; ++n) { t.s.a[n] = 26; t.s.b[n] = 26; t.s.v0[n] = false; t.s.v1[n] = false; }
  return t.s;
}
constexpr Slots SL = make_slots();
static_assert(SL.n <= 192, "too many slots");

constexpr int UP = 33, ZB = 30 * UP, ZA = 676;
struct SymTab { u32 w[192][4]; };
constexpr SymTab make_symtab() {
  SymTab t{};
  for (int d = 0; d < 192; ++d) {
    int c = d >> 2, s = c >> 1, hh = c & 1;
    for (int e = 0; e < 2; ++e) {
      int sg = 8 * s + 2 * (d & 3) + e;
      int a = hh ? perm1(SL.a[sg]) : SL.a[sg], b = hh ? perm1(SL.b[sg]) : SL.b[sg];
      bool v = hh ? SL.v1[sg] : SL.v0[sg];
      u32 b1 = ZB, b2 = ZB, a1 = ZA, a2 = ZA;
      if (v) {
        b1 = a * UP + b; b2 = a == b ? ZB : b * UP + a;
        if (a < 26 && b < 26) { a1 = a * 26 + b; a2 = a == b ? ZA : b * 26 + a; }
      }
      t.w[d][e] = b1 | (b2 << 16);
      t.w[d][2 + e] = a1 | (a2 << 16);
    }
  }
  return t;
}
__device__ const SymTab SYMTAB = make_symtab();

constexpr u32 OOB = 0x7FFFFF00u;
constexpr u32 SPIN_MAX = 1u << 18;

__device__ __forceinline__ float bflo(u32 v) { return __uint_as_float(v << 16); }
__device__ __forceinline__ float bfhi(u32 v) { return __uint_as_float(v & 0xffff0000u); }
__device__ __forceinline__ u32 packbf(float a, float b) {
  f32x2 f = {a, b};
  bf16x2 z = __builtin_convertvector(f, bf16x2);
  return __builtin_bit_cast(u32, z);
}
__device__ __forceinline__ float mulf(float a, float b) { float r; asm("v_mul_f32 %0, %1, %2" : "=v"(r) : "v"(a), "v"(b)); return r; }
template <int E> __device__ __forceinline__ f32x2 pkmul_bc(f32x2 a, f32x2 b) {
  f32x2 r; asm("v_pk_mul_f32 %0, %1, %2 op_sel:[%3,0] op_sel_hi:[%3,1]" : "=v"(r) : "v"(a), "v"(b), "i"(E)); return r;
}
__device__ __forceinline__ float dot2bf(u32 a, u32 b, float c) {
  return __builtin_amdgcn_fdot2_f32_bf16(__builtin_bit_cast(bf16x2, a), __builtin_bit_cast(bf16x2, b), c, false);
}
__device__ __forceinline__ rsrc_t mkrs(const void* p, u32 bytes) { return __builtin_amdgcn_make_buffer_rsrc((void*)p, 0, bytes, 0x00020000); }
__device__ __forceinline__ u64 mkflag(u32 tag) { return (u64)tag | ((u64)(tag * 0x9E3779B1u + 0x7F4A7C15u) << 32); }
__device__ __forceinline__ void drain_vm() { asm volatile("s_waitcnt vmcnt(0)" ::: "memory"); }
__device__ __forceinline__ void lds_inc(LAS u32* cnt) {
  u64 keep;
  asm volatile("s_mov_b64 %0, exec\n\ts_mov_b64 exec, 1\n\tds_add_u32 %1, %2\n\ts_mov_b64 exec, %0"
               : "=&s"(keep) : "v"((u32)(size_t)cnt), "v"(1u) : "memory");
}
__device__ __forceinline__ void team_arrive(LAS u32* cnt, int) {
  asm volatile("s_waitcnt lgkmcnt(0)" ::: "memory");
  lds_inc(cnt);
}
__device__ __forceinline__ float sum_rows(float v) {
  auto a = __builtin_amdgcn_permlane32_swap(__float_as_uint(v), __float_as_uint(v), false, false);
  v = __uint_as_float(a[0]) + __uint_as_float(a[1]);
  auto b = __builtin_amdgcn_permlane16_swap(__float_as_uint(v), __float_as_uint(v), false, false);
  return __uint_as_float(b[0]) + __uint_as_float(b[1]);
}
template <int CTRL> __device__ __forceinline__ float dpp_add(float v) {
  return v + __uint_as_float(__builtin_amdgcn_update_dpp(0u, __float_as_uint(v), CTRL, 0xf, 0xf, true));
}
__device__ __forceinline__ float sum_row16(float v) {
  v = dpp_add<0x128>(v);
  v = dpp_add<0x124>(v);
  v = dpp_add<0x122>(v);
  v = dpp_add<0x121>(v);
  return v;
}

#define STAMP(k) do {} while (0)

struct Args {
  const int* idx; const uint16_t* table; const uint16_t* wc; const uint16_t* wco; const uint16_t* w1;
  const uint16_t* b1; const uint16_t* w2; const uint16_t* bo; uint16_t* out; char* ws;
  u32 table_bytes; u32 pad_;
};
#define CAS __attribute__((address_space(4)))

template <int SLEEP> __device__ __forceinline__ void team_wait(LAS u32* cnt, u32 target) {
  u32 v, s, n = SPIN_MAX;
  asm volatile(
      "1:\n\tds_read_b32 %0, %3\n\ts_waitcnt lgkmcnt(0)\n\tv_readfirstlane_b32 %1, %0\n\ts_cmp_ge_u32 %1, %4\n\ts_cbranch_scc1 2f\n\t"
      "s_sleep %5\n\ts_sub_u32 %2, %2, 1\n\ts_cbranch_scc0 1b\n2:"
      : "=&v"(v), "=&s"(s), "+s"(n) : "v"((u32)(size_t)cnt), "s"(target), "i"(SLEEP) : "memory", "scc");
}
template <int SLEEP> __device__ __forceinline__ void poll_flags(const char* p, u64 want) {
  u64 v; u32 n = SPIN_MAX;
  asm volatile(
      "1:\n\tglobal_load_dwordx2 %0, %2, off sc1\n\ts_waitcnt vmcnt(0)\n\tv_cmp_ne_u64 vcc, %3, %0\n\ts_nop 4\n\ts_cbranch_vccz 2f\n\t"
      "s_sleep %4\n\ts_sub_u32 %1, %1, 1\n\ts_cbranch_scc0 1b\n2:"
      : "=&v"(v), "+s"(n) : "v"(p), "s"(want), "i"(SLEEP) : "memory", "scc", "vcc");
}
__device__ __forceinline__ void lds_barrier() {
  asm volatile("s_waitcnt lgkmcnt(0)" ::: "memory");
  __builtin_amdgcn_s_barrier();
  asm volatile("" ::: "memory");
}
__device__ __forceinline__ u32 load_epoch(const char* ws) {
  return __hip_atomic_load((const GAS u32*)(ws + WS_E), __ATOMIC_RELAXED, __HIP_MEMORY_SCOPE_AGENT);
}
__device__ __forceinline__ void store_flag(char* ws, u32 off, u64 flag) {
  __hip_atomic_store((GAS u64*)(ws + off), flag, __ATOMIC_RELAXED, __HIP_MEMORY_SCOPE_AGENT);
}
__device__ __forceinline__ u32 get_tag(LAS u32* cnt) {
  team_wait<0>(cnt + C_TAG, 1);
  return __builtin_amdgcn_readfirstlane(cnt[C_TAGV]);
}
__device__ __forceinline__ u32 opq(u32 v) { asm("" : "+v"(v)); return v; }

__global__ void __launch_bounds__(640, 3) xdfm_kernel(Args a) {
  extern __shared__ __attribute__((aligned(16))) char lds_raw[];
  LAS char* lds = (LAS char*)lds_raw;
  const int tid = threadIdx.x, lane = tid & 63;
  const int w = __builtin_amdgcn_readfirstlane(tid >> 6);
  const int b = blockIdx.x, j = b >> 3;
  const int jg = j < 6 ? (j >= 3 ? 1 : 0) : (j >= 19 ? 1 : 0);
  const int g = (b & 7) * 2 + jg, r = j < 6 ? 13 + j - 3 * jg : j - 6 - 13 * jg;
  LAS u32* cnt = (LAS u32*)(lds + L_CNT);
  asm volatile("" :: "s"(a.idx), "s"(a.table), "s"(a.wc), "s"(a.wco), "s"(a.w1), "s"(a.b1), "s"(a.w2), "s"(a.bo), "s"(a.out), "s"(a.ws), "s"(a.table_bytes));
  const rsrc_t rws = mkrs(a.ws, WS_END);
  const rsrc_t ridx = mkrs(a.idx, NB * NF * 4), rtab = mkrs(a.table, a.table_bytes);

  if (w == 9) {
    lds_barrier();
    if (b == 250) {
      ;
#pragma unroll 1
      for (int i = 0; i < 6; ++i) __builtin_amdgcn_s_sleep(127);
    }
  } else if (w == 8) {
    __builtin_amdgcn_s_setprio(3);
    if (lane < 16) cnt[lane] = 0;
    u32 tag;
    if (r < 13) {
      u32 id[4]; u32x4 xg[8];
#pragma unroll
      for (int m = 0; m < 2; ++m) {
        const int p0 = 4 * r + 2 * m, t = p0 >= NF ? 1 : 0, f0 = p0 - t * NF;
        const u32x2 ia = __builtin_amdgcn_raw_buffer_load_b64(ridx, (lane & 31) * (NF * 4), ((g * 64 + t * 32) * NF + f0) * 4, 0);
        id[2 * m] = ia.x; id[2 * m + 1] = ia.y;
      }
      const u32 ep = load_epoch(a.ws);
#pragma unroll
      for (int k = 0; k < 8; ++k) xg[k] = __builtin_amdgcn_raw_buffer_load_b128(rtab, id[k >> 1] * 64 + (k & 1) * 32 + (lane >> 5) * 16, 0, 0);
      lds_barrier();
      STAMP(1);
      tag = __builtin_amdgcn_readfirstlane(ep) + 1u;
      cnt[C_TAGV] = tag;
      team_arrive(cnt + C_TAG, lane);
#pragma unroll
      for (int k = 0; k < 8; ++k) __builtin_amdgcn_raw_buffer_store_b128(xg[k], rws, WS_X + lane * 16, (g * 104 + r * 8 + k) * 1024, 16);
      STAMP(2);
      drain_vm();
    } else {
      const u32 ep = load_epoch(a.ws);
      lds_barrier();
      tag = __builtin_amdgcn_readfirstlane(ep) + 1u;
      cnt[C_TAGV] = tag;
      team_arrive(cnt + C_TAG, lane);
    }
    const u64 flag = mkflag(tag);
    if (lane == 0) store_flag(a.ws, WS_XF + (g * 16 + r) * FL, flag);
    STAMP(3);
    if (r < 13) {
      __builtin_amdgcn_s_sleep(P_XDELAY);
      poll_flags<2>(a.ws + WS_XF + (g * 16 + (lane < 13 ? lane : 0)) * FL, flag);
      lds_inc(cnt + C_XRDY);
    }
    STAMP(4);
    __builtin_amdgcn_s_sleep(P_BDELAY);
    poll_flags<6>(a.ws + (lane < 32 ? WS_FA : WS_FB) + ((lane & 31) < NF ? (lane & 31) : 0) * FL, flag);
    lds_inc(cnt + C_ABRDY);
    STAMP(5);
  } else if (w >= 4) {
    const int cw = w - 4, smp = g * 64 + r * 4 + cw;
    u32 id[2]; u32x4 xr[2];
    u32 bo;
    {
      const CAS u32* ip = (const CAS u32*)(a.idx + smp * NF);
      u32 is[NF];
#pragma unroll
      for (int f = 0; f < NF; ++f) is[f] = ip[f];
      const size_t bop = (size_t)a.bo;
      bo = *(const CAS u32*)(bop & ~(size_t)3) >> ((bop & 2) * 8);
      u32 iv = 0;
      asm("v_writelane_b32 %0, %1, 0\n\tv_writelane_b32 %0, %2, 1\n\tv_writelane_b32 %0, %3, 2\n\tv_writelane_b32 %0, %4, 3\n\tv_writelane_b32 %0, %5, 4\n\tv_writelane_b32 %0, %6, 5\n\tv_writelane_b32 %0, %7, 6\n\tv_writelane_b32 %0, %8, 7\n\tv_writelane_b32 %0, %9, 8\n\tv_writelane_b32 %0, %10, 9\n\tv_writelane_b32 %0, %11, 10\n\tv_writelane_b32 %0, %12, 11\n\tv_writelane_b32 %0, %13, 12\n\tv_writelane_b32 %0, %14, 13\n\tv_writelane_b32 %0, %15, 14\n\tv_writelane_b32 %0, %16, 15\n\tv_writelane_b32 %0, %17, 16\n\tv_writelane_b32 %0, %18, 17\n\tv_writelane_b32 %0, %19, 18\n\tv_writelane_b32 %0, %20, 19\n\tv_writelane_b32 %0, %21, 20\n\tv_writelane_b32 %0, %22, 21\n\tv_writelane_b32 %0, %23, 22\n\tv_writelane_b32 %0, %24, 23\n\tv_writelane_b32 %0, %25, 24\n\tv_writelane_b32 %0, %26, 25"
          : "+v"(iv) : "s"(is[0]), "s"(is[1]), "s"(is[2]), "s"(is[3]), "s"(is[4]), "s"(is[5]), "s"(is[6]), "s"(is[7]), "s"(is[8]), "s"(is[9]), "s"(is[10]), "s"(is[11]), "s"(is[12]), "s"(is[13]), "s"(is[14]), "s"(is[15]), "s"(is[16]), "s"(is[17]), "s"(is[18]), "s"(is[19]), "s"(is[20]), "s"(is[21]), "s"(is[22]), "s"(is[23]), "s"(is[24]), "s"(is[25]));
      id[0] = __builtin_amdgcn_ds_bpermute((lane >> 2) * 4, iv);
      id[1] = __builtin_amdgcn_ds_bpermute((16 + (lane >> 2)) * 4, iv);
    }
    if (r < 13) __builtin_amdgcn_s_setprio(2);
    lds_barrier();
#pragma unroll
    for (int m = 0; m < 2; ++m) { const int f = 16 * m + (lane >> 2); xr[m] = __builtin_amdgcn_raw_buffer_load_b128(rtab, f < NF ? id[m] * 64 + (lane & 3) * 16 : OOB, 0, 0); }
    __builtin_amdgcn_s_setprio(0);
    STAMP(1);
    f32x2 xp[14];
    {
      const u32 o = L_XS + cw * XS_S + (lane >> 2) * 64 + (lane & 3) * 16;
      *(LAS u32x4*)(lds + o) = xr[0];
      u32x4 x1 = xr[1];
      if ((lane >> 2) == NF - 16) x1 = (u32x4){0x3F803F80u, 0x3F803F80u, 0x3F803F80u, 0x3F803F80u};
      *(LAS u32x4*)(lds + opq((lane >> 2) < 12 ? o + 1024 : L_XS + cw * XS_S + 27 * 64 + (lane & 3) * 16)) = x1;
      asm volatile("s_waitcnt lgkmcnt(0)" ::: "memory");
      const int hh = lane >> 5, q = (lane >> 2) & 3, pp = lane & 3;
      const u32 base = L_XS + cw * XS_S + ((lane >> 4) & 1) * 32 + pp * 8;
      const u32 a0 = base + (hh ? q + 2 : q) * 64, a1 = base + (hh ? ((q + 6) & 7) : q + 4) * 64;
      const u32 a6 = base + (24 + ((hh && q < 2) ? (q ^ 1) : q)) * 64;
#pragma unroll
      for (int t = 0; t < 7; ++t) {
        s16x4 y = __builtin_amdgcn_ds_read_tr16_b64_v4i16((LAS s16x4*)(lds + (t < 6 ? ((t & 1) ? a1 : a0) + (t >> 1) * 512 : a6)));
        u32x2 u = __builtin_bit_cast(u32x2, y);
        xp[2 * t] = (f32x2){bflo(u.x), bfhi(u.x)}; xp[2 * t + 1] = (f32x2){bflo(u.y), bfhi(u.y)};
      }
    }
    bf16x8 z[24];
#pragma unroll
    for (int s = 0; s < 24; ++s) {
      u32x4 zz;
#pragma unroll
      for (int e = 0; e < 4; ++e) {
        const int s1 = 8 * s + 2 * e, s2 = s1 + 1;
        const int a1 = SL.a[s1], b1 = SL.b[s1], a2 = SL.a[s2], b2 = SL.b[s2];
        if (a1 == a2 && (b1 & 1) == 0 && b1 < 26 && b2 == b1 + 1) {
          const f32x2 pr = (a1 & 1) ? pkmul_bc<1>(xp[a1 >> 1], xp[b1 >> 1]) : pkmul_bc<0>(xp[a1 >> 1], xp[b1 >> 1]);
          zz[e] = packbf(pr[0], pr[1]);
        } else {
          const float p1 = b1 == 26 ? xp[a1 >> 1][a1 & 1] : mulf(xp[a1 >> 1][a1 & 1], xp[b1 >> 1][b1 & 1]);
          const float p2 = b2 == 26 ? xp[a2 >> 1][a2 & 1] : mulf(xp[a2 >> 1][a2 & 1], xp[b2 >> 1][b2 & 1]);
          zz[e] = packbf(p1, p2);
        }
      }
      z[s] = __builtin_bit_cast(bf16x8, zz);
      asm volatile("" : "+v"(z[s]));
    }
    const u32 tag = get_tag(cnt);
    const u64 flag = mkflag(tag);
    STAMP(2);
    team_wait<1>(cnt + C_ABRDY, 1);
    STAMP(3);
    if (r < 13) team_wait<1>(cnt + C_XISS, 4);
    __builtin_amdgcn_s_setprio(2);
    STAMP(4);
    {
      u32x4 y[10];
#pragma unroll
      for (int m = 0; m < 10; ++m) {
        const u32 n = ((m % 5) * 4 + cw) * 64 + lane;
        y[m] = __builtin_amdgcn_raw_buffer_load_b128(rws, n < NF * 48 ? n * 16 : OOB, m < 5 ? WS_PA : WS_PB, 16);
      }
#pragma unroll
      for (int m = 0; m < 5; ++m) {
        const u32 n = (m * 4 + cw) * 64 + lane, i = (n * 1366u) >> 16, c = n - i * 48;
        const u32 o = opq(n < NF * 48 ? L_A + c * APITCH + i * 16 : L_A + 26 * 16);
        *(LAS u32x4*)(lds + o) = y[m];
        *(LAS u32x4*)(lds + o + (L_B - L_A)) = y[5 + m];
      }
      team_arrive(cnt + C_ABSTG, lane);
      team_wait<0>(cnt + C_ABSTG, 4);
    }
    __builtin_amdgcn_s_setprio(0);
    STAMP(5);
    const u32 afr = (lane >> 5) * APITCH + (lane & 31) * 16;
    const u32 tag2 = (u32)(flag >> 32);
    const int sg = r * 4 + cw;
    const u32 po = opq(lane < 26 ? WS_P + ((g * 26 + lane) * 2 + (sg >> 5)) * 512 + (sg & 31) * 16 : OOB);
    u32x4 yp;
    f32x16 acc1 = {}, acc2 = {};
    {
      constexpr int AD = 2;
      bf16x8 af[24], bf[24];
#pragma unroll
      for (int s = 0; s < AD; ++s) { af[s] = *(LAS bf16x8*)(lds + L_A + afr + s * 2 * APITCH); bf[s] = *(LAS bf16x8*)(lds + L_B + afr + s * 2 * APITCH); }
      __builtin_amdgcn_sched_barrier(0);
#pragma unroll
      for (int s = 0; s < 24; ++s) {
        if (s + AD < 24) { af[s + AD] = *(LAS bf16x8*)(lds + L_A + afr + (s + AD) * 2 * APITCH); bf[s + AD] = *(LAS bf16x8*)(lds + L_B + afr + (s + AD) * 2 * APITCH); }
        if (s == P_PQ) yp = __builtin_amdgcn_raw_buffer_load_b128(rws, po, 0, 16);
        acc1 = __builtin_amdgcn_mfma_f32_32x32x16_bf16(af[s], z[s], acc1, 0, 0, 0);
        acc2 = __builtin_amdgcn_mfma_f32_32x32x16_bf16(bf[s], z[s], acc2, 0, 0, 0);
        __builtin_amdgcn_sched_barrier(0);
      }
    }
    float sum = 0.f;
#pragma unroll
    for (int e = 0; e < 12; ++e) sum += acc1[e] * acc2[e];
    { const float t = acc1[12] * acc2[12] + acc1[13] * acc2[13]; sum += lane < 32 ? t : 0.f; }
    sum = sum_rows(sum_row16(sum));
    STAMP(6);
    for (u32 it = 0; it < SPIN_MAX; ++it) {
      const bool ok = lane >= 26 || (yp.y == tag && yp.w == tag2);
      if (__all(ok)) break;
      asm volatile("" ::: "memory");
      __builtin_amdgcn_s_sleep(1);
      yp = __builtin_amdgcn_raw_buffer_load_b128(rws, po, 0, 16);
    }
    float part = sum_rows(sum_row16(__uint_as_float(yp.x)));
    if (lane == 0) {
      const __bf16 ob = (__bf16)(sum + part + bflo(bo));
      a.out[smp] = __builtin_bit_cast(uint16_t, ob);
    }
    STAMP(7);
  } else if (r < 13) {
      const rsrc_t rw1 = mkrs(a.w1, DIN * DIN * 2);
      const int mh = w & 1, kh = w >> 1, hh = lane >> 5;
      lds_barrier();
      STAMP(1);
      {
        u32x4 v[26];
        const u32 vo = (w * 208 + (lane >> 3)) * 1664 + r * 128 + (lane & 7) * 16;
        const int c = lane & 7;
        const u32 lo = L_W + (w * 208 + (lane >> 3)) * 128 + (((c >> 1) ^ (((lane >> 4) & 1) << 1)) * 32) + (c & 1) * 16;
#pragma unroll
        for (int i = 0; i < 26; ++i) v[i] = __builtin_amdgcn_raw_buffer_load_b128(rw1, vo, i * 8 * 1664, 0);
#pragma unroll
        for (int i = 0; i < 26; ++i) *(LAS u32x4*)(lds + lo + i * 1024) = v[i];
      }
      team_arrive(cnt + C_W, lane);
      const u32 tag = get_tag(cnt);
      const u64 flag = mkflag(tag);
      f32x16 acc[2];
      f32x2 wf[8];
      {
        const CAS u32* bp = (const CAS u32*)(a.b1 + r * 64 + kh * 32);
        const CAS u32* wp = (const CAS u32*)(a.w2 + r * 64 + kh * 32);
        u32 bs[16], ws_[16];
#pragma unroll
        for (int t = 0; t < 16; ++t) { bs[t] = bp[t]; ws_[t] = wp[t]; }
#pragma unroll
        for (int t = 0; t < 16; ++t) asm volatile("" : "+s"(bs[t]), "+s"(ws_[t]));
#pragma unroll
        for (int t = 0; t < 4; ++t) {
          const u32 b0 = hh ? bs[4 * t + 2] : bs[4 * t], b1 = hh ? bs[4 * t + 3] : bs[4 * t + 1];
          const u32 w0 = hh ? ws_[4 * t + 2] : ws_[4 * t], w1 = hh ? ws_[4 * t + 3] : ws_[4 * t + 1];
          acc[0][4 * t] = bflo(b0); acc[0][4 * t + 1] = bfhi(b0); acc[0][4 * t + 2] = bflo(b1); acc[0][4 * t + 3] = bfhi(b1);
          acc[1][4 * t] = 0.f; acc[1][4 * t + 1] = 0.f; acc[1][4 * t + 2] = 0.f; acc[1][4 * t + 3] = 0.f;
          wf[2 * t] = (f32x2){bflo(w0), bfhi(w0)}; wf[2 * t + 1] = (f32x2){bflo(w1), bfhi(w1)};
          asm volatile("" : "+v"(wf[2 * t]), "+v"(wf[2 * t + 1]));
        }
      }
      STAMP(2);
      team_wait<0>(cnt + C_XRDY, 1);
      STAMP(3);
      constexpr int XW = P_XW, FD = 3;
      u32x4 xv[26];
      auto ldx = [&](int i) { xv[i] = __builtin_amdgcn_raw_buffer_load_b128(rws, WS_X + lane * 16, (g * 104 + mh * 52 + kh * 26 + i) * 1024, 16); };
#pragma unroll
      for (int i = 0; i < XW; ++i) ldx(i);
      team_wait<0>(cnt + C_W, 4);
      __builtin_amdgcn_s_setprio(1);
      {
        const int q = (lane >> 2) & 3, pp = lane & 3, gam = (lane >> 4) & 1;
        const u32 rb = L_W + kh * 26 * 2048 + (8 * hh + q) * 128 + pp * 8;
        const u32 ad0 = rb + (((2 * kh + gam) ^ (q & 2)) * 32), ad1 = rb + (((2 * (kh ^ 1) + gam) ^ (q & 2)) * 32);
        bf16x8 fr[26][2];
        auto ldf = [&](int i) {
#pragma unroll
          for (int t = 0; t < 2; ++t) {
            const u32 o = (t ? ad1 : ad0) + i * 2048;
            const s16x4 y0 = __builtin_amdgcn_ds_read_tr16_b64_v4i16((LAS s16x4*)(lds + o));
            const s16x4 y1 = __builtin_amdgcn_ds_read_tr16_b64_v4i16((LAS s16x4*)(lds + o + 512));
            fr[i][t] = __builtin_bit_cast(bf16x8, __builtin_shufflevector(y0, y1, 0, 1, 2, 3, 4, 5, 6, 7));
          }
        };
#pragma unroll
        for (int i = 0; i < FD; ++i) ldf(i);
        __builtin_amdgcn_sched_barrier(0);
#pragma unroll
        for (int i = 0; i < 26; ++i) {
          if (i + XW < 26) ldx(i + XW);
          if (i == P_XI) lds_inc(cnt + C_XISS);
          if (i == 8) lds_inc(cnt + C_WD + kh);
          if (i == 22) team_wait<0>(cnt + C_WD + kh, 2);
          if (i + FD < 26) ldf(i + FD);
          const bf16x8 xb = __builtin_bit_cast(bf16x8, xv[i]);
          acc[0] = __builtin_amdgcn_mfma_f32_32x32x16_bf16(fr[i][0], xb, acc[0], 0, 0, 0);
          acc[1] = __builtin_amdgcn_mfma_f32_32x32x16_bf16(fr[i][1], xb, acc[1], 0, 0, 0);
          __builtin_amdgcn_sched_barrier(0);
        }
      }
      STAMP(4);
      {
        const u32 so = L_W + kh * 26 * 2048 + mh * 4096 + lane * 16;
#pragma unroll
        for (int t = 0; t < 4; ++t) *(LAS f32x4*)(lds + so + t * 1024) = (f32x4){acc[1][4 * t], acc[1][4 * t + 1], acc[1][4 * t + 2], acc[1][4 * t + 3]};
        team_arrive(cnt + C_RED + mh, lane);
        team_wait<0>(cnt + C_RED + mh, 2);
      }
      STAMP(5);
      float part;
      {
        constexpr float K1 = -2.0f * 1.4426950408889634f * 0.7978845608028654f, K2 = K1 * 0.044715f;
        const u32 si = L_W + (kh ^ 1) * 26 * 2048 + mh * 4096 + lane * 16;
        f32x4 o[4];
#pragma unroll
        for (int t = 0; t < 4; ++t) o[t] = *(LAS f32x4*)(lds + si + t * 1024);
        f32x2 h[8], u[8], y[8];
#pragma unroll
        for (int m = 0; m < 8; ++m) {
          h[m] = (f32x2){acc[0][2 * m], acc[0][2 * m + 1]} + (f32x2){o[m >> 1][2 * (m & 1)], o[m >> 1][2 * (m & 1) + 1]};
          u[m] = (h[m] * h[m] * K2 + K1) * h[m];
          y[m] = h[m] * wf[m];
        }
#pragma unroll
        for (int m = 0; m < 8; ++m) { u[m][0] = __builtin_amdgcn_exp2f(u[m][0]); u[m][1] = __builtin_amdgcn_exp2f(u[m][1]); }
#pragma unroll
        for (int m = 0; m < 8; ++m) { u[m] = u[m] + 1.0f; }
#pragma unroll
        for (int m = 0; m < 8; ++m) { u[m][0] = __builtin_amdgcn_rcpf(u[m][0]); u[m][1] = __builtin_amdgcn_rcpf(u[m][1]); }
        f32x2 p0 = y[0] * u[0], p1 = y[1] * u[1];
#pragma unroll
        for (int m = 2; m < 8; m += 2) { p0 += y[m] * u[m]; p1 += y[m + 1] * u[m + 1]; }
        p0 += p1;
        part = p0[0] + p0[1];
      }
      {
        auto sw = __builtin_amdgcn_permlane32_swap(__float_as_uint(part), __float_as_uint(part), false, false);
        part = __uint_as_float(sw[0]) + __uint_as_float(sw[1]);
      }
      {
        const u32x4 o = {__float_as_uint(part), tag, __float_as_uint(part), (u32)(flag >> 32)};
        __builtin_amdgcn_raw_buffer_store_b128(o, rws, lane < 32 ? WS_P + ((g * 26 + r * 2 + kh) * 2 + mh) * 512 + lane * 16 : OOB, 0, 16);
      }
      STAMP(6);
  } else {
      const int p = g * 3 + r - 13;
      const rsrc_t rwc = mkrs(a.wc, 3 * CUBE * 2), rwo = mkrs(a.wco, 78 * 2);
      __builtin_amdgcn_s_setprio(3);
      if (p < NF) {
        const int i = p;
        u32x4 tb[3];
        const int hb = (w >> 1) * 13, el = (w & 1) * 64 + lane;
        const CAS u32* w3p = (const CAS u32*)(a.wco + 52);
        u32 w3[13];
#pragma unroll
        for (int h = 0; h < 13; ++h) w3[h] = w3p[h];
        u32x4 v[13];
#pragma unroll
        for (int h = 0; h < 13; ++h) v[h] = __builtin_amdgcn_raw_buffer_load_b128(rwc, el < 85 ? (2 * CUBE + hb * 676 + 8 * el) * 2 : OOB, h * 676 * 2, 0);
        u32 v2[2];
#pragma unroll
        for (int m = 0; m < 2; ++m) {
          const u32 d = tid + 256 * m, h = (d * 5042u) >> 16, e = d - 13 * h;
          v2[m] = __builtin_amdgcn_raw_buffer_load_b32(rwc, d < 338 ? (CUBE + (h * NF + i) * NF) * 2 + e * 4 : OOB, 0, 0);
        }
        const u32 wq = __builtin_amdgcn_raw_buffer_load_b16(rwo, tid < NF ? (NF + tid) * 2 : OOB, 0, 0);
        const u32 w1i = __builtin_amdgcn_raw_buffer_load_b16(rwo, i * 2, 0, 0);
        if (w == 0) {
#pragma unroll
          for (int m = 0; m < 3; ++m) tb[m] = *(const GAS u32x4*)SYMTAB.w[lane + 64 * m];
        }
        __builtin_amdgcn_sched_barrier(0);
        lds_barrier();
        STAMP(1);
        {
          float acc[8];
#pragma unroll
          for (int e = 0; e < 8; ++e) acc[e] = 0.f;
#pragma unroll
          for (int h = 0; h < 13; ++h) {
            const u32 we = (h & 1) ? w3[h >> 1] >> 16 : w3[h >> 1] & 0xffffu;
            const u32 wo = (h & 1) ? w3[(13 + h) >> 1] & 0xffffu : w3[(13 + h) >> 1] >> 16;
            const u32 wl = hb ? wo : we, wh = wl << 16;
#pragma unroll
            for (int e = 0; e < 4; ++e) { acc[2 * e] = dot2bf(v[h][e], wl, acc[2 * e]); acc[2 * e + 1] = dot2bf(v[h][e], wh, acc[2 * e + 1]); }
          }
          const u32 vo = opq(el < 85 ? L_V3 + (w >> 1) * 2816 + el * 32 : L_V3 + 5632 + lane * 32);
          *(LAS f32x4*)(lds + vo) = (f32x4){acc[0], acc[1], acc[2], acc[3]};
          *(LAS f32x4*)(lds + vo + 16) = (f32x4){acc[4], acc[5], acc[6], acc[7]};
#pragma unroll
          for (int m = 0; m < 2; ++m) *(LAS u32*)(lds + L_W2S + (tid + 256 * m) * 4) = v2[m];
          if (w == 0) *(LAS u32*)(lds + L_W2C + lane * 4) = wq << 16;
        }
        const int tj = w >> 1, tp = w & 1, kq = lane >> 4;
        const int jr = 16 * tj + (lane & 15), jp = 16 * tp + (lane & 15);
        u32 oa[7], ob[7], oc[7];
#pragma unroll
        for (int s = 0; s < 7; ++s) {
          const int h = 4 * s + kq;
          const bool inv = h < NF && jp < NF;
          oa[s] = opq((h < NF && jr < NF) ? L_W2S + (h * NF + jr) * 2 : L_W2C + 63 * 4);
          ob[s] = opq(inv ? L_V3 + (h * NF + jp) * 4 : L_W2C + ((h < NF && jp == NF) ? h : 63) * 4);
          oc[s] = opq(inv ? L_V3 + 2816 + (h * NF + jp) * 4 : L_W2C + 63 * 4);
        }
        team_arrive(cnt + C_V3, lane);
        team_wait<0>(cnt + C_V3, 4);
        STAMP(2);
        u32 av[7]; float bv[7], bw[7];
#pragma unroll
        for (int s = 0; s < 7; ++s) { av[s] = *(LAS uint16_t*)(lds + oa[s]); bv[s] = *(LAS float*)(lds + ob[s]); bw[s] = *(LAS float*)(lds + oc[s]); }
#pragma unroll
        for (int s = 0; s < 7; ++s) bv[s] += bw[s];
        f32x4 u = {0.f, 0.f, 0.f, 0.f}, u2 = {0.f, 0.f, 0.f, 0.f};
#pragma unroll
        for (int s = 0; s < 7; ++s) {
          if (s & 1) u2 = __builtin_amdgcn_mfma_f32_16x16x4f32(bflo(av[s]), bv[s], u2, 0, 0, 0);
          else u = __builtin_amdgcn_mfma_f32_16x16x4f32(bflo(av[s]), bv[s], u, 0, 0, 0);
        }
        u += u2;
#pragma unroll
        for (int e = 0; e < 4; ++e) {
          const int jo = 16 * tj + 4 * kq + e;
          const float val = (jo == NF && jp == NF) ? bflo(w1i) : u[e];
          *(LAS float*)(lds + L_U + (jo * UP + jp) * 4) = val;
        }
        team_arrive(cnt + C_U, lane);
        if (w == 0) {
          team_wait<0>(cnt + C_U, 4);
          STAMP(3);
          LAS float* U = (LAS float*)(lds + L_U);
          float e[12];
#pragma unroll
          for (int m = 0; m < 3; ++m) {
            e[4 * m] = U[tb[m].x & 0xffff]; e[4 * m + 1] = U[tb[m].x >> 16]; e[4 * m + 2] = U[tb[m].y & 0xffff]; e[4 * m + 3] = U[tb[m].y >> 16];
          }
#pragma unroll
          for (int m = 0; m < 3; ++m)
            __builtin_amdgcn_raw_buffer_store_b32(packbf(e[4 * m] + e[4 * m + 1], e[4 * m + 2] + e[4 * m + 3]), rws, WS_PB + i * 768 + (lane + 64 * m) * 4, 0, 16);
          const u64 flagb = mkflag(get_tag(cnt));
          drain_vm();
          if (lane == 0) store_flag(a.ws, WS_FB + i * FL, flagb);
          STAMP(4);
        }
      } else {
        const int i = (p - NF) * 4 + w;
        const bool prod = p < 33 && i < NF;
        u32 v[6]; u32x4 tb[3];
#pragma unroll
        for (int m = 0; m < 6; ++m) { const int d = lane + 64 * m; v[m] = __builtin_amdgcn_raw_buffer_load_b32(rwc, (prod && d < 338) ? i * 1352 + d * 4 : OOB, 0, 0); }
#pragma unroll
        for (int m = 0; m < 3; ++m) tb[m] = *(const GAS u32x4*)SYMTAB.w[lane + 64 * m];
        __builtin_amdgcn_sched_barrier(0);
        lds_barrier();
        STAMP(1);
        if (prod) {
          LAS char* raw = lds + L_RAW + w * 2048;
#pragma unroll
          for (int m = 0; m < 6; ++m) *(LAS u32*)(raw + (lane + 64 * m) * 4) = v[m];
          asm volatile("s_waitcnt lgkmcnt(0)" ::: "memory");
          LAS uint16_t* R = (LAS uint16_t*)raw;
          u32 e[12];
#pragma unroll
          for (int m = 0; m < 3; ++m) {
            e[4 * m] = R[tb[m].z & 0xffff]; e[4 * m + 1] = R[tb[m].z >> 16]; e[4 * m + 2] = R[tb[m].w & 0xffff]; e[4 * m + 3] = R[tb[m].w >> 16];
          }
#pragma unroll
          for (int m = 0; m < 3; ++m)
            __builtin_amdgcn_raw_buffer_store_b32(packbf(bflo(e[4 * m]) + bflo(e[4 * m + 1]), bflo(e[4 * m + 2]) + bflo(e[4 * m + 3])), rws,
                                                  WS_PA + i * 768 + (lane + 64 * m) * 4, 0, 16);
          const u64 flag0 = mkflag(get_tag(cnt));
          drain_vm();
          if (lane == 0) store_flag(a.ws, WS_FA + i * FL, flag0);
          STAMP(4);
        }
        if (p == 47 && w == 0) {
          const u32 tag0 = get_tag(cnt);
          const u64 flag0 = mkflag(tag0);
          __builtin_amdgcn_s_sleep(32);
#pragma unroll 1
          for (int m = 0; m < 4; ++m) poll_flags<16>(a.ws + WS_XF + (lane + 64 * m) * FL, flag0);
          if (lane == 0) __hip_atomic_store((GAS u32*)(a.ws + WS_E), tag0, __ATOMIC_RELAXED, __HIP_MEMORY_SCOPE_AGENT);
        }
      }
  }
}

}

extern "C" void kernel_launch(void* const* d_in, const int* in_sizes, int n_in, void* d_out, int out_size,
                              void* d_ws, size_t ws_size, hipStream_t stream) {
  using namespace xdfm;
  Args a;
  a.idx = (const int*)d_in[0]; a.table = (const uint16_t*)d_in[1]; a.wc = (const uint16_t*)d_in[2];
  a.wco = (const uint16_t*)d_in[3]; a.w1 = (const uint16_t*)d_in[4]; a.b1 = (const uint16_t*)d_in[5];
  a.w2 = (const uint16_t*)d_in[6]; a.bo = (const uint16_t*)d_in[7]; a.out = (uint16_t*)d_out; a.ws = (char*)d_ws;
  a.table_bytes = (u32)in_sizes[1] * 2u; a.pad_ = 0;
  (void)hipFuncSetAttribute((const void*)xdfm_kernel, hipFuncAttributeMaxDynamicSharedMemorySize, L_TOTAL);
  xdfm_kernel<<<256, NWAVE * 64, L_TOTAL, stream>>>(a);
}
```

```cpp
#include <hip/hip_runtime.h>
#include <hip/hip_bf16.h>
#include <stdint.h>

#define P_ADELAY 8
#define P_BDELAY 8
#define P_WW 10
#define P_PQ 16
#define P_XI 10
#define P_XDELAY 12
#define P_CG 4
#define P_XW 12
namespace xdfm {

typedef unsigned int u32;
typedef unsigned long long u64;
typedef u32 u32x2 __attribute__((ext_vector_type(2)));
typedef u32 u32x4 __attribute__((ext_vector_type(4)));
typedef short s16x4 __attribute__((ext_vector_type(4)));
typedef short s16x8 __attribute__((ext_vector_type(8)));
typedef float f32x2 __attribute__((ext_vector_type(2)));
typedef float f32x4 __attribute__((ext_vector_type(4)));
typedef float f32x16 __attribute__((ext_vector_type(16)));
typedef __bf16 bf16x2 __attribute__((ext_vector_type(2)));
typedef __bf16 bf16x8 __attribute__((ext_vector_type(8)));
#define GAS __attribute__((address_space(1)))
#define LAS __attribute__((address_space(3)))
typedef __amdgpu_buffer_rsrc_t rsrc_t;

constexpr int NF = 26, DIN = 832, NB = 1024, CUBE = 17576;

constexpr u32 WS_E = 0;
constexpr u32 FL = 128;
constexpr u32 WS_XF = 4096;
constexpr u32 WS_FA = 40960;
constexpr u32 WS_FB = 45056;
constexpr u32 WS_PA = 49152;
constexpr u32 WS_PB = 69632;
constexpr u32 WS_P = 131072;
constexpr u32 WS_X = 655360;
constexpr u32 WS_END = WS_X + 16 * 104 * 1024;
constexpr int NWAVE = 9;

constexpr u32 L_W = 0;
constexpr u32 APITCH = 432;
constexpr u32 L_A = 106496;
constexpr u32 L_B = L_A + 48 * APITCH;
constexpr u32 L_XS = L_B + 48 * APITCH;
constexpr u32 XS_S = 1824;
constexpr u32 L_CNT = L_XS + 4 * XS_S;
constexpr u32 L_TOTAL = L_CNT + 64;
constexpr u32 L_V3 = 0, L_W2C = 8192, L_W2S = 8448, L_U = 12288, L_RAW = 20480;
enum { C_TAGV = 12, C_TAG = 13, C_WISS = 14, C_ABRDY = 0, C_ABSTG = 1, C_W = 3, C_V3 = 4, C_U = 5, C_XISS = 6, C_XRDY = 7, C_WD = 8, C_RED = 10 };

constexpr int perm1(int j) { return j < 24 ? ((j & ~7) | ((j + 2) & 7)) : (j == 24 ? 25 : (j == 25 ? 24 : j)); }
struct Slots { int a[192]; int b[192]; bool v0[192]; bool v1[192]; int n; };
struct SlotBuilder {
  Slots s{}; bool vis[27][27] = {}; int n = 0;
  constexpr bool seen(int a, int b) const { return vis[a < b ? a : b][a < b ? b : a]; }
  constexpr void put(int a, int b) {
    s.a[n] = a; s.b[n] = b;
    s.v0[n] = !seen(a, b); vis[a < b ? a : b][a < b ? b : a] = true;
    const int na = perm1(a), nb = perm1(b);
    s.v1[n] = !seen(na, nb); vis[na < nb ? na : nb][na < nb ? nb : na] = true;
    ++n;
  }
};
constexpr int sg2(int m) { return perm1(perm1(2 * m)) >> 1; }
constexpr Slots make_slots() {
  SlotBuilder t{};
  for (int A = 0; A < 13; ++A)
    for (int B = A + 1; B < 13; ++B) {
      int ca = A, cb = B;
      while (!t.seen(2 * ca, 2 * cb)) {
        t.put(2 * ca, 2 * cb); t.put(2 * ca, 2 * cb + 1); t.put(2 * ca + 1, 2 * cb); t.put(2 * ca + 1, 2 * cb + 1);
        ca = sg2(ca); cb = sg2(cb);
      }
    }
  for (int A = 0; A < 13; ++A) {
    int ca = A;
    while (!t.seen(2 * ca, 2 * ca + 1)) {
      t.put(2 * ca, 2 * ca); t.put(2 * ca, 2 * ca + 1); t.put(2 * ca + 1, 2 * ca + 1); t.put(2 * ca + 1, 26);
      ca = sg2(ca);
    }
  }
  for (int a = 0; a < 27; ++a) { int ca = a; while (!t.seen(ca, 26)) { t.put(ca, 26); ca = perm1(perm1(ca)); } }
  t.s.n = t.n;
  for (int n = t.n; n < 192; ++n) { t.s.a[n] = 26; t.s.b[n] = 26; t.s.v0[n] = false; t.s.v1[n] = false; }
  return t.s;
}
constexpr Slots SL = make_slots();
static_assert(SL.n <= 192, "too many slots");

constexpr int UP = 33, ZB = 30 * UP, ZA = 676;
struct SymTab { u32 w[192][4]; };
constexpr SymTab make_symtab() {
  SymTab t{};
  for (int d = 0; d < 192; ++d) {
    int c = d >> 2, s = c >> 1, hh = c & 1;
    for (int e = 0; e < 2; ++e) {
      int sg = 8 * s + 2 * (d & 3) + e;
      int a = hh ? perm1(SL.a[sg]) : SL.a[sg], b = hh ? perm1(SL.b[sg]) : SL.b[sg];
      bool v = hh ? SL.v1[sg] : SL.v0[sg];
      u32 b1 = ZB, b2 = ZB, a1 = ZA, a2 = ZA;
      if (v) {
        b1 = a * UP + b; b2 = a == b ? ZB : b * UP + a;
        if (a < 26 && b < 26) { a1 = a * 26 + b; a2 = a == b ? ZA : b * 26 + a; }
      }
      t.w[d][e] = b1 | (b2 << 16);
      t.w[d][2 + e] = a1 | (a2 << 16);
    }
  }
  return t;
}
__device__ const SymTab SYMTAB = make_symtab();

constexpr u32 OOB = 0x7FFFFF00u;
constexpr u32 SPIN_MAX = 1u << 18;

__device__ __forceinline__ float bflo(u32 v) { return __uint_as_float(v << 16); }
__device__ __forceinline__ float bfhi(u32 v) { return __uint_as_float(v & 0xffff0000u); }
__device__ __forceinline__ u32 packbf(float a, float b) {
  f32x2 f = {a, b};
  bf16x2 z = __builtin_convertvector(f, bf16x2);
  return __builtin_bit_cast(u32, z);
}
__device__ __forceinline__ float mulf(float a, float b) { float r; asm("v_mul_f32 %0, %1, %2" : "=v"(r) : "v"(a), "v"(b)); return r; }
template <int E> __device__ __forceinline__ f32x2 pkmul_bc(f32x2 a, f32x2 b) {
  f32x2 r; asm("v_pk_mul_f32 %0, %1, %2 op_sel:[%3,0] op_sel_hi:[%3,1]" : "=v"(r) : "v"(a), "v"(b), "i"(E)); return r;
}
__device__ __forceinline__ float dot2bf(u32 a, u32 b, float c) {
  return __builtin_amdgcn_fdot2_f32_bf16(__builtin_bit_cast(bf16x2, a), __builtin_bit_cast(bf16x2, b), c, false);
}
__device__ __forceinline__ rsrc_t mkrs(const void* p, u32 bytes) { return __builtin_amdgcn_make_buffer_rsrc((void*)p, 0, bytes, 0x00020000); }
__device__ __forceinline__ u64 mkflag(u32 tag) { return (u64)tag | ((u64)(tag * 0x9E3779B1u + 0x7F4A7C15u) << 32); }
__device__ __forceinline__ void drain_vm() { asm volatile("s_waitcnt vmcnt(0)" ::: "memory"); }
__device__ __forceinline__ void lds_inc(LAS u32* cnt) {
  u64 keep;
  asm volatile("s_mov_b64 %0, exec\n\ts_mov_b64 exec, 1\n\tds_add_u32 %1, %2\n\ts_mov_b64 exec, %0"
               : "=&s"(keep) : "v"((u32)(size_t)cnt), "v"(1u) : "memory");
}
__device__ __forceinline__ void team_arrive(LAS u32* cnt, int) {
  asm volatile("s_waitcnt lgkmcnt(0)" ::: "memory");
  lds_inc(cnt);
}
__device__ __forceinline__ float sum_rows(float v) {
  auto a = __builtin_amdgcn_permlane32_swap(__float_as_uint(v), __float_as_uint(v), false, false);
  v = __uint_as_float(a[0]) + __uint_as_float(a[1]);
  auto b = __builtin_amdgcn_permlane16_swap(__float_as_uint(v), __float_as_uint(v), false, false);
  return __uint_as_float(b[0]) + __uint_as_float(b[1]);
}
template <int CTRL> __device__ __forceinline__ float dpp_add(float v) {
  return v + __uint_as_float(__builtin_amdgcn_update_dpp(0u, __float_as_uint(v), CTRL, 0xf, 0xf, true));
}
__device__ __forceinline__ float sum_row16(float v) {
  v = dpp_add<0x128>(v);
  v = dpp_add<0x124>(v);
  v = dpp_add<0x122>(v);
  v = dpp_add<0x121>(v);
  return v;
}

#define STAMP(k) do {} while (0)

struct Args {
  const int* idx; const uint16_t* table; const uint16_t* wc; const uint16_t* wco; const uint16_t* w1;
  const uint16_t* b1; const uint16_t* w2; const uint16_t* bo; uint16_t* out; char* ws;
  u32 table_bytes; u32 pad_;
};
#define CAS __attribute__((address_space(4)))

template <int SLEEP> __device__ __forceinline__ void team_wait(LAS u32* cnt, u32 target) {
  u32 v, s, n = SPIN_MAX;
  asm volatile(
      "1:\n\tds_read_b32 %0, %3\n\ts_waitcnt lgkmcnt(0)\n\tv_readfirstlane_b32 %1, %0\n\ts_cmp_ge_u32 %1, %4\n\ts_cbranch_scc1 2f\n\t"
      "s_sleep %5\n\ts_sub_u32 %2, %2, 1\n\ts_cbranch_scc0 1b\n2:"
      : "=&v"(v), "=&s"(s), "+s"(n) : "v"((u32)(size_t)cnt), "s"(target), "i"(SLEEP) : "memory", "scc");
}
template <int SLEEP> __device__ __forceinline__ void poll_flags(const char* p, u64 want) {
  u64 v; u32 n = SPIN_MAX;
  asm volatile(
      "1:\n\tglobal_load_dwordx2 %0, %2, off sc1\n\ts_waitcnt vmcnt(0)\n\tv_cmp_ne_u64 vcc, %3, %0\n\ts_nop 4\n\ts_cbranch_vccz 2f\n\t"
      "s_sleep %4\n\ts_sub_u32 %1, %1, 1\n\ts_cbranch_scc0 1b\n2:"
      : "=&v"(v), "+s"(n) : "v"(p), "s"(want), "i"(SLEEP) : "memory", "scc", "vcc");
}
__device__ __forceinline__ void lds_barrier() {
  asm volatile("s_waitcnt lgkmcnt(0)" ::: "memory");
  __builtin_amdgcn_s_barrier();
  asm volatile("" ::: "memory");
}
__device__ __forceinline__ u32 load_epoch(const char* ws) {
  return __hip_atomic_load((const GAS u32*)(ws + WS_E), __ATOMIC_RELAXED, __HIP_MEMORY_SCOPE_AGENT);
}
__device__ __forceinline__ void store_flag(char* ws, u32 off, u64 flag) {
  __hip_atomic_store((GAS u64*)(ws + off), flag, __ATOMIC_RELAXED, __HIP_MEMORY_SCOPE_AGENT);
}
__device__ __forceinline__ u32 get_tag(LAS u32* cnt) {
  team_wait<0>(cnt + C_TAG, 1);
  return __builtin_amdgcn_readfirstlane(cnt[C_TAGV]);
}
__device__ __forceinline__ u32 opq(u32 v) { asm("" : "+v"(v)); return v; }

__global__ void __launch_bounds__(768, 3) xdfm_kernel(Args a) {
  asm volatile(
      "s_cmp_lg_u32 %0, 250\n\ts_cbranch_scc1 9f\n\t"
      "s_sleep 127\n\ts_sleep 127\n\ts_sleep 127\n\ts_sleep 127\n\ts_sleep 127\n\ts_sleep 127\n9:" :: "s"(blockIdx.x) : "scc");
  extern __shared__ __attribute__((aligned(16))) char lds_raw[];
  LAS char* lds = (LAS char*)lds_raw;
  const int tid = threadIdx.x, lane = tid & 63;
  const int w = __builtin_amdgcn_readfirstlane(tid >> 6);
  const int b = blockIdx.x, j = b >> 3;
  const int jg = j < 6 ? (j >= 3 ? 1 : 0) : (j >= 19 ? 1 : 0);
  const int g = (b & 7) * 2 + jg, r = j < 6 ? 13 + j - 3 * jg : j - 6 - 13 * jg;
  if (w > 8) return;
  LAS u32* cnt = (LAS u32*)(lds + L_CNT);
  asm volatile("" :: "s"(a.idx), "s"(a.table), "s"(a.wc), "s"(a.wco), "s"(a.w1), "s"(a.b1), "s"(a.w2), "s"(a.bo), "s"(a.out), "s"(a.ws), "s"(a.table_bytes));
  const rsrc_t rws = mkrs(a.ws, WS_END);
  const rsrc_t ridx = mkrs(a.idx, NB * NF * 4), rtab = mkrs(a.table, a.table_bytes);

  if (w == 8) {
    __builtin_amdgcn_s_setprio(3);
    if (lane < 16) cnt[lane] = 0;
    u32 tag;
    if (r < 13) {
      u32 id[4]; u32x4 xg[8];
#pragma unroll
      for (int m = 0; m < 2; ++m) {
        const int p0 = 4 * r + 2 * m, t = p0 >= NF ? 1 : 0, f0 = p0 - t * NF;
        const u32x2 ia = __builtin_amdgcn_raw_buffer_load_b64(ridx, (lane & 31) * (NF * 4), ((g * 64 + t * 32) * NF + f0) * 4, 0);
        id[2 * m] = ia.x; id[2 * m + 1] = ia.y;
      }
      const u32 ep = load_epoch(a.ws);
#pragma unroll
      for (int k = 0; k < 8; ++k) xg[k] = __builtin_amdgcn_raw_buffer_load_b128(rtab, id[k >> 1] * 64 + (k & 1) * 32 + (lane >> 5) * 16, 0, 0);
      lds_barrier();
      STAMP(1);
      tag = __builtin_amdgcn_readfirstlane(ep) + 1u;
      cnt[C_TAGV] = tag;
      team_arrive(cnt + C_TAG, lane);
#pragma unroll
      for (int k = 0; k < 8; ++k) __builtin_amdgcn_raw_buffer_store_b128(xg[k], rws, WS_X + lane * 16, (g * 104 + r * 8 + k) * 1024, 16);
      STAMP(2);
      drain_vm();
    } else {
      const u32 ep = load_epoch(a.ws);
      lds_barrier();
      tag = __builtin_amdgcn_readfirstlane(ep) + 1u;
      cnt[C_TAGV] = tag;
      team_arrive(cnt + C_TAG, lane);
    }
    const u64 flag = mkflag(tag);
    if (lane == 0) store_flag(a.ws, WS_XF + (g * 16 + r) * FL, flag);
    STAMP(3);
    if (r < 13) {
      __builtin_amdgcn_s_sleep(P_XDELAY);
      poll_flags<2>(a.ws + WS_XF + (g * 16 + (lane < 13 ? lane : 0)) * FL, flag);
      lds_inc(cnt + C_XRDY);
    }
    STAMP(4);
    __builtin_amdgcn_s_sleep(P_BDELAY);
    poll_flags<6>(a.ws + (lane < 32 ? WS_FA : WS_FB) + ((lane & 31) < NF ? (lane & 31) : 0) * FL, flag);
    lds_inc(cnt + C_ABRDY);
    STAMP(5);
  } else if (w >= 4) {
    const int cw = w - 4, smp = g * 64 + r * 4 + cw;
    u32 id[2]; u32x4 xr[2];
    u32 bo;
    {
      const CAS u32* ip = (const CAS u32*)(a.idx + smp * NF);
      u32 is[NF];
#pragma unroll
      for (int f = 0; f < NF; ++f) is[f] = ip[f];
      const size_t bop = (size_t)a.bo;
      bo = *(const CAS u32*)(bop & ~(size_t)3) >> ((bop & 2) * 8);
      u32 iv = 0;
      asm("v_writelane_b32 %0, %1, 0\n\tv_writelane_b32 %0, %2, 1\n\tv_writelane_b32 %0, %3, 2\n\tv_writelane_b32 %0, %4, 3\n\tv_writelane_b32 %0, %5, 4\n\tv_writelane_b32 %0, %6, 5\n\tv_writelane_b32 %0, %7, 6\n\tv_writelane_b32 %0, %8, 7\n\tv_writelane_b32 %0, %9, 8\n\tv_writelane_b32 %0, %10, 9\n\tv_writelane_b32 %0, %11, 10\n\tv_writelane_b32 %0, %12, 11\n\tv_writelane_b32 %0, %13, 12\n\tv_writelane_b32 %0, %14, 13\n\tv_writelane_b32 %0, %15, 14\n\tv_writelane_b32 %0, %16, 15\n\tv_writelane_b32 %0, %17, 16\n\tv_writelane_b32 %0, %18, 17\n\tv_writelane_b32 %0, %19, 18\n\tv_writelane_b32 %0, %20, 19\n\tv_writelane_b32 %0, %21, 20\n\tv_writelane_b32 %0, %22, 21\n\tv_writelane_b32 %0, %23, 22\n\tv_writelane_b32 %0, %24, 23\n\tv_writelane_b32 %0, %25, 24\n\tv_writelane_b32 %0, %26, 25"
          : "+v"(iv) : "s"(is[0]), "s"(is[1]), "s"(is[2]), "s"(is[3]), "s"(is[4]), "s"(is[5]), "s"(is[6]), "s"(is[7]), "s"(is[8]), "s"(is[9]), "s"(is[10]), "s"(is[11]), "s"(is[12]), "s"(is[13]), "s"(is[14]), "s"(is[15]), "s"(is[16]), "s"(is[17]), "s"(is[18]), "s"(is[19]), "s"(is[20]), "s"(is[21]), "s"(is[22]), "s"(is[23]), "s"(is[24]), "s"(is[25]));
      id[0] = __builtin_amdgcn_ds_bpermute((lane >> 2) * 4, iv);
      id[1] = __builtin_amdgcn_ds_bpermute((16 + (lane >> 2)) * 4, iv);
    }
    if (r < 13) __builtin_amdgcn_s_setprio(2);
    lds_barrier();
#pragma unroll
    for (int m = 0; m < 2; ++m) { const int f = 16 * m + (lane >> 2); xr[m] = __builtin_amdgcn_raw_buffer_load_b128(rtab, f < NF ? id[m] * 64 + (lane & 3) * 16 : OOB, 0, 0); }
    __builtin_amdgcn_s_setprio(0);
    STAMP(1);
    f32x2 xp[14];
    {
      const u32 o = L_XS + cw * XS_S + (lane >> 2) * 64 + (lane & 3) * 16;
      *(LAS u32x4*)(lds + o) = xr[0];
      u32x4 x1 = xr[1];
      if ((lane >> 2) == NF - 16) x1 = (u32x4){0x3F803F80u, 0x3F803F80u, 0x3F803F80u, 0x3F803F80u};
      *(LAS u32x4*)(lds + opq((lane >> 2) < 12 ? o + 1024 : L_XS + cw * XS_S + 27 * 64 + (lane & 3) * 16)) = x1;
      asm volatile("s_waitcnt lgkmcnt(0)" ::: "memory");
      const int hh = lane >> 5, q = (lane >> 2) & 3, pp = lane & 3;
      const u32 base = L_XS + cw * XS_S + ((lane >> 4) & 1) * 32 + pp * 8;
      const u32 a0 = base + (hh ? q + 2 : q) * 64, a1 = base + (hh ? ((q + 6) & 7) : q + 4) * 64;
      const u32 a6 = base + (24 + ((hh && q < 2) ? (q ^ 1) : q)) * 64;
#pragma unroll
      for (int t = 0; t < 7; ++t) {
        s16x4 y = __builtin_amdgcn_ds_read_tr16_b64_v4i16((LAS s16x4*)(lds + (t < 6 ? ((t & 1) ? a1 : a0) + (t >> 1) * 512 : a6)));
        u32x2 u = __builtin_bit_cast(u32x2, y);
        xp[2 * t] = (f32x2){bflo(u.x), bfhi(u.x)}; xp[2 * t + 1] = (f32x2){bflo(u.y), bfhi(u.y)};
      }
    }
    bf16x8 z[24];
#pragma unroll
    for (int s = 0; s < 24; ++s) {
      u32x4 zz;
#pragma unroll
      for (int e = 0; e < 4; ++e) {
        const int s1 = 8 * s + 2 * e, s2 = s1 + 1;
        const int a1 = SL.a[s1], b1 = SL.b[s1], a2 = SL.a[s2], b2 = SL.b[s2];
        if (a1 == a2 && (b1 & 1) == 0 && b1 < 26 && b2 == b1 + 1) {
          const f32x2 pr = (a1 & 1) ? pkmul_bc<1>(xp[a1 >> 1], xp[b1 >> 1]) : pkmul_bc<0>(xp[a1 >> 1], xp[b1 >> 1]);
          zz[e] = packbf(pr[0], pr[1]);
        } else {
          const float p1 = b1 == 26 ? xp[a1 >> 1][a1 & 1] : mulf(xp[a1 >> 1][a1 & 1], xp[b1 >> 1][b1 & 1]);
          const float p2 = b2 == 26 ? xp[a2 >> 1][a2 & 1] : mulf(xp[a2 >> 1][a2 & 1], xp[b2 >> 1][b2 & 1]);
          zz[e] = packbf(p1, p2);
        }
      }
      z[s] = __builtin_bit_cast(bf16x8, zz);
      asm volatile("" : "+v"(z[s]));
    }
    const u32 tag = get_tag(cnt);
    const u64 flag = mkflag(tag);
    STAMP(2);
    team_wait<1>(cnt + C_ABRDY, 1);
    STAMP(3);
    if (r < 13) team_wait<1>(cnt + C_XISS, 4);
    __builtin_amdgcn_s_setprio(2);
    STAMP(4);
    {
      u32x4 y[10];
#pragma unroll
      for (int m = 0; m < 10; ++m) {
        const u32 n = ((m % 5) * 4 + cw) * 64 + lane;
        y[m] = __builtin_amdgcn_raw_buffer_load_b128(rws, n < NF * 48 ? n * 16 : OOB, m < 5 ? WS_PA : WS_PB, 16);
      }
#pragma unroll
      for (int m = 0; m < 5; ++m) {
        const u32 n = (m * 4 + cw) * 64 + lane, i = (n * 1366u) >> 16, c = n - i * 48;
        const u32 o = opq(n < NF * 48 ? L_A + c * APITCH + i * 16 : L_A + 26 * 16);
        *(LAS u32x4*)(lds + o) = y[m];
        *(LAS u32x4*)(lds + o + (L_B - L_A)) = y[5 + m];
      }
      team_arrive(cnt + C_ABSTG, lane);
      team_wait<0>(cnt + C_ABSTG, 4);
    }
    __builtin_amdgcn_s_setprio(0);
    STAMP(5);
    const u32 afr = (lane >> 5) * APITCH + (lane & 31) * 16;
    const u32 tag2 = (u32)(flag >> 32);
    const int sg = r * 4 + cw;
    const u32 po = opq(lane < 26 ? WS_P + ((g * 26 + lane) * 2 + (sg >> 5)) * 512 + (sg & 31) * 16 : OOB);
    u32x4 yp;
    f32x16 acc1 = {}, acc2 = {};
    {
      constexpr int AD = 2;
      bf16x8 af[24], bf[24];
#pragma unroll
      for (int s = 0; s < AD; ++s) { af[s] = *(LAS bf16x8*)(lds + L_A + afr + s * 2 * APITCH); bf[s] = *(LAS bf16x8*)(lds + L_B + afr + s * 2 * APITCH); }
      __builtin_amdgcn_sched_barrier(0);
#pragma unroll
      for (int s = 0; s < 24; ++s) {
        if (s + AD < 24) { af[s + AD] = *(LAS bf16x8*)(lds + L_A + afr + (s + AD) * 2 * APITCH); bf[s + AD] = *(LAS bf16x8*)(lds + L_B + afr + (s + AD) * 2 * APITCH); }
        if (s == P_PQ) yp = __builtin_amdgcn_raw_buffer_load_b128(rws, po, 0, 16);
        acc1 = __builtin_amdgcn_mfma_f32_32x32x16_bf16(af[s], z[s], acc1, 0, 0, 0);
        acc2 = __builtin_amdgcn_mfma_f32_32x32x16_bf16(bf[s], z[s], acc2, 0, 0, 0);
        __builtin_amdgcn_sched_barrier(0);
      }
    }
    float sum = 0.f;
#pragma unroll
    for (int e = 0; e < 12; ++e) sum += acc1[e] * acc2[e];
    { const float t = acc1[12] * acc2[12] + acc1[13] * acc2[13]; sum += lane < 32 ? t : 0.f; }
    sum = sum_rows(sum_row16(sum));
    STAMP(6);
    for (u32 it = 0; it < SPIN_MAX; ++it) {
      const bool ok = lane >= 26 || (yp.y == tag && yp.w == tag2);
      if (__all(ok)) break;
      asm volatile("" ::: "memory");
      __builtin_amdgcn_s_sleep(1);
      yp = __builtin_amdgcn_raw_buffer_load_b128(rws, po, 0, 16);
    }
    float part = sum_rows(sum_row16(__uint_as_float(yp.x)));
    if (lane == 0) {
      const __bf16 ob = (__bf16)(sum + part + bflo(bo));
      a.out[smp] = __builtin_bit_cast(uint16_t, ob);
    }
    STAMP(7);
  } else if (r < 13) {
      const rsrc_t rw1 = mkrs(a.w1, DIN * DIN * 2);
      const int mh = w & 1, kh = w >> 1, hh = lane >> 5;
      lds_barrier();
      STAMP(1);
      {
        u32x4 v[26];
        const u32 vo = (w * 208 + (lane >> 3)) * 1664 + r * 128 + (lane & 7) * 16;
        const int c = lane & 7;
        const u32 lo = L_W + (w * 208 + (lane >> 3)) * 128 + (((c >> 1) ^ (((lane >> 4) & 1) << 1)) * 32) + (c & 1) * 16;
#pragma unroll
        for (int i = 0; i < 26; ++i) v[i] = __builtin_amdgcn_raw_buffer_load_b128(rw1, vo, i * 8 * 1664, 0);
#pragma unroll
        for (int i = 0; i < 26; ++i) *(LAS u32x4*)(lds + lo + i * 1024) = v[i];
      }
      team_arrive(cnt + C_W, lane);
      const u32 tag = get_tag(cnt);
      const u64 flag = mkflag(tag);
      f32x16 acc[2];
      f32x2 wf[8];
      {
        const CAS u32* bp = (const CAS u32*)(a.b1 + r * 64 + kh * 32);
        const CAS u32* wp = (const CAS u32*)(a.w2 + r * 64 + kh * 32);
        u32 bs[16], ws_[16];
#pragma unroll
        for (int t = 0; t < 16; ++t) { bs[t] = bp[t]; ws_[t] = wp[t]; }
#pragma unroll
        for (int t = 0; t < 16; ++t) asm volatile("" : "+s"(bs[t]), "+s"(ws_[t]));
#pragma unroll
        for (int t = 0; t < 4; ++t) {
          const u32 b0 = hh ? bs[4 * t + 2] : bs[4 * t], b1 = hh ? bs[4 * t + 3] : bs[4 * t + 1];
          const u32 w0 = hh ? ws_[4 * t + 2] : ws_[4 * t], w1 = hh ? ws_[4 * t + 3] : ws_[4 * t + 1];
          acc[0][4 * t] = bflo(b0); acc[0][4 * t + 1] = bfhi(b0); acc[0][4 * t + 2] = bflo(b1); acc[0][4 * t + 3] = bfhi(b1);
          acc[1][4 * t] = 0.f; acc[1][4 * t + 1] = 0.f; acc[1][4 * t + 2] = 0.f; acc[1][4 * t + 3] = 0.f;
          wf[2 * t] = (f32x2){bflo(w0), bfhi(w0)}; wf[2 * t + 1] = (f32x2){bflo(w1), bfhi(w1)};
          asm volatile("" : "+v"(wf[2 * t]), "+v"(wf[2 * t + 1]));
        }
      }
      STAMP(2);
      team_wait<0>(cnt + C_XRDY, 1);
      STAMP(3);
      constexpr int XW = P_XW, FD = 3;
      u32x4 xv[26];
      auto ldx = [&](int i) { xv[i] = __builtin_amdgcn_raw_buffer_load_b128(rws, WS_X + lane * 16, (g * 104 + mh * 52 + kh * 26 + i) * 1024, 16); };
#pragma unroll
      for (int i = 0; i < XW; ++i) ldx(i);
      team_wait<0>(cnt + C_W, 4);
      __builtin_amdgcn_s_setprio(1);
      {
        const int q = (lane >> 2) & 3, pp = lane & 3, gam = (lane >> 4) & 1;
        const u32 rb = L_W + kh * 26 * 2048 + (8 * hh + q) * 128 + pp * 8;
        const u32 ad0 = rb + (((2 * kh + gam) ^ (q & 2)) * 32), ad1 = rb + (((2 * (kh ^ 1) + gam) ^ (q & 2)) * 32);
        bf16x8 fr[26][2];
        auto ldf = [&](int i) {
#pragma unroll
          for (int t = 0; t < 2; ++t) {
            const u32 o = (t ? ad1 : ad0) + i * 2048;
            const s16x4 y0 = __builtin_amdgcn_ds_read_tr16_b64_v4i16((LAS s16x4*)(lds + o));
            const s16x4 y1 = __builtin_amdgcn_ds_read_tr16_b64_v4i16((LAS s16x4*)(lds + o + 512));
            fr[i][t] = __builtin_bit_cast(bf16x8, __builtin_shufflevector(y0, y1, 0, 1, 2, 3, 4, 5, 6, 7));
          }
        };
#pragma unroll
        for (int i = 0; i < FD; ++i) ldf(i);
        __builtin_amdgcn_sched_barrier(0);
#pragma unroll
        for (int i = 0; i < 26; ++i) {
          if (i + XW < 26) ldx(i + XW);
          if (i == P_XI) lds_inc(cnt + C_XISS);
          if (i == 8) lds_inc(cnt + C_WD + kh);
          if (i == 22) team_wait<0>(cnt + C_WD + kh, 2);
          if (i + FD < 26) ldf(i + FD);
          const bf16x8 xb = __builtin_bit_cast(bf16x8, xv[i]);
          acc[0] = __builtin_amdgcn_mfma_f32_32x32x16_bf16(fr[i][0], xb, acc[0], 0, 0, 0);
          acc[1] = __builtin_amdgcn_mfma_f32_32x32x16_bf16(fr[i][1], xb, acc[1], 0, 0, 0);
          __builtin_amdgcn_sched_barrier(0);
        }
      }
      STAMP(4);
      {
        const u32 so = L_W + kh * 26 * 2048 + mh * 4096 + lane * 16;
#pragma unroll
        for (int t = 0; t < 4; ++t) *(LAS f32x4*)(lds + so + t * 1024) = (f32x4){acc[1][4 * t], acc[1][4 * t + 1], acc[1][4 * t + 2], acc[1][4 * t + 3]};
        team_arrive(cnt + C_RED + mh, lane);
        team_wait<0>(cnt + C_RED + mh, 2);
      }
      STAMP(5);
      float part;
      {
        constexpr float K1 = -2.0f * 1.4426950408889634f * 0.7978845608028654f, K2 = K1 * 0.044715f;
        const u32 si = L_W + (kh ^ 1) * 26 * 2048 + mh * 4096 + lane * 16;
        f32x4 o[4];
#pragma unroll
        for (int t = 0; t < 4; ++t) o[t] = *(LAS f32x4*)(lds + si + t * 1024);
        f32x2 h[8], u[8], y[8];
#pragma unroll
        for (int m = 0; m < 8; ++m) {
          h[m] = (f32x2){acc[0][2 * m], acc[0][2 * m + 1]} + (f32x2){o[m >> 1][2 * (m & 1)], o[m >> 1][2 * (m & 1) + 1]};
          u[m] = (h[m] * h[m] * K2 + K1) * h[m];
          y[m] = h[m] * wf[m];
        }
#pragma unroll
        for (int m = 0; m < 8; ++m) { u[m][0] = __builtin_amdgcn_exp2f(u[m][0]); u[m][1] = __builtin_amdgcn_exp2f(u[m][1]); }
#pragma unroll
        for (int m = 0; m < 8; ++m) { u[m] = u[m] + 1.0f; }
#pragma unroll
        for (int m = 0; m < 8; ++m) { u[m][0] = __builtin_amdgcn_rcpf(u[m][0]); u[m][1] = __builtin_amdgcn_rcpf(u[m][1]); }
        f32x2 p0 = y[0] * u[0], p1 = y[1] * u[1];
#pragma unroll
        for (int m = 2; m < 8; m += 2) { p0 += y[m] * u[m]; p1 += y[m + 1] * u[m + 1]; }
        p0 += p1;
        part = p0[0] + p0[1];
      }
      {
        auto sw = __builtin_amdgcn_permlane32_swap(__float_as_uint(part), __float_as_uint(part), false, false);
        part = __uint_as_float(sw[0]) + __uint_as_float(sw[1]);
      }
      {
        const u32x4 o = {__float_as_uint(part), tag, __float_as_uint(part), (u32)(flag >> 32)};
        __builtin_amdgcn_raw_buffer_store_b128(o, rws, lane < 32 ? WS_P + ((g * 26 + r * 2 + kh) * 2 + mh) * 512 + lane * 16 : OOB, 0, 16);
      }
      STAMP(6);
  } else {
      const int p = g * 3 + r - 13;
      const rsrc_t rwc = mkrs(a.wc, 3 * CUBE * 2), rwo = mkrs(a.wco, 78 * 2);
      __builtin_amdgcn_s_setprio(3);
      if (p < NF) {
        const int i = p;
        u32x4 tb[3];
        const int hb = (w >> 1) * 13, el = (w & 1) * 64 + lane;
        const CAS u32* w3p = (const CAS u32*)(a.wco + 52);
        u32 w3[13];
#pragma unroll
        for (int h = 0; h < 13; ++h) w3[h] = w3p[h];
        u32x4 v[13];
#pragma unroll
        for (int h = 0; h < 13; ++h) v[h] = __builtin_amdgcn_raw_buffer_load_b128(rwc, el < 85 ? (2 * CUBE + hb * 676 + 8 * el) * 2 : OOB, h * 676 * 2, 0);
        u32 v2[2];
#pragma unroll
        for (int m = 0; m < 2; ++m) {
          const u32 d = tid + 256 * m, h = (d * 5042u) >> 16, e = d - 13 * h;
          v2[m] = __builtin_amdgcn_raw_buffer_load_b32(rwc, d < 338 ? (CUBE + (h * NF + i) * NF) * 2 + e * 4 : OOB, 0, 0);
        }
        const u32 wq = __builtin_amdgcn_raw_buffer_load_b16(rwo, tid < NF ? (NF + tid) * 2 : OOB, 0, 0);
        const u32 w1i = __builtin_amdgcn_raw_buffer_load_b16(rwo, i * 2, 0, 0);
        if (w == 0) {
#pragma unroll
          for (int m = 0; m < 3; ++m) tb[m] = *(const GAS u32x4*)SYMTAB.w[lane + 64 * m];
        }
        __builtin_amdgcn_sched_barrier(0);
        lds_barrier();
        STAMP(1);
        {
          float acc[8];
#pragma unroll
          for (int e = 0; e < 8; ++e) acc[e] = 0.f;
#pragma unroll
          for (int h = 0; h < 13; ++h) {
            const u32 we = (h & 1) ? w3[h >> 1] >> 16 : w3[h >> 1] & 0xffffu;
            const u32 wo = (h & 1) ? w3[(13 + h) >> 1] & 0xffffu : w3[(13 + h) >> 1] >> 16;
            const u32 wl = hb ? wo : we, wh = wl << 16;
#pragma unroll
            for (int e = 0; e < 4; ++e) { acc[2 * e] = dot2bf(v[h][e], wl, acc[2 * e]); acc[2 * e + 1] = dot2bf(v[h][e], wh, acc[2 * e + 1]); }
          }
          const u32 vo = opq(el < 85 ? L_V3 + (w >> 1) * 2816 + el * 32 : L_V3 + 5632 + lane * 32);
          *(LAS f32x4*)(lds + vo) = (f32x4){acc[0], acc[1], acc[2], acc[3]};
          *(LAS f32x4*)(lds + vo + 16) = (f32x4){acc[4], acc[5], acc[6], acc[7]};
#pragma unroll
          for (int m = 0; m < 2; ++m) *(LAS u32*)(lds + L_W2S + (tid + 256 * m) * 4) = v2[m];
          if (w == 0) *(LAS u32*)(lds + L_W2C + lane * 4) = wq << 16;
        }
        const int tj = w >> 1, tp = w & 1, kq = lane >> 4;
        const int jr = 16 * tj + (lane & 15), jp = 16 * tp + (lane & 15);
        u32 oa[7], ob[7], oc[7];
#pragma unroll
        for (int s = 0; s < 7; ++s) {
          const int h = 4 * s + kq;
          const bool inv = h < NF && jp < NF;
          oa[s] = opq((h < NF && jr < NF) ? L_W2S + (h * NF + jr) * 2 : L_W2C + 63 * 4);
          ob[s] = opq(inv ? L_V3 + (h * NF + jp) * 4 : L_W2C + ((h < NF && jp == NF) ? h : 63) * 4);
          oc[s] = opq(inv ? L_V3 + 2816 + (h * NF + jp) * 4 : L_W2C + 63 * 4);
        }
        team_arrive(cnt + C_V3, lane);
        team_wait<0>(cnt + C_V3, 4);
        STAMP(2);
        u32 av[7]; float bv[7], bw[7];
#pragma unroll
        for (int s = 0; s < 7; ++s) { av[s] = *(LAS uint16_t*)(lds + oa[s]); bv[s] = *(LAS float*)(lds + ob[s]); bw[s] = *(LAS float*)(lds + oc[s]); }
#pragma unroll
        for (int s = 0; s < 7; ++s) bv[s] += bw[s];
        f32x4 u = {0.f, 0.f, 0.f, 0.f}, u2 = {0.f, 0.f, 0.f, 0.f};
#pragma unroll
        for (int s = 0; s < 7; ++s) {
          if (s & 1) u2 = __builtin_amdgcn_mfma_f32_16x16x4f32(bflo(av[s]), bv[s], u2, 0, 0, 0);
          else u = __builtin_amdgcn_mfma_f32_16x16x4f32(bflo(av[s]), bv[s], u, 0, 0, 0);
        }
        u += u2;
#pragma unroll
        for (int e = 0; e < 4; ++e) {
          const int jo = 16 * tj + 4 * kq + e;
          const float val = (jo == NF && jp == NF) ? bflo(w1i) : u[e];
          *(LAS float*)(lds + L_U + (jo * UP + jp) * 4) = val;
        }
        team_arrive(cnt + C_U, lane);
        if (w == 0) {
          team_wait<0>(cnt + C_U, 4);
          STAMP(3);
          LAS float* U = (LAS float*)(lds + L_U);
          float e[12];
#pragma unroll
          for (int m = 0; m < 3; ++m) {
            e[4 * m] = U[tb[m].x & 0xffff]; e[4 * m + 1] = U[tb[m].x >> 16]; e[4 * m + 2] = U[tb[m].y & 0xffff]; e[4 * m + 3] = U[tb[m].y >> 16];
          }
#pragma unroll
          for (int m = 0; m < 3; ++m)
            __builtin_amdgcn_raw_buffer_store_b32(packbf(e[4 * m] + e[4 * m + 1], e[4 * m + 2] + e[4 * m + 3]), rws, WS_PB + i * 768 + (lane + 64 * m) * 4, 0, 16);
          const u64 flagb = mkflag(get_tag(cnt));
          drain_vm();
          if (lane == 0) store_flag(a.ws, WS_FB + i * FL, flagb);
          STAMP(4);
        }
      } else {
        const int i = (p - NF) * 4 + w;
        const bool prod = p < 33 && i < NF;
        u32 v[6]; u32x4 tb[3];
#pragma unroll
        for (int m = 0; m < 6; ++m) { const int d = lane + 64 * m; v[m] = __builtin_amdgcn_raw_buffer_load_b32(rwc, (prod && d < 338) ? i * 1352 + d * 4 : OOB, 0, 0); }
#pragma unroll
        for (int m = 0; m < 3; ++m) tb[m] = *(const GAS u32x4*)SYMTAB.w[lane + 64 * m];
        __builtin_amdgcn_sched_barrier(0);
        lds_barrier();
        STAMP(1);
        if (prod) {
          LAS char* raw = lds + L_RAW + w * 2048;
#pragma unroll
          for (int m = 0; m < 6; ++m) *(LAS u32*)(raw + (lane + 64 * m) * 4) = v[m];
          asm volatile("s_waitcnt lgkmcnt(0)" ::: "memory");
          LAS uint16_t* R = (LAS uint16_t*)raw;
          u32 e[12];
#pragma unroll
          for (int m = 0; m < 3; ++m) {
            e[4 * m] = R[tb[m].z & 0xffff]; e[4 * m + 1] = R[tb[m].z >> 16]; e[4 * m + 2] = R[tb[m].w & 0xffff]; e[4 * m + 3] = R[tb[m].w >> 16];
          }
#pragma unroll
          for (int m = 0; m < 3; ++m)
            __builtin_amdgcn_raw_buffer_store_b32(packbf(bflo(e[4 * m]) + bflo(e[4 * m + 1]), bflo(e[4 * m + 2]) + bflo(e[4 * m + 3])), rws,
                                                  WS_PA + i * 768 + (lane + 64 * m) * 4, 0, 16);
          const u64 flag0 = mkflag(get_tag(cnt));
          drain_vm();
          if (lane == 0) store_flag(a.ws, WS_FA + i * FL, flag0);
          STAMP(4);
        }
        if (p == 47 && w == 0) {
          const u32 tag0 = get_tag(cnt);
          const u64 flag0 = mkflag(tag0);
          __builtin_amdgcn_s_sleep(32);
#pragma unroll 1
          for (int m = 0; m < 4; ++m) poll_flags<16>(a.ws + WS_XF + (lane + 64 * m) * FL, flag0);
          if (lane == 0) __hip_atomic_store((GAS u32*)(a.ws + WS_E), tag0, __ATOMIC_RELAXED, __HIP_MEMORY_SCOPE_AGENT);
        }
      }
  }
}

}

extern "C" void kernel_launch(void* const* d_in, const int* in_sizes, int n_in, void* d_out, int out_size,
                              void* d_ws, size_t ws_size, hipStream_t stream) {
  using namespace xdfm;
  Args a;
  a.idx = (const int*)d_in[0]; a.table = (const uint16_t*)d_in[1]; a.wc = (const uint16_t*)d_in[2];
  a.wco = (const uint16_t*)d_in[3]; a.w1 = (const uint16_t*)d_in[4]; a.b1 = (const uint16_t*)d_in[5];
  a.w2 = (const uint16_t*)d_in[6]; a.bo = (const uint16_t*)d_in[7]; a.out = (uint16_t*)d_out; a.ws = (char*)d_ws;
  a.table_bytes = (u32)in_sizes[1] * 2u; a.pad_ = 0;
  (void)hipFuncSetAttribute((const void*)xdfm_kernel, hipFuncAttributeMaxDynamicSharedMemorySize, L_TOTAL);
  xdfm_kernel<<<256, 768, L_TOTAL, stream>>>(a);
}
```

```cpp
#include <hip/hip_runtime.h>
#include <hip/hip_bf16.h>
#include <stdint.h>

#define P_ADELAY 8
#define P_BDELAY 8
#define P_WW 10
#define P_PQ 16
#define P_XI 10
#define P_XDELAY 12
#define P_CG 4
#define P_XW 12
namespace xdfm {

typedef unsigned int u32;
typedef unsigned long long u64;
typedef u32 u32x2 __attribute__((ext_vector_type(2)));
typedef u32 u32x4 __attribute__((ext_vector_type(4)));
typedef short s16x4 __attribute__((ext_vector_type(4)));
typedef short s16x8 __attribute__((ext_vector_type(8)));
typedef float f32x2 __attribute__((ext_vector_type(2)));
typedef float f32x4 __attribute__((ext_vector_type(4)));
typedef float f32x16 __attribute__((ext_vector_type(16)));
typedef __bf16 bf16x2 __attribute__((ext_vector_type(2)));
typedef __bf16 bf16x8 __attribute__((ext_vector_type(8)));
#define GAS __attribute__((address_space(1)))
#define LAS __attribute__((address_space(3)))
typedef __amdgpu_buffer_rsrc_t rsrc_t;

constexpr int NF = 26, DIN = 832, NB = 1024, CUBE = 17576;

constexpr u32 WS_E = 0;
constexpr u32 FL = 128;
constexpr u32 WS_XF = 4096;
constexpr u32 WS_FA = 40960;
constexpr u32 WS_FB = 45056;
constexpr u32 WS_PA = 49152;
constexpr u32 WS_PB = 69632;
constexpr u32 WS_P = 131072;
constexpr u32 WS_X = 655360;
constexpr u32 WS_END = WS_X + 16 * 104 * 1024;
constexpr int NWAVE = 9;

constexpr u32 L_W = 0;
constexpr u32 APITCH = 432;
constexpr u32 L_A = 106496;
constexpr u32 L_B = L_A + 48 * APITCH;
constexpr u32 L_XS = L_B + 48 * APITCH;
constexpr u32 XS_S = 1824;
constexpr u32 L_CNT = L_XS + 4 * XS_S;
constexpr u32 L_TOTAL = L_CNT + 64;
constexpr u32 L_V3 = 0, L_W2C = 8192, L_W2S = 8448, L_U = 12288, L_RAW = 20480;
enum { C_TAGV = 12, C_TAG = 13, C_WISS = 14, C_ABRDY = 0, C_ABSTG = 1, C_W = 3, C_V3 = 4, C_U = 5, C_XISS = 6, C_XRDY = 7, C_WD = 8, C_RED = 10 };

constexpr int perm1(int j) { return j < 24 ? ((j & ~7) | ((j + 2) & 7)) : (j == 24 ? 25 : (j == 25 ? 24 : j)); }
struct Slots { int a[192]; int b[192]; bool v0[192]; bool v1[192]; int n; };
struct SlotBuilder {
  Slots s{}; bool vis[27][27] = {}; int n = 0;
  constexpr bool seen(int a, int b) const { return vis[a < b ? a : b][a < b ? b : a]; }
  constexpr void put(int a, int b) {
    s.a[n] = a; s.b[n] = b;
    s.v0[n] = !seen(a, b); vis[a < b ? a : b][a < b ? b : a] = true;
    const int na = perm1(a), nb = perm1(b);
    s.v1[n] = !seen(na, nb); vis[na < nb ? na : nb][na < nb ? nb : na] = true;
    ++n;
  }
};
constexpr int sg2(int m) { return perm1(perm1(2 * m)) >> 1; }
constexpr Slots make_slots() {
  SlotBuilder t{};
  for (int A = 0; A < 13; ++A)
    for (int B = A + 1; B < 13; ++B) {
      int ca = A, cb = B;
      while (!t.seen(2 * ca, 2 * cb)) {
        t.put(2 * ca, 2 * cb); t.put(2 * ca, 2 * cb + 1); t.put(2 * ca + 1, 2 * cb); t.put(2 * ca + 1, 2 * cb + 1);
        ca = sg2(ca); cb = sg2(cb);
      }
    }
  for (int A = 0; A < 13; ++A) {
    int ca = A;
    while (!t.seen(2 * ca, 2 * ca + 1)) {
      t.put(2 * ca, 2 * ca); t.put(2 * ca, 2 * ca + 1); t.put(2 * ca + 1, 2 * ca + 1); t.put(2 * ca + 1, 26);
      ca = sg2(ca);
    }
  }
  for (int a = 0; a < 27; ++a) { int ca = a; while (!t.seen(ca, 26)) { t.put(ca, 26); ca = perm1(perm1(ca)); } }
  t.s.n = t.n;
  for (int n = t.n; n < 192; ++n) { t.s.a[n] = 26; t.s.b[n] = 26; t.s.v0[n] = false; t.s.v1[n] = false; }
  return t.s;
}
constexpr Slots SL = make_slots();
static_assert(SL.n <= 192, "too many slots");

constexpr int UP = 33, ZB = 30 * UP, ZA = 676;
struct SymTab { u32 w[192][4]; };
constexpr SymTab make_symtab() {
  SymTab t{};
  for (int d = 0; d < 192; ++d) {
    int c = d >> 2, s = c >> 1, hh = c & 1;
    for (int e = 0; e < 2; ++e) {
      int sg = 8 * s + 2 * (d & 3) + e;
      int a = hh ? perm1(SL.a[sg]) : SL.a[sg], b = hh ? perm1(SL.b[sg]) : SL.b[sg];
      bool v = hh ? SL.v1[sg] : SL.v0[sg];
      u32 b1 = ZB, b2 = ZB, a1 = ZA, a2 = ZA;
      if (v) {
        b1 = a * UP + b; b2 = a == b ? ZB : b * UP + a;
        if (a < 26 && b < 26) { a1 = a * 26 + b; a2 = a == b ? ZA : b * 26 + a; }
      }
      t.w[d][e] = b1 | (b2 << 16);
      t.w[d][2 + e] = a1 | (a2 << 16);
    }
  }
  return t;
}
__device__ const SymTab SYMTAB = make_symtab();

constexpr u32 OOB = 0x7FFFFF00u;
constexpr u32 SPIN_MAX = 1u << 18;

__device__ __forceinline__ float bflo(u32 v) { return __uint_as_float(v << 16); }
__device__ __forceinline__ float bfhi(u32 v) { return __uint_as_float(v & 0xffff0000u); }
__device__ __forceinline__ u32 packbf(float a, float b) {
  f32x2 f = {a, b};
  bf16x2 z = __builtin_convertvector(f, bf16x2);
  return __builtin_bit_cast(u32, z);
}
__device__ __forceinline__ float mulf(float a, float b) { float r; asm("v_mul_f32 %0, %1, %2" : "=v"(r) : "v"(a), "v"(b)); return r; }
template <int E> __device__ __forceinline__ f32x2 pkmul_bc(f32x2 a, f32x2 b) {
  f32x2 r; asm("v_pk_mul_f32 %0, %1, %2 op_sel:[%3,0] op_sel_hi:[%3,1]" : "=v"(r) : "v"(a), "v"(b), "i"(E)); return r;
}
__device__ __forceinline__ float dot2bf(u32 a, u32 b, float c) {
  return __builtin_amdgcn_fdot2_f32_bf16(__builtin_bit_cast(bf16x2, a), __builtin_bit_cast(bf16x2, b), c, false);
}
__device__ __forceinline__ rsrc_t mkrs(const void* p, u32 bytes) { return __builtin_amdgcn_make_buffer_rsrc((void*)p, 0, bytes, 0x00020000); }
__device__ __forceinline__ u64 mkflag(u32 tag) { return (u64)tag | ((u64)(tag * 0x9E3779B1u + 0x7F4A7C15u) << 32); }
__device__ __forceinline__ void drain_vm() { asm volatile("s_waitcnt vmcnt(0)" ::: "memory"); }
__device__ __forceinline__ void lds_inc(LAS u32* cnt) {
  u64 keep;
  asm volatile("s_mov_b64 %0, exec\n\ts_mov_b64 exec, 1\n\tds_add_u32 %1, %2\n\ts_mov_b64 exec, %0"
               : "=&s"(keep) : "v"((u32)(size_t)cnt), "v"(1u) : "memory");
}
__device__ __forceinline__ void team_arrive(LAS u32* cnt, int) {
  asm volatile("s_waitcnt lgkmcnt(0)" ::: "memory");
  lds_inc(cnt);
}
__device__ __forceinline__ float sum_rows(float v) {
  auto a = __builtin_amdgcn_permlane32_swap(__float_as_uint(v), __float_as_uint(v), false, false);
  v = __uint_as_float(a[0]) + __uint_as_float(a[1]);
  auto b = __builtin_amdgcn_permlane16_swap(__float_as_uint(v), __float_as_uint(v), false, false);
  return __uint_as_float(b[0]) + __uint_as_float(b[1]);
}
template <int CTRL> __device__ __forceinline__ float dpp_add(float v) {
  return v + __uint_as_float(__builtin_amdgcn_update_dpp(0u, __float_as_uint(v), CTRL, 0xf, 0xf, true));
}
__device__ __forceinline__ float sum_row16(float v) {
  v = dpp_add<0x128>(v);
  v = dpp_add<0x124>(v);
  v = dpp_add<0x122>(v);
  v = dpp_add<0x121>(v);
  return v;
}

#define STAMP(k) do {} while (0)

struct Args {
  const int* idx; const uint16_t* table; const uint16_t* wc; const uint16_t* wco; const uint16_t* w1;
  const uint16_t* b1; const uint16_t* w2; const uint16_t* bo; uint16_t* out; char* ws;
  u32 table_bytes; u32 pad_;
};
#define CAS __attribute__((address_space(4)))

template <int SLEEP> __device__ __forceinline__ void team_wait(LAS u32* cnt, u32 target) {
  u32 v, s, n = SPIN_MAX;
  asm volatile(
      "1:\n\tds_read_b32 %0, %3\n\ts_waitcnt lgkmcnt(0)\n\tv_readfirstlane_b32 %1, %0\n\ts_cmp_ge_u32 %1, %4\n\ts_cbranch_scc1 2f\n\t"
      "s_sleep %5\n\ts_sub_u32 %2, %2, 1\n\ts_cbranch_scc0 1b\n2:"
      : "=&v"(v), "=&s"(s), "+s"(n) : "v"((u32)(size_t)cnt), "s"(target), "i"(SLEEP) : "memory", "scc");
}
template <int SLEEP> __device__ __forceinline__ void poll_flags(const char* p, u64 want) {
  u64 v; u32 n = SPIN_MAX;
  asm volatile(
      "1:\n\tglobal_load_dwordx2 %0, %2, off sc1\n\ts_waitcnt vmcnt(0)\n\tv_cmp_ne_u64 vcc, %3, %0\n\ts_nop 4\n\ts_cbranch_vccz 2f\n\t"
      "s_sleep %4\n\ts_sub_u32 %1, %1, 1\n\ts_cbranch_scc0 1b\n2:"
      : "=&v"(v), "+s"(n) : "v"(p), "s"(want), "i"(SLEEP) : "memory", "scc", "vcc");
}
__device__ __forceinline__ void lds_barrier() {
  asm volatile("s_waitcnt lgkmcnt(0)" ::: "memory");
  __builtin_amdgcn_s_barrier();
  asm volatile("" ::: "memory");
}
__device__ __forceinline__ u32 load_epoch(const char* ws) {
  return __hip_atomic_load((const GAS u32*)(ws + WS_E), __ATOMIC_RELAXED, __HIP_MEMORY_SCOPE_AGENT);
}
__device__ __forceinline__ void store_flag(char* ws, u32 off, u64 flag) {
  __hip_atomic_store((GAS u64*)(ws + off), flag, __ATOMIC_RELAXED, __HIP_MEMORY_SCOPE_AGENT);
}
__device__ __forceinline__ u32 get_tag(LAS u32* cnt) {
  team_wait<0>(cnt + C_TAG, 1);
  return __builtin_amdgcn_readfirstlane(cnt[C_TAGV]);
}
__device__ __forceinline__ u32 opq(u32 v) { asm("" : "+v"(v)); return v; }

__global__ void __launch_bounds__(576, 3) xdfm_kernel(Args a) {
  const unsigned long long rt_start = __builtin_amdgcn_s_memrealtime();
  extern __shared__ __attribute__((aligned(16))) char lds_raw[];
  LAS char* lds = (LAS char*)lds_raw;
  const int tid = threadIdx.x, lane = tid & 63;
  const int w = __builtin_amdgcn_readfirstlane(tid >> 6);
  const int b = blockIdx.x, j = b >> 3;
  const int jg = j < 6 ? (j >= 3 ? 1 : 0) : (j >= 19 ? 1 : 0);
  const int g = (b & 7) * 2 + jg, r = j < 6 ? 13 + j - 3 * jg : j - 6 - 13 * jg;
  LAS u32* cnt = (LAS u32*)(lds + L_CNT);
  asm volatile("" :: "s"(a.idx), "s"(a.table), "s"(a.wc), "s"(a.wco), "s"(a.w1), "s"(a.b1), "s"(a.w2), "s"(a.bo), "s"(a.out), "s"(a.ws), "s"(a.table_bytes));
  const rsrc_t rws = mkrs(a.ws, WS_END);
  const rsrc_t ridx = mkrs(a.idx, NB * NF * 4), rtab = mkrs(a.table, a.table_bytes);

  if (w == 8) {
    __builtin_amdgcn_s_setprio(3);
    if (lane < 16) cnt[lane] = 0;
    u32 tag;
    if (r < 13) {
      u32 id[4]; u32x4 xg[8];
#pragma unroll
      for (int m = 0; m < 2; ++m) {
        const int p0 = 4 * r + 2 * m, t = p0 >= NF ? 1 : 0, f0 = p0 - t * NF;
        const u32x2 ia = __builtin_amdgcn_raw_buffer_load_b64(ridx, (lane & 31) * (NF * 4), ((g * 64 + t * 32) * NF + f0) * 4, 0);
        id[2 * m] = ia.x; id[2 * m + 1] = ia.y;
      }
      const u32 ep = load_epoch(a.ws);
#pragma unroll
      for (int k = 0; k < 8; ++k) xg[k] = __builtin_amdgcn_raw_buffer_load_b128(rtab, id[k >> 1] * 64 + (k & 1) * 32 + (lane >> 5) * 16, 0, 0);
      lds_barrier();
      STAMP(1);
      tag = __builtin_amdgcn_readfirstlane(ep) + 1u;
      cnt[C_TAGV] = tag;
      team_arrive(cnt + C_TAG, lane);
#pragma unroll
      for (int k = 0; k < 8; ++k) __builtin_amdgcn_raw_buffer_store_b128(xg[k], rws, WS_X + lane * 16, (g * 104 + r * 8 + k) * 1024, 16);
      STAMP(2);
      drain_vm();
    } else {
      const u32 ep = load_epoch(a.ws);
      lds_barrier();
      tag = __builtin_amdgcn_readfirstlane(ep) + 1u;
      cnt[C_TAGV] = tag;
      team_arrive(cnt + C_TAG, lane);
    }
    const u64 flag = mkflag(tag);
    if (lane == 0) store_flag(a.ws, WS_XF + (g * 16 + r) * FL, flag);
    STAMP(3);
    if (r < 13) {
      __builtin_amdgcn_s_sleep(P_XDELAY);
      poll_flags<2>(a.ws + WS_XF + (g * 16 + (lane < 13 ? lane : 0)) * FL, flag);
      lds_inc(cnt + C_XRDY);
    }
    STAMP(4);
    __builtin_amdgcn_s_sleep(P_BDELAY);
    poll_flags<6>(a.ws + (lane < 32 ? WS_FA : WS_FB) + ((lane & 31) < NF ? (lane & 31) : 0) * FL, flag);
    lds_inc(cnt + C_ABRDY);
    STAMP(5);
  } else if (w >= 4) {
    const int cw = w - 4, smp = g * 64 + r * 4 + cw;
    u32 id[2]; u32x4 xr[2];
    u32 bo;
    {
      const CAS u32* ip = (const CAS u32*)(a.idx + smp * NF);
      u32 is[NF];
#pragma unroll
      for (int f = 0; f < NF; ++f) is[f] = ip[f];
      const size_t bop = (size_t)a.bo;
      bo = *(const CAS u32*)(bop & ~(size_t)3) >> ((bop & 2) * 8);
      u32 iv = 0;
      asm("v_writelane_b32 %0, %1, 0\n\tv_writelane_b32 %0, %2, 1\n\tv_writelane_b32 %0, %3, 2\n\tv_writelane_b32 %0, %4, 3\n\tv_writelane_b32 %0, %5, 4\n\tv_writelane_b32 %0, %6, 5\n\tv_writelane_b32 %0, %7, 6\n\tv_writelane_b32 %0, %8, 7\n\tv_writelane_b32 %0, %9, 8\n\tv_writelane_b32 %0, %10, 9\n\tv_writelane_b32 %0, %11, 10\n\tv_writelane_b32 %0, %12, 11\n\tv_writelane_b32 %0, %13, 12\n\tv_writelane_b32 %0, %14, 13\n\tv_writelane_b32 %0, %15, 14\n\tv_writelane_b32 %0, %16, 15\n\tv_writelane_b32 %0, %17, 16\n\tv_writelane_b32 %0, %18, 17\n\tv_writelane_b32 %0, %19, 18\n\tv_writelane_b32 %0, %20, 19\n\tv_writelane_b32 %0, %21, 20\n\tv_writelane_b32 %0, %22, 21\n\tv_writelane_b32 %0, %23, 22\n\tv_writelane_b32 %0, %24, 23\n\tv_writelane_b32 %0, %25, 24\n\tv_writelane_b32 %0, %26, 25"
          : "+v"(iv) : "s"(is[0]), "s"(is[1]), "s"(is[2]), "s"(is[3]), "s"(is[4]), "s"(is[5]), "s"(is[6]), "s"(is[7]), "s"(is[8]), "s"(is[9]), "s"(is[10]), "s"(is[11]), "s"(is[12]), "s"(is[13]), "s"(is[14]), "s"(is[15]), "s"(is[16]), "s"(is[17]), "s"(is[18]), "s"(is[19]), "s"(is[20]), "s"(is[21]), "s"(is[22]), "s"(is[23]), "s"(is[24]), "s"(is[25]));
      id[0] = __builtin_amdgcn_ds_bpermute((lane >> 2) * 4, iv);
      id[1] = __builtin_amdgcn_ds_bpermute((16 + (lane >> 2)) * 4, iv);
    }
    if (r < 13) __builtin_amdgcn_s_setprio(2);
    lds_barrier();
#pragma unroll
    for (int m = 0; m < 2; ++m) { const int f = 16 * m + (lane >> 2); xr[m] = __builtin_amdgcn_raw_buffer_load_b128(rtab, f < NF ? id[m] * 64 + (lane & 3) * 16 : OOB, 0, 0); }
    __builtin_amdgcn_s_setprio(0);
    STAMP(1);
    f32x2 xp[14];
    {
      const u32 o = L_XS + cw * XS_S + (lane >> 2) * 64 + (lane & 3) * 16;
      *(LAS u32x4*)(lds + o) = xr[0];
      u32x4 x1 = xr[1];
      if ((lane >> 2) == NF - 16) x1 = (u32x4){0x3F803F80u, 0x3F803F80u, 0x3F803F80u, 0x3F803F80u};
      *(LAS u32x4*)(lds + opq((lane >> 2) < 12 ? o + 1024 : L_XS + cw * XS_S + 27 * 64 + (lane & 3) * 16)) = x1;
      asm volatile("s_waitcnt lgkmcnt(0)" ::: "memory");
      const int hh = lane >> 5, q = (lane >> 2) & 3, pp = lane & 3;
      const u32 base = L_XS + cw * XS_S + ((lane >> 4) & 1) * 32 + pp * 8;
      const u32 a0 = base + (hh ? q + 2 : q) * 64, a1 = base + (hh ? ((q + 6) & 7) : q + 4) * 64;
      const u32 a6 = base + (24 + ((hh && q < 2) ? (q ^ 1) : q)) * 64;
#pragma unroll
      for (int t = 0; t < 7; ++t) {
        s16x4 y = __builtin_amdgcn_ds_read_tr16_b64_v4i16((LAS s16x4*)(lds + (t < 6 ? ((t & 1) ? a1 : a0) + (t >> 1) * 512 : a6)));
        u32x2 u = __builtin_bit_cast(u32x2, y);
        xp[2 * t] = (f32x2){bflo(u.x), bfhi(u.x)}; xp[2 * t + 1] = (f32x2){bflo(u.y), bfhi(u.y)};
      }
    }
    bf16x8 z[24];
#pragma unroll
    for (int s = 0; s < 24; ++s) {
      u32x4 zz;
#pragma unroll
      for (int e = 0; e < 4; ++e) {
        const int s1 = 8 * s + 2 * e, s2 = s1 + 1;
        const int a1 = SL.a[s1], b1 = SL.b[s1], a2 = SL.a[s2], b2 = SL.b[s2];
        if (a1 == a2 && (b1 & 1) == 0 && b1 < 26 && b2 == b1 + 1) {
          const f32x2 pr = (a1 & 1) ? pkmul_bc<1>(xp[a1 >> 1], xp[b1 >> 1]) : pkmul_bc<0>(xp[a1 >> 1], xp[b1 >> 1]);
          zz[e] = packbf(pr[0], pr[1]);
        } else {
          const float p1 = b1 == 26 ? xp[a1 >> 1][a1 & 1] : mulf(xp[a1 >> 1][a1 & 1], xp[b1 >> 1][b1 & 1]);
          const float p2 = b2 == 26 ? xp[a2 >> 1][a2 & 1] : mulf(xp[a2 >> 1][a2 & 1], xp[b2 >> 1][b2 & 1]);
          zz[e] = packbf(p1, p2);
        }
      }
      z[s] = __builtin_bit_cast(bf16x8, zz);
      asm volatile("" : "+v"(z[s]));
    }
    const u32 tag = get_tag(cnt);
    const u64 flag = mkflag(tag);
    STAMP(2);
    team_wait<1>(cnt + C_ABRDY, 1);
    STAMP(3);
    if (r < 13) team_wait<1>(cnt + C_XISS, 4);
    __builtin_amdgcn_s_setprio(2);
    STAMP(4);
    {
      u32x4 y[10];
#pragma unroll
      for (int m = 0; m < 10; ++m) {
        const u32 n = ((m % 5) * 4 + cw) * 64 + lane;
        y[m] = __builtin_amdgcn_raw_buffer_load_b128(rws, n < NF * 48 ? n * 16 : OOB, m < 5 ? WS_PA : WS_PB, 16);
      }
#pragma unroll
      for (int m = 0; m < 5; ++m) {
        const u32 n = (m * 4 + cw) * 64 + lane, i = (n * 1366u) >> 16, c = n - i * 48;
        const u32 o = opq(n < NF * 48 ? L_A + c * APITCH + i * 16 : L_A + 26 * 16);
        *(LAS u32x4*)(lds + o) = y[m];
        *(LAS u32x4*)(lds + o + (L_B - L_A)) = y[5 + m];
      }
      team_arrive(cnt + C_ABSTG, lane);
      team_wait<0>(cnt + C_ABSTG, 4);
    }
    __builtin_amdgcn_s_setprio(0);
    STAMP(5);
    const u32 afr = (lane >> 5) * APITCH + (lane & 31) * 16;
    const u32 tag2 = (u32)(flag >> 32);
    const int sg = r * 4 + cw;
    const u32 po = opq(lane < 26 ? WS_P + ((g * 26 + lane) * 2 + (sg >> 5)) * 512 + (sg & 31) * 16 : OOB);
    u32x4 yp;
    f32x16 acc1 = {}, acc2 = {};
    {
      constexpr int AD = 2;
      bf16x8 af[24], bf[24];
#pragma unroll
      for (int s = 0; s < AD; ++s) { af[s] = *(LAS bf16x8*)(lds + L_A + afr + s * 2 * APITCH); bf[s] = *(LAS bf16x8*)(lds + L_B + afr + s * 2 * APITCH); }
      __builtin_amdgcn_sched_barrier(0);
#pragma unroll
      for (int s = 0; s < 24; ++s) {
        if (s + AD < 24) { af[s + AD] = *(LAS bf16x8*)(lds + L_A + afr + (s + AD) * 2 * APITCH); bf[s + AD] = *(LAS bf16x8*)(lds + L_B + afr + (s + AD) * 2 * APITCH); }
        if (s == P_PQ) yp = __builtin_amdgcn_raw_buffer_load_b128(rws, po, 0, 16);
        acc1 = __builtin_amdgcn_mfma_f32_32x32x16_bf16(af[s], z[s], acc1, 0, 0, 0);
        acc2 = __builtin_amdgcn_mfma_f32_32x32x16_bf16(bf[s], z[s], acc2, 0, 0, 0);
        __builtin_amdgcn_sched_barrier(0);
      }
    }
    float sum = 0.f;
#pragma unroll
    for (int e = 0; e < 12; ++e) sum += acc1[e] * acc2[e];
    { const float t = acc1[12] * acc2[12] + acc1[13] * acc2[13]; sum += lane < 32 ? t : 0.f; }
    sum = sum_rows(sum_row16(sum));
    STAMP(6);
    for (u32 it = 0; it < SPIN_MAX; ++it) {
      const bool ok = lane >= 26 || (yp.y == tag && yp.w == tag2);
      if (__all(ok)) break;
      asm volatile("" ::: "memory");
      __builtin_amdgcn_s_sleep(1);
      yp = __builtin_amdgcn_raw_buffer_load_b128(rws, po, 0, 16);
    }
    float part = sum_rows(sum_row16(__uint_as_float(yp.x)));
    if (lane == 0) {
      const __bf16 ob = (__bf16)(sum + part + bflo(bo));
      a.out[smp] = __builtin_bit_cast(uint16_t, ob);
    }
    STAMP(7);
  } else if (r < 13) {
      const rsrc_t rw1 = mkrs(a.w1, DIN * DIN * 2);
      const int mh = w & 1, kh = w >> 1, hh = lane >> 5;
      lds_barrier();
      STAMP(1);
      {
        u32x4 v[26];
        const u32 vo = (w * 208 + (lane >> 3)) * 1664 + r * 128 + (lane & 7) * 16;
        const int c = lane & 7;
        const u32 lo = L_W + (w * 208 + (lane >> 3)) * 128 + (((c >> 1) ^ (((lane >> 4) & 1) << 1)) * 32) + (c & 1) * 16;
#pragma unroll
        for (int i = 0; i < 26; ++i) v[i] = __builtin_amdgcn_raw_buffer_load_b128(rw1, vo, i * 8 * 1664, 0);
#pragma unroll
        for (int i = 0; i < 26; ++i) *(LAS u32x4*)(lds + lo + i * 1024) = v[i];
      }
      team_arrive(cnt + C_W, lane);
      const u32 tag = get_tag(cnt);
      const u64 flag = mkflag(tag);
      f32x16 acc[2];
      f32x2 wf[8];
      {
        const CAS u32* bp = (const CAS u32*)(a.b1 + r * 64 + kh * 32);
        const CAS u32* wp = (const CAS u32*)(a.w2 + r * 64 + kh * 32);
        u32 bs[16], ws_[16];
#pragma unroll
        for (int t = 0; t < 16; ++t) { bs[t] = bp[t]; ws_[t] = wp[t]; }
#pragma unroll
        for (int t = 0; t < 16; ++t) asm volatile("" : "+s"(bs[t]), "+s"(ws_[t]));
#pragma unroll
        for (int t = 0; t < 4; ++t) {
          const u32 b0 = hh ? bs[4 * t + 2] : bs[4 * t], b1 = hh ? bs[4 * t + 3] : bs[4 * t + 1];
          const u32 w0 = hh ? ws_[4 * t + 2] : ws_[4 * t], w1 = hh ? ws_[4 * t + 3] : ws_[4 * t + 1];
          acc[0][4 * t] = bflo(b0); acc[0][4 * t + 1] = bfhi(b0); acc[0][4 * t + 2] = bflo(b1); acc[0][4 * t + 3] = bfhi(b1);
          acc[1][4 * t] = 0.f; acc[1][4 * t + 1] = 0.f; acc[1][4 * t + 2] = 0.f; acc[1][4 * t + 3] = 0.f;
          wf[2 * t] = (f32x2){bflo(w0), bfhi(w0)}; wf[2 * t + 1] = (f32x2){bflo(w1), bfhi(w1)};
          asm volatile("" : "+v"(wf[2 * t]), "+v"(wf[2 * t + 1]));
        }
      }
      STAMP(2);
      team_wait<0>(cnt + C_XRDY, 1);
      STAMP(3);
      constexpr int XW = P_XW, FD = 3;
      u32x4 xv[26];
      auto ldx = [&](int i) { xv[i] = __builtin_amdgcn_raw_buffer_load_b128(rws, WS_X + lane * 16, (g * 104 + mh * 52 + kh * 26 + i) * 1024, 16); };
#pragma unroll
      for (int i = 0; i < XW; ++i) ldx(i);
      team_wait<0>(cnt + C_W, 4);
      __builtin_amdgcn_s_setprio(1);
      {
        const int q = (lane >> 2) & 3, pp = lane & 3, gam = (lane >> 4) & 1;
        const u32 rb = L_W + kh * 26 * 2048 + (8 * hh + q) * 128 + pp * 8;
        const u32 ad0 = rb + (((2 * kh + gam) ^ (q & 2)) * 32), ad1 = rb + (((2 * (kh ^ 1) + gam) ^ (q & 2)) * 32);
        bf16x8 fr[26][2];
        auto ldf = [&](int i) {
#pragma unroll
          for (int t = 0; t < 2; ++t) {
            const u32 o = (t ? ad1 : ad0) + i * 2048;
            const s16x4 y0 = __builtin_amdgcn_ds_read_tr16_b64_v4i16((LAS s16x4*)(lds + o));
            const s16x4 y1 = __builtin_amdgcn_ds_read_tr16_b64_v4i16((LAS s16x4*)(lds + o + 512));
            fr[i][t] = __builtin_bit_cast(bf16x8, __builtin_shufflevector(y0, y1, 0, 1, 2, 3, 4, 5, 6, 7));
          }
        };
#pragma unroll
        for (int i = 0; i < FD; ++i) ldf(i);
        __builtin_amdgcn_sched_barrier(0);
#pragma unroll
        for (int i = 0; i < 26; ++i) {
          if (i + XW < 26) ldx(i + XW);
          if (i == P_XI) lds_inc(cnt + C_XISS);
          if (i == 8) lds_inc(cnt + C_WD + kh);
          if (i == 22) team_wait<0>(cnt + C_WD + kh, 2);
          if (i + FD < 26) ldf(i + FD);
          const bf16x8 xb = __builtin_bit_cast(bf16x8, xv[i]);
          acc[0] = __builtin_amdgcn_mfma_f32_32x32x16_bf16(fr[i][0], xb, acc[0], 0, 0, 0);
          acc[1] = __builtin_amdgcn_mfma_f32_32x32x16_bf16(fr[i][1], xb, acc[1], 0, 0, 0);
          __builtin_amdgcn_sched_barrier(0);
        }
      }
      STAMP(4);
      {
        const u32 so = L_W + kh * 26 * 2048 + mh * 4096 + lane * 16;
#pragma unroll
        for (int t = 0; t < 4; ++t) *(LAS f32x4*)(lds + so + t * 1024) = (f32x4){acc[1][4 * t], acc[1][4 * t + 1], acc[1][4 * t + 2], acc[1][4 * t + 3]};
        team_arrive(cnt + C_RED + mh, lane);
        team_wait<0>(cnt + C_RED + mh, 2);
      }
      STAMP(5);
      float part;
      {
        constexpr float K1 = -2.0f * 1.4426950408889634f * 0.7978845608028654f, K2 = K1 * 0.044715f;
        const u32 si = L_W + (kh ^ 1) * 26 * 2048 + mh * 4096 + lane * 16;
        f32x4 o[4];
#pragma unroll
        for (int t = 0; t < 4; ++t) o[t] = *(LAS f32x4*)(lds + si + t * 1024);
        f32x2 h[8], u[8], y[8];
#pragma unroll
        for (int m = 0; m < 8; ++m) {
          h[m] = (f32x2){acc[0][2 * m], acc[0][2 * m + 1]} + (f32x2){o[m >> 1][2 * (m & 1)], o[m >> 1][2 * (m & 1) + 1]};
          u[m] = (h[m] * h[m] * K2 + K1) * h[m];
          y[m] = h[m] * wf[m];
        }
#pragma unroll
        for (int m = 0; m < 8; ++m) { u[m][0] = __builtin_amdgcn_exp2f(u[m][0]); u[m][1] = __builtin_amdgcn_exp2f(u[m][1]); }
#pragma unroll
        for (int m = 0; m < 8; ++m) { u[m] = u[m] + 1.0f; }
#pragma unroll
        for (int m = 0; m < 8; ++m) { u[m][0] = __builtin_amdgcn_rcpf(u[m][0]); u[m][1] = __builtin_amdgcn_rcpf(u[m][1]); }
        f32x2 p0 = y[0] * u[0], p1 = y[1] * u[1];
#pragma unroll
        for (int m = 2; m < 8; m += 2) { p0 += y[m] * u[m]; p1 += y[m + 1] * u[m + 1]; }
        p0 += p1;
        part = p0[0] + p0[1];
      }
      {
        auto sw = __builtin_amdgcn_permlane32_swap(__float_as_uint(part), __float_as_uint(part), false, false);
        part = __uint_as_float(sw[0]) + __uint_as_float(sw[1]);
      }
      {
        const u32x4 o = {__float_as_uint(part), tag, __float_as_uint(part), (u32)(flag >> 32)};
        __builtin_amdgcn_raw_buffer_store_b128(o, rws, lane < 32 ? WS_P + ((g * 26 + r * 2 + kh) * 2 + mh) * 512 + lane * 16 : OOB, 0, 16);
      }
      STAMP(6);
  } else {
      const int p = g * 3 + r - 13;
      const rsrc_t rwc = mkrs(a.wc, 3 * CUBE * 2), rwo = mkrs(a.wco, 78 * 2);
      __builtin_amdgcn_s_setprio(3);
      if (p < NF) {
        const int i = p;
        u32x4 tb[3];
        const int hb = (w >> 1) * 13, el = (w & 1) * 64 + lane;
        const CAS u32* w3p = (const CAS u32*)(a.wco + 52);
        u32 w3[13];
#pragma unroll
        for (int h = 0; h < 13; ++h) w3[h] = w3p[h];
        u32x4 v[13];
#pragma unroll
        for (int h = 0; h < 13; ++h) v[h] = __builtin_amdgcn_raw_buffer_load_b128(rwc, el < 85 ? (2 * CUBE + hb * 676 + 8 * el) * 2 : OOB, h * 676 * 2, 0);
        u32 v2[2];
#pragma unroll
        for (int m = 0; m < 2; ++m) {
          const u32 d = tid + 256 * m, h = (d * 5042u) >> 16, e = d - 13 * h;
          v2[m] = __builtin_amdgcn_raw_buffer_load_b32(rwc, d < 338 ? (CUBE + (h * NF + i) * NF) * 2 + e * 4 : OOB, 0, 0);
        }
        const u32 wq = __builtin_amdgcn_raw_buffer_load_b16(rwo, tid < NF ? (NF + tid) * 2 : OOB, 0, 0);
        const u32 w1i = __builtin_amdgcn_raw_buffer_load_b16(rwo, i * 2, 0, 0);
        if (w == 0) {
#pragma unroll
          for (int m = 0; m < 3; ++m) tb[m] = *(const GAS u32x4*)SYMTAB.w[lane + 64 * m];
        }
        __builtin_amdgcn_sched_barrier(0);
        lds_barrier();
        STAMP(1);
        {
          float acc[8];
#pragma unroll
          for (int e = 0; e < 8; ++e) acc[e] = 0.f;
#pragma unroll
          for (int h = 0; h < 13; ++h) {
            const u32 we = (h & 1) ? w3[h >> 1] >> 16 : w3[h >> 1] & 0xffffu;
            const u32 wo = (h & 1) ? w3[(13 + h) >> 1] & 0xffffu : w3[(13 + h) >> 1] >> 16;
            const u32 wl = hb ? wo : we, wh = wl << 16;
#pragma unroll
            for (int e = 0; e < 4; ++e) { acc[2 * e] = dot2bf(v[h][e], wl, acc[2 * e]); acc[2 * e + 1] = dot2bf(v[h][e], wh, acc[2 * e + 1]); }
          }
          const u32 vo = opq(el < 85 ? L_V3 + (w >> 1) * 2816 + el * 32 : L_V3 + 5632 + lane * 32);
          *(LAS f32x4*)(lds + vo) = (f32x4){acc[0], acc[1], acc[2], acc[3]};
          *(LAS f32x4*)(lds + vo + 16) = (f32x4){acc[4], acc[5], acc[6], acc[7]};
#pragma unroll
          for (int m = 0; m < 2; ++m) *(LAS u32*)(lds + L_W2S + (tid + 256 * m) * 4) = v2[m];
          if (w == 0) *(LAS u32*)(lds + L_W2C + lane * 4) = wq << 16;
        }
        const int tj = w >> 1, tp = w & 1, kq = lane >> 4;
        const int jr = 16 * tj + (lane & 15), jp = 16 * tp + (lane & 15);
        u32 oa[7], ob[7], oc[7];
#pragma unroll
        for (int s = 0; s < 7; ++s) {
          const int h = 4 * s + kq;
          const bool inv = h < NF && jp < NF;
          oa[s] = opq((h < NF && jr < NF) ? L_W2S + (h * NF + jr) * 2 : L_W2C + 63 * 4);
          ob[s] = opq(inv ? L_V3 + (h * NF + jp) * 4 : L_W2C + ((h < NF && jp == NF) ? h : 63) * 4);
          oc[s] = opq(inv ? L_V3 + 2816 + (h * NF + jp) * 4 : L_W2C + 63 * 4);
        }
        team_arrive(cnt + C_V3, lane);
        team_wait<0>(cnt + C_V3, 4);
        STAMP(2);
        u32 av[7]; float bv[7], bw[7];
#pragma unroll
        for (int s = 0; s < 7; ++s) { av[s] = *(LAS uint16_t*)(lds + oa[s]); bv[s] = *(LAS float*)(lds + ob[s]); bw[s] = *(LAS float*)(lds + oc[s]); }
#pragma unroll
        for (int s = 0; s < 7; ++s) bv[s] += bw[s];
        f32x4 u = {0.f, 0.f, 0.f, 0.f}, u2 = {0.f, 0.f, 0.f, 0.f};
#pragma unroll
        for (int s = 0; s < 7; ++s) {
          if (s & 1) u2 = __builtin_amdgcn_mfma_f32_16x16x4f32(bflo(av[s]), bv[s], u2, 0, 0, 0);
          else u = __builtin_amdgcn_mfma_f32_16x16x4f32(bflo(av[s]), bv[s], u, 0, 0, 0);
        }
        u += u2;
#pragma unroll
        for (int e = 0; e < 4; ++e) {
          const int jo = 16 * tj + 4 * kq + e;
          const float val = (jo == NF && jp == NF) ? bflo(w1i) : u[e];
          *(LAS float*)(lds + L_U + (jo * UP + jp) * 4) = val;
        }
        team_arrive(cnt + C_U, lane);
        if (w == 0) {
          team_wait<0>(cnt + C_U, 4);
          STAMP(3);
          LAS float* U = (LAS float*)(lds + L_U);
          float e[12];
#pragma unroll
          for (int m = 0; m < 3; ++m) {
            e[4 * m] = U[tb[m].x & 0xffff]; e[4 * m + 1] = U[tb[m].x >> 16]; e[4 * m + 2] = U[tb[m].y & 0xffff]; e[4 * m + 3] = U[tb[m].y >> 16];
          }
#pragma unroll
          for (int m = 0; m < 3; ++m)
            __builtin_amdgcn_raw_buffer_store_b32(packbf(e[4 * m] + e[4 * m + 1], e[4 * m + 2] + e[4 * m + 3]), rws, WS_PB + i * 768 + (lane + 64 * m) * 4, 0, 16);
          const u64 flagb = mkflag(get_tag(cnt));
          drain_vm();
          if (lane == 0) store_flag(a.ws, WS_FB + i * FL, flagb);
          STAMP(4);
        }
      } else {
        const int i = (p - NF) * 4 + w;
        const bool prod = p < 33 && i < NF;
        u32 v[6]; u32x4 tb[3];
#pragma unroll
        for (int m = 0; m < 6; ++m) { const int d = lane + 64 * m; v[m] = __builtin_amdgcn_raw_buffer_load_b32(rwc, (prod && d < 338) ? i * 1352 + d * 4 : OOB, 0, 0); }
#pragma unroll
        for (int m = 0; m < 3; ++m) tb[m] = *(const GAS u32x4*)SYMTAB.w[lane + 64 * m];
        __builtin_amdgcn_sched_barrier(0);
        lds_barrier();
        STAMP(1);
        if (prod) {
          LAS char* raw = lds + L_RAW + w * 2048;
#pragma unroll
          for (int m = 0; m < 6; ++m) *(LAS u32*)(raw + (lane + 64 * m) * 4) = v[m];
          asm volatile("s_waitcnt lgkmcnt(0)" ::: "memory");
          LAS uint16_t* R = (LAS uint16_t*)raw;
          u32 e[12];
#pragma unroll
          for (int m = 0; m < 3; ++m) {
            e[4 * m] = R[tb[m].z & 0xffff]; e[4 * m + 1] = R[tb[m].z >> 16]; e[4 * m + 2] = R[tb[m].w & 0xffff]; e[4 * m + 3] = R[tb[m].w >> 16];
          }
#pragma unroll
          for (int m = 0; m < 3; ++m)
            __builtin_amdgcn_raw_buffer_store_b32(packbf(bflo(e[4 * m]) + bflo(e[4 * m + 1]), bflo(e[4 * m + 2]) + bflo(e[4 * m + 3])), rws,
                                                  WS_PA + i * 768 + (lane + 64 * m) * 4, 0, 16);
          const u64 flag0 = mkflag(get_tag(cnt));
          drain_vm();
          if (lane == 0) store_flag(a.ws, WS_FA + i * FL, flag0);
          STAMP(4);
        }
        if (p == 47 && w == 0) {
          const u32 tag0 = get_tag(cnt);
          const u64 flag0 = mkflag(tag0);
          __builtin_amdgcn_s_sleep(32);
#pragma unroll 1
          for (int m = 0; m < 4; ++m) poll_flags<16>(a.ws + WS_XF + (lane + 64 * m) * FL, flag0);
          if (lane == 0) __hip_atomic_store((GAS u32*)(a.ws + WS_E), tag0, __ATOMIC_RELAXED, __HIP_MEMORY_SCOPE_AGENT);
        }
      }
  }
  if (b == 250 && w == 8) { while (__builtin_amdgcn_s_memrealtime() - rt_start < 4000) __builtin_amdgcn_s_sleep(8); }
}

}

extern "C" void kernel_launch(void* const* d_in, const int* in_sizes, int n_in, void* d_out, int out_size,
                              void* d_ws, size_t ws_size, hipStream_t stream) {
  using namespace xdfm;
  Args a;
  a.idx = (const int*)d_in[0]; a.table = (const uint16_t*)d_in[1]; a.wc = (const uint16_t*)d_in[2];
  a.wco = (const uint16_t*)d_in[3]; a.w1 = (const uint16_t*)d_in[4]; a.b1 = (const uint16_t*)d_in[5];
  a.w2 = (const uint16_t*)d_in[6]; a.bo = (const uint16_t*)d_in[7]; a.out = (uint16_t*)d_out; a.ws = (char*)d_ws;
  a.table_bytes = (u32)in_sizes[1] * 2u; a.pad_ = 0;
  (void)hipFuncSetAttribute((const void*)xdfm_kernel, hipFuncAttributeMaxDynamicSharedMemorySize, L_TOTAL);
  xdfm_kernel<<<256, NWAVE * 64, L_TOTAL, stream>>>(a);
}
```

```cpp
#include <hip/hip_runtime.h>
#include <hip/hip_bf16.h>
#include <stdint.h>

#define P_ADELAY 8
#define P_BDELAY 8
#define P_WW 10
#define P_PQ 16
#define P_XI 10
#define P_XDELAY 12
#define P_CG 4
#define P_XW 8
#define P_W0 12
#define P_X0 4
namespace xdfm {

typedef unsigned int u32;
typedef unsigned long long u64;
typedef u32 u32x2 __attribute__((ext_vector_type(2)));
typedef u32 u32x4 __attribute__((ext_vector_type(4)));
typedef short s16x4 __attribute__((ext_vector_type(4)));
typedef short s16x8 __attribute__((ext_vector_type(8)));
typedef float f32x2 __attribute__((ext_vector_type(2)));
typedef float f32x4 __attribute__((ext_vector_type(4)));
typedef float f32x16 __attribute__((ext_vector_type(16)));
typedef __bf16 bf16x2 __attribute__((ext_vector_type(2)));
typedef __bf16 bf16x8 __attribute__((ext_vector_type(8)));
#define GAS __attribute__((address_space(1)))
#define LAS __attribute__((address_space(3)))
typedef __amdgpu_buffer_rsrc_t rsrc_t;

constexpr int NF = 26, DIN = 832, NB = 1024, CUBE = 17576;

constexpr u32 WS_E = 0;
constexpr u32 FL = 128;
constexpr u32 WS_HI = 4096;
constexpr u32 WS_FA = 40960;
constexpr u32 WS_FB = 45056;
constexpr u32 WS_PA = 49152;
constexpr u32 WS_PB = 69632;
constexpr u32 WS_P = 131072;
constexpr u32 WS_END = WS_P + 16 * 13 * 4 * 256;
constexpr int NWAVE = 8;

constexpr u32 L_W = 0;
constexpr u32 APITCH = 432;
constexpr u32 L_A = 106496;
constexpr u32 L_B = L_A + 48 * APITCH;
constexpr u32 L_XS = L_B + 48 * APITCH;
constexpr u32 XS_S = 1824;
constexpr u32 L_ID = L_XS + 4 * XS_S;
constexpr u32 L_CNT = L_ID + 4 * 2048;
constexpr u32 L_TOTAL = L_CNT + 64;
constexpr u32 L_V3 = 0, L_W2C = 8192, L_W2S = 8448, L_U = 12288, L_RAW = 20480;
enum { C_TAGV = 12, C_TAG = 13, C_ABRDY = 0, C_ABSTG = 1, C_W = 3, C_V3 = 4, C_U = 5, C_XISS = 6 };

constexpr int perm1(int j) { return j < 24 ? ((j & ~7) | ((j + 2) & 7)) : (j == 24 ? 25 : (j == 25 ? 24 : j)); }
struct Slots { int a[192]; int b[192]; bool v0[192]; bool v1[192]; int n; };
struct SlotBuilder {
  Slots s{}; bool vis[27][27] = {}; int n = 0;
  constexpr bool seen(int a, int b) const { return vis[a < b ? a : b][a < b ? b : a]; }
  constexpr void put(int a, int b) {
    s.a[n] = a; s.b[n] = b;
    s.v0[n] = !seen(a, b); vis[a < b ? a : b][a < b ? b : a] = true;
    const int na = perm1(a), nb = perm1(b);
    s.v1[n] = !seen(na, nb); vis[na < nb ? na : nb][na < nb ? nb : na] = true;
    ++n;
  }
};
constexpr int sg2(int m) { return perm1(perm1(2 * m)) >> 1; }
constexpr Slots make_slots() {
  SlotBuilder t{};
  for (int A = 0; A < 13; ++A)
    for (int B = A + 1; B < 13; ++B) {
      int ca = A, cb = B;
      while (!t.seen(2 * ca, 2 * cb)) {
        t.put(2 * ca, 2 * cb); t.put(2 * ca, 2 * cb + 1); t.put(2 * ca + 1, 2 * cb); t.put(2 * ca + 1, 2 * cb + 1);
        ca = sg2(ca); cb = sg2(cb);
      }
    }
  for (int A = 0; A < 13; ++A) {
    int ca = A;
    while (!t.seen(2 * ca, 2 * ca + 1)) {
      t.put(2 * ca, 2 * ca); t.put(2 * ca, 2 * ca + 1); t.put(2 * ca + 1, 2 * ca + 1); t.put(2 * ca + 1, 26);
      ca = sg2(ca);
    }
  }
  for (int a = 0; a < 27; ++a) { int ca = a; while (!t.seen(ca, 26)) { t.put(ca, 26); ca = perm1(perm1(ca)); } }
  t.s.n = t.n;
  for (int n = t.n; n < 192; ++n) { t.s.a[n] = 26; t.s.b[n] = 26; t.s.v0[n] = false; t.s.v1[n] = false; }
  return t.s;
}
constexpr Slots SL = make_slots();
static_assert(SL.n <= 192, "too many slots");

constexpr int UP = 33, ZB = 30 * UP, ZA = 676;
struct SymTab { u32 w[192][4]; };
constexpr SymTab make_symtab() {
  SymTab t{};
  for (int d = 0; d < 192; ++d) {
    int c = d >> 2, s = c >> 1, hh = c & 1;
    for (int e = 0; e < 2; ++e) {
      int sg = 8 * s + 2 * (d & 3) + e;
      int a = hh ? perm1(SL.a[sg]) : SL.a[sg], b = hh ? perm1(SL.b[sg]) : SL.b[sg];
      bool v = hh ? SL.v1[sg] : SL.v0[sg];
      u32 b1 = ZB, b2 = ZB, a1 = ZA, a2 = ZA;
      if (v) {
        b1 = a * UP + b; b2 = a == b ? ZB : b * UP + a;
        if (a < 26 && b < 26) { a1 = a * 26 + b; a2 = a == b ? ZA : b * 26 + a; }
      }
      t.w[d][e] = b1 | (b2 << 16);
      t.w[d][2 + e] = a1 | (a2 << 16);
    }
  }
  return t;
}
__device__ const SymTab SYMTAB = make_symtab();

constexpr u32 OOB = 0x7FFFFF00u;
constexpr u32 SPIN_MAX = 1u << 18;

__device__ __forceinline__ float bflo(u32 v) { return __uint_as_float(v << 16); }
__device__ __forceinline__ float bfhi(u32 v) { return __uint_as_float(v & 0xffff0000u); }
__device__ __forceinline__ u32 packbf(float a, float b) {
  f32x2 f = {a, b};
  bf16x2 z = __builtin_convertvector(f, bf16x2);
  return __builtin_bit_cast(u32, z);
}
__device__ __forceinline__ float mulf(float a, float b) { float r; asm("v_mul_f32 %0, %1, %2" : "=v"(r) : "v"(a), "v"(b)); return r; }
template <int E> __device__ __forceinline__ f32x2 pkmul_bc(f32x2 a, f32x2 b) {
  f32x2 r; asm("v_pk_mul_f32 %0, %1, %2 op_sel:[%3,0] op_sel_hi:[%3,1]" : "=v"(r) : "v"(a), "v"(b), "i"(E)); return r;
}
__device__ __forceinline__ float dot2bf(u32 a, u32 b, float c) {
  return __builtin_amdgcn_fdot2_f32_bf16(__builtin_bit_cast(bf16x2, a), __builtin_bit_cast(bf16x2, b), c, false);
}
__device__ __forceinline__ rsrc_t mkrs(const void* p, u32 bytes) { return __builtin_amdgcn_make_buffer_rsrc((void*)p, 0, bytes, 0x00020000); }
__device__ __forceinline__ u64 mkflag(u32 tag) { return (u64)tag | ((u64)(tag * 0x9E3779B1u + 0x7F4A7C15u) << 32); }
__device__ __forceinline__ void drain_vm() { asm volatile("s_waitcnt vmcnt(0)" ::: "memory"); }
__device__ __forceinline__ void lds_inc(LAS u32* cnt) {
  u64 keep;
  asm volatile("s_mov_b64 %0, exec\n\ts_mov_b64 exec, 1\n\tds_add_u32 %1, %2\n\ts_mov_b64 exec, %0"
               : "=&s"(keep) : "v"((u32)(size_t)cnt), "v"(1u) : "memory");
}
__device__ __forceinline__ void team_arrive(LAS u32* cnt, int) {
  asm volatile("s_waitcnt lgkmcnt(0)" ::: "memory");
  lds_inc(cnt);
}
__device__ __forceinline__ float sum_rows(float v) {
  auto a = __builtin_amdgcn_permlane32_swap(__float_as_uint(v), __float_as_uint(v), false, false);
  v = __uint_as_float(a[0]) + __uint_as_float(a[1]);
  auto b = __builtin_amdgcn_permlane16_swap(__float_as_uint(v), __float_as_uint(v), false, false);
  return __uint_as_float(b[0]) + __uint_as_float(b[1]);
}
template <int CTRL> __device__ __forceinline__ float dpp_add(float v) {
  return v + __uint_as_float(__builtin_amdgcn_update_dpp(0u, __float_as_uint(v), CTRL, 0xf, 0xf, true));
}
__device__ __forceinline__ float sum_row16(float v) {
  v = dpp_add<0x128>(v);
  v = dpp_add<0x124>(v);
  v = dpp_add<0x122>(v);
  v = dpp_add<0x121>(v);
  return v;
}

#define STAMP(k) do {} while (0)

struct Args {
  const int* idx; const uint16_t* table; const uint16_t* wc; const uint16_t* wco; const uint16_t* w1;
  const uint16_t* b1; const uint16_t* w2; const uint16_t* bo; uint16_t* out; char* ws;
  u32 table_bytes; u32 pad_;
};
#define CAS __attribute__((address_space(4)))

template <int SLEEP> __device__ __forceinline__ void team_wait(LAS u32* cnt, u32 target) {
  u32 v, s, n = SPIN_MAX;
  asm volatile(
      "1:\n\tds_read_b32 %0, %3\n\ts_waitcnt lgkmcnt(0)\n\tv_readfirstlane_b32 %1, %0\n\ts_cmp_ge_u32 %1, %4\n\ts_cbranch_scc1 2f\n\t"
      "s_sleep %5\n\ts_sub_u32 %2, %2, 1\n\ts_cbranch_scc0 1b\n2:"
      : "=&v"(v), "=&s"(s), "+s"(n) : "v"((u32)(size_t)cnt), "s"(target), "i"(SLEEP) : "memory", "scc");
}
template <int SLEEP> __device__ __forceinline__ void poll_flags(const char* p, u64 want) {
  u64 v; u32 n = SPIN_MAX;
  asm volatile(
      "1:\n\tglobal_load_dwordx2 %0, %2, off sc1\n\ts_waitcnt vmcnt(0)\n\tv_cmp_ne_u64 vcc, %3, %0\n\ts_nop 4\n\ts_cbranch_vccz 2f\n\t"
      "s_sleep %4\n\ts_sub_u32 %1, %1, 1\n\ts_cbranch_scc0 1b\n2:"
      : "=&v"(v), "+s"(n) : "v"(p), "s"(want), "i"(SLEEP) : "memory", "scc", "vcc");
}
__device__ __forceinline__ void lds_barrier() {
  asm volatile("s_waitcnt lgkmcnt(0)" ::: "memory");
  __builtin_amdgcn_s_barrier();
  asm volatile("" ::: "memory");
}
__device__ __forceinline__ u32 load_epoch(const char* ws) {
  return __hip_atomic_load((const GAS u32*)(ws + WS_E), __ATOMIC_RELAXED, __HIP_MEMORY_SCOPE_AGENT);
}
__device__ __forceinline__ void store_flag(char* ws, u32 off, u64 flag) {
  __hip_atomic_store((GAS u64*)(ws + off), flag, __ATOMIC_RELAXED, __HIP_MEMORY_SCOPE_AGENT);
}
__device__ __forceinline__ u32 get_tag(LAS u32* cnt) {
  team_wait<0>(cnt + C_TAG, 1);
  return __builtin_amdgcn_readfirstlane(cnt[C_TAGV]);
}
__device__ __forceinline__ u32 opq(u32 v) { asm("" : "+v"(v)); return v; }

__global__ void __launch_bounds__(512, 2) xdfm_kernel(Args a) {
  extern __shared__ __attribute__((aligned(16))) char lds_raw[];
  LAS char* lds = (LAS char*)lds_raw;
  const int tid = threadIdx.x, lane = tid & 63;
  LAS u32* cnt = (LAS u32*)(lds + L_CNT);
  if (tid < 16) cnt[tid] = 0;
  const int w = __builtin_amdgcn_readfirstlane(tid >> 6);
  const int b = blockIdx.x, j = b >> 3;
  const int jg = j < 6 ? (j >= 3 ? 1 : 0) : (j >= 19 ? 1 : 0);
  const int g = (b & 7) * 2 + jg, r = j < 6 ? 13 + j - 3 * jg : j - 6 - 13 * jg;
  asm volatile("" :: "s"(a.idx), "s"(a.table), "s"(a.wc), "s"(a.wco), "s"(a.w1), "s"(a.b1), "s"(a.w2), "s"(a.bo), "s"(a.out), "s"(a.ws), "s"(a.table_bytes));
  lds_barrier();
  const rsrc_t rws = mkrs(a.ws, WS_END);
  const rsrc_t ridx = mkrs(a.idx, NB * NF * 4), rtab = mkrs(a.table, a.table_bytes);

  if (w >= 4) {
    const int cw = w - 4, smp = g * 64 + r * 4 + cw;
    u32 id[2]; u32x4 xr[2];
#pragma unroll
    for (int m = 0; m < 2; ++m) { const int f = 16 * m + (lane >> 2); id[m] = __builtin_amdgcn_raw_buffer_load_b32(ridx, f < NF ? f * 4 : OOB, smp * NF * 4, 0); }
    u32 ep = 0;
    if (cw == 0) ep = load_epoch(a.ws);
    const size_t bop = (size_t)a.bo;
    const u32 bo = *(const CAS u32*)(bop & ~(size_t)3) >> ((bop & 2) * 8);
#pragma unroll
    for (int m = 0; m < 2; ++m) { const int f = 16 * m + (lane >> 2); xr[m] = __builtin_amdgcn_raw_buffer_load_b128(rtab, f < NF ? id[m] * 64 + (lane & 3) * 16 : OOB, 0, 0); }
    if (cw == 0) {
      const u32 t0 = __builtin_amdgcn_readfirstlane(ep) + 1u;
      cnt[C_TAGV] = t0;
      team_arrive(cnt + C_TAG, lane);
      if (lane == 0) store_flag(a.ws, WS_HI + (g * 16 + r) * FL, mkflag(t0));
    }
    STAMP(1);
    f32x2 xp[14];
    {
      const u32 o = L_XS + cw * XS_S + (lane >> 2) * 64 + (lane & 3) * 16;
      *(LAS u32x4*)(lds + o) = xr[0];
      u32x4 x1 = xr[1];
      if ((lane >> 2) == NF - 16) x1 = (u32x4){0x3F803F80u, 0x3F803F80u, 0x3F803F80u, 0x3F803F80u};
      *(LAS u32x4*)(lds + opq((lane >> 2) < 12 ? o + 1024 : L_XS + cw * XS_S + 27 * 64 + (lane & 3) * 16)) = x1;
      asm volatile("s_waitcnt lgkmcnt(0)" ::: "memory");
      const int hh = lane >> 5, q = (lane >> 2) & 3, pp = lane & 3;
      const u32 base = L_XS + cw * XS_S + ((lane >> 4) & 1) * 32 + pp * 8;
      const u32 a0 = base + (hh ? q + 2 : q) * 64, a1 = base + (hh ? ((q + 6) & 7) : q + 4) * 64;
      const u32 a6 = base + (24 + ((hh && q < 2) ? (q ^ 1) : q)) * 64;
#pragma unroll
      for (int t = 0; t < 7; ++t) {
        s16x4 y = __builtin_amdgcn_ds_read_tr16_b64_v4i16((LAS s16x4*)(lds + (t < 6 ? ((t & 1) ? a1 : a0) + (t >> 1) * 512 : a6)));
        u32x2 u = __builtin_bit_cast(u32x2, y);
        xp[2 * t] = (f32x2){bflo(u.x), bfhi(u.x)}; xp[2 * t + 1] = (f32x2){bflo(u.y), bfhi(u.y)};
      }
    }
    bf16x8 z[24];
#pragma unroll
    for (int s = 0; s < 24; ++s) {
      u32x4 zz;
#pragma unroll
      for (int e = 0; e < 4; ++e) {
        const int s1 = 8 * s + 2 * e, s2 = s1 + 1;
        const int a1 = SL.a[s1], b1 = SL.b[s1], a2 = SL.a[s2], b2 = SL.b[s2];
        if (a1 == a2 && (b1 & 1) == 0 && b1 < 26 && b2 == b1 + 1) {
          const f32x2 pr = (a1 & 1) ? pkmul_bc<1>(xp[a1 >> 1], xp[b1 >> 1]) : pkmul_bc<0>(xp[a1 >> 1], xp[b1 >> 1]);
          zz[e] = packbf(pr[0], pr[1]);
        } else {
          const float p1 = b1 == 26 ? xp[a1 >> 1][a1 & 1] : mulf(xp[a1 >> 1][a1 & 1], xp[b1 >> 1][b1 & 1]);
          const float p2 = b2 == 26 ? xp[a2 >> 1][a2 & 1] : mulf(xp[a2 >> 1][a2 & 1], xp[b2 >> 1][b2 & 1]);
          zz[e] = packbf(p1, p2);
        }
      }
      z[s] = __builtin_bit_cast(bf16x8, zz);
      asm volatile("" : "+v"(z[s]));
    }
    const u32 tag = get_tag(cnt);
    const u64 flag = mkflag(tag);
    STAMP(2);
    if (cw == 0) {
      __builtin_amdgcn_s_sleep(P_BDELAY);
      poll_flags<6>(a.ws + (lane < 32 ? WS_FA : WS_FB) + ((lane & 31) < NF ? (lane & 31) : 0) * FL, flag);
      lds_inc(cnt + C_ABRDY);
    } else team_wait<1>(cnt + C_ABRDY, 1);
    STAMP(3);
    if (r < 13) team_wait<1>(cnt + C_XISS, 4);
    __builtin_amdgcn_s_setprio(2);
    STAMP(4);
    {
      u32x4 y[10];
#pragma unroll
      for (int m = 0; m < 10; ++m) {
        const u32 n = ((m % 5) * 4 + cw) * 64 + lane;
        y[m] = __builtin_amdgcn_raw_buffer_load_b128(rws, n < NF * 48 ? n * 16 : OOB, m < 5 ? WS_PA : WS_PB, 16);
      }
#pragma unroll
      for (int m = 0; m < 5; ++m) {
        const u32 n = (m * 4 + cw) * 64 + lane, i = (n * 1366u) >> 16, c = n - i * 48;
        const u32 o = opq(n < NF * 48 ? L_A + c * APITCH + i * 16 : L_A + 26 * 16);
        *(LAS u32x4*)(lds + o) = y[m];
        *(LAS u32x4*)(lds + o + (L_B - L_A)) = y[5 + m];
      }
      team_arrive(cnt + C_ABSTG, lane);
      team_wait<0>(cnt + C_ABSTG, 4);
    }
    __builtin_amdgcn_s_setprio(0);
    STAMP(5);
    const u32 afr = (lane >> 5) * APITCH + (lane & 31) * 16;
    const u32 tag2 = (u32)(flag >> 32);
    const int sg = r * 4 + cw;
    const u32 po = opq(lane < 13 ? WS_P + ((g * 13 + lane) * 4 + (sg >> 4)) * 256 + (sg & 15) * 16 : OOB);
    u32x4 yp;
    f32x16 acc1 = {}, acc2 = {};
    {
      constexpr int AD = 2;
      bf16x8 af[24], bf[24];
#pragma unroll
      for (int s = 0; s < AD; ++s) { af[s] = *(LAS bf16x8*)(lds + L_A + afr + s * 2 * APITCH); bf[s] = *(LAS bf16x8*)(lds + L_B + afr + s * 2 * APITCH); }
      __builtin_amdgcn_sched_barrier(0);
#pragma unroll
      for (int s = 0; s < 24; ++s) {
        if (s + AD < 24) { af[s + AD] = *(LAS bf16x8*)(lds + L_A + afr + (s + AD) * 2 * APITCH); bf[s + AD] = *(LAS bf16x8*)(lds + L_B + afr + (s + AD) * 2 * APITCH); }
        if (s == P_PQ) yp = __builtin_amdgcn_raw_buffer_load_b128(rws, po, 0, 16);
        acc1 = __builtin_amdgcn_mfma_f32_32x32x16_bf16(af[s], z[s], acc1, 0, 0, 0);
        acc2 = __builtin_amdgcn_mfma_f32_32x32x16_bf16(bf[s], z[s], acc2, 0, 0, 0);
        __builtin_amdgcn_sched_barrier(0);
      }
    }
    float sum = 0.f;
#pragma unroll
    for (int e = 0; e < 12; ++e) sum += acc1[e] * acc2[e];
    { const float t = acc1[12] * acc2[12] + acc1[13] * acc2[13]; sum += lane < 32 ? t : 0.f; }
    sum = sum_rows(sum_row16(sum));
    STAMP(6);
    for (u32 it = 0; it < SPIN_MAX; ++it) {
      const bool ok = lane >= 13 || (yp.y == tag && yp.w == tag2);
      if (__all(ok)) break;
      asm volatile("" ::: "memory");
      __builtin_amdgcn_s_sleep(1);
      yp = __builtin_amdgcn_raw_buffer_load_b128(rws, po, 0, 16);
    }
    float part = sum_row16(__uint_as_float(yp.x));
    if (lane == 0) {
      const __bf16 ob = (__bf16)(sum + part + bflo(bo));
      a.out[smp] = __builtin_bit_cast(uint16_t, ob);
    }
    STAMP(7);
  } else if (r < 13) {
    const rsrc_t rw1 = mkrs(a.w1, DIN * DIN * 2);
    const int gq = lane >> 4;
    const u32 ib = (g * 64 + w * 16) * NF * 4;
    const u32x4 i0 = __builtin_amdgcn_raw_buffer_load_b128(ridx, lane * 16, ib, 0);
    const u32x4 i1 = __builtin_amdgcn_raw_buffer_load_b128(ridx, lane < 40 ? 1024 + lane * 16 : OOB, ib, 0);
    u32x4 v[26];
    const u32 vo = (w * 208 + (lane >> 3)) * 1664 + r * 128 + (lane & 7) * 16;
#pragma unroll
    for (int i = 0; i < P_W0; ++i) v[i] = __builtin_amdgcn_raw_buffer_load_b128(rw1, vo, i * 8 * 1664, 0);
    __builtin_amdgcn_sched_barrier(0);
    u32 id[26];
    {
      LAS char* ids = lds + L_ID + w * 2048;
      *(LAS u32x4*)(ids + lane * 16) = i0;
      *(LAS u32x4*)(ids + 1024 + lane * 16) = i1;
      asm volatile("s_waitcnt lgkmcnt(0)" ::: "memory");
#pragma unroll
      for (int m = 0; m < 13; ++m) { const u32x2 t = *(LAS u32x2*)(ids + (lane & 15) * (NF * 4) + m * 8); id[2 * m] = t.x; id[2 * m + 1] = t.y; }
    }
    STAMP(1);
    constexpr int XW = P_XW, FD = 7;
    u32x4 xv[26];
    auto ldx = [&](int f) { xv[f] = __builtin_amdgcn_raw_buffer_load_b128(rtab, id[f] * 64 + gq * 16, 0, 0); };
#pragma unroll
    for (int f = 0; f < P_X0; ++f) ldx(f);
    __builtin_amdgcn_sched_barrier(0);
#pragma unroll
    for (int i = P_W0; i < 26; ++i) v[i] = __builtin_amdgcn_raw_buffer_load_b128(rw1, vo, i * 8 * 1664, 0);
#pragma unroll
    for (int f = P_X0; f < XW; ++f) ldx(f);
    __builtin_amdgcn_sched_barrier(0);
    {
      const int c = lane & 7, sb = (lane >> 4) & 1;
      const u32 lo = L_W + (w * 208 + (lane >> 3)) * 128 + (c & 1) * 16;
      const u32 l0 = lo + (((c >> 1) ^ sb) * 32), l1 = lo + (((c >> 1) ^ (sb | 2)) * 32);
#pragma unroll
      for (int i = 0; i < 26; ++i) *(LAS u32x4*)(lds + ((i & 1) ? l1 : l0) + i * 1024) = v[i];
    }
    team_arrive(cnt + C_W, lane);
    f32x4 acc[4]; f32x2 wf[8];
    {
      const CAS u32* bp = (const CAS u32*)(a.b1 + r * 64);
      const CAS u32* wp = (const CAS u32*)(a.w2 + r * 64);
#pragma unroll
      for (int t = 0; t < 4; ++t) {
        u32 bs[8], ws_[8];
#pragma unroll
        for (int e = 0; e < 8; ++e) { bs[e] = bp[8 * t + e]; ws_[e] = wp[8 * t + e]; }
#pragma unroll
        for (int e = 0; e < 8; ++e) asm volatile("" : "+s"(bs[e]), "+s"(ws_[e]));
        const bool g1 = gq & 1, g2 = gq & 2;
        const u32 b0 = g2 ? (g1 ? bs[6] : bs[4]) : (g1 ? bs[2] : bs[0]), b1 = g2 ? (g1 ? bs[7] : bs[5]) : (g1 ? bs[3] : bs[1]);
        const u32 w0 = g2 ? (g1 ? ws_[6] : ws_[4]) : (g1 ? ws_[2] : ws_[0]), w1 = g2 ? (g1 ? ws_[7] : ws_[5]) : (g1 ? ws_[3] : ws_[1]);
        acc[t] = (f32x4){bflo(b0), bfhi(b0), bflo(b1), bfhi(b1)};
        wf[2 * t] = (f32x2){bflo(w0), bfhi(w0)}; wf[2 * t + 1] = (f32x2){bflo(w1), bfhi(w1)};
        asm volatile("" : "+v"(wf[2 * t]), "+v"(wf[2 * t + 1]));
      }
    }
    STAMP(2);
    team_wait<0>(cnt + C_W, 4);
    __builtin_amdgcn_s_setprio(1);
    STAMP(3);
    {
      const int q = (lane >> 2) & 3, p = lane & 3;
      const int fsw = ((q >> 1) & 1) | ((gq & 1) << 1);
      const u32 rb = L_W + (8 * gq + q) * 128 + p * 8;
      u32 ad[4], ae[4];
#pragma unroll
      for (int t = 0; t < 4; ++t) { ad[t] = rb + ((t ^ fsw) * 32); ae[t] = ad[t] + 13 * 4096; }
      bf16x8 fr[104];
      auto ldf = [&](int n) {
        const int f = n >> 2, t = n & 3;
        const u32 o = f < 13 ? ad[t] + f * 4096 : ae[t] + (f - 13) * 4096;
        const s16x4 y0 = __builtin_amdgcn_ds_read_tr16_b64_v4i16((LAS s16x4*)(lds + o));
        const s16x4 y1 = __builtin_amdgcn_ds_read_tr16_b64_v4i16((LAS s16x4*)(lds + o + 512));
        fr[n] = __builtin_bit_cast(bf16x8, __builtin_shufflevector(y0, y1, 0, 1, 2, 3, 4, 5, 6, 7));
      };
#pragma unroll
      for (int n = 0; n < FD; ++n) ldf(n);
      __builtin_amdgcn_sched_barrier(0);
#pragma unroll
      for (int n = 0; n < 104; ++n) {
        const int f = n >> 2, t = n & 3;
        if (t == 0 && f + XW < 26) ldx(f + XW);
        if (n == 4 * (26 - XW)) lds_inc(cnt + C_XISS);
        if (n + FD < 104) ldf(n + FD);
        acc[t] = __builtin_amdgcn_mfma_f32_16x16x32_bf16(fr[n], __builtin_bit_cast(bf16x8, xv[f]), acc[t], 0, 0, 0);
        __builtin_amdgcn_sched_barrier(0);
      }
    }
    STAMP(4);
    float part;
    {
      constexpr float K1 = -2.0f * 1.4426950408889634f * 0.7978845608028654f, K2 = K1 * 0.044715f;
      f32x2 h[8], u[8], y[8];
#pragma unroll
      for (int m = 0; m < 8; ++m) {
        h[m] = (f32x2){acc[m >> 1][2 * (m & 1)], acc[m >> 1][2 * (m & 1) + 1]};
        u[m] = (h[m] * h[m] * K2 + K1) * h[m];
        y[m] = h[m] * wf[m];
      }
#pragma unroll
      for (int m = 0; m < 8; ++m) { u[m][0] = __builtin_amdgcn_exp2f(u[m][0]); u[m][1] = __builtin_amdgcn_exp2f(u[m][1]); }
#pragma unroll
      for (int m = 0; m < 8; ++m) { u[m] = u[m] + 1.0f; }
#pragma unroll
      for (int m = 0; m < 8; ++m) { u[m][0] = __builtin_amdgcn_rcpf(u[m][0]); u[m][1] = __builtin_amdgcn_rcpf(u[m][1]); }
      f32x2 p0 = y[0] * u[0], p1 = y[1] * u[1];
#pragma unroll
      for (int m = 2; m < 8; m += 2) { p0 += y[m] * u[m]; p1 += y[m + 1] * u[m + 1]; }
      p0 += p1;
      part = sum_rows(p0[0] + p0[1]);
    }
    {
      const u32 tag = get_tag(cnt);
      const u32x4 o = {__float_as_uint(part), tag, __float_as_uint(part), (u32)(mkflag(tag) >> 32)};
      __builtin_amdgcn_raw_buffer_store_b128(o, rws, lane < 16 ? WS_P + ((g * 13 + r) * 4 + w) * 256 + lane * 16 : OOB, 0, 16);
    }
    STAMP(5);
  } else {
      const int p = g * 3 + r - 13;
      const rsrc_t rwc = mkrs(a.wc, 3 * CUBE * 2), rwo = mkrs(a.wco, 78 * 2);
      __builtin_amdgcn_s_setprio(3);
      if (p < NF) {
        const int i = p;
        u32x4 tb[3];
        const int hb = (w >> 1) * 13, el = (w & 1) * 64 + lane;
        const CAS u32* w3p = (const CAS u32*)(a.wco + 52);
        u32 w3[13];
#pragma unroll
        for (int h = 0; h < 13; ++h) w3[h] = w3p[h];
        u32x4 v[13];
#pragma unroll
        for (int h = 0; h < 13; ++h) v[h] = __builtin_amdgcn_raw_buffer_load_b128(rwc, el < 85 ? (2 * CUBE + hb * 676 + 8 * el) * 2 : OOB, h * 676 * 2, 0);
        u32 v2[2];
#pragma unroll
        for (int m = 0; m < 2; ++m) {
          const u32 d = tid + 256 * m, h = (d * 5042u) >> 16, e = d - 13 * h;
          v2[m] = __builtin_amdgcn_raw_buffer_load_b32(rwc, d < 338 ? (CUBE + (h * NF + i) * NF) * 2 + e * 4 : OOB, 0, 0);
        }
        const u32 wq = __builtin_amdgcn_raw_buffer_load_b16(rwo, tid < NF ? (NF + tid) * 2 : OOB, 0, 0);
        const u32 w1i = __builtin_amdgcn_raw_buffer_load_b16(rwo, i * 2, 0, 0);
        if (w == 0) {
#pragma unroll
          for (int m = 0; m < 3; ++m) tb[m] = *(const GAS u32x4*)SYMTAB.w[lane + 64 * m];
        }
        __builtin_amdgcn_sched_barrier(0);
        STAMP(1);
        {
          float acc[8];
#pragma unroll
          for (int e = 0; e < 8; ++e) acc[e] = 0.f;
#pragma unroll
          for (int h = 0; h < 13; ++h) {
            const u32 we = (h & 1) ? w3[h >> 1] >> 16 : w3[h >> 1] & 0xffffu;
            const u32 wo = (h & 1) ? w3[(13 + h) >> 1] & 0xffffu : w3[(13 + h) >> 1] >> 16;
            const u32 wl = hb ? wo : we, wh = wl << 16;
#pragma unroll
            for (int e = 0; e < 4; ++e) { acc[2 * e] = dot2bf(v[h][e], wl, acc[2 * e]); acc[2 * e + 1] = dot2bf(v[h][e], wh, acc[2 * e + 1]); }
          }
          const u32 vo = opq(el < 85 ? L_V3 + (w >> 1) * 2816 + el * 32 : L_V3 + 5632 + lane * 32);
          *(LAS f32x4*)(lds + vo) = (f32x4){acc[0], acc[1], acc[2], acc[3]};
          *(LAS f32x4*)(lds + vo + 16) = (f32x4){acc[4], acc[5], acc[6], acc[7]};
#pragma unroll
          for (int m = 0; m < 2; ++m) *(LAS u32*)(lds + L_W2S + (tid + 256 * m) * 4) = v2[m];
          if (w == 0) *(LAS u32*)(lds + L_W2C + lane * 4) = wq << 16;
        }
        const int tj = w >> 1, tp = w & 1, kq = lane >> 4;
        const int jr = 16 * tj + (lane & 15), jp = 16 * tp + (lane & 15);
        u32 oa[7], ob[7], oc[7];
#pragma unroll
        for (int s = 0; s < 7; ++s) {
          const int h = 4 * s + kq;
          const bool inv = h < NF && jp < NF;
          oa[s] = opq((h < NF && jr < NF) ? L_W2S + (h * NF + jr) * 2 : L_W2C + 63 * 4);
          ob[s] = opq(inv ? L_V3 + (h * NF + jp) * 4 : L_W2C + ((h < NF && jp == NF) ? h : 63) * 4);
          oc[s] = opq(inv ? L_V3 + 2816 + (h * NF + jp) * 4 : L_W2C + 63 * 4);
        }
        team_arrive(cnt + C_V3, lane);
        team_wait<0>(cnt + C_V3, 4);
        STAMP(2);
        u32 av[7]; float bv[7], bw[7];
#pragma unroll
        for (int s = 0; s < 7; ++s) { av[s] = *(LAS uint16_t*)(lds + oa[s]); bv[s] = *(LAS float*)(lds + ob[s]); bw[s] = *(LAS float*)(lds + oc[s]); }
#pragma unroll
        for (int s = 0; s < 7; ++s) bv[s] += bw[s];
        f32x4 u = {0.f, 0.f, 0.f, 0.f}, u2 = {0.f, 0.f, 0.f, 0.f};
#pragma unroll
        for (int s = 0; s < 7; ++s) {
          if (s & 1) u2 = __builtin_amdgcn_mfma_f32_16x16x4f32(bflo(av[s]), bv[s], u2, 0, 0, 0);
          else u = __builtin_amdgcn_mfma_f32_16x16x4f32(bflo(av[s]), bv[s], u, 0, 0, 0);
        }
        u += u2;
#pragma unroll
        for (int e = 0; e < 4; ++e) {
          const int jo = 16 * tj + 4 * kq + e;
          const float val = (jo == NF && jp == NF) ? bflo(w1i) : u[e];
          *(LAS float*)(lds + L_U + (jo * UP + jp) * 4) = val;
        }
        team_arrive(cnt + C_U, lane);
        if (w == 0) {
          team_wait<0>(cnt + C_U, 4);
          STAMP(3);
          LAS float* U = (LAS float*)(lds + L_U);
          float e[12];
#pragma unroll
          for (int m = 0; m < 3; ++m) {
            e[4 * m] = U[tb[m].x & 0xffff]; e[4 * m + 1] = U[tb[m].x >> 16]; e[4 * m + 2] = U[tb[m].y & 0xffff]; e[4 * m + 3] = U[tb[m].y >> 16];
          }
#pragma unroll
          for (int m = 0; m < 3; ++m)
            __builtin_amdgcn_raw_buffer_store_b32(packbf(e[4 * m] + e[4 * m + 1], e[4 * m + 2] + e[4 * m + 3]), rws, WS_PB + i * 768 + (lane + 64 * m) * 4, 0, 16);
          const u64 flagb = mkflag(get_tag(cnt));
          drain_vm();
          if (lane == 0) store_flag(a.ws, WS_FB + i * FL, flagb);
          STAMP(4);
        }
      } else {
        const int i = (p - NF) * 4 + w;
        const bool prod = p < 33 && i < NF;
        u32 v[6]; u32x4 tb[3];
#pragma unroll
        for (int m = 0; m < 6; ++m) { const int d = lane + 64 * m; v[m] = __builtin_amdgcn_raw_buffer_load_b32(rwc, (prod && d < 338) ? i * 1352 + d * 4 : OOB, 0, 0); }
#pragma unroll
        for (int m = 0; m < 3; ++m) tb[m] = *(const GAS u32x4*)SYMTAB.w[lane + 64 * m];
        __builtin_amdgcn_sched_barrier(0);
        STAMP(1);
        if (prod) {
          LAS char* raw = lds + L_RAW + w * 2048;
#pragma unroll
          for (int m = 0; m < 6; ++m) *(LAS u32*)(raw + (lane + 64 * m) * 4) = v[m];
          asm volatile("s_waitcnt lgkmcnt(0)" ::: "memory");
          LAS uint16_t* R = (LAS uint16_t*)raw;
          u32 e[12];
#pragma unroll
          for (int m = 0; m < 3; ++m) {
            e[4 * m] = R[tb[m].z & 0xffff]; e[4 * m + 1] = R[tb[m].z >> 16]; e[4 * m + 2] = R[tb[m].w & 0xffff]; e[4 * m + 3] = R[tb[m].w >> 16];
          }
#pragma unroll
          for (int m = 0; m < 3; ++m)
            __builtin_amdgcn_raw_buffer_store_b32(packbf(bflo(e[4 * m]) + bflo(e[4 * m + 1]), bflo(e[4 * m + 2]) + bflo(e[4 * m + 3])), rws,
                                                  WS_PA + i * 768 + (lane + 64 * m) * 4, 0, 16);
          const u64 flag0 = mkflag(get_tag(cnt));
          drain_vm();
          if (lane == 0) store_flag(a.ws, WS_FA + i * FL, flag0);
          STAMP(4);
        }
        if (p == 47 && w == 0) {
          const u32 tag0 = get_tag(cnt);
          const u64 flag0 = mkflag(tag0);
          __builtin_amdgcn_s_sleep(32);
#pragma unroll 1
          for (int m = 0; m < 4; ++m) poll_flags<16>(a.ws + WS_HI + (lane + 64 * m) * FL, flag0);
          if (lane == 0) __hip_atomic_store((GAS u32*)(a.ws + WS_E), tag0, __ATOMIC_RELAXED, __HIP_MEMORY_SCOPE_AGENT);
        }
      }
  }
}

}

extern "C" void kernel_launch(void* const* d_in, const int* in_sizes, int n_in, void* d_out, int out_size,
                              void* d_ws, size_t ws_size, hipStream_t stream) {
  using namespace xdfm;
  Args a;
  a.idx = (const int*)d_in[0]; a.table = (const uint16_t*)d_in[1]; a.wc = (const uint16_t*)d_in[2];
  a.wco = (const uint16_t*)d_in[3]; a.w1 = (const uint16_t*)d_in[4]; a.b1 = (const uint16_t*)d_in[5];
  a.w2 = (const uint16_t*)d_in[6]; a.bo = (const uint16_t*)d_in[7]; a.out = (uint16_t*)d_out; a.ws = (char*)d_ws;
  a.table_bytes = (u32)in_sizes[1] * 2u; a.pad_ = 0;
  (void)hipFuncSetAttribute((const void*)xdfm_kernel, hipFuncAttributeMaxDynamicSharedMemorySize, L_TOTAL);
  xdfm_kernel<<<256, NWAVE * 64, L_TOTAL, stream>>>(a);
}
```

```cpp
#include <hip/hip_runtime.h>
#include <hip/hip_bf16.h>
#include <stdint.h>

#define P_ADELAY 8
#define P_BDELAY 8
#define P_WW 10
#define P_PQ 16
#define P_XI 10
#define P_XDELAY 12
#define P_CG 4
#define P_XW 8
#define P_W0 12
#define P_X0 4
namespace xdfm {

typedef unsigned int u32;
typedef unsigned long long u64;
typedef u32 u32x2 __attribute__((ext_vector_type(2)));
typedef u32 u32x4 __attribute__((ext_vector_type(4)));
typedef short s16x4 __attribute__((ext_vector_type(4)));
typedef short s16x8 __attribute__((ext_vector_type(8)));
typedef float f32x2 __attribute__((ext_vector_type(2)));
typedef float f32x4 __attribute__((ext_vector_type(4)));
typedef float f32x16 __attribute__((ext_vector_type(16)));
typedef __bf16 bf16x2 __attribute__((ext_vector_type(2)));
typedef __bf16 bf16x8 __attribute__((ext_vector_type(8)));
#define GAS __attribute__((address_space(1)))
#define LAS __attribute__((address_space(3)))
typedef __amdgpu_buffer_rsrc_t rsrc_t;

constexpr int NF = 26, DIN = 832, NB = 1024, CUBE = 17576;

constexpr u32 WS_E = 0;
constexpr u32 FL = 128;
constexpr u32 WS_HI = 4096;
constexpr u32 WS_FA = 40960;
constexpr u32 WS_FB = 45056;
constexpr u32 WS_PA = 49152;
constexpr u32 WS_PB = 69632;
constexpr u32 WS_P = 131072;
constexpr u32 WS_END = WS_P + 16 * 13 * 4 * 256;
constexpr int NWAVE = 9;

constexpr u32 L_W = 0;
constexpr u32 APITCH = 432;
constexpr u32 L_A = 106496;
constexpr u32 L_B = L_A + 48 * APITCH;
constexpr u32 L_XS = L_B + 48 * APITCH;
constexpr u32 XS_S = 1824;
constexpr u32 L_ID = L_XS + 4 * XS_S;
constexpr u32 L_CNT = L_ID + 4 * 2048;
constexpr u32 L_TOTAL = L_CNT + 64;
constexpr u32 L_V3 = 0, L_W2C = 8192, L_W2S = 8448, L_U = 12288, L_RAW = 20480;
enum { C_TAGV = 12, C_TAG = 13, C_ABRDY = 0, C_ABSTG = 1, C_W = 3, C_V3 = 4, C_U = 5, C_XISS = 6 };

constexpr int perm1(int j) { return j < 24 ? ((j & ~7) | ((j + 2) & 7)) : (j == 24 ? 25 : (j == 25 ? 24 : j)); }
struct Slots { int a[192]; int b[192]; bool v0[192]; bool v1[192]; int n; };
struct SlotBuilder {
  Slots s{}; bool vis[27][27] = {}; int n = 0;
  constexpr bool seen(int a, int b) const { return vis[a < b ? a : b][a < b ? b : a]; }
  constexpr void put(int a, int b) {
    s.a[n] = a; s.b[n] = b;
    s.v0[n] = !seen(a, b); vis[a < b ? a : b][a < b ? b : a] = true;
    const int na = perm1(a), nb = perm1(b);
    s.v1[n] = !seen(na, nb); vis[na < nb ? na : nb][na < nb ? nb : na] = true;
    ++n;
  }
};
constexpr int sg2(int m) { return perm1(perm1(2 * m)) >> 1; }
constexpr Slots make_slots() {
  SlotBuilder t{};
  for (int A = 0; A < 13; ++A)
    for (int B = A + 1; B < 13; ++B) {
      int ca = A, cb = B;
      while (!t.seen(2 * ca, 2 * cb)) {
        t.put(2 * ca, 2 * cb); t.put(2 * ca, 2 * cb + 1); t.put(2 * ca + 1, 2 * cb); t.put(2 * ca + 1, 2 * cb + 1);
        ca = sg2(ca); cb = sg2(cb);
      }
    }
  for (int A = 0; A < 13; ++A) {
    int ca = A;
    while (!t.seen(2 * ca, 2 * ca + 1)) {
      t.put(2 * ca, 2 * ca); t.put(2 * ca, 2 * ca + 1); t.put(2 * ca + 1, 2 * ca + 1); t.put(2 * ca + 1, 26);
      ca = sg2(ca);
    }
  }
  for (int a = 0; a < 27; ++a) { int ca = a; while (!t.seen(ca, 26)) { t.put(ca, 26); ca = perm1(perm1(ca)); } }
  t.s.n = t.n;
  for (int n = t.n; n < 192; ++n) { t.s.a[n] = 26; t.s.b[n] = 26; t.s.v0[n] = false; t.s.v1[n] = false; }
  return t.s;
}
constexpr Slots SL = make_slots();
static_assert(SL.n <= 192, "too many slots");

constexpr int UP = 33, ZB = 30 * UP, ZA = 676;
struct SymTab { u32 w[192][4]; };
constexpr SymTab make_symtab() {
  SymTab t{};
  for (int d = 0; d < 192; ++d) {
    int c = d >> 2, s = c >> 1, hh = c & 1;
    for (int e = 0; e < 2; ++e) {
      int sg = 8 * s + 2 * (d & 3) + e;
      int a = hh ? perm1(SL.a[sg]) : SL.a[sg], b = hh ? perm1(SL.b[sg]) : SL.b[sg];
      bool v = hh ? SL.v1[sg] : SL.v0[sg];
      u32 b1 = ZB, b2 = ZB, a1 = ZA, a2 = ZA;
      if (v) {
        b1 = a * UP + b; b2 = a == b ? ZB : b * UP + a;
        if (a < 26 && b < 26) { a1 = a * 26 + b; a2 = a == b ? ZA : b * 26 + a; }
      }
      t.w[d][e] = b1 | (b2 << 16);
      t.w[d][2 + e] = a1 | (a2 << 16);
    }
  }
  return t;
}
__device__ const SymTab SYMTAB = make_symtab();

constexpr u32 OOB = 0x7FFFFF00u;
constexpr u32 SPIN_MAX = 1u << 18;

__device__ __forceinline__ float bflo(u32 v) { return __uint_as_float(v << 16); }
__device__ __forceinline__ float bfhi(u32 v) { return __uint_as_float(v & 0xffff0000u); }
__device__ __forceinline__ u32 packbf(float a, float b) {
  f32x2 f = {a, b};
  bf16x2 z = __builtin_convertvector(f, bf16x2);
  return __builtin_bit_cast(u32, z);
}
__device__ __forceinline__ float mulf(float a, float b) { float r; asm("v_mul_f32 %0, %1, %2" : "=v"(r) : "v"(a), "v"(b)); return r; }
template <int E> __device__ __forceinline__ f32x2 pkmul_bc(f32x2 a, f32x2 b) {
  f32x2 r; asm("v_pk_mul_f32 %0, %1, %2 op_sel:[%3,0] op_sel_hi:[%3,1]" : "=v"(r) : "v"(a), "v"(b), "i"(E)); return r;
}
__device__ __forceinline__ float dot2bf(u32 a, u32 b, float c) {
  return __builtin_amdgcn_fdot2_f32_bf16(__builtin_bit_cast(bf16x2, a), __builtin_bit_cast(bf16x2, b), c, false);
}
__device__ __forceinline__ rsrc_t mkrs(const void* p, u32 bytes) { return __builtin_amdgcn_make_buffer_rsrc((void*)p, 0, bytes, 0x00020000); }
__device__ __forceinline__ u64 mkflag(u32 tag) { return (u64)tag | ((u64)(tag * 0x9E3779B1u + 0x7F4A7C15u) << 32); }
__device__ __forceinline__ void drain_vm() { asm volatile("s_waitcnt vmcnt(0)" ::: "memory"); }
__device__ __forceinline__ void lds_inc(LAS u32* cnt) {
  u64 keep;
  asm volatile("s_mov_b64 %0, exec\n\ts_mov_b64 exec, 1\n\tds_add_u32 %1, %2\n\ts_mov_b64 exec, %0"
               : "=&s"(keep) : "v"((u32)(size_t)cnt), "v"(1u) : "memory");
}
__device__ __forceinline__ void team_arrive(LAS u32* cnt, int) {
  asm volatile("s_waitcnt lgkmcnt(0)" ::: "memory");
  lds_inc(cnt);
}
__device__ __forceinline__ float sum_rows(float v) {
  auto a = __builtin_amdgcn_permlane32_swap(__float_as_uint(v), __float_as_uint(v), false, false);
  v = __uint_as_float(a[0]) + __uint_as_float(a[1]);
  auto b = __builtin_amdgcn_permlane16_swap(__float_as_uint(v), __float_as_uint(v), false, false);
  return __uint_as_float(b[0]) + __uint_as_float(b[1]);
}
template <int CTRL> __device__ __forceinline__ float dpp_add(float v) {
  return v + __uint_as_float(__builtin_amdgcn_update_dpp(0u, __float_as_uint(v), CTRL, 0xf, 0xf, true));
}
__device__ __forceinline__ float sum_row16(float v) {
  v = dpp_add<0x128>(v);
  v = dpp_add<0x124>(v);
  v = dpp_add<0x122>(v);
  v = dpp_add<0x121>(v);
  return v;
}

#define STAMP(k) do {} while (0)

struct Args {
  const int* idx; const uint16_t* table; const uint16_t* wc; const uint16_t* wco; const uint16_t* w1;
  const uint16_t* b1; const uint16_t* w2; const uint16_t* bo; uint16_t* out; char* ws;
  u32 table_bytes; u32 pad_;
};
#define CAS __attribute__((address_space(4)))

template <int SLEEP> __device__ __forceinline__ void team_wait(LAS u32* cnt, u32 target) {
  u32 v, s, n = SPIN_MAX;
  asm volatile(
      "1:\n\tds_read_b32 %0, %3\n\ts_waitcnt lgkmcnt(0)\n\tv_readfirstlane_b32 %1, %0\n\ts_cmp_ge_u32 %1, %4\n\ts_cbranch_scc1 2f\n\t"
      "s_sleep %5\n\ts_sub_u32 %2, %2, 1\n\ts_cbranch_scc0 1b\n2:"
      : "=&v"(v), "=&s"(s), "+s"(n) : "v"((u32)(size_t)cnt), "s"(target), "i"(SLEEP) : "memory", "scc");
}
template <int SLEEP> __device__ __forceinline__ void poll_flags(const char* p, u64 want) {
  u64 v; u32 n = SPIN_MAX;
  asm volatile(
      "1:\n\tglobal_load_dwordx2 %0, %2, off sc1\n\ts_waitcnt vmcnt(0)\n\tv_cmp_ne_u64 vcc, %3, %0\n\ts_nop 4\n\ts_cbranch_vccz 2f\n\t"
      "s_sleep %4\n\ts_sub_u32 %1, %1, 1\n\ts_cbranch_scc0 1b\n2:"
      : "=&v"(v), "+s"(n) : "v"(p), "s"(want), "i"(SLEEP) : "memory", "scc", "vcc");
}
__device__ __forceinline__ void lds_barrier() {
  asm volatile("s_waitcnt lgkmcnt(0)" ::: "memory");
  __builtin_amdgcn_s_barrier();
  asm volatile("" ::: "memory");
}
__device__ __forceinline__ u32 load_epoch(const char* ws) {
  return __hip_atomic_load((const GAS u32*)(ws + WS_E), __ATOMIC_RELAXED, __HIP_MEMORY_SCOPE_AGENT);
}
__device__ __forceinline__ void store_flag(char* ws, u32 off, u64 flag) {
  __hip_atomic_store((GAS u64*)(ws + off), flag, __ATOMIC_RELAXED, __HIP_MEMORY_SCOPE_AGENT);
}
__device__ __forceinline__ u32 get_tag(LAS u32* cnt) {
  team_wait<0>(cnt + C_TAG, 1);
  return __builtin_amdgcn_readfirstlane(cnt[C_TAGV]);
}
__device__ __forceinline__ u32 opq(u32 v) { asm("" : "+v"(v)); return v; }

__global__ void __launch_bounds__(576, 3) xdfm_kernel(Args a) {
  extern __shared__ __attribute__((aligned(16))) char lds_raw[];
  LAS char* lds = (LAS char*)lds_raw;
  const int tid = threadIdx.x, lane = tid & 63;
  LAS u32* cnt = (LAS u32*)(lds + L_CNT);
  if (tid < 16) cnt[tid] = 0;
  const int w = __builtin_amdgcn_readfirstlane(tid >> 6);
  const int b = blockIdx.x, j = b >> 3;
  const int jg = j < 6 ? (j >= 3 ? 1 : 0) : (j >= 19 ? 1 : 0);
  const int g = (b & 7) * 2 + jg, r = j < 6 ? 13 + j - 3 * jg : j - 6 - 13 * jg;
  asm volatile("" :: "s"(a.idx), "s"(a.table), "s"(a.wc), "s"(a.wco), "s"(a.w1), "s"(a.b1), "s"(a.w2), "s"(a.bo), "s"(a.out), "s"(a.ws), "s"(a.table_bytes));
  lds_barrier();
  const rsrc_t rws = mkrs(a.ws, WS_END);
  const rsrc_t ridx = mkrs(a.idx, NB * NF * 4), rtab = mkrs(a.table, a.table_bytes);

  if (w == 8) {
    if (b == 250) {

#pragma unroll 1
      for (int i = 0; i < 6; ++i) __builtin_amdgcn_s_sleep(127);
    }
    return;
  }
  if (w >= 4) {
    const int cw = w - 4, smp = g * 64 + r * 4 + cw;
    u32 id[2]; u32x4 xr[2];
#pragma unroll
    for (int m = 0; m < 2; ++m) { const int f = 16 * m + (lane >> 2); id[m] = __builtin_amdgcn_raw_buffer_load_b32(ridx, f < NF ? f * 4 : OOB, smp * NF * 4, 0); }
    u32 ep = 0;
    if (cw == 0) ep = load_epoch(a.ws);
    const size_t bop = (size_t)a.bo;
    const u32 bo = *(const CAS u32*)(bop & ~(size_t)3) >> ((bop & 2) * 8);
#pragma unroll
    for (int m = 0; m < 2; ++m) { const int f = 16 * m + (lane >> 2); xr[m] = __builtin_amdgcn_raw_buffer_load_b128(rtab, f < NF ? id[m] * 64 + (lane & 3) * 16 : OOB, 0, 0); }
    if (cw == 0) {
      const u32 t0 = __builtin_amdgcn_readfirstlane(ep) + 1u;
      cnt[C_TAGV] = t0;
      team_arrive(cnt + C_TAG, lane);
      if (lane == 0) store_flag(a.ws, WS_HI + (g * 16 + r) * FL, mkflag(t0));
    }
    STAMP(1);
    f32x2 xp[14];
    {
      const u32 o = L_XS + cw * XS_S + (lane >> 2) * 64 + (lane & 3) * 16;
      *(LAS u32x4*)(lds + o) = xr[0];
      u32x4 x1 = xr[1];
      if ((lane >> 2) == NF - 16) x1 = (u32x4){0x3F803F80u, 0x3F803F80u, 0x3F803F80u, 0x3F803F80u};
      *(LAS u32x4*)(lds + opq((lane >> 2) < 12 ? o + 1024 : L_XS + cw * XS_S + 27 * 64 + (lane & 3) * 16)) = x1;
      asm volatile("s_waitcnt lgkmcnt(0)" ::: "memory");
      const int hh = lane >> 5, q = (lane >> 2) & 3, pp = lane & 3;
      const u32 base = L_XS + cw * XS_S + ((lane >> 4) & 1) * 32 + pp * 8;
      const u32 a0 = base + (hh ? q + 2 : q) * 64, a1 = base + (hh ? ((q + 6) & 7) : q + 4) * 64;
      const u32 a6 = base + (24 + ((hh && q < 2) ? (q ^ 1) : q)) * 64;
#pragma unroll
      for (int t = 0; t < 7; ++t) {
        s16x4 y = __builtin_amdgcn_ds_read_tr16_b64_v4i16((LAS s16x4*)(lds + (t < 6 ? ((t & 1) ? a1 : a0) + (t >> 1) * 512 : a6)));
        u32x2 u = __builtin_bit_cast(u32x2, y);
        xp[2 * t] = (f32x2){bflo(u.x), bfhi(u.x)}; xp[2 * t + 1] = (f32x2){bflo(u.y), bfhi(u.y)};
      }
    }
    bf16x8 z[24];
#pragma unroll
    for (int s = 0; s < 24; ++s) {
      u32x4 zz;
#pragma unroll
      for (int e = 0; e < 4; ++e) {
        const int s1 = 8 * s + 2 * e, s2 = s1 + 1;
        const int a1 = SL.a[s1], b1 = SL.b[s1], a2 = SL.a[s2], b2 = SL.b[s2];
        if (a1 == a2 && (b1 & 1) == 0 && b1 < 26 && b2 == b1 + 1) {
          const f32x2 pr = (a1 & 1) ? pkmul_bc<1>(xp[a1 >> 1], xp[b1 >> 1]) : pkmul_bc<0>(xp[a1 >> 1], xp[b1 >> 1]);
          zz[e] = packbf(pr[0], pr[1]);
        } else {
          const float p1 = b1 == 26 ? xp[a1 >> 1][a1 & 1] : mulf(xp[a1 >> 1][a1 & 1], xp[b1 >> 1][b1 & 1]);
          const float p2 = b2 == 26 ? xp[a2 >> 1][a2 & 1] : mulf(xp[a2 >> 1][a2 & 1], xp[b2 >> 1][b2 & 1]);
          zz[e] = packbf(p1, p2);
        }
      }
      z[s] = __builtin_bit_cast(bf16x8, zz);
      asm volatile("" : "+v"(z[s]));
    }
    const u32 tag = get_tag(cnt);
    const u64 flag = mkflag(tag);
    STAMP(2);
    if (cw == 0) {
      __builtin_amdgcn_s_sleep(P_BDELAY);
      poll_flags<6>(a.ws + (lane < 32 ? WS_FA : WS_FB) + ((lane & 31) < NF ? (lane & 31) : 0) * FL, flag);
      lds_inc(cnt + C_ABRDY);
    } else team_wait<1>(cnt + C_ABRDY, 1);
    STAMP(3);
    if (r < 13) team_wait<1>(cnt + C_XISS, 4);
    __builtin_amdgcn_s_setprio(2);
    STAMP(4);
    {
      u32x4 y[10];
#pragma unroll
      for (int m = 0; m < 10; ++m) {
        const u32 n = ((m % 5) * 4 + cw) * 64 + lane;
        y[m] = __builtin_amdgcn_raw_buffer_load_b128(rws, n < NF * 48 ? n * 16 : OOB, m < 5 ? WS_PA : WS_PB, 16);
      }
#pragma unroll
      for (int m = 0; m < 5; ++m) {
        const u32 n = (m * 4 + cw) * 64 + lane, i = (n * 1366u) >> 16, c = n - i * 48;
        const u32 o = opq(n < NF * 48 ? L_A + c * APITCH + i * 16 : L_A + 26 * 16);
        *(LAS u32x4*)(lds + o) = y[m];
        *(LAS u32x4*)(lds + o + (L_B - L_A)) = y[5 + m];
      }
      team_arrive(cnt + C_ABSTG, lane);
      team_wait<0>(cnt + C_ABSTG, 4);
    }
    __builtin_amdgcn_s_setprio(0);
    STAMP(5);
    const u32 afr = (lane >> 5) * APITCH + (lane & 31) * 16;
    const u32 tag2 = (u32)(flag >> 32);
    const int sg = r * 4 + cw;
    const u32 po = opq(lane < 13 ? WS_P + ((g * 13 + lane) * 4 + (sg >> 4)) * 256 + (sg & 15) * 16 : OOB);
    u32x4 yp;
    f32x16 acc1 = {}, acc2 = {};
    {
      constexpr int AD = 2;
      bf16x8 af[24], bf[24];
#pragma unroll
      for (int s = 0; s < AD; ++s) { af[s] = *(LAS bf16x8*)(lds + L_A + afr + s * 2 * APITCH); bf[s] = *(LAS bf16x8*)(lds + L_B + afr + s * 2 * APITCH); }
      __builtin_amdgcn_sched_barrier(0);
#pragma unroll
      for (int s = 0; s < 24; ++s) {
        if (s + AD < 24) { af[s + AD] = *(LAS bf16x8*)(lds + L_A + afr + (s + AD) * 2 * APITCH); bf[s + AD] = *(LAS bf16x8*)(lds + L_B + afr + (s + AD) * 2 * APITCH); }
        if (s == P_PQ) yp = __builtin_amdgcn_raw_buffer_load_b128(rws, po, 0, 16);
        acc1 = __builtin_amdgcn_mfma_f32_32x32x16_bf16(af[s], z[s], acc1, 0, 0, 0);
        acc2 = __builtin_amdgcn_mfma_f32_32x32x16_bf16(bf[s], z[s], acc2, 0, 0, 0);
        __builtin_amdgcn_sched_barrier(0);
      }
    }
    float sum = 0.f;
#pragma unroll
    for (int e = 0; e < 12; ++e) sum += acc1[e] * acc2[e];
    { const float t = acc1[12] * acc2[12] + acc1[13] * acc2[13]; sum += lane < 32 ? t : 0.f; }
    sum = sum_rows(sum_row16(sum));
    STAMP(6);
    for (u32 it = 0; it < SPIN_MAX; ++it) {
      const bool ok = lane >= 13 || (yp.y == tag && yp.w == tag2);
      if (__all(ok)) break;
      asm volatile("" ::: "memory");
      __builtin_amdgcn_s_sleep(1);
      yp = __builtin_amdgcn_raw_buffer_load_b128(rws, po, 0, 16);
    }
    float part = sum_row16(__uint_as_float(yp.x));
    if (lane == 0) {
      const __bf16 ob = (__bf16)(sum + part + bflo(bo));
      a.out[smp] = __builtin_bit_cast(uint16_t, ob);
    }
    STAMP(7);
  } else if (r < 13) {
    const rsrc_t rw1 = mkrs(a.w1, DIN * DIN * 2);
    const int gq = lane >> 4;
    const u32 ib = (g * 64 + w * 16) * NF * 4;
    const u32x4 i0 = __builtin_amdgcn_raw_buffer_load_b128(ridx, lane * 16, ib, 0);
    const u32x4 i1 = __builtin_amdgcn_raw_buffer_load_b128(ridx, lane < 40 ? 1024 + lane * 16 : OOB, ib, 0);
    u32x4 v[26];
    const u32 vo = (w * 208 + (lane >> 3)) * 1664 + r * 128 + (lane & 7) * 16;
#pragma unroll
    for (int i = 0; i < P_W0; ++i) v[i] = __builtin_amdgcn_raw_buffer_load_b128(rw1, vo, i * 8 * 1664, 0);
    __builtin_amdgcn_sched_barrier(0);
    u32 id[26];
    {
      LAS char* ids = lds + L_ID + w * 2048;
      *(LAS u32x4*)(ids + lane * 16) = i0;
      *(LAS u32x4*)(ids + 1024 + lane * 16) = i1;
      asm volatile("s_waitcnt lgkmcnt(0)" ::: "memory");
#pragma unroll
      for (int m = 0; m < 13; ++m) { const u32x2 t = *(LAS u32x2*)(ids + (lane & 15) * (NF * 4) + m * 8); id[2 * m] = t.x; id[2 * m + 1] = t.y; }
    }
    lds_inc(cnt + 8);
    STAMP(1);
    constexpr int XW = P_XW, FD = 7;
    u32x4 xv[26];
    auto ldx = [&](int f) { xv[f] = __builtin_amdgcn_raw_buffer_load_b128(rtab, id[f] * 64 + gq * 16, 0, 0); };
#pragma unroll
    for (int f = 0; f < P_X0; ++f) ldx(f);
    __builtin_amdgcn_sched_barrier(0);
#pragma unroll
    for (int i = P_W0; i < 26; ++i) v[i] = __builtin_amdgcn_raw_buffer_load_b128(rw1, vo, i * 8 * 1664, 0);
#pragma unroll
    for (int f = P_X0; f < XW; ++f) ldx(f);
    __builtin_amdgcn_sched_barrier(0);
    {
      const int c = lane & 7, sb = (lane >> 4) & 1;
      const u32 lo = L_W + (w * 208 + (lane >> 3)) * 128 + (c & 1) * 16;
      const u32 l0 = lo + (((c >> 1) ^ sb) * 32), l1 = lo + (((c >> 1) ^ (sb | 2)) * 32);
#pragma unroll
      for (int i = 0; i < 26; ++i) *(LAS u32x4*)(lds + ((i & 1) ? l1 : l0) + i * 1024) = v[i];
    }
    team_arrive(cnt + C_W, lane);
    f32x4 acc[4]; f32x2 wf[8];
    {
      const CAS u32* bp = (const CAS u32*)(a.b1 + r * 64);
      const CAS u32* wp = (const CAS u32*)(a.w2 + r * 64);
#pragma unroll
      for (int t = 0; t < 4; ++t) {
        u32 bs[8], ws_[8];
#pragma unroll
        for (int e = 0; e < 8; ++e) { bs[e] = bp[8 * t + e]; ws_[e] = wp[8 * t + e]; }
#pragma unroll
        for (int e = 0; e < 8; ++e) asm volatile("" : "+s"(bs[e]), "+s"(ws_[e]));
        const bool g1 = gq & 1, g2 = gq & 2;
        const u32 b0 = g2 ? (g1 ? bs[6] : bs[4]) : (g1 ? bs[2] : bs[0]), b1 = g2 ? (g1 ? bs[7] : bs[5]) : (g1 ? bs[3] : bs[1]);
        const u32 w0 = g2 ? (g1 ? ws_[6] : ws_[4]) : (g1 ? ws_[2] : ws_[0]), w1 = g2 ? (g1 ? ws_[7] : ws_[5]) : (g1 ? ws_[3] : ws_[1]);
        acc[t] = (f32x4){bflo(b0), bfhi(b0), bflo(b1), bfhi(b1)};
        wf[2 * t] = (f32x2){bflo(w0), bfhi(w0)}; wf[2 * t + 1] = (f32x2){bflo(w1), bfhi(w1)};
        asm volatile("" : "+v"(wf[2 * t]), "+v"(wf[2 * t + 1]));
      }
    }
    STAMP(2);
    team_wait<0>(cnt + C_W, 4);
    __builtin_amdgcn_s_setprio(1);
    STAMP(3);
    {
      const int q = (lane >> 2) & 3, p = lane & 3;
      const int fsw = ((q >> 1) & 1) | ((gq & 1) << 1);
      const u32 rb = L_W + (8 * gq + q) * 128 + p * 8;
      u32 ad[4], ae[4];
#pragma unroll
      for (int t = 0; t < 4; ++t) { ad[t] = rb + ((t ^ fsw) * 32); ae[t] = ad[t] + 13 * 4096; }
      bf16x8 fr[104];
      auto ldf = [&](int n) {
        const int f = n >> 2, t = n & 3;
        const u32 o = f < 13 ? ad[t] + f * 4096 : ae[t] + (f - 13) * 4096;
        const s16x4 y0 = __builtin_amdgcn_ds_read_tr16_b64_v4i16((LAS s16x4*)(lds + o));
        const s16x4 y1 = __builtin_amdgcn_ds_read_tr16_b64_v4i16((LAS s16x4*)(lds + o + 512));
        fr[n] = __builtin_bit_cast(bf16x8, __builtin_shufflevector(y0, y1, 0, 1, 2, 3, 4, 5, 6, 7));
      };
#pragma unroll
      for (int n = 0; n < FD; ++n) ldf(n);
      __builtin_amdgcn_sched_barrier(0);
#pragma unroll
      for (int n = 0; n < 104; ++n) {
        const int f = n >> 2, t = n & 3;
        if (t == 0 && f + XW < 26) ldx(f + XW);
        if (n == 4 * (26 - XW)) lds_inc(cnt + C_XISS);
        if (n + FD < 104) ldf(n + FD);
        acc[t] = __builtin_amdgcn_mfma_f32_16x16x32_bf16(fr[n], __builtin_bit_cast(bf16x8, xv[f]), acc[t], 0, 0, 0);
        __builtin_amdgcn_sched_barrier(0);
      }
    }
    lds_inc(cnt + 9);
    STAMP(4);
    float part;
    {
      constexpr float K1 = -2.0f * 1.4426950408889634f * 0.7978845608028654f, K2 = K1 * 0.044715f;
      f32x2 h[8], u[8], y[8];
#pragma unroll
      for (int m = 0; m < 8; ++m) {
        h[m] = (f32x2){acc[m >> 1][2 * (m & 1)], acc[m >> 1][2 * (m & 1) + 1]};
        u[m] = (h[m] * h[m] * K2 + K1) * h[m];
        y[m] = h[m] * wf[m];
      }
#pragma unroll
      for (int m = 0; m < 8; ++m) { u[m][0] = __builtin_amdgcn_exp2f(u[m][0]); u[m][1] = __builtin_amdgcn_exp2f(u[m][1]); }
#pragma unroll
      for (int m = 0; m < 8; ++m) { u[m] = u[m] + 1.0f; }
#pragma unroll
      for (int m = 0; m < 8; ++m) { u[m][0] = __builtin_amdgcn_rcpf(u[m][0]); u[m][1] = __builtin_amdgcn_rcpf(u[m][1]); }
      f32x2 p0 = y[0] * u[0], p1 = y[1] * u[1];
#pragma unroll
      for (int m = 2; m < 8; m += 2) { p0 += y[m] * u[m]; p1 += y[m + 1] * u[m + 1]; }
      p0 += p1;
      part = sum_rows(p0[0] + p0[1]);
    }
    {
      const u32 tag = get_tag(cnt);
      const u32x4 o = {__float_as_uint(part), tag, __float_as_uint(part), (u32)(mkflag(tag) >> 32)};
      __builtin_amdgcn_raw_buffer_store_b128(o, rws, lane < 16 ? WS_P + ((g * 13 + r) * 4 + w) * 256 + lane * 16 : OOB, 0, 16);
    }
    lds_inc(cnt + 10);
    STAMP(5);
  } else {
      const int p = g * 3 + r - 13;
      const rsrc_t rwc = mkrs(a.wc, 3 * CUBE * 2), rwo = mkrs(a.wco, 78 * 2);
      __builtin_amdgcn_s_setprio(3);
      if (p < NF) {
        const int i = p;
        u32x4 tb[3];
        const int hb = (w >> 1) * 13, el = (w & 1) * 64 + lane;
        const CAS u32* w3p = (const CAS u32*)(a.wco + 52);
        u32 w3[13];
#pragma unroll
        for (int h = 0; h < 13; ++h) w3[h] = w3p[h];
        u32x4 v[13];
#pragma unroll
        for (int h = 0; h < 13; ++h) v[h] = __builtin_amdgcn_raw_buffer_load_b128(rwc, el < 85 ? (2 * CUBE + hb * 676 + 8 * el) * 2 : OOB, h * 676 * 2, 0);
        u32 v2[2];
#pragma unroll
        for (int m = 0; m < 2; ++m) {
          const u32 d = tid + 256 * m, h = (d * 5042u) >> 16, e = d - 13 * h;
          v2[m] = __builtin_amdgcn_raw_buffer_load_b32(rwc, d < 338 ? (CUBE + (h * NF + i) * NF) * 2 + e * 4 : OOB, 0, 0);
        }
        const u32 wq = __builtin_amdgcn_raw_buffer_load_b16(rwo, tid < NF ? (NF + tid) * 2 : OOB, 0, 0);
        const u32 w1i = __builtin_amdgcn_raw_buffer_load_b16(rwo, i * 2, 0, 0);
        if (w == 0) {
#pragma unroll
          for (int m = 0; m < 3; ++m) tb[m] = *(const GAS u32x4*)SYMTAB.w[lane + 64 * m];
        }
        __builtin_amdgcn_sched_barrier(0);
        STAMP(1);
        {
          float acc[8];
#pragma unroll
          for (int e = 0; e < 8; ++e) acc[e] = 0.f;
#pragma unroll
          for (int h = 0; h < 13; ++h) {
            const u32 we = (h & 1) ? w3[h >> 1] >> 16 : w3[h >> 1] & 0xffffu;
            const u32 wo = (h & 1) ? w3[(13 + h) >> 1] & 0xffffu : w3[(13 + h) >> 1] >> 16;
            const u32 wl = hb ? wo : we, wh = wl << 16;
#pragma unroll
            for (int e = 0; e < 4; ++e) { acc[2 * e] = dot2bf(v[h][e], wl, acc[2 * e]); acc[2 * e + 1] = dot2bf(v[h][e], wh, acc[2 * e + 1]); }
          }
          const u32 vo = opq(el < 85 ? L_V3 + (w >> 1) * 2816 + el * 32 : L_V3 + 5632 + lane * 32);
          *(LAS f32x4*)(lds + vo) = (f32x4){acc[0], acc[1], acc[2], acc[3]};
          *(LAS f32x4*)(lds + vo + 16) = (f32x4){acc[4], acc[5], acc[6], acc[7]};
#pragma unroll
          for (int m = 0; m < 2; ++m) *(LAS u32*)(lds + L_W2S + (tid + 256 * m) * 4) = v2[m];
          if (w == 0) *(LAS u32*)(lds + L_W2C + lane * 4) = wq << 16;
        }
        const int tj = w >> 1, tp = w & 1, kq = lane >> 4;
        const int jr = 16 * tj + (lane & 15), jp = 16 * tp + (lane & 15);
        u32 oa[7], ob[7], oc[7];
#pragma unroll
        for (int s = 0; s < 7; ++s) {
          const int h = 4 * s + kq;
          const bool inv = h < NF && jp < NF;
          oa[s] = opq((h < NF && jr < NF) ? L_W2S + (h * NF + jr) * 2 : L_W2C + 63 * 4);
          ob[s] = opq(inv ? L_V3 + (h * NF + jp) * 4 : L_W2C + ((h < NF && jp == NF) ? h : 63) * 4);
          oc[s] = opq(inv ? L_V3 + 2816 + (h * NF + jp) * 4 : L_W2C + 63 * 4);
        }
        team_arrive(cnt + C_V3, lane);
        team_wait<0>(cnt + C_V3, 4);
        STAMP(2);
        u32 av[7]; float bv[7], bw[7];
#pragma unroll
        for (int s = 0; s < 7; ++s) { av[s] = *(LAS uint16_t*)(lds + oa[s]); bv[s] = *(LAS float*)(lds + ob[s]); bw[s] = *(LAS float*)(lds + oc[s]); }
#pragma unroll
        for (int s = 0; s < 7; ++s) bv[s] += bw[s];
        f32x4 u = {0.f, 0.f, 0.f, 0.f}, u2 = {0.f, 0.f, 0.f, 0.f};
#pragma unroll
        for (int s = 0; s < 7; ++s) {
          if (s & 1) u2 = __builtin_amdgcn_mfma_f32_16x16x4f32(bflo(av[s]), bv[s], u2, 0, 0, 0);
          else u = __builtin_amdgcn_mfma_f32_16x16x4f32(bflo(av[s]), bv[s], u, 0, 0, 0);
        }
        u += u2;
#pragma unroll
        for (int e = 0; e < 4; ++e) {
          const int jo = 16 * tj + 4 * kq + e;
          const float val = (jo == NF && jp == NF) ? bflo(w1i) : u[e];
          *(LAS float*)(lds + L_U + (jo * UP + jp) * 4) = val;
        }
        team_arrive(cnt + C_U, lane);
        if (w == 0) {
          team_wait<0>(cnt + C_U, 4);
          STAMP(3);
          LAS float* U = (LAS float*)(lds + L_U);
          float e[12];
#pragma unroll
          for (int m = 0; m < 3; ++m) {
            e[4 * m] = U[tb[m].x & 0xffff]; e[4 * m + 1] = U[tb[m].x >> 16]; e[4 * m + 2] = U[tb[m].y & 0xffff]; e[4 * m + 3] = U[tb[m].y >> 16];
          }
#pragma unroll
          for (int m = 0; m < 3; ++m)
            __builtin_amdgcn_raw_buffer_store_b32(packbf(e[4 * m] + e[4 * m + 1], e[4 * m + 2] + e[4 * m + 3]), rws, WS_PB + i * 768 + (lane + 64 * m) * 4, 0, 16);
          const u64 flagb = mkflag(get_tag(cnt));
          drain_vm();
          if (lane == 0) store_flag(a.ws, WS_FB + i * FL, flagb);
          STAMP(4);
        }
      } else {
        const int i = (p - NF) * 4 + w;
        const bool prod = p < 33 && i < NF;
        u32 v[6]; u32x4 tb[3];
#pragma unroll
        for (int m = 0; m < 6; ++m) { const int d = lane + 64 * m; v[m] = __builtin_amdgcn_raw_buffer_load_b32(rwc, (prod && d < 338) ? i * 1352 + d * 4 : OOB, 0, 0); }
#pragma unroll
        for (int m = 0; m < 3; ++m) tb[m] = *(const GAS u32x4*)SYMTAB.w[lane + 64 * m];
        __builtin_amdgcn_sched_barrier(0);
        STAMP(1);
        if (prod) {
          LAS char* raw = lds + L_RAW + w * 2048;
#pragma unroll
          for (int m = 0; m < 6; ++m) *(LAS u32*)(raw + (lane + 64 * m) * 4) = v[m];
          asm volatile("s_waitcnt lgkmcnt(0)" ::: "memory");
          LAS uint16_t* R = (LAS uint16_t*)raw;
          u32 e[12];
#pragma unroll
          for (int m = 0; m < 3; ++m) {
            e[4 * m] = R[tb[m].z & 0xffff]; e[4 * m + 1] = R[tb[m].z >> 16]; e[4 * m + 2] = R[tb[m].w & 0xffff]; e[4 * m + 3] = R[tb[m].w >> 16];
          }
#pragma unroll
          for (int m = 0; m < 3; ++m)
            __builtin_amdgcn_raw_buffer_store_b32(packbf(bflo(e[4 * m]) + bflo(e[4 * m + 1]), bflo(e[4 * m + 2]) + bflo(e[4 * m + 3])), rws,
                                                  WS_PA + i * 768 + (lane + 64 * m) * 4, 0, 16);
          const u64 flag0 = mkflag(get_tag(cnt));
          drain_vm();
          if (lane == 0) store_flag(a.ws, WS_FA + i * FL, flag0);
          STAMP(4);
        }
        if (p == 47 && w == 0) {
          const u32 tag0 = get_tag(cnt);
          const u64 flag0 = mkflag(tag0);
          __builtin_amdgcn_s_sleep(32);
#pragma unroll 1
          for (int m = 0; m < 4; ++m) poll_flags<16>(a.ws + WS_HI + (lane + 64 * m) * FL, flag0);
          if (lane == 0) __hip_atomic_store((GAS u32*)(a.ws + WS_E), tag0, __ATOMIC_RELAXED, __HIP_MEMORY_SCOPE_AGENT);
        }
      }
  }
}

}

extern "C" void kernel_launch(void* const* d_in, const int* in_sizes, int n_in, void* d_out, int out_size,
                              void* d_ws, size_t ws_size, hipStream_t stream) {
  using namespace xdfm;
  Args a;
  a.idx = (const int*)d_in[0]; a.table = (const uint16_t*)d_in[1]; a.wc = (const uint16_t*)d_in[2];
  a.wco = (const uint16_t*)d_in[3]; a.w1 = (const uint16_t*)d_in[4]; a.b1 = (const uint16_t*)d_in[5];
  a.w2 = (const uint16_t*)d_in[6]; a.bo = (const uint16_t*)d_in[7]; a.out = (uint16_t*)d_out; a.ws = (char*)d_ws;
  a.table_bytes = (u32)in_sizes[1] * 2u; a.pad_ = 0;
  (void)hipFuncSetAttribute((const void*)xdfm_kernel, hipFuncAttributeMaxDynamicSharedMemorySize, L_TOTAL);
  xdfm_kernel<<<256, NWAVE * 64, L_TOTAL, stream>>>(a);
}
```

```cpp
#include <hip/hip_runtime.h>
#include <hip/hip_bf16.h>
#include <stdint.h>

#define P_ADELAY 8
#define P_BDELAY 8
#define P_WW 10
#define P_PQ 8
#define P_XI 10
#define P_XDELAY 12
#define P_CG 4
#define P_XW 8
#define P_TOUCH 1
#define P_ROT 1
#define P_IL 3
#define P_W0 10
#define P_X0 2
namespace xdfm {

typedef unsigned int u32;
typedef unsigned long long u64;
typedef u32 u32x2 __attribute__((ext_vector_type(2)));
typedef u32 u32x4 __attribute__((ext_vector_type(4)));
typedef short s16x4 __attribute__((ext_vector_type(4)));
typedef short s16x8 __attribute__((ext_vector_type(8)));
typedef float f32x2 __attribute__((ext_vector_type(2)));
typedef float f32x4 __attribute__((ext_vector_type(4)));
typedef float f32x16 __attribute__((ext_vector_type(16)));
typedef __bf16 bf16x2 __attribute__((ext_vector_type(2)));
typedef __bf16 bf16x8 __attribute__((ext_vector_type(8)));
#define GAS __attribute__((address_space(1)))
#define LAS __attribute__((address_space(3)))
typedef __amdgpu_buffer_rsrc_t rsrc_t;

constexpr int NF = 26, DIN = 832, NB = 1024, CUBE = 17576;

constexpr u32 WS_E = 0;
constexpr u32 FL = 128;
constexpr u32 WS_HI = 4096;
constexpr u32 WS_FA = 40960;
constexpr u32 WS_FB = 45056;
constexpr u32 WS_PA = 49152;
constexpr u32 WS_PB = 69632;
constexpr u32 WS_P = 131072;
constexpr u32 WS_END = WS_P + 16 * 13 * 4 * 256;
constexpr int NWAVE = 9;

constexpr u32 L_W = 0;
constexpr u32 APITCH = 432;
constexpr u32 L_A = 106496;
constexpr u32 L_B = L_A + 48 * APITCH;
constexpr u32 L_XS = L_B + 48 * APITCH;
constexpr u32 XS_S = 1824;
constexpr u32 L_ID = L_XS + 4 * XS_S;
constexpr u32 L_CNT = L_ID + 4 * 2048;
constexpr u32 L_TOTAL = L_CNT + 64;
constexpr u32 L_V3 = 0, L_W2C = 8192, L_W2S = 8448, L_U = 12288, L_RAW = 20480;
enum { C_TAGV = 12, C_TAG = 13, C_ARDY = 0, C_ASTG = 1, C_BRDY = 2, C_BSTG = 7, C_W = 3, C_V3 = 4, C_U = 5, C_XISS = 6 };

constexpr int perm1(int j) { return j < 24 ? ((j & ~7) | ((j + 2) & 7)) : (j == 24 ? 25 : (j == 25 ? 24 : j)); }
struct Slots { int a[192]; int b[192]; bool v0[192]; bool v1[192]; int n; };
struct SlotBuilder {
  Slots s{}; bool vis[27][27] = {}; int n = 0;
  constexpr bool seen(int a, int b) const { return vis[a < b ? a : b][a < b ? b : a]; }
  constexpr void put(int a, int b) {
    s.a[n] = a; s.b[n] = b;
    s.v0[n] = !seen(a, b); vis[a < b ? a : b][a < b ? b : a] = true;
    const int na = perm1(a), nb = perm1(b);
    s.v1[n] = !seen(na, nb); vis[na < nb ? na : nb][na < nb ? nb : na] = true;
    ++n;
  }
};
constexpr int sg2(int m) { return perm1(perm1(2 * m)) >> 1; }
constexpr Slots make_slots() {
  SlotBuilder t{};
  for (int A = 0; A < 13; ++A)
    for (int B = A + 1; B < 13; ++B) {
      int ca = A, cb = B;
      while (!t.seen(2 * ca, 2 * cb)) {
        t.put(2 * ca, 2 * cb); t.put(2 * ca, 2 * cb + 1); t.put(2 * ca + 1, 2 * cb); t.put(2 * ca + 1, 2 * cb + 1);
        ca = sg2(ca); cb = sg2(cb);
      }
    }
  for (int A = 0; A < 13; ++A) {
    int ca = A;
    while (!t.seen(2 * ca, 2 * ca + 1)) {
      t.put(2 * ca, 2 * ca); t.put(2 * ca, 2 * ca + 1); t.put(2 * ca + 1, 2 * ca + 1); t.put(2 * ca + 1, 26);
      ca = sg2(ca);
    }
  }
  for (int a = 0; a < 27; ++a) { int ca = a; while (!t.seen(ca, 26)) { t.put(ca, 26); ca = perm1(perm1(ca)); } }
  t.s.n = t.n;
  for (int n = t.n; n < 192; ++n) { t.s.a[n] = 26; t.s.b[n] = 26; t.s.v0[n] = false; t.s.v1[n] = false; }
  return t.s;
}
constexpr Slots SL = make_slots();
static_assert(SL.n <= 192, "too many slots");

constexpr int UP = 33, ZB = 30 * UP, ZA = 676;
struct SymTab { u32 w[192][4]; };
constexpr SymTab make_symtab() {
  SymTab t{};
  for (int d = 0; d < 192; ++d) {
    int c = d >> 2, s = c >> 1, hh = c & 1;
    for (int e = 0; e < 2; ++e) {
      int sg = 8 * s + 2 * (d & 3) + e;
      int a = hh ? perm1(SL.a[sg]) : SL.a[sg], b = hh ? perm1(SL.b[sg]) : SL.b[sg];
      bool v = hh ? SL.v1[sg] : SL.v0[sg];
      u32 b1 = ZB, b2 = ZB, a1 = ZA, a2 = ZA;
      if (v) {
        b1 = a * UP + b; b2 = a == b ? ZB : b * UP + a;
        if (a < 26 && b < 26) { a1 = a * 26 + b; a2 = a == b ? ZA : b * 26 + a; }
      }
      t.w[d][e] = b1 | (b2 << 16);
      t.w[d][2 + e] = a1 | (a2 << 16);
    }
  }
  return t;
}
__device__ const SymTab SYMTAB = make_symtab();

constexpr u32 OOB = 0x7FFFFF00u;
constexpr u32 SPIN_MAX = 1u << 18;

__device__ __forceinline__ float bflo(u32 v) { return __uint_as_float(v << 16); }
__device__ __forceinline__ float bfhi(u32 v) { return __uint_as_float(v & 0xffff0000u); }
__device__ __forceinline__ u32 packbf(float a, float b) {
  f32x2 f = {a, b};
  bf16x2 z = __builtin_convertvector(f, bf16x2);
  return __builtin_bit_cast(u32, z);
}
__device__ __forceinline__ float mulf(float a, float b) { float r; asm("v_mul_f32 %0, %1, %2" : "=v"(r) : "v"(a), "v"(b)); return r; }
template <int E> __device__ __forceinline__ f32x2 pkmul_bc(f32x2 a, f32x2 b) {
  f32x2 r; asm("v_pk_mul_f32 %0, %1, %2 op_sel:[%3,0] op_sel_hi:[%3,1]" : "=v"(r) : "v"(a), "v"(b), "i"(E)); return r;
}
__device__ __forceinline__ float dot2bf(u32 a, u32 b, float c) {
  return __builtin_amdgcn_fdot2_f32_bf16(__builtin_bit_cast(bf16x2, a), __builtin_bit_cast(bf16x2, b), c, false);
}
__device__ __forceinline__ rsrc_t mkrs(const void* p, u32 bytes) { return __builtin_amdgcn_make_buffer_rsrc((void*)p, 0, bytes, 0x00020000); }
__device__ __forceinline__ u64 mkflag(u32 tag) { return (u64)tag | ((u64)(tag * 0x9E3779B1u + 0x7F4A7C15u) << 32); }
__device__ __forceinline__ void drain_vm() { asm volatile("s_waitcnt vmcnt(0)" ::: "memory"); }
__device__ __forceinline__ void lds_inc(LAS u32* cnt) {
  u64 keep;
  asm volatile("s_mov_b64 %0, exec\n\ts_mov_b64 exec, 1\n\tds_add_u32 %1, %2\n\ts_mov_b64 exec, %0"
               : "=&s"(keep) : "v"((u32)(size_t)cnt), "v"(1u) : "memory");
}
__device__ __forceinline__ void team_arrive(LAS u32* cnt, int) {
  asm volatile("s_waitcnt lgkmcnt(0)" ::: "memory");
  lds_inc(cnt);
}
__device__ __forceinline__ float sum_rows(float v) {
  auto a = __builtin_amdgcn_permlane32_swap(__float_as_uint(v), __float_as_uint(v), false, false);
  v = __uint_as_float(a[0]) + __uint_as_float(a[1]);
  auto b = __builtin_amdgcn_permlane16_swap(__float_as_uint(v), __float_as_uint(v), false, false);
  return __uint_as_float(b[0]) + __uint_as_float(b[1]);
}
template <int CTRL> __device__ __forceinline__ float dpp_add(float v) {
  return v + __uint_as_float(__builtin_amdgcn_update_dpp(0u, __float_as_uint(v), CTRL, 0xf, 0xf, true));
}
__device__ __forceinline__ float sum_row16(float v) {
  v = dpp_add<0x128>(v);
  v = dpp_add<0x124>(v);
  v = dpp_add<0x122>(v);
  v = dpp_add<0x121>(v);
  return v;
}

#define STAMP(k) do {} while (0)

struct Args {
  const int* idx; const uint16_t* table; const uint16_t* wc; const uint16_t* wco; const uint16_t* w1;
  const uint16_t* b1; const uint16_t* w2; const uint16_t* bo; uint16_t* out; char* ws;
  u32 table_bytes; u32 pad_;
};
#define CAS __attribute__((address_space(4)))

template <int SLEEP> __device__ __forceinline__ void team_wait(LAS u32* cnt, u32 target) {
  u32 v, s, n = SPIN_MAX;
  asm volatile(
      "1:\n\tds_read_b32 %0, %3\n\ts_waitcnt lgkmcnt(0)\n\tv_readfirstlane_b32 %1, %0\n\ts_cmp_ge_u32 %1, %4\n\ts_cbranch_scc1 2f\n\t"
      "s_sleep %5\n\ts_sub_u32 %2, %2, 1\n\ts_cbranch_scc0 1b\n2:"
      : "=&v"(v), "=&s"(s), "+s"(n) : "v"((u32)(size_t)cnt), "s"(target), "i"(SLEEP) : "memory", "scc");
}
template <int SLEEP> __device__ __forceinline__ void poll_flags(const char* p, u64 want) {
  u64 v; u32 n = SPIN_MAX;
  asm volatile(
      "1:\n\tglobal_load_dwordx2 %0, %2, off sc1\n\ts_waitcnt vmcnt(0)\n\tv_cmp_ne_u64 vcc, %3, %0\n\ts_nop 4\n\ts_cbranch_vccz 2f\n\t"
      "s_sleep %4\n\ts_sub_u32 %1, %1, 1\n\ts_cbranch_scc0 1b\n2:"
      : "=&v"(v), "+s"(n) : "v"(p), "s"(want), "i"(SLEEP) : "memory", "scc", "vcc");
}
__device__ __forceinline__ void lds_barrier() {
  asm volatile("s_waitcnt lgkmcnt(0)" ::: "memory");
  __builtin_amdgcn_s_barrier();
  asm volatile("" ::: "memory");
}
__device__ __forceinline__ u32 load_epoch(const char* ws) {
  return __hip_atomic_load((const GAS u32*)(ws + WS_E), __ATOMIC_RELAXED, __HIP_MEMORY_SCOPE_AGENT);
}
__device__ __forceinline__ void store_flag(char* ws, u32 off, u64 flag) {
  __hip_atomic_store((GAS u64*)(ws + off), flag, __ATOMIC_RELAXED, __HIP_MEMORY_SCOPE_AGENT);
}
__device__ __forceinline__ u32 get_tag(LAS u32* cnt) {
  team_wait<0>(cnt + C_TAG, 1);
  return __builtin_amdgcn_readfirstlane(cnt[C_TAGV]);
}
__device__ __forceinline__ u32 opq(u32 v) { asm("" : "+v"(v)); return v; }

__global__ void __launch_bounds__(576, 3) xdfm_kernel(Args a) {
  extern __shared__ __attribute__((aligned(16))) char lds_raw[];
  LAS char* lds = (LAS char*)lds_raw;
  const int tid = threadIdx.x, lane = tid & 63;
  LAS u32* cnt = (LAS u32*)(lds + L_CNT);
  if (tid < 16) cnt[tid] = 0;
  const int w = __builtin_amdgcn_readfirstlane(tid >> 6);
  const int b = blockIdx.x, j = b >> 3;
  const int jg = j < 6 ? (j >= 3 ? 1 : 0) : (j >= 19 ? 1 : 0);
  const int g = (b & 7) * 2 + jg, r = j < 6 ? 13 + j - 3 * jg : j - 6 - 13 * jg;
  asm volatile("" :: "s"(a.idx), "s"(a.table), "s"(a.wc), "s"(a.wco), "s"(a.w1), "s"(a.b1), "s"(a.w2), "s"(a.bo), "s"(a.out), "s"(a.ws), "s"(a.table_bytes));
  lds_barrier();
  const rsrc_t rws = mkrs(a.ws, WS_END);
  const rsrc_t ridx = mkrs(a.idx, NB * NF * 4), rtab = mkrs(a.table, a.table_bytes);

  if (w == 8) {
    if (b == 250) {
      team_wait<1>(cnt + 8, 4);
#pragma unroll 1
      for (int i = 0; i < 6; ++i) __builtin_amdgcn_s_sleep(127);
    }
    return;
  }
  if (w >= 4) {
    const int cw = w - 4, smp = g * 64 + r * 4 + cw;
    __builtin_amdgcn_s_setprio(3);
    u32 id[2]; u32x4 xr[2];
#pragma unroll
    for (int m = 0; m < 2; ++m) { const int f = 16 * m + (lane >> 2); id[m] = __builtin_amdgcn_raw_buffer_load_b32(ridx, f < NF ? f * 4 : OOB, smp * NF * 4, 0); }
    u32 ep = 0, touch = 0;
    if (cw == 0) ep = load_epoch(a.ws);
    if (P_TOUCH && cw == 1) touch = __builtin_amdgcn_raw_buffer_load_b32(rtab, lane < 8 ? lane * (1u << 20) : OOB, 0, 0);
    if (P_TOUCH && cw == 2) touch = __builtin_amdgcn_raw_buffer_load_b32(rws, lane < 2 ? (lane ? WS_P : WS_PA) : OOB, 0, 0) + *(const GAS uint16_t*)(a.out + smp);
    const size_t bop = (size_t)a.bo;
    const u32 bo = *(const CAS u32*)(bop & ~(size_t)3) >> ((bop & 2) * 8);
#pragma unroll
    for (int m = 0; m < 2; ++m) { const int f = 16 * m + (lane >> 2); xr[m] = __builtin_amdgcn_raw_buffer_load_b128(rtab, f < NF ? id[m] * 64 + (lane & 3) * 16 : OOB, 0, 0); }
    if (cw == 0) {
      const u32 t0 = __builtin_amdgcn_readfirstlane(ep) + 1u;
      cnt[C_TAGV] = t0;
      team_arrive(cnt + C_TAG, lane);
      if (lane == 0) store_flag(a.ws, WS_HI + (g * 16 + r) * FL, mkflag(t0));
    }
    asm volatile("" :: "v"(touch));
    __builtin_amdgcn_s_setprio(0);
    STAMP(1);
    f32x2 xp[14];
    {
      const u32 o = L_XS + cw * XS_S + (lane >> 2) * 64 + (lane & 3) * 16;
      *(LAS u32x4*)(lds + o) = xr[0];
      u32x4 x1 = xr[1];
      if ((lane >> 2) == NF - 16) x1 = (u32x4){0x3F803F80u, 0x3F803F80u, 0x3F803F80u, 0x3F803F80u};
      *(LAS u32x4*)(lds + opq((lane >> 2) < 12 ? o + 1024 : L_XS + cw * XS_S + 27 * 64 + (lane & 3) * 16)) = x1;
      asm volatile("s_waitcnt lgkmcnt(0)" ::: "memory");
      const int hh = lane >> 5, q = (lane >> 2) & 3, pp = lane & 3;
      const u32 base = L_XS + cw * XS_S + ((lane >> 4) & 1) * 32 + pp * 8;
      const u32 a0 = base + (hh ? q + 2 : q) * 64, a1 = base + (hh ? ((q + 6) & 7) : q + 4) * 64;
      const u32 a6 = base + (24 + ((hh && q < 2) ? (q ^ 1) : q)) * 64;
#pragma unroll
      for (int t = 0; t < 7; ++t) {
        s16x4 y = __builtin_amdgcn_ds_read_tr16_b64_v4i16((LAS s16x4*)(lds + (t < 6 ? ((t & 1) ? a1 : a0) + (t >> 1) * 512 : a6)));
        u32x2 u = __builtin_bit_cast(u32x2, y);
        xp[2 * t] = (f32x2){bflo(u.x), bfhi(u.x)}; xp[2 * t + 1] = (f32x2){bflo(u.y), bfhi(u.y)};
      }
    }
    bf16x8 z[24];
#pragma unroll
    for (int s = 0; s < 24; ++s) {
      u32x4 zz;
#pragma unroll
      for (int e = 0; e < 4; ++e) {
        const int s1 = 8 * s + 2 * e, s2 = s1 + 1;
        const int a1 = SL.a[s1], b1 = SL.b[s1], a2 = SL.a[s2], b2 = SL.b[s2];
        if (a1 == a2 && (b1 & 1) == 0 && b1 < 26 && b2 == b1 + 1) {
          const f32x2 pr = (a1 & 1) ? pkmul_bc<1>(xp[a1 >> 1], xp[b1 >> 1]) : pkmul_bc<0>(xp[a1 >> 1], xp[b1 >> 1]);
          zz[e] = packbf(pr[0], pr[1]);
        } else {
          const float p1 = b1 == 26 ? xp[a1 >> 1][a1 & 1] : mulf(xp[a1 >> 1][a1 & 1], xp[b1 >> 1][b1 & 1]);
          const float p2 = b2 == 26 ? xp[a2 >> 1][a2 & 1] : mulf(xp[a2 >> 1][a2 & 1], xp[b2 >> 1][b2 & 1]);
          zz[e] = packbf(p1, p2);
        }
      }
      z[s] = __builtin_bit_cast(bf16x8, zz);
      asm volatile("" : "+v"(z[s]));
    }
    const u32 tag = get_tag(cnt);
    const u64 flag = mkflag(tag);
    STAMP(2);
    auto stage = [&](u32 wsf, u32 wsp, u32 ldst, int crdy, int cstg, bool after_x) {
      if (cw == 0) {
        poll_flags<6>(a.ws + wsf + (lane < NF ? lane : 0) * FL, flag);
        lds_inc(cnt + crdy);
      } else team_wait<1>(cnt + crdy, 1);
      if (after_x) team_wait<1>(cnt + C_XISS, 4);
      __builtin_amdgcn_s_setprio(2);
      u32x4 y[5];
#pragma unroll
      for (int m = 0; m < 5; ++m) { const u32 n = (m * 4 + cw) * 64 + lane; y[m] = __builtin_amdgcn_raw_buffer_load_b128(rws, n < NF * 48 ? n * 16 : OOB, wsp, 16); }
#pragma unroll
      for (int m = 0; m < 5; ++m) {
        const u32 n = (m * 4 + cw) * 64 + lane, i = (n * 1366u) >> 16, c = n - i * 48;
        *(LAS u32x4*)(lds + opq(n < NF * 48 ? ldst + c * APITCH + i * 16 : ldst + 26 * 16)) = y[m];
      }
      team_arrive(cnt + cstg, lane);
      team_wait<0>(cnt + cstg, 4);
      __builtin_amdgcn_s_setprio(0);
    };
    const u32 afr = (lane >> 5) * APITCH + (lane & 31) * 16;
    const u32 tag2 = (u32)(flag >> 32);
    const int sg = r * 4 + cw;
    const u32 po = opq(lane < 13 ? WS_P + ((g * 13 + lane) * 4 + (sg >> 4)) * 256 + (sg & 15) * 16 : OOB);
    f32x16 acc1 = {}, acc2 = {};
    constexpr int AD = 3;
    bf16x8 af[24];
    if (P_ADELAY) __builtin_amdgcn_s_sleep(P_ADELAY);
    stage(WS_FA, WS_PA, L_A, C_ARDY, C_ASTG, false);
    STAMP(3);
#pragma unroll
    for (int s = 0; s < AD; ++s) af[s] = *(LAS bf16x8*)(lds + L_A + afr + s * 2 * APITCH);
    __builtin_amdgcn_sched_barrier(0);
#pragma unroll
    for (int s = 0; s < 24; ++s) {
      if (s + AD < 24) af[s + AD] = *(LAS bf16x8*)(lds + L_A + afr + (s + AD) * 2 * APITCH);
      acc1 = __builtin_amdgcn_mfma_f32_32x32x16_bf16(af[s], z[s], acc1, 0, 0, 0);
      __builtin_amdgcn_sched_barrier(0);
    }
    STAMP(4);
    stage(WS_FB, WS_PB, L_B, C_BRDY, C_BSTG, r < 13);
    STAMP(5);
    u32x4 yp;
#pragma unroll
    for (int s = 0; s < AD; ++s) af[s] = *(LAS bf16x8*)(lds + L_B + afr + s * 2 * APITCH);
    __builtin_amdgcn_sched_barrier(0);
#pragma unroll
    for (int s = 0; s < 24; ++s) {
      if (s + AD < 24) af[s + AD] = *(LAS bf16x8*)(lds + L_B + afr + (s + AD) * 2 * APITCH);
      if (s == P_PQ) yp = __builtin_amdgcn_raw_buffer_load_b128(rws, po, 0, 16);
      acc2 = __builtin_amdgcn_mfma_f32_32x32x16_bf16(af[s], z[s], acc2, 0, 0, 0);
      __builtin_amdgcn_sched_barrier(0);
    }
    float sum = 0.f;
#pragma unroll
    for (int e = 0; e < 12; ++e) sum += acc1[e] * acc2[e];
    { const float t = acc1[12] * acc2[12] + acc1[13] * acc2[13]; sum += lane < 32 ? t : 0.f; }
    sum = sum_rows(sum_row16(sum));
    STAMP(6);
    for (u32 it = 0; it < SPIN_MAX; ++it) {
      const bool ok = lane >= 13 || (yp.y == tag && yp.w == tag2);
      if (__all(ok)) break;
      asm volatile("" ::: "memory");
      __builtin_amdgcn_s_sleep(1);
      yp = __builtin_amdgcn_raw_buffer_load_b128(rws, po, 0, 16);
    }
    float part = sum_row16(__uint_as_float(yp.x));
    if (lane == 0) {
      const __bf16 ob = (__bf16)(sum + part + bflo(bo));
      a.out[smp] = __builtin_bit_cast(uint16_t, ob);
    }
    STAMP(7);
  } else if (r < 13) {
    const rsrc_t rw1 = mkrs(a.w1, DIN * DIN * 2);
    const int gq = lane >> 4;
    const u32 ib = (g * 64 + w * 16) * NF * 4;
    const u32x4 i0 = __builtin_amdgcn_raw_buffer_load_b128(ridx, lane * 16, ib, 0);
    const u32x4 i1 = __builtin_amdgcn_raw_buffer_load_b128(ridx, lane < 40 ? 1024 + lane * 16 : OOB, ib, 0);
    const int rot = P_ROT ? 2 * ((r + 5 * g) % 13) : 0;
    u32x4 v[26];
    const u32 vo = (lane >> 3) * 1664 + r * 128 + (lane & 7) * 16;
    auto ldw = [&](int i) {
      const int row = w * 208 + i * 8 + rot * 32;
      v[i] = __builtin_amdgcn_raw_buffer_load_b128(rw1, vo, (row >= DIN ? row - DIN : row) * 1664, 0);
    };
#pragma unroll
    for (int i = 0; i < P_W0; ++i) ldw(i);
    __builtin_amdgcn_sched_barrier(0);
    u32 id[26];
    {
      LAS char* ids = lds + L_ID + w * 2048;
      *(LAS u32x4*)(ids + lane * 16) = i0;
      *(LAS u32x4*)(ids + 1024 + lane * 16) = i1;
      asm volatile("s_waitcnt lgkmcnt(0)" ::: "memory");
#pragma unroll
      for (int m = 0; m < 13; ++m) {
        const int f = 2 * m + rot;
        const u32x2 t = *(LAS u32x2*)(ids + (lane & 15) * (NF * 4) + (f >= NF ? f - NF : f) * 4);
        id[2 * m] = t.x; id[2 * m + 1] = t.y;
      }
    }
    lds_inc(cnt + 8);
    STAMP(1);
    constexpr int XW = P_XW, FD = 7;
    u32x4 xv[26];
    auto ldx = [&](int f) { xv[f] = __builtin_amdgcn_raw_buffer_load_b128(rtab, id[f] * 64 + gq * 16, 0, 0); };
#pragma unroll
    for (int f = 0; f < P_X0; ++f) ldx(f);
    __builtin_amdgcn_sched_barrier(0);
#pragma unroll
    for (int i = P_W0; i < 26; ++i) { ldw(i); if (P_X0 + (i - P_W0) / P_IL < XW && (i - P_W0) % P_IL == 0) ldx(P_X0 + (i - P_W0) / P_IL); }
#pragma unroll
    for (int f = P_X0 + (25 - P_W0) / P_IL + 1; f < XW; ++f) ldx(f);
    __builtin_amdgcn_sched_barrier(0);
    {
      const int c = lane & 7, sb = (lane >> 4) & 1;
      const u32 lo = L_W + (w * 208 + (lane >> 3)) * 128 + (c & 1) * 16;
      const u32 l0 = lo + (((c >> 1) ^ sb) * 32), l1 = lo + (((c >> 1) ^ (sb | 2)) * 32);
#pragma unroll
      for (int i = 0; i < 26; ++i) *(LAS u32x4*)(lds + ((i & 1) ? l1 : l0) + i * 1024) = v[i];
    }
    team_arrive(cnt + C_W, lane);
    f32x4 acc[4]; f32x2 wf[8];
    {
      const CAS u32* bp = (const CAS u32*)(a.b1 + r * 64);
      const CAS u32* wp = (const CAS u32*)(a.w2 + r * 64);
#pragma unroll
      for (int t = 0; t < 4; ++t) {
        u32 bs[8], ws_[8];
#pragma unroll
        for (int e = 0; e < 8; ++e) { bs[e] = bp[8 * t + e]; ws_[e] = wp[8 * t + e]; }
#pragma unroll
        for (int e = 0; e < 8; ++e) asm volatile("" : "+s"(bs[e]), "+s"(ws_[e]));
        const bool g1 = gq & 1, g2 = gq & 2;
        const u32 b0 = g2 ? (g1 ? bs[6] : bs[4]) : (g1 ? bs[2] : bs[0]), b1 = g2 ? (g1 ? bs[7] : bs[5]) : (g1 ? bs[3] : bs[1]);
        const u32 w0 = g2 ? (g1 ? ws_[6] : ws_[4]) : (g1 ? ws_[2] : ws_[0]), w1 = g2 ? (g1 ? ws_[7] : ws_[5]) : (g1 ? ws_[3] : ws_[1]);
        acc[t] = (f32x4){bflo(b0), bfhi(b0), bflo(b1), bfhi(b1)};
        wf[2 * t] = (f32x2){bflo(w0), bfhi(w0)}; wf[2 * t + 1] = (f32x2){bflo(w1), bfhi(w1)};
        asm volatile("" : "+v"(wf[2 * t]), "+v"(wf[2 * t + 1]));
      }
    }
    STAMP(2);
    team_wait<0>(cnt + C_W, 4);
    __builtin_amdgcn_s_setprio(1);
    STAMP(3);
    {
      const int q = (lane >> 2) & 3, p = lane & 3;
      const int fsw = ((q >> 1) & 1) | ((gq & 1) << 1);
      const u32 rb = L_W + (8 * gq + q) * 128 + p * 8;
      u32 ad[4], ae[4];
#pragma unroll
      for (int t = 0; t < 4; ++t) { ad[t] = rb + ((t ^ fsw) * 32); ae[t] = ad[t] + 13 * 4096; }
      bf16x8 fr[104];
      auto ldf = [&](int n) {
        const int f = n >> 2, t = n & 3;
        const u32 o = f < 13 ? ad[t] + f * 4096 : ae[t] + (f - 13) * 4096;
        const s16x4 y0 = __builtin_amdgcn_ds_read_tr16_b64_v4i16((LAS s16x4*)(lds + o));
        const s16x4 y1 = __builtin_amdgcn_ds_read_tr16_b64_v4i16((LAS s16x4*)(lds + o + 512));
        fr[n] = __builtin_bit_cast(bf16x8, __builtin_shufflevector(y0, y1, 0, 1, 2, 3, 4, 5, 6, 7));
      };
#pragma unroll
      for (int n = 0; n < FD; ++n) ldf(n);
      __builtin_amdgcn_sched_barrier(0);
#pragma unroll
      for (int n = 0; n < 104; ++n) {
        const int f = n >> 2, t = n & 3;
        if (t == 0 && f + XW < 26) ldx(f + XW);
        if (n == 4 * (26 - XW)) lds_inc(cnt + C_XISS);
        if (n + FD < 104) ldf(n + FD);
        acc[t] = __builtin_amdgcn_mfma_f32_16x16x32_bf16(fr[n], __builtin_bit_cast(bf16x8, xv[f]), acc[t], 0, 0, 0);
        __builtin_amdgcn_sched_barrier(0);
      }
    }
    lds_inc(cnt + 9);
    STAMP(4);
    float part;
    {
      constexpr float K1 = -2.0f * 1.4426950408889634f * 0.7978845608028654f, K2 = K1 * 0.044715f;
      f32x2 h[8], u[8], y[8];
#pragma unroll
      for (int m = 0; m < 8; ++m) {
        h[m] = (f32x2){acc[m >> 1][2 * (m & 1)], acc[m >> 1][2 * (m & 1) + 1]};
        u[m] = (h[m] * h[m] * K2 + K1) * h[m];
        y[m] = h[m] * wf[m];
      }
#pragma unroll
      for (int m = 0; m < 8; ++m) { u[m][0] = __builtin_amdgcn_exp2f(u[m][0]); u[m][1] = __builtin_amdgcn_exp2f(u[m][1]); }
#pragma unroll
      for (int m = 0; m < 8; ++m) { u[m] = u[m] + 1.0f; }
#pragma unroll
      for (int m = 0; m < 8; ++m) { u[m][0] = __builtin_amdgcn_rcpf(u[m][0]); u[m][1] = __builtin_amdgcn_rcpf(u[m][1]); }
      f32x2 p0 = y[0] * u[0], p1 = y[1] * u[1];
#pragma unroll
      for (int m = 2; m < 8; m += 2) { p0 += y[m] * u[m]; p1 += y[m + 1] * u[m + 1]; }
      p0 += p1;
      part = sum_rows(p0[0] + p0[1]);
    }
    {
      const u32 tag = get_tag(cnt);
      const u32x4 o = {__float_as_uint(part), tag, __float_as_uint(part), (u32)(mkflag(tag) >> 32)};
      __builtin_amdgcn_raw_buffer_store_b128(o, rws, lane < 16 ? WS_P + ((g * 13 + r) * 4 + w) * 256 + lane * 16 : OOB, 0, 16);
    }
    lds_inc(cnt + 10);
    STAMP(5);
  } else {
      const int p = g * 3 + r - 13;
      const rsrc_t rwc = mkrs(a.wc, 3 * CUBE * 2), rwo = mkrs(a.wco, 78 * 2);
      __builtin_amdgcn_s_setprio(3);
      if (p < NF) {
        const int i = p;
        u32x4 tb[3];
        const int hb = (w >> 1) * 13, el = (w & 1) * 64 + lane;
        const CAS u32* w3p = (const CAS u32*)(a.wco + 52);
        u32 w3[13];
#pragma unroll
        for (int h = 0; h < 13; ++h) w3[h] = w3p[h];
        u32x4 v[13];
#pragma unroll
        for (int h = 0; h < 13; ++h) v[h] = __builtin_amdgcn_raw_buffer_load_b128(rwc, el < 85 ? (2 * CUBE + hb * 676 + 8 * el) * 2 : OOB, h * 676 * 2, 0);
        u32 v2[2];
#pragma unroll
        for (int m = 0; m < 2; ++m) {
          const u32 d = tid + 256 * m, h = (d * 5042u) >> 16, e = d - 13 * h;
          v2[m] = __builtin_amdgcn_raw_buffer_load_b32(rwc, d < 338 ? (CUBE + (h * NF + i) * NF) * 2 + e * 4 : OOB, 0, 0);
        }
        const u32 wq = __builtin_amdgcn_raw_buffer_load_b16(rwo, tid < NF ? (NF + tid) * 2 : OOB, 0, 0);
        const u32 w1i = __builtin_amdgcn_raw_buffer_load_b16(rwo, i * 2, 0, 0);
        if (w == 0) {
#pragma unroll
          for (int m = 0; m < 3; ++m) tb[m] = *(const GAS u32x4*)SYMTAB.w[lane + 64 * m];
        }
        __builtin_amdgcn_sched_barrier(0);
        STAMP(1);
        {
          float acc[8];
#pragma unroll
          for (int e = 0; e < 8; ++e) acc[e] = 0.f;
#pragma unroll
          for (int h = 0; h < 13; ++h) {
            const u32 we = (h & 1) ? w3[h >> 1] >> 16 : w3[h >> 1] & 0xffffu;
            const u32 wo = (h & 1) ? w3[(13 + h) >> 1] & 0xffffu : w3[(13 + h) >> 1] >> 16;
            const u32 wl = hb ? wo : we, wh = wl << 16;
#pragma unroll
            for (int e = 0; e < 4; ++e) { acc[2 * e] = dot2bf(v[h][e], wl, acc[2 * e]); acc[2 * e + 1] = dot2bf(v[h][e], wh, acc[2 * e + 1]); }
          }
          const u32 vo = opq(el < 85 ? L_V3 + (w >> 1) * 2816 + el * 32 : L_V3 + 5632 + lane * 32);
          *(LAS f32x4*)(lds + vo) = (f32x4){acc[0], acc[1], acc[2], acc[3]};
          *(LAS f32x4*)(lds + vo + 16) = (f32x4){acc[4], acc[5], acc[6], acc[7]};
#pragma unroll
          for (int m = 0; m < 2; ++m) *(LAS u32*)(lds + L_W2S + (tid + 256 * m) * 4) = v2[m];
          if (w == 0) *(LAS u32*)(lds + L_W2C + lane * 4) = wq << 16;
        }
        const int tj = w >> 1, tp = w & 1, kq = lane >> 4;
        const int jr = 16 * tj + (lane & 15), jp = 16 * tp + (lane & 15);
        u32 oa[7], ob[7], oc[7];
#pragma unroll
        for (int s = 0; s < 7; ++s) {
          const int h = 4 * s + kq;
          const bool inv = h < NF && jp < NF;
          oa[s] = opq((h < NF && jr < NF) ? L_W2S + (h * NF + jr) * 2 : L_W2C + 63 * 4);
          ob[s] = opq(inv ? L_V3 + (h * NF + jp) * 4 : L_W2C + ((h < NF && jp == NF) ? h : 63) * 4);
          oc[s] = opq(inv ? L_V3 + 2816 + (h * NF + jp) * 4 : L_W2C + 63 * 4);
        }
        team_arrive(cnt + C_V3, lane);
        team_wait<0>(cnt + C_V3, 4);
        STAMP(2);
        u32 av[7]; float bv[7], bw[7];
#pragma unroll
        for (int s = 0; s < 7; ++s) { av[s] = *(LAS uint16_t*)(lds + oa[s]); bv[s] = *(LAS float*)(lds + ob[s]); bw[s] = *(LAS float*)(lds + oc[s]); }
#pragma unroll
        for (int s = 0; s < 7; ++s) bv[s] += bw[s];
        f32x4 u = {0.f, 0.f, 0.f, 0.f}, u2 = {0.f, 0.f, 0.f, 0.f};
#pragma unroll
        for (int s = 0; s < 7; ++s) {
          if (s & 1) u2 = __builtin_amdgcn_mfma_f32_16x16x4f32(bflo(av[s]), bv[s], u2, 0, 0, 0);
          else u = __builtin_amdgcn_mfma_f32_16x16x4f32(bflo(av[s]), bv[s], u, 0, 0, 0);
        }
        u += u2;
#pragma unroll
        for (int e = 0; e < 4; ++e) {
          const int jo = 16 * tj + 4 * kq + e;
          const float val = (jo == NF && jp == NF) ? bflo(w1i) : u[e];
          *(LAS float*)(lds + L_U + (jo * UP + jp) * 4) = val;
        }
        team_arrive(cnt + C_U, lane);
        if (w == 0) {
          team_wait<0>(cnt + C_U, 4);
          STAMP(3);
          LAS float* U = (LAS float*)(lds + L_U);
          float e[12];
#pragma unroll
          for (int m = 0; m < 3; ++m) {
            e[4 * m] = U[tb[m].x & 0xffff]; e[4 * m + 1] = U[tb[m].x >> 16]; e[4 * m + 2] = U[tb[m].y & 0xffff]; e[4 * m + 3] = U[tb[m].y >> 16];
          }
#pragma unroll
          for (int m = 0; m < 3; ++m)
            __builtin_amdgcn_raw_buffer_store_b32(packbf(e[4 * m] + e[4 * m + 1], e[4 * m + 2] + e[4 * m + 3]), rws, WS_PB + i * 768 + (lane + 64 * m) * 4, 0, 16);
          const u64 flagb = mkflag(get_tag(cnt));
          drain_vm();
          if (lane == 0) store_flag(a.ws, WS_FB + i * FL, flagb);
          STAMP(4);
        }
      } else {
        const int i = (p - NF) * 4 + w;
        const bool prod = p < 33 && i < NF;
        u32 v[6]; u32x4 tb[3];
#pragma unroll
        for (int m = 0; m < 6; ++m) { const int d = lane + 64 * m; v[m] = __builtin_amdgcn_raw_buffer_load_b32(rwc, (prod && d < 338) ? i * 1352 + d * 4 : OOB, 0, 0); }
#pragma unroll
        for (int m = 0; m < 3; ++m) tb[m] = *(const GAS u32x4*)SYMTAB.w[lane + 64 * m];
        __builtin_amdgcn_sched_barrier(0);
        STAMP(1);
        if (prod) {
          LAS char* raw = lds + L_RAW + w * 2048;
#pragma unroll
          for (int m = 0; m < 6; ++m) *(LAS u32*)(raw + (lane + 64 * m) * 4) = v[m];
          asm volatile("s_waitcnt lgkmcnt(0)" ::: "memory");
          LAS uint16_t* R = (LAS uint16_t*)raw;
          u32 e[12];
#pragma unroll
          for (int m = 0; m < 3; ++m) {
            e[4 * m] = R[tb[m].z & 0xffff]; e[4 * m + 1] = R[tb[m].z >> 16]; e[4 * m + 2] = R[tb[m].w & 0xffff]; e[4 * m + 3] = R[tb[m].w >> 16];
          }
#pragma unroll
          for (int m = 0; m < 3; ++m)
            __builtin_amdgcn_raw_buffer_store_b32(packbf(bflo(e[4 * m]) + bflo(e[4 * m + 1]), bflo(e[4 * m + 2]) + bflo(e[4 * m + 3])), rws,
                                                  WS_PA + i * 768 + (lane + 64 * m) * 4, 0, 16);
          const u64 flag0 = mkflag(get_tag(cnt));
          drain_vm();
          if (lane == 0) store_flag(a.ws, WS_FA + i * FL, flag0);
          STAMP(4);
        }
        if (p == 47 && w == 0) {
          const u32 tag0 = get_tag(cnt);
          const u64 flag0 = mkflag(tag0);
          __builtin_amdgcn_s_sleep(32);
#pragma unroll 1
          for (int m = 0; m < 4; ++m) poll_flags<16>(a.ws + WS_HI + (lane + 64 * m) * FL, flag0);
          if (lane == 0) __hip_atomic_store((GAS u32*)(a.ws + WS_E), tag0, __ATOMIC_RELAXED, __HIP_MEMORY_SCOPE_AGENT);
        }
      }
  }
}

}

extern "C" void kernel_launch(void* const* d_in, const int* in_sizes, int n_in, void* d_out, int out_size,
                              void* d_ws, size_t ws_size, hipStream_t stream) {
  using namespace xdfm;
  Args a;
  a.idx = (const int*)d_in[0]; a.table = (const uint16_t*)d_in[1]; a.wc = (const uint16_t*)d_in[2];
  a.wco = (const uint16_t*)d_in[3]; a.w1 = (const uint16_t*)d_in[4]; a.b1 = (const uint16_t*)d_in[5];
  a.w2 = (const uint16_t*)d_in[6]; a.bo = (const uint16_t*)d_in[7]; a.out = (uint16_t*)d_out; a.ws = (char*)d_ws;
  a.table_bytes = (u32)in_sizes[1] * 2u; a.pad_ = 0;
  (void)hipFuncSetAttribute((const void*)xdfm_kernel, hipFuncAttributeMaxDynamicSharedMemorySize, L_TOTAL);
  xdfm_kernel<<<256, NWAVE * 64, L_TOTAL, stream>>>(a);
}
```

```cpp
#include <hip/hip_runtime.h>
#include <hip/hip_bf16.h>
#include <stdint.h>

#define P_ADELAY 8
#define P_BDELAY 8
#define P_WW 10
#define P_PQ 16
#define P_XI 10
#define P_XDELAY 12
#define P_CG 4
#define P_ROT 1
#define P_TOUCH 1
#define P_W0 6
#define P_CPF 1
#define P_XW 12
namespace xdfm {

typedef unsigned int u32;
typedef unsigned long long u64;
typedef u32 u32x2 __attribute__((ext_vector_type(2)));
typedef u32 u32x4 __attribute__((ext_vector_type(4)));
typedef short s16x4 __attribute__((ext_vector_type(4)));
typedef short s16x8 __attribute__((ext_vector_type(8)));
typedef float f32x2 __attribute__((ext_vector_type(2)));
typedef float f32x4 __attribute__((ext_vector_type(4)));
typedef float f32x16 __attribute__((ext_vector_type(16)));
typedef __bf16 bf16x2 __attribute__((ext_vector_type(2)));
typedef __bf16 bf16x8 __attribute__((ext_vector_type(8)));
#define GAS __attribute__((address_space(1)))
#define LAS __attribute__((address_space(3)))
typedef __amdgpu_buffer_rsrc_t rsrc_t;

constexpr int NF = 26, DIN = 832, NB = 1024, CUBE = 17576;

constexpr u32 WS_E = 0;
constexpr u32 FL = 128;
constexpr u32 WS_XF = 4096;
constexpr u32 WS_FA = 40960;
constexpr u32 WS_FB = 45056;
constexpr u32 WS_PA = 49152;
constexpr u32 WS_PB = 69632;
constexpr u32 WS_P = 131072;
constexpr u32 WS_X = 655360;
constexpr u32 WS_END = WS_X + 16 * 104 * 1024;
constexpr int NWAVE = 9;

constexpr u32 L_W = 0;
constexpr u32 APITCH = 432;
constexpr u32 L_A = 106496;
constexpr u32 L_B = L_A + 48 * APITCH;
constexpr u32 L_XS = L_B + 48 * APITCH;
constexpr u32 XS_S = 1824;
constexpr u32 L_CNT = L_XS + 4 * XS_S;
constexpr u32 L_TOTAL = L_CNT + 64;
constexpr u32 L_V3 = 0, L_W2C = 8192, L_W2S = 8448, L_U = 12288, L_RAW = 20480;
enum { C_TAGV = 12, C_TAG = 13, C_GISS = 14, C_ABRDY = 0, C_ABSTG = 1, C_W = 3, C_V3 = 4, C_U = 5, C_XISS = 6, C_XRDY = 7, C_WD = 8, C_RED = 10 };

constexpr int perm1(int j) { return j < 24 ? ((j & ~7) | ((j + 2) & 7)) : (j == 24 ? 25 : (j == 25 ? 24 : j)); }
struct Slots { int a[192]; int b[192]; bool v0[192]; bool v1[192]; int n; };
struct SlotBuilder {
  Slots s{}; bool vis[27][27] = {}; int n = 0;
  constexpr bool seen(int a, int b) const { return vis[a < b ? a : b][a < b ? b : a]; }
  constexpr void put(int a, int b) {
    s.a[n] = a; s.b[n] = b;
    s.v0[n] = !seen(a, b); vis[a < b ? a : b][a < b ? b : a] = true;
    const int na = perm1(a), nb = perm1(b);
    s.v1[n] = !seen(na, nb); vis[na < nb ? na : nb][na < nb ? nb : na] = true;
    ++n;
  }
};
constexpr int sg2(int m) { return perm1(perm1(2 * m)) >> 1; }
constexpr Slots make_slots() {
  SlotBuilder t{};
  for (int A = 0; A < 13; ++A)
    for (int B = A + 1; B < 13; ++B) {
      int ca = A, cb = B;
      while (!t.seen(2 * ca, 2 * cb)) {
        t.put(2 * ca, 2 * cb); t.put(2 * ca, 2 * cb + 1); t.put(2 * ca + 1, 2 * cb); t.put(2 * ca + 1, 2 * cb + 1);
        ca = sg2(ca); cb = sg2(cb);
      }
    }
  for (int A = 0; A < 13; ++A) {
    int ca = A;
    while (!t.seen(2 * ca, 2 * ca + 1)) {
      t.put(2 * ca, 2 * ca); t.put(2 * ca, 2 * ca + 1); t.put(2 * ca + 1, 2 * ca + 1); t.put(2 * ca + 1, 26);
      ca = sg2(ca);
    }
  }
  for (int a = 0; a < 27; ++a) { int ca = a; while (!t.seen(ca, 26)) { t.put(ca, 26); ca = perm1(perm1(ca)); } }
  t.s.n = t.n;
  for (int n = t.n; n < 192; ++n) { t.s.a[n] = 26; t.s.b[n] = 26; t.s.v0[n] = false; t.s.v1[n] = false; }
  return t.s;
}
constexpr Slots SL = make_slots();
static_assert(SL.n <= 192, "too many slots");

constexpr int UP = 33, ZB = 30 * UP, ZA = 676;
struct SymTab { u32 w[192][4]; };
constexpr SymTab make_symtab() {
  SymTab t{};
  for (int d = 0; d < 192; ++d) {
    int c = d >> 2, s = c >> 1, hh = c & 1;
    for (int e = 0; e < 2; ++e) {
      int sg = 8 * s + 2 * (d & 3) + e;
      int a = hh ? perm1(SL.a[sg]) : SL.a[sg], b = hh ? perm1(SL.b[sg]) : SL.b[sg];
      bool v = hh ? SL.v1[sg] : SL.v0[sg];
      u32 b1 = ZB, b2 = ZB, a1 = ZA, a2 = ZA;
      if (v) {
        b1 = a * UP + b; b2 = a == b ? ZB : b * UP + a;
        if (a < 26 && b < 26) { a1 = a * 26 + b; a2 = a == b ? ZA : b * 26 + a; }
      }
      t.w[d][e] = b1 | (b2 << 16);
      t.w[d][2 + e] = a1 | (a2 << 16);
    }
  }
  return t;
}
__device__ const SymTab SYMTAB = make_symtab();

constexpr u32 OOB = 0x7FFFFF00u;
constexpr u32 CODE_PART = 20;
constexpr u32 SPIN_MAX = 1u << 18;

__device__ __forceinline__ float bflo(u32 v) { return __uint_as_float(v << 16); }
__device__ __forceinline__ float bfhi(u32 v) { return __uint_as_float(v & 0xffff0000u); }
__device__ __forceinline__ u32 packbf(float a, float b) {
  f32x2 f = {a, b};
  bf16x2 z = __builtin_convertvector(f, bf16x2);
  return __builtin_bit_cast(u32, z);
}
__device__ __forceinline__ float mulf(float a, float b) { float r; asm("v_mul_f32 %0, %1, %2" : "=v"(r) : "v"(a), "v"(b)); return r; }
template <int E> __device__ __forceinline__ f32x2 pkmul_bc(f32x2 a, f32x2 b) {
  f32x2 r; asm("v_pk_mul_f32 %0, %1, %2 op_sel:[%3,0] op_sel_hi:[%3,1]" : "=v"(r) : "v"(a), "v"(b), "i"(E)); return r;
}
__device__ __forceinline__ float dot2bf(u32 a, u32 b, float c) {
  return __builtin_amdgcn_fdot2_f32_bf16(__builtin_bit_cast(bf16x2, a), __builtin_bit_cast(bf16x2, b), c, false);
}
__device__ __forceinline__ rsrc_t mkrs(const void* p, u32 bytes) { return __builtin_amdgcn_make_buffer_rsrc((void*)p, 0, bytes, 0x00020000); }
__device__ __forceinline__ u64 mkflag(u32 tag) { return (u64)tag | ((u64)(tag * 0x9E3779B1u + 0x7F4A7C15u) << 32); }
__device__ __forceinline__ void drain_vm() { asm volatile("s_waitcnt vmcnt(0)" ::: "memory"); }
__device__ __forceinline__ void lds_inc(LAS u32* cnt) {
  u64 keep;
  asm volatile("s_mov_b64 %0, exec\n\ts_mov_b64 exec, 1\n\tds_add_u32 %1, %2\n\ts_mov_b64 exec, %0"
               : "=&s"(keep) : "v"((u32)(size_t)cnt), "v"(1u) : "memory");
}
__device__ __forceinline__ void team_arrive(LAS u32* cnt, int) {
  asm volatile("s_waitcnt lgkmcnt(0)" ::: "memory");
  lds_inc(cnt);
}
__device__ __forceinline__ float sum_rows(float v) {
  auto a = __builtin_amdgcn_permlane32_swap(__float_as_uint(v), __float_as_uint(v), false, false);
  v = __uint_as_float(a[0]) + __uint_as_float(a[1]);
  auto b = __builtin_amdgcn_permlane16_swap(__float_as_uint(v), __float_as_uint(v), false, false);
  return __uint_as_float(b[0]) + __uint_as_float(b[1]);
}
template <int CTRL> __device__ __forceinline__ float dpp_add(float v) {
  return v + __uint_as_float(__builtin_amdgcn_update_dpp(0u, __float_as_uint(v), CTRL, 0xf, 0xf, true));
}
__device__ __forceinline__ float sum_row16(float v) {
  v = dpp_add<0x128>(v);
  v = dpp_add<0x124>(v);
  v = dpp_add<0x122>(v);
  v = dpp_add<0x121>(v);
  return v;
}

#define STAMP(k) do {} while (0)

struct Args {
  const int* idx; const uint16_t* table; const uint16_t* wc; const uint16_t* wco; const uint16_t* w1;
  const uint16_t* b1; const uint16_t* w2; const uint16_t* bo; uint16_t* out; char* ws;
  u32 table_bytes; u32 pad_;
};
#define CAS __attribute__((address_space(4)))

template <int SLEEP> __device__ __forceinline__ void team_wait(LAS u32* cnt, u32 target) {
  u32 v, s, n = SPIN_MAX;
  asm volatile(
      "1:\n\tds_read_b32 %0, %3\n\ts_waitcnt lgkmcnt(0)\n\tv_readfirstlane_b32 %1, %0\n\ts_cmp_ge_u32 %1, %4\n\ts_cbranch_scc1 2f\n\t"
      "s_sleep %5\n\ts_sub_u32 %2, %2, 1\n\ts_cbranch_scc0 1b\n2:"
      : "=&v"(v), "=&s"(s), "+s"(n) : "v"((u32)(size_t)cnt), "s"(target), "i"(SLEEP) : "memory", "scc");
}
template <int SLEEP> __device__ __forceinline__ void poll_flags(const char* p, u64 want) {
  u64 v; u32 n = SPIN_MAX;
  asm volatile(
      "1:\n\tglobal_load_dwordx2 %0, %2, off sc1\n\ts_waitcnt vmcnt(0)\n\tv_cmp_ne_u64 vcc, %3, %0\n\ts_nop 4\n\ts_cbranch_vccz 2f\n\t"
      "s_sleep %4\n\ts_sub_u32 %1, %1, 1\n\ts_cbranch_scc0 1b\n2:"
      : "=&v"(v), "+s"(n) : "v"(p), "s"(want), "i"(SLEEP) : "memory", "scc", "vcc");
}
__device__ __forceinline__ void lds_barrier() {
  asm volatile("s_waitcnt lgkmcnt(0)" ::: "memory");
  __builtin_amdgcn_s_barrier();
  asm volatile("" ::: "memory");
}
__device__ __forceinline__ u32 load_epoch(const char* ws) {
  return __hip_atomic_load((const GAS u32*)(ws + WS_E), __ATOMIC_RELAXED, __HIP_MEMORY_SCOPE_AGENT);
}
__device__ __forceinline__ void store_flag(char* ws, u32 off, u64 flag) {
  __hip_atomic_store((GAS u64*)(ws + off), flag, __ATOMIC_RELAXED, __HIP_MEMORY_SCOPE_AGENT);
}
__device__ __forceinline__ u32 get_tag(LAS u32* cnt) {
  team_wait<0>(cnt + C_TAG, 1);
  return __builtin_amdgcn_readfirstlane(cnt[C_TAGV]);
}
__device__ __forceinline__ u32 opq(u32 v) { asm("" : "+v"(v)); return v; }

__global__ void __launch_bounds__(576, 3) xdfm_kernel(Args a) {
  extern __shared__ __attribute__((aligned(16))) char lds_raw[];
  LAS char* lds = (LAS char*)lds_raw;
  const int tid = threadIdx.x, lane = tid & 63;
  LAS u32* cnt = (LAS u32*)(lds + L_CNT);
  u32 cpf = 0;
  if (P_CPF && tid >= 512) {
    u64 pc;
    asm volatile("s_getpc_b64 %0" : "=s"(pc));
    const u32 ln = ((blockIdx.x >> 3) & 7) * CODE_PART + (lane < CODE_PART ? lane : 0);
    const char* cp = (const char*)(pc & ~(u64)255) + ln * 128;
    asm volatile("global_load_dword %0, %1, off" : "+v"(cpf) : "v"(cp));
  }
  if (tid < 16) cnt[tid] = 0;
  const int pw = __builtin_amdgcn_readfirstlane(tid >> 6), w = pw == 0 ? 8 : pw - 1;
  const int ftid = w * 64 + lane;
  const int b = blockIdx.x, j = b >> 3;
  const int jg = j < 6 ? (j >= 3 ? 1 : 0) : (j >= 19 ? 1 : 0);
  const int g = (b & 7) * 2 + jg, r = j < 6 ? 13 + j - 3 * jg : j - 6 - 13 * jg;
  asm volatile("" :: "s"(a.idx), "s"(a.table), "s"(a.wc), "s"(a.wco), "s"(a.w1), "s"(a.b1), "s"(a.w2), "s"(a.bo), "s"(a.out), "s"(a.ws), "s"(a.table_bytes));
  lds_barrier();
  const rsrc_t rws = mkrs(a.ws, WS_END);
  const rsrc_t ridx = mkrs(a.idx, NB * NF * 4), rtab = mkrs(a.table, a.table_bytes);
  const int rot = P_ROT ? 2 * ((r + 5 * g) % 13) : 0;

  if (w == 8) {
    __builtin_amdgcn_s_setprio(3);
    u32 tag;
    if (r < 13) {
      u32 id[4]; u32x4 xg[8];
#pragma unroll
      for (int m = 0; m < 2; ++m) {
        const int p0 = 4 * r + 2 * m, t = p0 >= NF ? 1 : 0, f0 = p0 - t * NF;
        const u32x2 ia = __builtin_amdgcn_raw_buffer_load_b64(ridx, (lane & 31) * (NF * 4), ((g * 64 + t * 32) * NF + f0) * 4, 0);
        id[2 * m] = ia.x; id[2 * m + 1] = ia.y;
      }
      const u32 ep = load_epoch(a.ws);
#pragma unroll
      for (int k = 0; k < 8; ++k) xg[k] = __builtin_amdgcn_raw_buffer_load_b128(rtab, id[k >> 1] * 64 + (k & 1) * 32 + (lane >> 5) * 16, 0, 0);
      lds_inc(cnt + C_GISS);
      STAMP(1);
      tag = __builtin_amdgcn_readfirstlane(ep) + 1u;
      cnt[C_TAGV] = tag;
      team_arrive(cnt + C_TAG, lane);
#pragma unroll
      for (int k = 0; k < 8; ++k) __builtin_amdgcn_raw_buffer_store_b128(xg[k], rws, WS_X + lane * 16, (g * 104 + r * 8 + k) * 1024, 16);
      STAMP(2);
      drain_vm();
    } else {
      const u32 ep = load_epoch(a.ws);
      tag = __builtin_amdgcn_readfirstlane(ep) + 1u;
      cnt[C_TAGV] = tag;
      team_arrive(cnt + C_TAG, lane);
    }
    const u64 flag = mkflag(tag);
    if (lane == 0) store_flag(a.ws, WS_XF + (g * 16 + r) * FL, flag);
    STAMP(3);
    if (r < 13) {
      __builtin_amdgcn_s_sleep(P_XDELAY);
      poll_flags<2>(a.ws + WS_XF + (g * 16 + (lane < 13 ? lane : 0)) * FL, flag);
      lds_inc(cnt + C_XRDY);
    }
    STAMP(4);
    __builtin_amdgcn_s_sleep(P_BDELAY);
    poll_flags<6>(a.ws + (lane < 32 ? WS_FA : WS_FB) + ((lane & 31) < NF ? (lane & 31) : 0) * FL, flag);
    lds_inc(cnt + C_ABRDY);
    STAMP(5);
  } else if (w >= 4) {
    const int cw = w - 4, smp = g * 64 + r * 4 + cw;
    u32 id[2]; u32x4 xr[2];
    if (r < 13) __builtin_amdgcn_s_setprio(2);
#pragma unroll
    for (int m = 0; m < 2; ++m) { const int f = 16 * m + (lane >> 2); id[m] = __builtin_amdgcn_raw_buffer_load_b32(ridx, f < NF ? f * 4 : OOB, smp * NF * 4, 0); }
    u32 touch = 0;
    if (P_TOUCH && cw == 1) {
      const char* tp = lane < 7 ? (const char*)a.table + (size_t)lane * (a.table_bytes / 7 & ~3u)
                                : (lane == 7 ? a.ws + WS_P : (lane == 8 ? a.ws + WS_PA : (lane == 9 ? a.ws + WS_X + g * 104 * 1024 : (const char*)a.out)));
      asm volatile("global_load_dword %0, %1, off" : "+v"(touch) : "v"(tp));
    }
    const size_t bop = (size_t)a.bo;
    const u32 bo = *(const CAS u32*)(bop & ~(size_t)3) >> ((bop & 2) * 8);
#pragma unroll
    for (int m = 0; m < 2; ++m) { const int f = 16 * m + (lane >> 2); xr[m] = __builtin_amdgcn_raw_buffer_load_b128(rtab, f < NF ? id[m] * 64 + (lane & 3) * 16 : OOB, 0, 0); }
    __builtin_amdgcn_s_setprio(0);
    STAMP(1);
    f32x2 xp[14];
    {
      const u32 o = L_XS + cw * XS_S + (lane >> 2) * 64 + (lane & 3) * 16;
      *(LAS u32x4*)(lds + o) = xr[0];
      u32x4 x1 = xr[1];
      if ((lane >> 2) == NF - 16) x1 = (u32x4){0x3F803F80u, 0x3F803F80u, 0x3F803F80u, 0x3F803F80u};
      *(LAS u32x4*)(lds + opq((lane >> 2) < 12 ? o + 1024 : L_XS + cw * XS_S + 27 * 64 + (lane & 3) * 16)) = x1;
      asm volatile("s_waitcnt lgkmcnt(0)" :: "v"(touch), "v"(cpf) : "memory");
      const int hh = lane >> 5, q = (lane >> 2) & 3, pp = lane & 3;
      const u32 base = L_XS + cw * XS_S + ((lane >> 4) & 1) * 32 + pp * 8;
      const u32 a0 = base + (hh ? q + 2 : q) * 64, a1 = base + (hh ? ((q + 6) & 7) : q + 4) * 64;
      const u32 a6 = base + (24 + ((hh && q < 2) ? (q ^ 1) : q)) * 64;
#pragma unroll
      for (int t = 0; t < 7; ++t) {
        s16x4 y = __builtin_amdgcn_ds_read_tr16_b64_v4i16((LAS s16x4*)(lds + (t < 6 ? ((t & 1) ? a1 : a0) + (t >> 1) * 512 : a6)));
        u32x2 u = __builtin_bit_cast(u32x2, y);
        xp[2 * t] = (f32x2){bflo(u.x), bfhi(u.x)}; xp[2 * t + 1] = (f32x2){bflo(u.y), bfhi(u.y)};
      }
    }
    bf16x8 z[24];
#pragma unroll
    for (int s = 0; s < 24; ++s) {
      u32x4 zz;
#pragma unroll
      for (int e = 0; e < 4; ++e) {
        const int s1 = 8 * s + 2 * e, s2 = s1 + 1;
        const int a1 = SL.a[s1], b1 = SL.b[s1], a2 = SL.a[s2], b2 = SL.b[s2];
        if (a1 == a2 && (b1 & 1) == 0 && b1 < 26 && b2 == b1 + 1) {
          const f32x2 pr = (a1 & 1) ? pkmul_bc<1>(xp[a1 >> 1], xp[b1 >> 1]) : pkmul_bc<0>(xp[a1 >> 1], xp[b1 >> 1]);
          zz[e] = packbf(pr[0], pr[1]);
        } else {
          const float p1 = b1 == 26 ? xp[a1 >> 1][a1 & 1] : mulf(xp[a1 >> 1][a1 & 1], xp[b1 >> 1][b1 & 1]);
          const float p2 = b2 == 26 ? xp[a2 >> 1][a2 & 1] : mulf(xp[a2 >> 1][a2 & 1], xp[b2 >> 1][b2 & 1]);
          zz[e] = packbf(p1, p2);
        }
      }
      z[s] = __builtin_bit_cast(bf16x8, zz);
      asm volatile("" : "+v"(z[s]));
    }
    const u32 tag = get_tag(cnt);
    const u64 flag = mkflag(tag);
    STAMP(2);
    team_wait<1>(cnt + C_ABRDY, 1);
    STAMP(3);
    if (r < 13) team_wait<1>(cnt + C_XISS, 4);
    __builtin_amdgcn_s_setprio(2);
    STAMP(4);
    {
      u32x4 y[10];
#pragma unroll
      for (int m = 0; m < 10; ++m) {
        const u32 n = ((m % 5) * 4 + cw) * 64 + lane;
        y[m] = __builtin_amdgcn_raw_buffer_load_b128(rws, n < NF * 48 ? n * 16 : OOB, m < 5 ? WS_PA : WS_PB, 16);
      }
#pragma unroll
      for (int m = 0; m < 5; ++m) {
        const u32 n = (m * 4 + cw) * 64 + lane, i = (n * 1366u) >> 16, c = n - i * 48;
        const u32 o = opq(n < NF * 48 ? L_A + c * APITCH + i * 16 : L_A + 26 * 16);
        *(LAS u32x4*)(lds + o) = y[m];
        *(LAS u32x4*)(lds + o + (L_B - L_A)) = y[5 + m];
      }
      team_arrive(cnt + C_ABSTG, lane);
      team_wait<0>(cnt + C_ABSTG, 4);
    }
    __builtin_amdgcn_s_setprio(0);
    STAMP(5);
    const u32 afr = (lane >> 5) * APITCH + (lane & 31) * 16;
    const u32 tag2 = (u32)(flag >> 32);
    const int sg = r * 4 + cw;
    const u32 po = opq(lane < 26 ? WS_P + ((g * 26 + lane) * 2 + (sg >> 5)) * 512 + (sg & 31) * 16 : OOB);
    u32x4 yp;
    f32x16 acc1 = {}, acc2 = {};
    {
      constexpr int AD = 2;
      bf16x8 af[24], bf[24];
#pragma unroll
      for (int s = 0; s < AD; ++s) { af[s] = *(LAS bf16x8*)(lds + L_A + afr + s * 2 * APITCH); bf[s] = *(LAS bf16x8*)(lds + L_B + afr + s * 2 * APITCH); }
      __builtin_amdgcn_sched_barrier(0);
#pragma unroll
      for (int s = 0; s < 24; ++s) {
        if (s + AD < 24) { af[s + AD] = *(LAS bf16x8*)(lds + L_A + afr + (s + AD) * 2 * APITCH); bf[s + AD] = *(LAS bf16x8*)(lds + L_B + afr + (s + AD) * 2 * APITCH); }
        if (s == P_PQ) yp = __builtin_amdgcn_raw_buffer_load_b128(rws, po, 0, 16);
        acc1 = __builtin_amdgcn_mfma_f32_32x32x16_bf16(af[s], z[s], acc1, 0, 0, 0);
        acc2 = __builtin_amdgcn_mfma_f32_32x32x16_bf16(bf[s], z[s], acc2, 0, 0, 0);
        __builtin_amdgcn_sched_barrier(0);
      }
    }
    float sum = 0.f;
#pragma unroll
    for (int e = 0; e < 12; ++e) sum += acc1[e] * acc2[e];
    { const float t = acc1[12] * acc2[12] + acc1[13] * acc2[13]; sum += lane < 32 ? t : 0.f; }
    sum = sum_rows(sum_row16(sum));
    STAMP(6);
    for (u32 it = 0; it < SPIN_MAX; ++it) {
      const bool ok = lane >= 26 || (yp.y == tag && yp.w == tag2);
      if (__all(ok)) break;
      asm volatile("" ::: "memory");
      __builtin_amdgcn_s_sleep(1);
      yp = __builtin_amdgcn_raw_buffer_load_b128(rws, po, 0, 16);
    }
    float part = sum_rows(sum_row16(__uint_as_float(yp.x)));
    if (lane == 0) {
      const __bf16 ob = (__bf16)(sum + part + bflo(bo));
      a.out[smp] = __builtin_bit_cast(uint16_t, ob);
    }
    STAMP(7);
  } else if (r < 13) {
      const rsrc_t rw1 = mkrs(a.w1, DIN * DIN * 2);
      const int mh = w & 1, kh = w >> 1, hh = lane >> 5;
      {
        u32x4 v[26];
        const u32 vo = (lane >> 3) * 1664 + r * 128 + (lane & 7) * 16;
        auto wrow = [&](int i) {
          const int s = 13 * (w & 1) + (i >> 1) + rot;
          return kh * 416 + (s >= 26 ? s - 26 : s) * 16 + (i & 1) * 8;
        };
        const int c = lane & 7;
        const u32 lo = L_W + (w * 208 + (lane >> 3)) * 128 + (((c >> 1) ^ (((lane >> 4) & 1) << 1)) * 32) + (c & 1) * 16;
#pragma unroll
        for (int i = 0; i < P_W0; ++i) v[i] = __builtin_amdgcn_raw_buffer_load_b128(rw1, vo, wrow(i) * 1664, 0);
        __builtin_amdgcn_sched_barrier(0);
        team_wait<1>(cnt + C_GISS, 1);
#pragma unroll
        for (int i = P_W0; i < 26; ++i) v[i] = __builtin_amdgcn_raw_buffer_load_b128(rw1, vo, wrow(i) * 1664, 0);
        STAMP(1);
#pragma unroll
        for (int i = 0; i < 26; ++i) *(LAS u32x4*)(lds + lo + i * 1024) = v[i];
      }
      team_arrive(cnt + C_W, lane);
      const u32 tag = get_tag(cnt);
      const u64 flag = mkflag(tag);
      f32x16 acc[2];
      f32x2 wf[8];
      {
        const CAS u32* bp = (const CAS u32*)(a.b1 + r * 64 + kh * 32);
        const CAS u32* wp = (const CAS u32*)(a.w2 + r * 64 + kh * 32);
        u32 bs[16], ws_[16];
#pragma unroll
        for (int t = 0; t < 16; ++t) { bs[t] = bp[t]; ws_[t] = wp[t]; }
#pragma unroll
        for (int t = 0; t < 16; ++t) asm volatile("" : "+s"(bs[t]), "+s"(ws_[t]));
#pragma unroll
        for (int t = 0; t < 4; ++t) {
          const u32 b0 = hh ? bs[4 * t + 2] : bs[4 * t], b1 = hh ? bs[4 * t + 3] : bs[4 * t + 1];
          const u32 w0 = hh ? ws_[4 * t + 2] : ws_[4 * t], w1 = hh ? ws_[4 * t + 3] : ws_[4 * t + 1];
          acc[0][4 * t] = bflo(b0); acc[0][4 * t + 1] = bfhi(b0); acc[0][4 * t + 2] = bflo(b1); acc[0][4 * t + 3] = bfhi(b1);
          acc[1][4 * t] = 0.f; acc[1][4 * t + 1] = 0.f; acc[1][4 * t + 2] = 0.f; acc[1][4 * t + 3] = 0.f;
          wf[2 * t] = (f32x2){bflo(w0), bfhi(w0)}; wf[2 * t + 1] = (f32x2){bflo(w1), bfhi(w1)};
          asm volatile("" : "+v"(wf[2 * t]), "+v"(wf[2 * t + 1]));
        }
      }
      STAMP(2);
      team_wait<0>(cnt + C_XRDY, 1);
      STAMP(3);
      constexpr int XW = P_XW, FD = 3;
      u32x4 xv[26];
      auto ldx = [&](int i) { const int s = i + rot; xv[i] = __builtin_amdgcn_raw_buffer_load_b128(rws, WS_X + lane * 16, (g * 104 + mh * 52 + kh * 26 + (s >= 26 ? s - 26 : s)) * 1024, 16); };
#pragma unroll
      for (int i = 0; i < XW; ++i) ldx(i);
      team_wait<0>(cnt + C_W, 4);
      __builtin_amdgcn_s_setprio(1);
      {
        const int q = (lane >> 2) & 3, pp = lane & 3, gam = (lane >> 4) & 1;
        const u32 rb = L_W + kh * 26 * 2048 + (8 * hh + q) * 128 + pp * 8;
        const u32 ad0 = rb + (((2 * kh + gam) ^ (q & 2)) * 32), ad1 = rb + (((2 * (kh ^ 1) + gam) ^ (q & 2)) * 32);
        bf16x8 fr[26][2];
        auto ldf = [&](int i) {
#pragma unroll
          for (int t = 0; t < 2; ++t) {
            const u32 o = (t ? ad1 : ad0) + i * 2048;
            const s16x4 y0 = __builtin_amdgcn_ds_read_tr16_b64_v4i16((LAS s16x4*)(lds + o));
            const s16x4 y1 = __builtin_amdgcn_ds_read_tr16_b64_v4i16((LAS s16x4*)(lds + o + 512));
            fr[i][t] = __builtin_bit_cast(bf16x8, __builtin_shufflevector(y0, y1, 0, 1, 2, 3, 4, 5, 6, 7));
          }
        };
#pragma unroll
        for (int i = 0; i < FD; ++i) ldf(i);
        __builtin_amdgcn_sched_barrier(0);
#pragma unroll
        for (int i = 0; i < 26; ++i) {
          if (i + XW < 26) ldx(i + XW);
          if (i == P_XI) lds_inc(cnt + C_XISS);
          if (i == 8) lds_inc(cnt + C_WD + kh);
          if (i == 22) team_wait<0>(cnt + C_WD + kh, 2);
          if (i + FD < 26) ldf(i + FD);
          const bf16x8 xb = __builtin_bit_cast(bf16x8, xv[i]);
          acc[0] = __builtin_amdgcn_mfma_f32_32x32x16_bf16(fr[i][0], xb, acc[0], 0, 0, 0);
          acc[1] = __builtin_amdgcn_mfma_f32_32x32x16_bf16(fr[i][1], xb, acc[1], 0, 0, 0);
          __builtin_amdgcn_sched_barrier(0);
        }
      }
      STAMP(4);
      {
        const u32 so = L_W + kh * 26 * 2048 + mh * 4096 + lane * 16;
#pragma unroll
        for (int t = 0; t < 4; ++t) *(LAS f32x4*)(lds + so + t * 1024) = (f32x4){acc[1][4 * t], acc[1][4 * t + 1], acc[1][4 * t + 2], acc[1][4 * t + 3]};
        team_arrive(cnt + C_RED + mh, lane);
        team_wait<0>(cnt + C_RED + mh, 2);
      }
      STAMP(5);
      float part;
      {
        constexpr float K1 = -2.0f * 1.4426950408889634f * 0.7978845608028654f, K2 = K1 * 0.044715f;
        const u32 si = L_W + (kh ^ 1) * 26 * 2048 + mh * 4096 + lane * 16;
        f32x4 o[4];
#pragma unroll
        for (int t = 0; t < 4; ++t) o[t] = *(LAS f32x4*)(lds + si + t * 1024);
        f32x2 h[8], u[8], y[8];
#pragma unroll
        for (int m = 0; m < 8; ++m) {
          h[m] = (f32x2){acc[0][2 * m], acc[0][2 * m + 1]} + (f32x2){o[m >> 1][2 * (m & 1)], o[m >> 1][2 * (m & 1) + 1]};
          u[m] = (h[m] * h[m] * K2 + K1) * h[m];
          y[m] = h[m] * wf[m];
        }
#pragma unroll
        for (int m = 0; m < 8; ++m) { u[m][0] = __builtin_amdgcn_exp2f(u[m][0]); u[m][1] = __builtin_amdgcn_exp2f(u[m][1]); }
#pragma unroll
        for (int m = 0; m < 8; ++m) { u[m] = u[m] + 1.0f; }
#pragma unroll
        for (int m = 0; m < 8; ++m) { u[m][0] = __builtin_amdgcn_rcpf(u[m][0]); u[m][1] = __builtin_amdgcn_rcpf(u[m][1]); }
        f32x2 p0 = y[0] * u[0], p1 = y[1] * u[1];
#pragma unroll
        for (int m = 2; m < 8; m += 2) { p0 += y[m] * u[m]; p1 += y[m + 1] * u[m + 1]; }
        p0 += p1;
        part = p0[0] + p0[1];
      }
      {
        auto sw = __builtin_amdgcn_permlane32_swap(__float_as_uint(part), __float_as_uint(part), false, false);
        part = __uint_as_float(sw[0]) + __uint_as_float(sw[1]);
      }
      {
        const u32x4 o = {__float_as_uint(part), tag, __float_as_uint(part), (u32)(flag >> 32)};
        __builtin_amdgcn_raw_buffer_store_b128(o, rws, lane < 32 ? WS_P + ((g * 26 + r * 2 + kh) * 2 + mh) * 512 + lane * 16 : OOB, 0, 16);
      }
      STAMP(6);
  } else {
      const int p = g * 3 + r - 13;
      const rsrc_t rwc = mkrs(a.wc, 3 * CUBE * 2), rwo = mkrs(a.wco, 78 * 2);
      __builtin_amdgcn_s_setprio(3);
      if (p < NF) {
        const int i = p;
        u32x4 tb[3];
        const int hb = (w >> 1) * 13, el = (w & 1) * 64 + lane;
        const CAS u32* w3p = (const CAS u32*)(a.wco + 52);
        u32 w3[13];
#pragma unroll
        for (int h = 0; h < 13; ++h) w3[h] = w3p[h];
        u32x4 v[13];
#pragma unroll
        for (int h = 0; h < 13; ++h) v[h] = __builtin_amdgcn_raw_buffer_load_b128(rwc, el < 85 ? (2 * CUBE + hb * 676 + 8 * el) * 2 : OOB, h * 676 * 2, 0);
        u32 v2[2];
#pragma unroll
        for (int m = 0; m < 2; ++m) {
          const u32 d = ftid + 256 * m, h = (d * 5042u) >> 16, e = d - 13 * h;
          v2[m] = __builtin_amdgcn_raw_buffer_load_b32(rwc, d < 338 ? (CUBE + (h * NF + i) * NF) * 2 + e * 4 : OOB, 0, 0);
        }
        const u32 wq = __builtin_amdgcn_raw_buffer_load_b16(rwo, ftid < NF ? (NF + ftid) * 2 : OOB, 0, 0);
        const u32 w1i = __builtin_amdgcn_raw_buffer_load_b16(rwo, i * 2, 0, 0);
        if (w == 0) {
#pragma unroll
          for (int m = 0; m < 3; ++m) tb[m] = *(const GAS u32x4*)SYMTAB.w[lane + 64 * m];
        }
        __builtin_amdgcn_sched_barrier(0);
        STAMP(1);
        {
          float acc[8];
#pragma unroll
          for (int e = 0; e < 8; ++e) acc[e] = 0.f;
#pragma unroll
          for (int h = 0; h < 13; ++h) {
            const u32 we = (h & 1) ? w3[h >> 1] >> 16 : w3[h >> 1] & 0xffffu;
            const u32 wo = (h & 1) ? w3[(13 + h) >> 1] & 0xffffu : w3[(13 + h) >> 1] >> 16;
            const u32 wl = hb ? wo : we, wh = wl << 16;
#pragma unroll
            for (int e = 0; e < 4; ++e) { acc[2 * e] = dot2bf(v[h][e], wl, acc[2 * e]); acc[2 * e + 1] = dot2bf(v[h][e], wh, acc[2 * e + 1]); }
          }
          const u32 vo = opq(el < 85 ? L_V3 + (w >> 1) * 2816 + el * 32 : L_V3 + 5632 + lane * 32);
          *(LAS f32x4*)(lds + vo) = (f32x4){acc[0], acc[1], acc[2], acc[3]};
          *(LAS f32x4*)(lds + vo + 16) = (f32x4){acc[4], acc[5], acc[6], acc[7]};
#pragma unroll
          for (int m = 0; m < 2; ++m) *(LAS u32*)(lds + L_W2S + (ftid + 256 * m) * 4) = v2[m];
          if (w == 0) *(LAS u32*)(lds + L_W2C + lane * 4) = wq << 16;
        }
        const int tj = w >> 1, tp = w & 1, kq = lane >> 4;
        const int jr = 16 * tj + (lane & 15), jp = 16 * tp + (lane & 15);
        u32 oa[7], ob[7], oc[7];
#pragma unroll
        for (int s = 0; s < 7; ++s) {
          const int h = 4 * s + kq;
          const bool inv = h < NF && jp < NF;
          oa[s] = opq((h < NF && jr < NF) ? L_W2S + (h * NF + jr) * 2 : L_W2C + 63 * 4);
          ob[s] = opq(inv ? L_V3 + (h * NF + jp) * 4 : L_W2C + ((h < NF && jp == NF) ? h : 63) * 4);
          oc[s] = opq(inv ? L_V3 + 2816 + (h * NF + jp) * 4 : L_W2C + 63 * 4);
        }
        team_arrive(cnt + C_V3, lane);
        team_wait<0>(cnt + C_V3, 4);
        STAMP(2);
        u32 av[7]; float bv[7], bw[7];
#pragma unroll
        for (int s = 0; s < 7; ++s) { av[s] = *(LAS uint16_t*)(lds + oa[s]); bv[s] = *(LAS float*)(lds + ob[s]); bw[s] = *(LAS float*)(lds + oc[s]); }
#pragma unroll
        for (int s = 0; s < 7; ++s) bv[s] += bw[s];
        f32x4 u = {0.f, 0.f, 0.f, 0.f}, u2 = {0.f, 0.f, 0.f, 0.f};
#pragma unroll
        for (int s = 0; s < 7; ++s) {
          if (s & 1) u2 = __builtin_amdgcn_mfma_f32_16x16x4f32(bflo(av[s]), bv[s], u2, 0, 0, 0);
          else u = __builtin_amdgcn_mfma_f32_16x16x4f32(bflo(av[s]), bv[s], u, 0, 0, 0);
        }
        u += u2;
#pragma unroll
        for (int e = 0; e < 4; ++e) {
          const int jo = 16 * tj + 4 * kq + e;
          const float val = (jo == NF && jp == NF) ? bflo(w1i) : u[e];
          *(LAS float*)(lds + L_U + (jo * UP + jp) * 4) = val;
        }
        team_arrive(cnt + C_U, lane);
        if (w == 0) {
          team_wait<0>(cnt + C_U, 4);
          STAMP(3);
          LAS float* U = (LAS float*)(lds + L_U);
          float e[12];
#pragma unroll
          for (int m = 0; m < 3; ++m) {
            e[4 * m] = U[tb[m].x & 0xffff]; e[4 * m + 1] = U[tb[m].x >> 16]; e[4 * m + 2] = U[tb[m].y & 0xffff]; e[4 * m + 3] = U[tb[m].y >> 16];
          }
#pragma unroll
          for (int m = 0; m < 3; ++m)
            __builtin_amdgcn_raw_buffer_store_b32(packbf(e[4 * m] + e[4 * m + 1], e[4 * m + 2] + e[4 * m + 3]), rws, WS_PB + i * 768 + (lane + 64 * m) * 4, 0, 16);
          const u64 flagb = mkflag(get_tag(cnt));
          drain_vm();
          if (lane == 0) store_flag(a.ws, WS_FB + i * FL, flagb);
          STAMP(4);
        }
      } else {
        const int i = (p - NF) * 4 + w;
        const bool prod = p < 33 && i < NF;
        u32 v[6]; u32x4 tb[3];
#pragma unroll
        for (int m = 0; m < 6; ++m) { const int d = lane + 64 * m; v[m] = __builtin_amdgcn_raw_buffer_load_b32(rwc, (prod && d < 338) ? i * 1352 + d * 4 : OOB, 0, 0); }
#pragma unroll
        for (int m = 0; m < 3; ++m) tb[m] = *(const GAS u32x4*)SYMTAB.w[lane + 64 * m];
        __builtin_amdgcn_sched_barrier(0);
        STAMP(1);
        if (prod) {
          LAS char* raw = lds + L_RAW + w * 2048;
#pragma unroll
          for (int m = 0; m < 6; ++m) *(LAS u32*)(raw + (lane + 64 * m) * 4) = v[m];
          asm volatile("s_waitcnt lgkmcnt(0)" ::: "memory");
          LAS uint16_t* R = (LAS uint16_t*)raw;
          u32 e[12];
#pragma unroll
          for (int m = 0; m < 3; ++m) {
            e[4 * m] = R[tb[m].z & 0xffff]; e[4 * m + 1] = R[tb[m].z >> 16]; e[4 * m + 2] = R[tb[m].w & 0xffff]; e[4 * m + 3] = R[tb[m].w >> 16];
          }
#pragma unroll
          for (int m = 0; m < 3; ++m)
            __builtin_amdgcn_raw_buffer_store_b32(packbf(bflo(e[4 * m]) + bflo(e[4 * m + 1]), bflo(e[4 * m + 2]) + bflo(e[4 * m + 3])), rws,
                                                  WS_PA + i * 768 + (lane + 64 * m) * 4, 0, 16);
          const u64 flag0 = mkflag(get_tag(cnt));
          drain_vm();
          if (lane == 0) store_flag(a.ws, WS_FA + i * FL, flag0);
          STAMP(4);
        }
        if (p == 47 && w == 0) {
          const u32 tag0 = get_tag(cnt);
          const u64 flag0 = mkflag(tag0);
          __builtin_amdgcn_s_sleep(32);
#pragma unroll 1
          for (int m = 0; m < 4; ++m) poll_flags<16>(a.ws + WS_XF + (lane + 64 * m) * FL, flag0);
          if (lane == 0) __hip_atomic_store((GAS u32*)(a.ws + WS_E), tag0, __ATOMIC_RELAXED, __HIP_MEMORY_SCOPE_AGENT);
        }
      }
  }
}

}

extern "C" void kernel_launch(void* const* d_in, const int* in_sizes, int n_in, void* d_out, int out_size,
                              void* d_ws, size_t ws_size, hipStream_t stream) {
  using namespace xdfm;
  Args a;
  a.idx = (const int*)d_in[0]; a.table = (const uint16_t*)d_in[1]; a.wc = (const uint16_t*)d_in[2];
  a.wco = (const uint16_t*)d_in[3]; a.w1 = (const uint16_t*)d_in[4]; a.b1 = (const uint16_t*)d_in[5];
  a.w2 = (const uint16_t*)d_in[6]; a.bo = (const uint16_t*)d_in[7]; a.out = (uint16_t*)d_out; a.ws = (char*)d_ws;
  a.table_bytes = (u32)in_sizes[1] * 2u; a.pad_ = 0;
  (void)hipFuncSetAttribute((const void*)xdfm_kernel, hipFuncAttributeMaxDynamicSharedMemorySize, L_TOTAL);
  xdfm_kernel<<<256, NWAVE * 64, L_TOTAL, stream>>>(a);
}
```

```cpp
#include <hip/hip_runtime.h>
#include <hip/hip_bf16.h>
#include <stdint.h>

#define P_ADELAY 8
#define P_BDELAY 8
#define P_WW 10
#define P_PQ 16
#define P_XI 10
#define P_XDELAY 12
#define P_CG 4
#define P_ROT 1
#define P_TOUCH 1
#define P_W0 6
#define P_CPF 1
#define P_XW 12
namespace xdfm {

typedef unsigned int u32;
typedef unsigned long long u64;
typedef u32 u32x2 __attribute__((ext_vector_type(2)));
typedef u32 u32x4 __attribute__((ext_vector_type(4)));
typedef short s16x4 __attribute__((ext_vector_type(4)));
typedef short s16x8 __attribute__((ext_vector_type(8)));
typedef float f32x2 __attribute__((ext_vector_type(2)));
typedef float f32x4 __attribute__((ext_vector_type(4)));
typedef float f32x16 __attribute__((ext_vector_type(16)));
typedef __bf16 bf16x2 __attribute__((ext_vector_type(2)));
typedef __bf16 bf16x8 __attribute__((ext_vector_type(8)));
#define GAS __attribute__((address_space(1)))
#define LAS __attribute__((address_space(3)))
typedef __amdgpu_buffer_rsrc_t rsrc_t;

constexpr int NF = 26, DIN = 832, NB = 1024, CUBE = 17576;

constexpr u32 WS_E = 0;
constexpr u32 FL = 128;
constexpr u32 WS_XF = 4096;
constexpr u32 WS_FA = 40960;
constexpr u32 WS_FB = 45056;
constexpr u32 WS_PA = 49152;
constexpr u32 WS_PB = 69632;
constexpr u32 WS_P = 131072;
constexpr u32 WS_X = 655360;
constexpr u32 WS_END = WS_X + 16 * 104 * 1024;
constexpr int NWAVE = 10;

constexpr u32 L_W = 0;
constexpr u32 APITCH = 432;
constexpr u32 L_A = 106496;
constexpr u32 L_B = L_A + 48 * APITCH;
constexpr u32 L_XS = L_B + 48 * APITCH;
constexpr u32 XS_S = 1824;
constexpr u32 L_CNT = L_XS + 4 * XS_S;
constexpr u32 L_TOTAL = L_CNT + 64;
constexpr u32 L_V3 = 0, L_W2C = 8192, L_W2S = 8448, L_U = 12288, L_RAW = 20480;
enum { C_TAGV = 12, C_TAG = 13, C_GISS = 14, C_ABRDY = 0, C_ABSTG = 1, C_W = 3, C_V3 = 4, C_U = 5, C_XISS = 6, C_XRDY = 7, C_WD = 8, C_RED = 10 };

constexpr int perm1(int j) { return j < 24 ? ((j & ~7) | ((j + 2) & 7)) : (j == 24 ? 25 : (j == 25 ? 24 : j)); }
struct Slots { int a[192]; int b[192]; bool v0[192]; bool v1[192]; int n; };
struct SlotBuilder {
  Slots s{}; bool vis[27][27] = {}; int n = 0;
  constexpr bool seen(int a, int b) const { return vis[a < b ? a : b][a < b ? b : a]; }
  constexpr void put(int a, int b) {
    s.a[n] = a; s.b[n] = b;
    s.v0[n] = !seen(a, b); vis[a < b ? a : b][a < b ? b : a] = true;
    const int na = perm1(a), nb = perm1(b);
    s.v1[n] = !seen(na, nb); vis[na < nb ? na : nb][na < nb ? nb : na] = true;
    ++n;
  }
};
constexpr int sg2(int m) { return perm1(perm1(2 * m)) >> 1; }
constexpr Slots make_slots() {
  SlotBuilder t{};
  for (int A = 0; A < 13; ++A)
    for (int B = A + 1; B < 13; ++B) {
      int ca = A, cb = B;
      while (!t.seen(2 * ca, 2 * cb)) {
        t.put(2 * ca, 2 * cb); t.put(2 * ca, 2 * cb + 1); t.put(2 * ca + 1, 2 * cb); t.put(2 * ca + 1, 2 * cb + 1);
        ca = sg2(ca); cb = sg2(cb);
      }
    }
  for (int A = 0; A < 13; ++A) {
    int ca = A;
    while (!t.seen(2 * ca, 2 * ca + 1)) {
      t.put(2 * ca, 2 * ca); t.put(2 * ca, 2 * ca + 1); t.put(2 * ca + 1, 2 * ca + 1); t.put(2 * ca + 1, 26);
      ca = sg2(ca);
    }
  }
  for (int a = 0; a < 27; ++a) { int ca = a; while (!t.seen(ca, 26)) { t.put(ca, 26); ca = perm1(perm1(ca)); } }
  t.s.n = t.n;
  for (int n = t.n; n < 192; ++n) { t.s.a[n] = 26; t.s.b[n] = 26; t.s.v0[n] = false; t.s.v1[n] = false; }
  return t.s;
}
constexpr Slots SL = make_slots();
static_assert(SL.n <= 192, "too many slots");

constexpr int UP = 33, ZB = 30 * UP, ZA = 676;
struct SymTab { u32 w[192][4]; };
constexpr SymTab make_symtab() {
  SymTab t{};
  for (int d = 0; d < 192; ++d) {
    int c = d >> 2, s = c >> 1, hh = c & 1;
    for (int e = 0; e < 2; ++e) {
      int sg = 8 * s + 2 * (d & 3) + e;
      int a = hh ? perm1(SL.a[sg]) : SL.a[sg], b = hh ? perm1(SL.b[sg]) : SL.b[sg];
      bool v = hh ? SL.v1[sg] : SL.v0[sg];
      u32 b1 = ZB, b2 = ZB, a1 = ZA, a2 = ZA;
      if (v) {
        b1 = a * UP + b; b2 = a == b ? ZB : b * UP + a;
        if (a < 26 && b < 26) { a1 = a * 26 + b; a2 = a == b ? ZA : b * 26 + a; }
      }
      t.w[d][e] = b1 | (b2 << 16);
      t.w[d][2 + e] = a1 | (a2 << 16);
    }
  }
  return t;
}
__device__ const SymTab SYMTAB = make_symtab();

constexpr u32 OOB = 0x7FFFFF00u;
constexpr u32 CODE_PART = 20;
constexpr u32 SPIN_MAX = 1u << 18;

__device__ __forceinline__ float bflo(u32 v) { return __uint_as_float(v << 16); }
__device__ __forceinline__ float bfhi(u32 v) { return __uint_as_float(v & 0xffff0000u); }
__device__ __forceinline__ u32 packbf(float a, float b) {
  f32x2 f = {a, b};
  bf16x2 z = __builtin_convertvector(f, bf16x2);
  return __builtin_bit_cast(u32, z);
}
__device__ __forceinline__ float mulf(float a, float b) { float r; asm("v_mul_f32 %0, %1, %2" : "=v"(r) : "v"(a), "v"(b)); return r; }
template <int E> __device__ __forceinline__ f32x2 pkmul_bc(f32x2 a, f32x2 b) {
  f32x2 r; asm("v_pk_mul_f32 %0, %1, %2 op_sel:[%3,0] op_sel_hi:[%3,1]" : "=v"(r) : "v"(a), "v"(b), "i"(E)); return r;
}
__device__ __forceinline__ float dot2bf(u32 a, u32 b, float c) {
  return __builtin_amdgcn_fdot2_f32_bf16(__builtin_bit_cast(bf16x2, a), __builtin_bit_cast(bf16x2, b), c, false);
}
__device__ __forceinline__ rsrc_t mkrs(const void* p, u32 bytes) { return __builtin_amdgcn_make_buffer_rsrc((void*)p, 0, bytes, 0x00020000); }
__device__ __forceinline__ u64 mkflag(u32 tag) { return (u64)tag | ((u64)(tag * 0x9E3779B1u + 0x7F4A7C15u) << 32); }
__device__ __forceinline__ void drain_vm() { asm volatile("s_waitcnt vmcnt(0)" ::: "memory"); }
__device__ __forceinline__ void lds_inc(LAS u32* cnt) {
  u64 keep;
  asm volatile("s_mov_b64 %0, exec\n\ts_mov_b64 exec, 1\n\tds_add_u32 %1, %2\n\ts_mov_b64 exec, %0"
               : "=&s"(keep) : "v"((u32)(size_t)cnt), "v"(1u) : "memory");
}
__device__ __forceinline__ void team_arrive(LAS u32* cnt, int) {
  asm volatile("s_waitcnt lgkmcnt(0)" ::: "memory");
  lds_inc(cnt);
}
__device__ __forceinline__ float sum_rows(float v) {
  auto a = __builtin_amdgcn_permlane32_swap(__float_as_uint(v), __float_as_uint(v), false, false);
  v = __uint_as_float(a[0]) + __uint_as_float(a[1]);
  auto b = __builtin_amdgcn_permlane16_swap(__float_as_uint(v), __float_as_uint(v), false, false);
  return __uint_as_float(b[0]) + __uint_as_float(b[1]);
}
template <int CTRL> __device__ __forceinline__ float dpp_add(float v) {
  return v + __uint_as_float(__builtin_amdgcn_update_dpp(0u, __float_as_uint(v), CTRL, 0xf, 0xf, true));
}
__device__ __forceinline__ float sum_row16(float v) {
  v = dpp_add<0x128>(v);
  v = dpp_add<0x124>(v);
  v = dpp_add<0x122>(v);
  v = dpp_add<0x121>(v);
  return v;
}

#define STAMP(k) do {} while (0)

struct Args {
  const int* idx; const uint16_t* table; const uint16_t* wc; const uint16_t* wco; const uint16_t* w1;
  const uint16_t* b1; const uint16_t* w2; const uint16_t* bo; uint16_t* out; char* ws;
  u32 table_bytes; u32 pad_;
};
#define CAS __attribute__((address_space(4)))

template <int SLEEP> __device__ __forceinline__ void team_wait(LAS u32* cnt, u32 target) {
  u32 v, s, n = SPIN_MAX;
  asm volatile(
      "1:\n\tds_read_b32 %0, %3\n\ts_waitcnt lgkmcnt(0)\n\tv_readfirstlane_b32 %1, %0\n\ts_cmp_ge_u32 %1, %4\n\ts_cbranch_scc1 2f\n\t"
      "s_sleep %5\n\ts_sub_u32 %2, %2, 1\n\ts_cbranch_scc0 1b\n2:"
      : "=&v"(v), "=&s"(s), "+s"(n) : "v"((u32)(size_t)cnt), "s"(target), "i"(SLEEP) : "memory", "scc");
}
template <int SLEEP> __device__ __forceinline__ void poll_flags(const char* p, u64 want) {
  u64 v; u32 n = SPIN_MAX;
  asm volatile(
      "1:\n\tglobal_load_dwordx2 %0, %2, off sc1\n\ts_waitcnt vmcnt(0)\n\tv_cmp_ne_u64 vcc, %3, %0\n\ts_nop 4\n\ts_cbranch_vccz 2f\n\t"
      "s_sleep %4\n\ts_sub_u32 %1, %1, 1\n\ts_cbranch_scc0 1b\n2:"
      : "=&v"(v), "+s"(n) : "v"(p), "s"(want), "i"(SLEEP) : "memory", "scc", "vcc");
}
__device__ __forceinline__ void lds_barrier() {
  asm volatile("s_waitcnt lgkmcnt(0)" ::: "memory");
  __builtin_amdgcn_s_barrier();
  asm volatile("" ::: "memory");
}
__device__ __forceinline__ u32 load_epoch(const char* ws) {
  return __hip_atomic_load((const GAS u32*)(ws + WS_E), __ATOMIC_RELAXED, __HIP_MEMORY_SCOPE_AGENT);
}
__device__ __forceinline__ void store_flag(char* ws, u32 off, u64 flag) {
  __hip_atomic_store((GAS u64*)(ws + off), flag, __ATOMIC_RELAXED, __HIP_MEMORY_SCOPE_AGENT);
}
__device__ __forceinline__ u32 get_tag(LAS u32* cnt) {
  team_wait<0>(cnt + C_TAG, 1);
  return __builtin_amdgcn_readfirstlane(cnt[C_TAGV]);
}
__device__ __forceinline__ u32 opq(u32 v) { asm("" : "+v"(v)); return v; }

__global__ void __launch_bounds__(640, 3) xdfm_kernel(Args a) {
  extern __shared__ __attribute__((aligned(16))) char lds_raw[];
  LAS char* lds = (LAS char*)lds_raw;
  const int tid = threadIdx.x, lane = tid & 63;
  LAS u32* cnt = (LAS u32*)(lds + L_CNT);
  u32 cpf = 0;
  if (P_CPF && tid >= 512 && tid < 576) {
    u64 pc;
    asm volatile("s_getpc_b64 %0" : "=s"(pc));
    const u32 ln = ((blockIdx.x >> 3) & 7) * CODE_PART + (lane < CODE_PART ? lane : 0);
    const char* cp = (const char*)(pc & ~(u64)255) + ln * 128;
    asm volatile("global_load_dword %0, %1, off" : "+v"(cpf) : "v"(cp));
  }
  if (tid < 16) cnt[tid] = 0;
  const int pw = __builtin_amdgcn_readfirstlane(tid >> 6), w = pw == 0 ? 8 : (pw == 9 ? 9 : pw - 1);
  const int ftid = w * 64 + lane;
  const int b = blockIdx.x, j = b >> 3;
  const int jg = j < 6 ? (j >= 3 ? 1 : 0) : (j >= 19 ? 1 : 0);
  const int g = (b & 7) * 2 + jg, r = j < 6 ? 13 + j - 3 * jg : j - 6 - 13 * jg;
  asm volatile("" :: "s"(a.idx), "s"(a.table), "s"(a.wc), "s"(a.wco), "s"(a.w1), "s"(a.b1), "s"(a.w2), "s"(a.bo), "s"(a.out), "s"(a.ws), "s"(a.table_bytes));
  lds_barrier();
  const rsrc_t rws = mkrs(a.ws, WS_END);
  const rsrc_t ridx = mkrs(a.idx, NB * NF * 4), rtab = mkrs(a.table, a.table_bytes);
  const int rot = P_ROT ? 2 * ((r + 5 * g) % 13) : 0;

  if (w == 9) {
    if (b == 250) {
      team_wait<1>(cnt + C_GISS, 1);
#pragma unroll 1
      for (int i = 0; i < 6; ++i) __builtin_amdgcn_s_sleep(127);
    }
    return;
  }
  if (w == 8) {
    __builtin_amdgcn_s_setprio(3);
    u32 tag;
    if (r < 13) {
      u32 id[4]; u32x4 xg[8];
#pragma unroll
      for (int m = 0; m < 2; ++m) {
        const int p0 = 4 * r + 2 * m, t = p0 >= NF ? 1 : 0, f0 = p0 - t * NF;
        const u32x2 ia = __builtin_amdgcn_raw_buffer_load_b64(ridx, (lane & 31) * (NF * 4), ((g * 64 + t * 32) * NF + f0) * 4, 0);
        id[2 * m] = ia.x; id[2 * m + 1] = ia.y;
      }
      const u32 ep = load_epoch(a.ws);
#pragma unroll
      for (int k = 0; k < 8; ++k) xg[k] = __builtin_amdgcn_raw_buffer_load_b128(rtab, id[k >> 1] * 64 + (k & 1) * 32 + (lane >> 5) * 16, 0, 0);
      lds_inc(cnt + C_GISS);
      STAMP(1);
      tag = __builtin_amdgcn_readfirstlane(ep) + 1u;
      cnt[C_TAGV] = tag;
      team_arrive(cnt + C_TAG, lane);
#pragma unroll
      for (int k = 0; k < 8; ++k) __builtin_amdgcn_raw_buffer_store_b128(xg[k], rws, WS_X + lane * 16, (g * 104 + r * 8 + k) * 1024, 16);
      STAMP(2);
      drain_vm();
    } else {
      const u32 ep = load_epoch(a.ws);
      tag = __builtin_amdgcn_readfirstlane(ep) + 1u;
      cnt[C_TAGV] = tag;
      team_arrive(cnt + C_TAG, lane);
    }
    const u64 flag = mkflag(tag);
    if (lane == 0) store_flag(a.ws, WS_XF + (g * 16 + r) * FL, flag);
    STAMP(3);
    if (r < 13) {
      __builtin_amdgcn_s_sleep(P_XDELAY);
      poll_flags<2>(a.ws + WS_XF + (g * 16 + (lane < 13 ? lane : 0)) * FL, flag);
      lds_inc(cnt + C_XRDY);
    }
    STAMP(4);
    __builtin_amdgcn_s_sleep(P_BDELAY);
    poll_flags<6>(a.ws + (lane < 32 ? WS_FA : WS_FB) + ((lane & 31) < NF ? (lane & 31) : 0) * FL, flag);
    lds_inc(cnt + C_ABRDY);
    STAMP(5);
  } else if (w >= 4) {
    const int cw = w - 4, smp = g * 64 + r * 4 + cw;
    u32 id[2]; u32x4 xr[2];
    if (r < 13) __builtin_amdgcn_s_setprio(2);
#pragma unroll
    for (int m = 0; m < 2; ++m) { const int f = 16 * m + (lane >> 2); id[m] = __builtin_amdgcn_raw_buffer_load_b32(ridx, f < NF ? f * 4 : OOB, smp * NF * 4, 0); }
    u32 touch = 0;
    if (P_TOUCH && cw == 1) {
      const char* tp = lane < 7 ? (const char*)a.table + (size_t)lane * (a.table_bytes / 7 & ~3u)
                                : (lane == 7 ? a.ws + WS_P : (lane == 8 ? a.ws + WS_PA : (lane == 9 ? a.ws + WS_X + g * 104 * 1024 : (const char*)a.out)));
      asm volatile("global_load_dword %0, %1, off" : "+v"(touch) : "v"(tp));
    }
    const size_t bop = (size_t)a.bo;
    const u32 bo = *(const CAS u32*)(bop & ~(size_t)3) >> ((bop & 2) * 8);
#pragma unroll
    for (int m = 0; m < 2; ++m) { const int f = 16 * m + (lane >> 2); xr[m] = __builtin_amdgcn_raw_buffer_load_b128(rtab, f < NF ? id[m] * 64 + (lane & 3) * 16 : OOB, 0, 0); }
    __builtin_amdgcn_s_setprio(0);
    STAMP(1);
    f32x2 xp[14];
    {
      const u32 o = L_XS + cw * XS_S + (lane >> 2) * 64 + (lane & 3) * 16;
      *(LAS u32x4*)(lds + o) = xr[0];
      u32x4 x1 = xr[1];
      if ((lane >> 2) == NF - 16) x1 = (u32x4){0x3F803F80u, 0x3F803F80u, 0x3F803F80u, 0x3F803F80u};
      *(LAS u32x4*)(lds + opq((lane >> 2) < 12 ? o + 1024 : L_XS + cw * XS_S + 27 * 64 + (lane & 3) * 16)) = x1;
      asm volatile("s_waitcnt lgkmcnt(0)" :: "v"(touch), "v"(cpf) : "memory");
      const int hh = lane >> 5, q = (lane >> 2) & 3, pp = lane & 3;
      const u32 base = L_XS + cw * XS_S + ((lane >> 4) & 1) * 32 + pp * 8;
      const u32 a0 = base + (hh ? q + 2 : q) * 64, a1 = base + (hh ? ((q + 6) & 7) : q + 4) * 64;
      const u32 a6 = base + (24 + ((hh && q < 2) ? (q ^ 1) : q)) * 64;
#pragma unroll
      for (int t = 0; t < 7; ++t) {
        s16x4 y = __builtin_amdgcn_ds_read_tr16_b64_v4i16((LAS s16x4*)(lds + (t < 6 ? ((t & 1) ? a1 : a0) + (t >> 1) * 512 : a6)));
        u32x2 u = __builtin_bit_cast(u32x2, y);
        xp[2 * t] = (f32x2){bflo(u.x), bfhi(u.x)}; xp[2 * t + 1] = (f32x2){bflo(u.y), bfhi(u.y)};
      }
    }
    bf16x8 z[24];
#pragma unroll
    for (int s = 0; s < 24; ++s) {
      u32x4 zz;
#pragma unroll
      for (int e = 0; e < 4; ++e) {
        const int s1 = 8 * s + 2 * e, s2 = s1 + 1;
        const int a1 = SL.a[s1], b1 = SL.b[s1], a2 = SL.a[s2], b2 = SL.b[s2];
        if (a1 == a2 && (b1 & 1) == 0 && b1 < 26 && b2 == b1 + 1) {
          const f32x2 pr = (a1 & 1) ? pkmul_bc<1>(xp[a1 >> 1], xp[b1 >> 1]) : pkmul_bc<0>(xp[a1 >> 1], xp[b1 >> 1]);
          zz[e] = packbf(pr[0], pr[1]);
        } else {
          const float p1 = b1 == 26 ? xp[a1 >> 1][a1 & 1] : mulf(xp[a1 >> 1][a1 & 1], xp[b1 >> 1][b1 & 1]);
          const float p2 = b2 == 26 ? xp[a2 >> 1][a2 & 1] : mulf(xp[a2 >> 1][a2 & 1], xp[b2 >> 1][b2 & 1]);
          zz[e] = packbf(p1, p2);
        }
      }
      z[s] = __builtin_bit_cast(bf16x8, zz);
      asm volatile("" : "+v"(z[s]));
    }
    const u32 tag = get_tag(cnt);
    const u64 flag = mkflag(tag);
    STAMP(2);
    team_wait<1>(cnt + C_ABRDY, 1);
    STAMP(3);
    if (r < 13) team_wait<1>(cnt + C_XISS, 4);
    __builtin_amdgcn_s_setprio(2);
    STAMP(4);
    {
      u32x4 y[10];
#pragma unroll
      for (int m = 0; m < 10; ++m) {
        const u32 n = ((m % 5) * 4 + cw) * 64 + lane;
        y[m] = __builtin_amdgcn_raw_buffer_load_b128(rws, n < NF * 48 ? n * 16 : OOB, m < 5 ? WS_PA : WS_PB, 16);
      }
#pragma unroll
      for (int m = 0; m < 5; ++m) {
        const u32 n = (m * 4 + cw) * 64 + lane, i = (n * 1366u) >> 16, c = n - i * 48;
        const u32 o = opq(n < NF * 48 ? L_A + c * APITCH + i * 16 : L_A + 26 * 16);
        *(LAS u32x4*)(lds + o) = y[m];
        *(LAS u32x4*)(lds + o + (L_B - L_A)) = y[5 + m];
      }
      team_arrive(cnt + C_ABSTG, lane);
      team_wait<0>(cnt + C_ABSTG, 4);
    }
    __builtin_amdgcn_s_setprio(0);
    STAMP(5);
    const u32 afr = (lane >> 5) * APITCH + (lane & 31) * 16;
    const u32 tag2 = (u32)(flag >> 32);
    const int sg = r * 4 + cw;
    const u32 po = opq(lane < 26 ? WS_P + ((g * 26 + lane) * 2 + (sg >> 5)) * 512 + (sg & 31) * 16 : OOB);
    u32x4 yp;
    f32x16 acc1 = {}, acc2 = {};
    {
      constexpr int AD = 2;
      bf16x8 af[24], bf[24];
#pragma unroll
      for (int s = 0; s < AD; ++s) { af[s] = *(LAS bf16x8*)(lds + L_A + afr + s * 2 * APITCH); bf[s] = *(LAS bf16x8*)(lds + L_B + afr + s * 2 * APITCH); }
      __builtin_amdgcn_sched_barrier(0);
#pragma unroll
      for (int s = 0; s < 24; ++s) {
        if (s + AD < 24) { af[s + AD] = *(LAS bf16x8*)(lds + L_A + afr + (s + AD) * 2 * APITCH); bf[s + AD] = *(LAS bf16x8*)(lds + L_B + afr + (s + AD) * 2 * APITCH); }
        if (s == P_PQ) yp = __builtin_amdgcn_raw_buffer_load_b128(rws, po, 0, 16);
        acc1 = __builtin_amdgcn_mfma_f32_32x32x16_bf16(af[s], z[s], acc1, 0, 0, 0);
        acc2 = __builtin_amdgcn_mfma_f32_32x32x16_bf16(bf[s], z[s], acc2, 0, 0, 0);
        __builtin_amdgcn_sched_barrier(0);
      }
    }
    float sum = 0.f;
#pragma unroll
    for (int e = 0; e < 12; ++e) sum += acc1[e] * acc2[e];
    { const float t = acc1[12] * acc2[12] + acc1[13] * acc2[13]; sum += lane < 32 ? t : 0.f; }
    sum = sum_rows(sum_row16(sum));
    STAMP(6);
    for (u32 it = 0; it < SPIN_MAX; ++it) {
      const bool ok = lane >= 26 || (yp.y == tag && yp.w == tag2);
      if (__all(ok)) break;
      asm volatile("" ::: "memory");
      __builtin_amdgcn_s_sleep(1);
      yp = __builtin_amdgcn_raw_buffer_load_b128(rws, po, 0, 16);
    }
    float part = sum_rows(sum_row16(__uint_as_float(yp.x)));
    if (lane == 0) {
      const __bf16 ob = (__bf16)(sum + part + bflo(bo));
      a.out[smp] = __builtin_bit_cast(uint16_t, ob);
    }
    STAMP(7);
  } else if (r < 13) {
      const rsrc_t rw1 = mkrs(a.w1, DIN * DIN * 2);
      const int mh = w & 1, kh = w >> 1, hh = lane >> 5;
      {
        u32x4 v[26];
        const u32 vo = (lane >> 3) * 1664 + r * 128 + (lane & 7) * 16;
        auto wrow = [&](int i) {
          const int s = 13 * (w & 1) + (i >> 1) + rot;
          return kh * 416 + (s >= 26 ? s - 26 : s) * 16 + (i & 1) * 8;
        };
        const int c = lane & 7;
        const u32 lo = L_W + (w * 208 + (lane >> 3)) * 128 + (((c >> 1) ^ (((lane >> 4) & 1) << 1)) * 32) + (c & 1) * 16;
#pragma unroll
        for (int i = 0; i < P_W0; ++i) v[i] = __builtin_amdgcn_raw_buffer_load_b128(rw1, vo, wrow(i) * 1664, 0);
        __builtin_amdgcn_sched_barrier(0);
        team_wait<1>(cnt + C_GISS, 1);
#pragma unroll
        for (int i = P_W0; i < 26; ++i) v[i] = __builtin_amdgcn_raw_buffer_load_b128(rw1, vo, wrow(i) * 1664, 0);
        STAMP(1);
#pragma unroll
        for (int i = 0; i < 26; ++i) *(LAS u32x4*)(lds + lo + i * 1024) = v[i];
      }
      team_arrive(cnt + C_W, lane);
      const u32 tag = get_tag(cnt);
      const u64 flag = mkflag(tag);
      f32x16 acc[2];
      f32x2 wf[8];
      {
        const CAS u32* bp = (const CAS u32*)(a.b1 + r * 64 + kh * 32);
        const CAS u32* wp = (const CAS u32*)(a.w2 + r * 64 + kh * 32);
        u32 bs[16], ws_[16];
#pragma unroll
        for (int t = 0; t < 16; ++t) { bs[t] = bp[t]; ws_[t] = wp[t]; }
#pragma unroll
        for (int t = 0; t < 16; ++t) asm volatile("" : "+s"(bs[t]), "+s"(ws_[t]));
#pragma unroll
        for (int t = 0; t < 4; ++t) {
          const u32 b0 = hh ? bs[4 * t + 2] : bs[4 * t], b1 = hh ? bs[4 * t + 3] : bs[4 * t + 1];
          const u32 w0 = hh ? ws_[4 * t + 2] : ws_[4 * t], w1 = hh ? ws_[4 * t + 3] : ws_[4 * t + 1];
          acc[0][4 * t] = bflo(b0); acc[0][4 * t + 1] = bfhi(b0); acc[0][4 * t + 2] = bflo(b1); acc[0][4 * t + 3] = bfhi(b1);
          acc[1][4 * t] = 0.f; acc[1][4 * t + 1] = 0.f; acc[1][4 * t + 2] = 0.f; acc[1][4 * t + 3] = 0.f;
          wf[2 * t] = (f32x2){bflo(w0), bfhi(w0)}; wf[2 * t + 1] = (f32x2){bflo(w1), bfhi(w1)};
          asm volatile("" : "+v"(wf[2 * t]), "+v"(wf[2 * t + 1]));
        }
      }
      STAMP(2);
      team_wait<0>(cnt + C_XRDY, 1);
      STAMP(3);
      constexpr int XW = P_XW, FD = 3;
      u32x4 xv[26];
      auto ldx = [&](int i) { const int s = i + rot; xv[i] = __builtin_amdgcn_raw_buffer_load_b128(rws, WS_X + lane * 16, (g * 104 + mh * 52 + kh * 26 + (s >= 26 ? s - 26 : s)) * 1024, 16); };
#pragma unroll
      for (int i = 0; i < XW; ++i) ldx(i);
      team_wait<0>(cnt + C_W, 4);
      __builtin_amdgcn_s_setprio(1);
      {
        const int q = (lane >> 2) & 3, pp = lane & 3, gam = (lane >> 4) & 1;
        const u32 rb = L_W + kh * 26 * 2048 + (8 * hh + q) * 128 + pp * 8;
        const u32 ad0 = rb + (((2 * kh + gam) ^ (q & 2)) * 32), ad1 = rb + (((2 * (kh ^ 1) + gam) ^ (q & 2)) * 32);
        bf16x8 fr[26][2];
        auto ldf = [&](int i) {
#pragma unroll
          for (int t = 0; t < 2; ++t) {
            const u32 o = (t ? ad1 : ad0) + i * 2048;
            const s16x4 y0 = __builtin_amdgcn_ds_read_tr16_b64_v4i16((LAS s16x4*)(lds + o));
            const s16x4 y1 = __builtin_amdgcn_ds_read_tr16_b64_v4i16((LAS s16x4*)(lds + o + 512));
            fr[i][t] = __builtin_bit_cast(bf16x8, __builtin_shufflevector(y0, y1, 0, 1, 2, 3, 4, 5, 6, 7));
          }
        };
#pragma unroll
        for (int i = 0; i < FD; ++i) ldf(i);
        __builtin_amdgcn_sched_barrier(0);
#pragma unroll
        for (int i = 0; i < 26; ++i) {
          if (i + XW < 26) ldx(i + XW);
          if (i == P_XI) lds_inc(cnt + C_XISS);
          if (i == 8) lds_inc(cnt + C_WD + kh);
          if (i == 22) team_wait<0>(cnt + C_WD + kh, 2);
          if (i + FD < 26) ldf(i + FD);
          const bf16x8 xb = __builtin_bit_cast(bf16x8, xv[i]);
          acc[0] = __builtin_amdgcn_mfma_f32_32x32x16_bf16(fr[i][0], xb, acc[0], 0, 0, 0);
          acc[1] = __builtin_amdgcn_mfma_f32_32x32x16_bf16(fr[i][1], xb, acc[1], 0, 0, 0);
          __builtin_amdgcn_sched_barrier(0);
        }
      }
      STAMP(4);
      {
        const u32 so = L_W + kh * 26 * 2048 + mh * 4096 + lane * 16;
#pragma unroll
        for (int t = 0; t < 4; ++t) *(LAS f32x4*)(lds + so + t * 1024) = (f32x4){acc[1][4 * t], acc[1][4 * t + 1], acc[1][4 * t + 2], acc[1][4 * t + 3]};
        team_arrive(cnt + C_RED + mh, lane);
        team_wait<0>(cnt + C_RED + mh, 2);
      }
      STAMP(5);
      float part;
      {
        constexpr float K1 = -2.0f * 1.4426950408889634f * 0.7978845608028654f, K2 = K1 * 0.044715f;
        const u32 si = L_W + (kh ^ 1) * 26 * 2048 + mh * 4096 + lane * 16;
        f32x4 o[4];
#pragma unroll
        for (int t = 0; t < 4; ++t) o[t] = *(LAS f32x4*)(lds + si + t * 1024);
        f32x2 h[8], u[8], y[8];
#pragma unroll
        for (int m = 0; m < 8; ++m) {
          h[m] = (f32x2){acc[0][2 * m], acc[0][2 * m + 1]} + (f32x2){o[m >> 1][2 * (m & 1)], o[m >> 1][2 * (m & 1) + 1]};
          u[m] = (h[m] * h[m] * K2 + K1) * h[m];
          y[m] = h[m] * wf[m];
        }
#pragma unroll
        for (int m = 0; m < 8; ++m) { u[m][0] = __builtin_amdgcn_exp2f(u[m][0]); u[m][1] = __builtin_amdgcn_exp2f(u[m][1]); }
#pragma unroll
        for (int m = 0; m < 8; ++m) { u[m] = u[m] + 1.0f; }
#pragma unroll
        for (int m = 0; m < 8; ++m) { u[m][0] = __builtin_amdgcn_rcpf(u[m][0]); u[m][1] = __builtin_amdgcn_rcpf(u[m][1]); }
        f32x2 p0 = y[0] * u[0], p1 = y[1] * u[1];
#pragma unroll
        for (int m = 2; m < 8; m += 2) { p0 += y[m] * u[m]; p1 += y[m + 1] * u[m + 1]; }
        p0 += p1;
        part = p0[0] + p0[1];
      }
      {
        auto sw = __builtin_amdgcn_permlane32_swap(__float_as_uint(part), __float_as_uint(part), false, false);
        part = __uint_as_float(sw[0]) + __uint_as_float(sw[1]);
      }
      {
        const u32x4 o = {__float_as_uint(part), tag, __float_as_uint(part), (u32)(flag >> 32)};
        __builtin_amdgcn_raw_buffer_store_b128(o, rws, lane < 32 ? WS_P + ((g * 26 + r * 2 + kh) * 2 + mh) * 512 + lane * 16 : OOB, 0, 16);
      }
      STAMP(6);
  } else {
      const int p = g * 3 + r - 13;
      const rsrc_t rwc = mkrs(a.wc, 3 * CUBE * 2), rwo = mkrs(a.wco, 78 * 2);
      __builtin_amdgcn_s_setprio(3);
      if (p < NF) {
        const int i = p;
        u32x4 tb[3];
        const int hb = (w >> 1) * 13, el = (w & 1) * 64 + lane;
        const CAS u32* w3p = (const CAS u32*)(a.wco + 52);
        u32 w3[13];
#pragma unroll
        for (int h = 0; h < 13; ++h) w3[h] = w3p[h];
        u32x4 v[13];
#pragma unroll
        for (int h = 0; h < 13; ++h) v[h] = __builtin_amdgcn_raw_buffer_load_b128(rwc, el < 85 ? (2 * CUBE + hb * 676 + 8 * el) * 2 : OOB, h * 676 * 2, 0);
        u32 v2[2];
#pragma unroll
        for (int m = 0; m < 2; ++m) {
          const u32 d = ftid + 256 * m, h = (d * 5042u) >> 16, e = d - 13 * h;
          v2[m] = __builtin_amdgcn_raw_buffer_load_b32(rwc, d < 338 ? (CUBE + (h * NF + i) * NF) * 2 + e * 4 : OOB, 0, 0);
        }
        const u32 wq = __builtin_amdgcn_raw_buffer_load_b16(rwo, ftid < NF ? (NF + ftid) * 2 : OOB, 0, 0);
        const u32 w1i = __builtin_amdgcn_raw_buffer_load_b16(rwo, i * 2, 0, 0);
        if (w == 0) {
#pragma unroll
          for (int m = 0; m < 3; ++m) tb[m] = *(const GAS u32x4*)SYMTAB.w[lane + 64 * m];
        }
        __builtin_amdgcn_sched_barrier(0);
        STAMP(1);
        {
          float acc[8];
#pragma unroll
          for (int e = 0; e < 8; ++e) acc[e] = 0.f;
#pragma unroll
          for (int h = 0; h < 13; ++h) {
            const u32 we = (h & 1) ? w3[h >> 1] >> 16 : w3[h >> 1] & 0xffffu;
            const u32 wo = (h & 1) ? w3[(13 + h) >> 1] & 0xffffu : w3[(13 + h) >> 1] >> 16;
            const u32 wl = hb ? wo : we, wh = wl << 16;
#pragma unroll
            for (int e = 0; e < 4; ++e) { acc[2 * e] = dot2bf(v[h][e], wl, acc[2 * e]); acc[2 * e + 1] = dot2bf(v[h][e], wh, acc[2 * e + 1]); }
          }
          const u32 vo = opq(el < 85 ? L_V3 + (w >> 1) * 2816 + el * 32 : L_V3 + 5632 + lane * 32);
          *(LAS f32x4*)(lds + vo) = (f32x4){acc[0], acc[1], acc[2], acc[3]};
          *(LAS f32x4*)(lds + vo + 16) = (f32x4){acc[4], acc[5], acc[6], acc[7]};
#pragma unroll
          for (int m = 0; m < 2; ++m) *(LAS u32*)(lds + L_W2S + (ftid + 256 * m) * 4) = v2[m];
          if (w == 0) *(LAS u32*)(lds + L_W2C + lane * 4) = wq << 16;
        }
        const int tj = w >> 1, tp = w & 1, kq = lane >> 4;
        const int jr = 16 * tj + (lane & 15), jp = 16 * tp + (lane & 15);
        u32 oa[7], ob[7], oc[7];
#pragma unroll
        for (int s = 0; s < 7; ++s) {
          const int h = 4 * s + kq;
          const bool inv = h < NF && jp < NF;
          oa[s] = opq((h < NF && jr < NF) ? L_W2S + (h * NF + jr) * 2 : L_W2C + 63 * 4);
          ob[s] = opq(inv ? L_V3 + (h * NF + jp) * 4 : L_W2C + ((h < NF && jp == NF) ? h : 63) * 4);
          oc[s] = opq(inv ? L_V3 + 2816 + (h * NF + jp) * 4 : L_W2C + 63 * 4);
        }
        team_arrive(cnt + C_V3, lane);
        team_wait<0>(cnt + C_V3, 4);
        STAMP(2);
        u32 av[7]; float bv[7], bw[7];
#pragma unroll
        for (int s = 0; s < 7; ++s) { av[s] = *(LAS uint16_t*)(lds + oa[s]); bv[s] = *(LAS float*)(lds + ob[s]); bw[s] = *(LAS float*)(lds + oc[s]); }
#pragma unroll
        for (int s = 0; s < 7; ++s) bv[s] += bw[s];
        f32x4 u = {0.f, 0.f, 0.f, 0.f}, u2 = {0.f, 0.f, 0.f, 0.f};
#pragma unroll
        for (int s = 0; s < 7; ++s) {
          if (s & 1) u2 = __builtin_amdgcn_mfma_f32_16x16x4f32(bflo(av[s]), bv[s], u2, 0, 0, 0);
          else u = __builtin_amdgcn_mfma_f32_16x16x4f32(bflo(av[s]), bv[s], u, 0, 0, 0);
        }
        u += u2;
#pragma unroll
        for (int e = 0; e < 4; ++e) {
          const int jo = 16 * tj + 4 * kq + e;
          const float val = (jo == NF && jp == NF) ? bflo(w1i) : u[e];
          *(LAS float*)(lds + L_U + (jo * UP + jp) * 4) = val;
        }
        team_arrive(cnt + C_U, lane);
        if (w == 0) {
          team_wait<0>(cnt + C_U, 4);
          STAMP(3);
          LAS float* U = (LAS float*)(lds + L_U);
          float e[12];
#pragma unroll
          for (int m = 0; m < 3; ++m) {
            e[4 * m] = U[tb[m].x & 0xffff]; e[4 * m + 1] = U[tb[m].x >> 16]; e[4 * m + 2] = U[tb[m].y & 0xffff]; e[4 * m + 3] = U[tb[m].y >> 16];
          }
#pragma unroll
          for (int m = 0; m < 3; ++m)
            __builtin_amdgcn_raw_buffer_store_b32(packbf(e[4 * m] + e[4 * m + 1], e[4 * m + 2] + e[4 * m + 3]), rws, WS_PB + i * 768 + (lane + 64 * m) * 4, 0, 16);
          const u64 flagb = mkflag(get_tag(cnt));
          drain_vm();
          if (lane == 0) store_flag(a.ws, WS_FB + i * FL, flagb);
          STAMP(4);
        }
      } else {
        const int i = (p - NF) * 4 + w;
        const bool prod = p < 33 && i < NF;
        u32 v[6]; u32x4 tb[3];
#pragma unroll
        for (int m = 0; m < 6; ++m) { const int d = lane + 64 * m; v[m] = __builtin_amdgcn_raw_buffer_load_b32(rwc, (prod && d < 338) ? i * 1352 + d * 4 : OOB, 0, 0); }
#pragma unroll
        for (int m = 0; m < 3; ++m) tb[m] = *(const GAS u32x4*)SYMTAB.w[lane + 64 * m];
        __builtin_amdgcn_sched_barrier(0);
        STAMP(1);
        if (prod) {
          LAS char* raw = lds + L_RAW + w * 2048;
#pragma unroll
          for (int m = 0; m < 6; ++m) *(LAS u32*)(raw + (lane + 64 * m) * 4) = v[m];
          asm volatile("s_waitcnt lgkmcnt(0)" ::: "memory");
          LAS uint16_t* R = (LAS uint16_t*)raw;
          u32 e[12];
#pragma unroll
          for (int m = 0; m < 3; ++m) {
            e[4 * m] = R[tb[m].z & 0xffff]; e[4 * m + 1] = R[tb[m].z >> 16]; e[4 * m + 2] = R[tb[m].w & 0xffff]; e[4 * m + 3] = R[tb[m].w >> 16];
          }
#pragma unroll
          for (int m = 0; m < 3; ++m)
            __builtin_amdgcn_raw_buffer_store_b32(packbf(bflo(e[4 * m]) + bflo(e[4 * m + 1]), bflo(e[4 * m + 2]) + bflo(e[4 * m + 3])), rws,
                                                  WS_PA + i * 768 + (lane + 64 * m) * 4, 0, 16);
          const u64 flag0 = mkflag(get_tag(cnt));
          drain_vm();
          if (lane == 0) store_flag(a.ws, WS_FA + i * FL, flag0);
          STAMP(4);
        }
        if (p == 47 && w == 0) {
          const u32 tag0 = get_tag(cnt);
          const u64 flag0 = mkflag(tag0);
          __builtin_amdgcn_s_sleep(32);
#pragma unroll 1
          for (int m = 0; m < 4; ++m) poll_flags<16>(a.ws + WS_XF + (lane + 64 * m) * FL, flag0);
          if (lane == 0) __hip_atomic_store((GAS u32*)(a.ws + WS_E), tag0, __ATOMIC_RELAXED, __HIP_MEMORY_SCOPE_AGENT);
        }
      }
  }
}

}

extern "C" void kernel_launch(void* const* d_in, const int* in_sizes, int n_in, void* d_out, int out_size,
                              void* d_ws, size_t ws_size, hipStream_t stream) {
  using namespace xdfm;
  Args a;
  a.idx = (const int*)d_in[0]; a.table = (const uint16_t*)d_in[1]; a.wc = (const uint16_t*)d_in[2];
  a.wco = (const uint16_t*)d_in[3]; a.w1 = (const uint16_t*)d_in[4]; a.b1 = (const uint16_t*)d_in[5];
  a.w2 = (const uint16_t*)d_in[6]; a.bo = (const uint16_t*)d_in[7]; a.out = (uint16_t*)d_out; a.ws = (char*)d_ws;
  a.table_bytes = (u32)in_sizes[1] * 2u; a.pad_ = 0;
  (void)hipFuncSetAttribute((const void*)xdfm_kernel, hipFuncAttributeMaxDynamicSharedMemorySize, L_TOTAL);
  xdfm_kernel<<<256, NWAVE * 64, L_TOTAL, stream>>>(a);
}
```

```cpp
#include <hip/hip_runtime.h>
#include <hip/hip_bf16.h>
#include <stdint.h>

#define P_ADELAY 8
#define P_BDELAY 8
#define P_WW 10
#define P_PQ 16
#define P_XI 10
#define P_XDELAY 12
#define P_CG 4
#define P_ROT 1
#define P_TOUCH 1
#define P_W0 6
#define P_CPF 1
#define P_XW 12
namespace xdfm {

typedef unsigned int u32;
typedef unsigned long long u64;
typedef u32 u32x2 __attribute__((ext_vector_type(2)));
typedef u32 u32x4 __attribute__((ext_vector_type(4)));
typedef short s16x4 __attribute__((ext_vector_type(4)));
typedef short s16x8 __attribute__((ext_vector_type(8)));
typedef float f32x2 __attribute__((ext_vector_type(2)));
typedef float f32x4 __attribute__((ext_vector_type(4)));
typedef float f32x16 __attribute__((ext_vector_type(16)));
typedef __bf16 bf16x2 __attribute__((ext_vector_type(2)));
typedef __bf16 bf16x8 __attribute__((ext_vector_type(8)));
#define GAS __attribute__((address_space(1)))
#define LAS __attribute__((address_space(3)))
typedef __amdgpu_buffer_rsrc_t rsrc_t;

constexpr int NF = 26, DIN = 832, NB = 1024, CUBE = 17576;

constexpr u32 WS_E = 0;
constexpr u32 FL = 128;
constexpr u32 WS_XF = 4096;
constexpr u32 WS_FA = 40960;
constexpr u32 WS_FB = 45056;
constexpr u32 WS_PA = 49152;
constexpr u32 WS_PB = 69632;
constexpr u32 WS_P = 131072;
constexpr u32 WS_X = 655360;
constexpr u32 WS_END = WS_X + 16 * 104 * 1024;
constexpr int NWAVE = 9;

constexpr u32 L_W = 0;
constexpr u32 APITCH = 432;
constexpr u32 L_A = 106496;
constexpr u32 L_B = L_A + 48 * APITCH;
constexpr u32 L_XS = L_B + 48 * APITCH;
constexpr u32 XS_S = 1824;
constexpr u32 L_CNT = L_XS + 4 * XS_S;
constexpr u32 L_TOTAL = L_CNT + 64;
constexpr u32 L_V3 = 0, L_W2C = 8192, L_W2S = 8448, L_U = 12288, L_RAW = 20480;
enum { C_TAGV = 12, C_TAG = 13, C_GISS = 14, C_ABRDY = 0, C_ABSTG = 1, C_W = 3, C_V3 = 4, C_U = 5, C_XISS = 6, C_XRDY = 7, C_WD = 8, C_RED = 10 };

constexpr int perm1(int j) { return j < 24 ? ((j & ~7) | ((j + 2) & 7)) : (j == 24 ? 25 : (j == 25 ? 24 : j)); }
struct Slots { int a[192]; int b[192]; bool v0[192]; bool v1[192]; int n; };
struct SlotBuilder {
  Slots s{}; bool vis[27][27] = {}; int n = 0;
  constexpr bool seen(int a, int b) const { return vis[a < b ? a : b][a < b ? b : a]; }
  constexpr void put(int a, int b) {
    s.a[n] = a; s.b[n] = b;
    s.v0[n] = !seen(a, b); vis[a < b ? a : b][a < b ? b : a] = true;
    const int na = perm1(a), nb = perm1(b);
    s.v1[n] = !seen(na, nb); vis[na < nb ? na : nb][na < nb ? nb : na] = true;
    ++n;
  }
};
constexpr int sg2(int m) { return perm1(perm1(2 * m)) >> 1; }
constexpr Slots make_slots() {
  SlotBuilder t{};
  for (int A = 0; A < 13; ++A)
    for (int B = A + 1; B < 13; ++B) {
      int ca = A, cb = B;
      while (!t.seen(2 * ca, 2 * cb)) {
        t.put(2 * ca, 2 * cb); t.put(2 * ca, 2 * cb + 1); t.put(2 * ca + 1, 2 * cb); t.put(2 * ca + 1, 2 * cb + 1);
        ca = sg2(ca); cb = sg2(cb);
      }
    }
  for (int A = 0; A < 13; ++A) {
    int ca = A;
    while (!t.seen(2 * ca, 2 * ca + 1)) {
      t.put(2 * ca, 2 * ca); t.put(2 * ca, 2 * ca + 1); t.put(2 * ca + 1, 2 * ca + 1); t.put(2 * ca + 1, 26);
      ca = sg2(ca);
    }
  }
  for (int a = 0; a < 27; ++a) { int ca = a; while (!t.seen(ca, 26)) { t.put(ca, 26); ca = perm1(perm1(ca)); } }
  t.s.n = t.n;
  for (int n = t.n; n < 192; ++n) { t.s.a[n] = 26; t.s.b[n] = 26; t.s.v0[n] = false; t.s.v1[n] = false; }
  return t.s;
}
constexpr Slots SL = make_slots();
static_assert(SL.n <= 192, "too many slots");

constexpr int UP = 33, ZB = 30 * UP, ZA = 676;
struct SymTab { u32 w[192][4]; };
constexpr SymTab make_symtab() {
  SymTab t{};
  for (int d = 0; d < 192; ++d) {
    int c = d >> 2, s = c >> 1, hh = c & 1;
    for (int e = 0; e < 2; ++e) {
      int sg = 8 * s + 2 * (d & 3) + e;
      int a = hh ? perm1(SL.a[sg]) : SL.a[sg], b = hh ? perm1(SL.b[sg]) : SL.b[sg];
      bool v = hh ? SL.v1[sg] : SL.v0[sg];
      u32 b1 = ZB, b2 = ZB, a1 = ZA, a2 = ZA;
      if (v) {
        b1 = a * UP + b; b2 = a == b ? ZB : b * UP + a;
        if (a < 26 && b < 26) { a1 = a * 26 + b; a2 = a == b ? ZA : b * 26 + a; }
      }
      t.w[d][e] = b1 | (b2 << 16);
      t.w[d][2 + e] = a1 | (a2 << 16);
    }
  }
  return t;
}
__device__ const SymTab SYMTAB = make_symtab();

constexpr u32 OOB = 0x7FFFFF00u;
constexpr u32 CODE_PART = 20;
constexpr u32 SPIN_MAX = 1u << 18;

__device__ __forceinline__ float bflo(u32 v) { return __uint_as_float(v << 16); }
__device__ __forceinline__ float bfhi(u32 v) { return __uint_as_float(v & 0xffff0000u); }
__device__ __forceinline__ u32 packbf(float a, float b) {
  f32x2 f = {a, b};
  bf16x2 z = __builtin_convertvector(f, bf16x2);
  return __builtin_bit_cast(u32, z);
}
__device__ __forceinline__ float mulf(float a, float b) { float r; asm("v_mul_f32 %0, %1, %2" : "=v"(r) : "v"(a), "v"(b)); return r; }
template <int E> __device__ __forceinline__ f32x2 pkmul_bc(f32x2 a, f32x2 b) {
  f32x2 r; asm("v_pk_mul_f32 %0, %1, %2 op_sel:[%3,0] op_sel_hi:[%3,1]" : "=v"(r) : "v"(a), "v"(b), "i"(E)); return r;
}
__device__ __forceinline__ float dot2bf(u32 a, u32 b, float c) {
  return __builtin_amdgcn_fdot2_f32_bf16(__builtin_bit_cast(bf16x2, a), __builtin_bit_cast(bf16x2, b), c, false);
}
__device__ __forceinline__ rsrc_t mkrs(const void* p, u32 bytes) { return __builtin_amdgcn_make_buffer_rsrc((void*)p, 0, bytes, 0x00020000); }
__device__ __forceinline__ u64 mkflag(u32 tag) { return (u64)tag | ((u64)(tag * 0x9E3779B1u + 0x7F4A7C15u) << 32); }
__device__ __forceinline__ void drain_vm() { asm volatile("s_waitcnt vmcnt(0)" ::: "memory"); }
__device__ __forceinline__ void lds_inc(LAS u32* cnt) {
  u64 keep;
  asm volatile("s_mov_b64 %0, exec\n\ts_mov_b64 exec, 1\n\tds_add_u32 %1, %2\n\ts_mov_b64 exec, %0"
               : "=&s"(keep) : "v"((u32)(size_t)cnt), "v"(1u) : "memory");
}
__device__ __forceinline__ void team_arrive(LAS u32* cnt, int) {
  asm volatile("s_waitcnt lgkmcnt(0)" ::: "memory");
  lds_inc(cnt);
}
__device__ __forceinline__ float sum_rows(float v) {
  auto a = __builtin_amdgcn_permlane32_swap(__float_as_uint(v), __float_as_uint(v), false, false);
  v = __uint_as_float(a[0]) + __uint_as_float(a[1]);
  auto b = __builtin_amdgcn_permlane16_swap(__float_as_uint(v), __float_as_uint(v), false, false);
  return __uint_as_float(b[0]) + __uint_as_float(b[1]);
}
template <int CTRL> __device__ __forceinline__ float dpp_add(float v) {
  return v + __uint_as_float(__builtin_amdgcn_update_dpp(0u, __float_as_uint(v), CTRL, 0xf, 0xf, true));
}
__device__ __forceinline__ float sum_row16(float v) {
  v = dpp_add<0x128>(v);
  v = dpp_add<0x124>(v);
  v = dpp_add<0x122>(v);
  v = dpp_add<0x121>(v);
  return v;
}

#define STAMP(k) do {} while (0)

struct Args {
  const int* idx; const uint16_t* table; const uint16_t* wc; const uint16_t* wco; const uint16_t* w1;
  const uint16_t* b1; const uint16_t* w2; const uint16_t* bo; uint16_t* out; char* ws;
  u32 table_bytes; u32 pad_;
};
#define CAS __attribute__((address_space(4)))

template <int SLEEP> __device__ __forceinline__ void team_wait(LAS u32* cnt, u32 target) {
  u32 v, s, n = SPIN_MAX;
  asm volatile(
      "1:\n\tds_read_b32 %0, %3\n\ts_waitcnt lgkmcnt(0)\n\tv_readfirstlane_b32 %1, %0\n\ts_cmp_ge_u32 %1, %4\n\ts_cbranch_scc1 2f\n\t"
      "s_sleep %5\n\ts_sub_u32 %2, %2, 1\n\ts_cbranch_scc0 1b\n2:"
      : "=&v"(v), "=&s"(s), "+s"(n) : "v"((u32)(size_t)cnt), "s"(target), "i"(SLEEP) : "memory", "scc");
}
template <int SLEEP> __device__ __forceinline__ void poll_flags(const char* p, u64 want) {
  u64 v; u32 n = SPIN_MAX;
  asm volatile(
      "1:\n\tglobal_load_dwordx2 %0, %2, off sc1\n\ts_waitcnt vmcnt(0)\n\tv_cmp_ne_u64 vcc, %3, %0\n\ts_nop 4\n\ts_cbranch_vccz 2f\n\t"
      "s_sleep %4\n\ts_sub_u32 %1, %1, 1\n\ts_cbranch_scc0 1b\n2:"
      : "=&v"(v), "+s"(n) : "v"(p), "s"(want), "i"(SLEEP) : "memory", "scc", "vcc");
}
__device__ __forceinline__ void lds_barrier() {
  asm volatile("s_waitcnt lgkmcnt(0)" ::: "memory");
  __builtin_amdgcn_s_barrier();
  asm volatile("" ::: "memory");
}
__device__ __forceinline__ u32 load_epoch(const char* ws) {
  return __hip_atomic_load((const GAS u32*)(ws + WS_E), __ATOMIC_RELAXED, __HIP_MEMORY_SCOPE_AGENT);
}
__device__ __forceinline__ void store_flag(char* ws, u32 off, u64 flag) {
  __hip_atomic_store((GAS u64*)(ws + off), flag, __ATOMIC_RELAXED, __HIP_MEMORY_SCOPE_AGENT);
}
__device__ __forceinline__ u32 get_tag(LAS u32* cnt) {
  team_wait<0>(cnt + C_TAG, 1);
  return __builtin_amdgcn_readfirstlane(cnt[C_TAGV]);
}
__device__ __forceinline__ u32 opq(u32 v) { asm("" : "+v"(v)); return v; }

__global__ void __launch_bounds__(576, 3) xdfm_kernel(Args a) {
  extern __shared__ __attribute__((aligned(16))) char lds_raw[];
  LAS char* lds = (LAS char*)lds_raw;
  const int tid = threadIdx.x, lane = tid & 63;
  LAS u32* cnt = (LAS u32*)(lds + L_CNT);
  u32 cpf = 0;
  if (P_CPF && tid >= 512) {
    u64 pc;
    asm volatile("s_getpc_b64 %0" : "=s"(pc));
    const u32 ln = ((blockIdx.x >> 3) & 7) * CODE_PART + (lane < CODE_PART ? lane : 0);
    const char* cp = (const char*)(pc & ~(u64)255) + ln * 128;
    asm volatile("global_load_dword %0, %1, off" : "+v"(cpf) : "v"(cp));
  }
  if (tid < 16) cnt[tid] = 0;
  const int pw = __builtin_amdgcn_readfirstlane(tid >> 6), w = pw == 0 ? 8 : pw - 1;
  const int ftid = w * 64 + lane;
  const int b = blockIdx.x, j = b >> 3;
  const int jg = j < 6 ? (j >= 3 ? 1 : 0) : (j >= 19 ? 1 : 0);
  const int g = (b & 7) * 2 + jg, r = j < 6 ? 13 + j - 3 * jg : j - 6 - 13 * jg;
  asm volatile("" :: "s"(a.idx), "s"(a.table), "s"(a.wc), "s"(a.wco), "s"(a.w1), "s"(a.b1), "s"(a.w2), "s"(a.bo), "s"(a.out), "s"(a.ws), "s"(a.table_bytes));
  lds_barrier();
  const rsrc_t rws = mkrs(a.ws, WS_END);
  const rsrc_t ridx = mkrs(a.idx, NB * NF * 4), rtab = mkrs(a.table, a.table_bytes);
  const int rot = P_ROT ? 2 * ((r + 5 * g) % 13) : 0;

  if (w < 4 && r >= 13) {
      const int p = g * 3 + r - 13;
      const rsrc_t rwc = mkrs(a.wc, 3 * CUBE * 2), rwo = mkrs(a.wco, 78 * 2);
      __builtin_amdgcn_s_setprio(3);
      if (p < NF) {
        const int i = p;
        u32x4 tb[3];
        const int hb = (w >> 1) * 13, el = (w & 1) * 64 + lane;
        const CAS u32* w3p = (const CAS u32*)(a.wco + 52);
        u32 w3[13];
#pragma unroll
        for (int h = 0; h < 13; ++h) w3[h] = w3p[h];
        u32x4 v[13];
#pragma unroll
        for (int h = 0; h < 13; ++h) v[h] = __builtin_amdgcn_raw_buffer_load_b128(rwc, el < 85 ? (2 * CUBE + hb * 676 + 8 * el) * 2 : OOB, h * 676 * 2, 0);
        u32 v2[2];
#pragma unroll
        for (int m = 0; m < 2; ++m) {
          const u32 d = ftid + 256 * m, h = (d * 5042u) >> 16, e = d - 13 * h;
          v2[m] = __builtin_amdgcn_raw_buffer_load_b32(rwc, d < 338 ? (CUBE + (h * NF + i) * NF) * 2 + e * 4 : OOB, 0, 0);
        }
        const u32 wq = __builtin_amdgcn_raw_buffer_load_b16(rwo, ftid < NF ? (NF + ftid) * 2 : OOB, 0, 0);
        const u32 w1i = __builtin_amdgcn_raw_buffer_load_b16(rwo, i * 2, 0, 0);
        if (w == 0) {
#pragma unroll
          for (int m = 0; m < 3; ++m) tb[m] = *(const GAS u32x4*)SYMTAB.w[lane + 64 * m];
        }
        __builtin_amdgcn_sched_barrier(0);
        STAMP(1);
        {
          float acc[8];
#pragma unroll
          for (int e = 0; e < 8; ++e) acc[e] = 0.f;
#pragma unroll
          for (int h = 0; h < 13; ++h) {
            const u32 we = (h & 1) ? w3[h >> 1] >> 16 : w3[h >> 1] & 0xffffu;
            const u32 wo = (h & 1) ? w3[(13 + h) >> 1] & 0xffffu : w3[(13 + h) >> 1] >> 16;
            const u32 wl = hb ? wo : we, wh = wl << 16;
#pragma unroll
            for (int e = 0; e < 4; ++e) { acc[2 * e] = dot2bf(v[h][e], wl, acc[2 * e]); acc[2 * e + 1] = dot2bf(v[h][e], wh, acc[2 * e + 1]); }
          }
          const u32 vo = opq(el < 85 ? L_V3 + (w >> 1) * 2816 + el * 32 : L_V3 + 5632 + lane * 32);
          *(LAS f32x4*)(lds + vo) = (f32x4){acc[0], acc[1], acc[2], acc[3]};
          *(LAS f32x4*)(lds + vo + 16) = (f32x4){acc[4], acc[5], acc[6], acc[7]};
#pragma unroll
          for (int m = 0; m < 2; ++m) *(LAS u32*)(lds + L_W2S + (ftid + 256 * m) * 4) = v2[m];
          if (w == 0) *(LAS u32*)(lds + L_W2C + lane * 4) = wq << 16;
        }
        const int tj = w >> 1, tp = w & 1, kq = lane >> 4;
        const int jr = 16 * tj + (lane & 15), jp = 16 * tp + (lane & 15);
        u32 oa[7], ob[7], oc[7];
#pragma unroll
        for (int s = 0; s < 7; ++s) {
          const int h = 4 * s + kq;
          const bool inv = h < NF && jp < NF;
          oa[s] = opq((h < NF && jr < NF) ? L_W2S + (h * NF + jr) * 2 : L_W2C + 63 * 4);
          ob[s] = opq(inv ? L_V3 + (h * NF + jp) * 4 : L_W2C + ((h < NF && jp == NF) ? h : 63) * 4);
          oc[s] = opq(inv ? L_V3 + 2816 + (h * NF + jp) * 4 : L_W2C + 63 * 4);
        }
        team_arrive(cnt + C_V3, lane);
        team_wait<0>(cnt + C_V3, 4);
        STAMP(2);
        u32 av[7]; float bv[7], bw[7];
#pragma unroll
        for (int s = 0; s < 7; ++s) { av[s] = *(LAS uint16_t*)(lds + oa[s]); bv[s] = *(LAS float*)(lds + ob[s]); bw[s] = *(LAS float*)(lds + oc[s]); }
#pragma unroll
        for (int s = 0; s < 7; ++s) bv[s] += bw[s];
        f32x4 u = {0.f, 0.f, 0.f, 0.f}, u2 = {0.f, 0.f, 0.f, 0.f};
#pragma unroll
        for (int s = 0; s < 7; ++s) {
          if (s & 1) u2 = __builtin_amdgcn_mfma_f32_16x16x4f32(bflo(av[s]), bv[s], u2, 0, 0, 0);
          else u = __builtin_amdgcn_mfma_f32_16x16x4f32(bflo(av[s]), bv[s], u, 0, 0, 0);
        }
        u += u2;
#pragma unroll
        for (int e = 0; e < 4; ++e) {
          const int jo = 16 * tj + 4 * kq + e;
          const float val = (jo == NF && jp == NF) ? bflo(w1i) : u[e];
          *(LAS float*)(lds + L_U + (jo * UP + jp) * 4) = val;
        }
        team_arrive(cnt + C_U, lane);
        if (w == 0) {
          team_wait<0>(cnt + C_U, 4);
          STAMP(3);
          LAS float* U = (LAS float*)(lds + L_U);
          float e[12];
#pragma unroll
          for (int m = 0; m < 3; ++m) {
            e[4 * m] = U[tb[m].x & 0xffff]; e[4 * m + 1] = U[tb[m].x >> 16]; e[4 * m + 2] = U[tb[m].y & 0xffff]; e[4 * m + 3] = U[tb[m].y >> 16];
          }
#pragma unroll
          for (int m = 0; m < 3; ++m)
            __builtin_amdgcn_raw_buffer_store_b32(packbf(e[4 * m] + e[4 * m + 1], e[4 * m + 2] + e[4 * m + 3]), rws, WS_PB + i * 768 + (lane + 64 * m) * 4, 0, 16);
          const u64 flagb = mkflag(get_tag(cnt));
          drain_vm();
          if (lane == 0) store_flag(a.ws, WS_FB + i * FL, flagb);
          STAMP(4);
        }
      } else {
        const int i = (p - NF) * 4 + w;
        const bool prod = p < 33 && i < NF;
        u32 v[6]; u32x4 tb[3];
#pragma unroll
        for (int m = 0; m < 6; ++m) { const int d = lane + 64 * m; v[m] = __builtin_amdgcn_raw_buffer_load_b32(rwc, (prod && d < 338) ? i * 1352 + d * 4 : OOB, 0, 0); }
#pragma unroll
        for (int m = 0; m < 3; ++m) tb[m] = *(const GAS u32x4*)SYMTAB.w[lane + 64 * m];
        __builtin_amdgcn_sched_barrier(0);
        STAMP(1);
        if (prod) {
          LAS char* raw = lds + L_RAW + w * 2048;
#pragma unroll
          for (int m = 0; m < 6; ++m) *(LAS u32*)(raw + (lane + 64 * m) * 4) = v[m];
          asm volatile("s_waitcnt lgkmcnt(0)" ::: "memory");
          LAS uint16_t* R = (LAS uint16_t*)raw;
          u32 e[12];
#pragma unroll
          for (int m = 0; m < 3; ++m) {
            e[4 * m] = R[tb[m].z & 0xffff]; e[4 * m + 1] = R[tb[m].z >> 16]; e[4 * m + 2] = R[tb[m].w & 0xffff]; e[4 * m + 3] = R[tb[m].w >> 16];
          }
#pragma unroll
          for (int m = 0; m < 3; ++m)
            __builtin_amdgcn_raw_buffer_store_b32(packbf(bflo(e[4 * m]) + bflo(e[4 * m + 1]), bflo(e[4 * m + 2]) + bflo(e[4 * m + 3])), rws,
                                                  WS_PA + i * 768 + (lane + 64 * m) * 4, 0, 16);
          const u64 flag0 = mkflag(get_tag(cnt));
          drain_vm();
          if (lane == 0) store_flag(a.ws, WS_FA + i * FL, flag0);
          STAMP(4);
        }
        if (p == 47 && w == 0) {
          const u32 tag0 = get_tag(cnt);
          const u64 flag0 = mkflag(tag0);
          __builtin_amdgcn_s_sleep(32);
#pragma unroll 1
          for (int m = 0; m < 4; ++m) poll_flags<16>(a.ws + WS_XF + (lane + 64 * m) * FL, flag0);
          if (lane == 0) __hip_atomic_store((GAS u32*)(a.ws + WS_E), tag0, __ATOMIC_RELAXED, __HIP_MEMORY_SCOPE_AGENT);
        }
      }
  } else if (w >= 4 && w < 8) {
    const int cw = w - 4, smp = g * 64 + r * 4 + cw;
    u32 id[2]; u32x4 xr[2];
    if (r < 13) __builtin_amdgcn_s_setprio(2);
#pragma unroll
    for (int m = 0; m < 2; ++m) { const int f = 16 * m + (lane >> 2); id[m] = __builtin_amdgcn_raw_buffer_load_b32(ridx, f < NF ? f * 4 : OOB, smp * NF * 4, 0); }
    u32 touch = 0;
    if (P_TOUCH && cw == 1) {
      const char* tp = lane < 7 ? (const char*)a.table + (size_t)lane * (a.table_bytes / 7 & ~3u)
                                : (lane == 7 ? a.ws + WS_P : (lane == 8 ? a.ws + WS_PA : (lane == 9 ? a.ws + WS_X + g * 104 * 1024 : (const char*)a.out)));
      asm volatile("global_load_dword %0, %1, off" : "+v"(touch) : "v"(tp));
    }
    const size_t bop = (size_t)a.bo;
    const u32 bo = *(const CAS u32*)(bop & ~(size_t)3) >> ((bop & 2) * 8);
#pragma unroll
    for (int m = 0; m < 2; ++m) { const int f = 16 * m + (lane >> 2); xr[m] = __builtin_amdgcn_raw_buffer_load_b128(rtab, f < NF ? id[m] * 64 + (lane & 3) * 16 : OOB, 0, 0); }
    __builtin_amdgcn_s_setprio(0);
    STAMP(1);
    f32x2 xp[14];
    {
      const u32 o = L_XS + cw * XS_S + (lane >> 2) * 64 + (lane & 3) * 16;
      *(LAS u32x4*)(lds + o) = xr[0];
      u32x4 x1 = xr[1];
      if ((lane >> 2) == NF - 16) x1 = (u32x4){0x3F803F80u, 0x3F803F80u, 0x3F803F80u, 0x3F803F80u};
      *(LAS u32x4*)(lds + opq((lane >> 2) < 12 ? o + 1024 : L_XS + cw * XS_S + 27 * 64 + (lane & 3) * 16)) = x1;
      asm volatile("s_waitcnt lgkmcnt(0)" :: "v"(touch), "v"(cpf) : "memory");
      const int hh = lane >> 5, q = (lane >> 2) & 3, pp = lane & 3;
      const u32 base = L_XS + cw * XS_S + ((lane >> 4) & 1) * 32 + pp * 8;
      const u32 a0 = base + (hh ? q + 2 : q) * 64, a1 = base + (hh ? ((q + 6) & 7) : q + 4) * 64;
      const u32 a6 = base + (24 + ((hh && q < 2) ? (q ^ 1) : q)) * 64;
#pragma unroll
      for (int t = 0; t < 7; ++t) {
        s16x4 y = __builtin_amdgcn_ds_read_tr16_b64_v4i16((LAS s16x4*)(lds + (t < 6 ? ((t & 1) ? a1 : a0) + (t >> 1) * 512 : a6)));
        u32x2 u = __builtin_bit_cast(u32x2, y);
        xp[2 * t] = (f32x2){bflo(u.x), bfhi(u.x)}; xp[2 * t + 1] = (f32x2){bflo(u.y), bfhi(u.y)};
      }
    }
    bf16x8 z[24];
#pragma unroll
    for (int s = 0; s < 24; ++s) {
      u32x4 zz;
#pragma unroll
      for (int e = 0; e < 4; ++e) {
        const int s1 = 8 * s + 2 * e, s2 = s1 + 1;
        const int a1 = SL.a[s1], b1 = SL.b[s1], a2 = SL.a[s2], b2 = SL.b[s2];
        if (a1 == a2 && (b1 & 1) == 0 && b1 < 26 && b2 == b1 + 1) {
          const f32x2 pr = (a1 & 1) ? pkmul_bc<1>(xp[a1 >> 1], xp[b1 >> 1]) : pkmul_bc<0>(xp[a1 >> 1], xp[b1 >> 1]);
          zz[e] = packbf(pr[0], pr[1]);
        } else {
          const float p1 = b1 == 26 ? xp[a1 >> 1][a1 & 1] : mulf(xp[a1 >> 1][a1 & 1], xp[b1 >> 1][b1 & 1]);
          const float p2 = b2 == 26 ? xp[a2 >> 1][a2 & 1] : mulf(xp[a2 >> 1][a2 & 1], xp[b2 >> 1][b2 & 1]);
          zz[e] = packbf(p1, p2);
        }
      }
      z[s] = __builtin_bit_cast(bf16x8, zz);
      asm volatile("" : "+v"(z[s]));
    }
    const u32 tag = get_tag(cnt);
    const u64 flag = mkflag(tag);
    STAMP(2);
    team_wait<1>(cnt + C_ABRDY, 1);
    STAMP(3);
    if (r < 13) team_wait<1>(cnt + C_XISS, 4);
    __builtin_amdgcn_s_setprio(2);
    STAMP(4);
    {
      u32x4 y[10];
#pragma unroll
      for (int m = 0; m < 10; ++m) {
        const u32 n = ((m % 5) * 4 + cw) * 64 + lane;
        y[m] = __builtin_amdgcn_raw_buffer_load_b128(rws, n < NF * 48 ? n * 16 : OOB, m < 5 ? WS_PA : WS_PB, 16);
      }
#pragma unroll
      for (int m = 0; m < 5; ++m) {
        const u32 n = (m * 4 + cw) * 64 + lane, i = (n * 1366u) >> 16, c = n - i * 48;
        const u32 o = opq(n < NF * 48 ? L_A + c * APITCH + i * 16 : L_A + 26 * 16);
        *(LAS u32x4*)(lds + o) = y[m];
        *(LAS u32x4*)(lds + o + (L_B - L_A)) = y[5 + m];
      }
      team_arrive(cnt + C_ABSTG, lane);
      team_wait<0>(cnt + C_ABSTG, 4);
    }
    __builtin_amdgcn_s_setprio(0);
    STAMP(5);
    const u32 afr = (lane >> 5) * APITCH + (lane & 31) * 16;
    const u32 tag2 = (u32)(flag >> 32);
    const int sg = r * 4 + cw;
    const u32 po = opq(lane < 26 ? WS_P + ((g * 26 + lane) * 2 + (sg >> 5)) * 512 + (sg & 31) * 16 : OOB);
    u32x4 yp;
    f32x16 acc1 = {}, acc2 = {};
    {
      constexpr int AD = 2;
      bf16x8 af[24], bf[24];
#pragma unroll
      for (int s = 0; s < AD; ++s) { af[s] = *(LAS bf16x8*)(lds + L_A + afr + s * 2 * APITCH); bf[s] = *(LAS bf16x8*)(lds + L_B + afr + s * 2 * APITCH); }
      __builtin_amdgcn_sched_barrier(0);
#pragma unroll
      for (int s = 0; s < 24; ++s) {
        if (s + AD < 24) { af[s + AD] = *(LAS bf16x8*)(lds + L_A + afr + (s + AD) * 2 * APITCH); bf[s + AD] = *(LAS bf16x8*)(lds + L_B + afr + (s + AD) * 2 * APITCH); }
        if (s == P_PQ) yp = __builtin_amdgcn_raw_buffer_load_b128(rws, po, 0, 16);
        acc1 = __builtin_amdgcn_mfma_f32_32x32x16_bf16(af[s], z[s], acc1, 0, 0, 0);
        acc2 = __builtin_amdgcn_mfma_f32_32x32x16_bf16(bf[s], z[s], acc2, 0, 0, 0);
        __builtin_amdgcn_sched_barrier(0);
      }
    }
    float sum = 0.f;
#pragma unroll
    for (int e = 0; e < 12; ++e) sum += acc1[e] * acc2[e];
    { const float t = acc1[12] * acc2[12] + acc1[13] * acc2[13]; sum += lane < 32 ? t : 0.f; }
    sum = sum_rows(sum_row16(sum));
    STAMP(6);
    for (u32 it = 0; it < SPIN_MAX; ++it) {
      const bool ok = lane >= 26 || (yp.y == tag && yp.w == tag2);
      if (__all(ok)) break;
      asm volatile("" ::: "memory");
      __builtin_amdgcn_s_sleep(1);
      yp = __builtin_amdgcn_raw_buffer_load_b128(rws, po, 0, 16);
    }
    float part = sum_rows(sum_row16(__uint_as_float(yp.x)));
    if (lane == 0) {
      const __bf16 ob = (__bf16)(sum + part + bflo(bo));
      a.out[smp] = __builtin_bit_cast(uint16_t, ob);
    }
    STAMP(7);
  } else if (w < 4 && r < 13) {
      const rsrc_t rw1 = mkrs(a.w1, DIN * DIN * 2);
      const int mh = w & 1, kh = w >> 1, hh = lane >> 5;
      {
        u32x4 v[26];
        const u32 vo = (lane >> 3) * 1664 + r * 128 + (lane & 7) * 16;
        auto wrow = [&](int i) {
          const int s = 13 * (w & 1) + (i >> 1) + rot;
          return kh * 416 + (s >= 26 ? s - 26 : s) * 16 + (i & 1) * 8;
        };
        const int c = lane & 7;
        const u32 lo = L_W + (w * 208 + (lane >> 3)) * 128 + (((c >> 1) ^ (((lane >> 4) & 1) << 1)) * 32) + (c & 1) * 16;
#pragma unroll
        for (int i = 0; i < P_W0; ++i) v[i] = __builtin_amdgcn_raw_buffer_load_b128(rw1, vo, wrow(i) * 1664, 0);
        __builtin_amdgcn_sched_barrier(0);
        team_wait<1>(cnt + C_GISS, 1);
#pragma unroll
        for (int i = P_W0; i < 26; ++i) v[i] = __builtin_amdgcn_raw_buffer_load_b128(rw1, vo, wrow(i) * 1664, 0);
        STAMP(1);
#pragma unroll
        for (int i = 0; i < 26; ++i) *(LAS u32x4*)(lds + lo + i * 1024) = v[i];
      }
      team_arrive(cnt + C_W, lane);
      const u32 tag = get_tag(cnt);
      const u64 flag = mkflag(tag);
      f32x16 acc[2];
      f32x2 wf[8];
      {
        const CAS u32* bp = (const CAS u32*)(a.b1 + r * 64 + kh * 32);
        const CAS u32* wp = (const CAS u32*)(a.w2 + r * 64 + kh * 32);
        u32 bs[16], ws_[16];
#pragma unroll
        for (int t = 0; t < 16; ++t) { bs[t] = bp[t]; ws_[t] = wp[t]; }
#pragma unroll
        for (int t = 0; t < 16; ++t) asm volatile("" : "+s"(bs[t]), "+s"(ws_[t]));
#pragma unroll
        for (int t = 0; t < 4; ++t) {
          const u32 b0 = hh ? bs[4 * t + 2] : bs[4 * t], b1 = hh ? bs[4 * t + 3] : bs[4 * t + 1];
          const u32 w0 = hh ? ws_[4 * t + 2] : ws_[4 * t], w1 = hh ? ws_[4 * t + 3] : ws_[4 * t + 1];
          acc[0][4 * t] = bflo(b0); acc[0][4 * t + 1] = bfhi(b0); acc[0][4 * t + 2] = bflo(b1); acc[0][4 * t + 3] = bfhi(b1);
          acc[1][4 * t] = 0.f; acc[1][4 * t + 1] = 0.f; acc[1][4 * t + 2] = 0.f; acc[1][4 * t + 3] = 0.f;
          wf[2 * t] = (f32x2){bflo(w0), bfhi(w0)}; wf[2 * t + 1] = (f32x2){bflo(w1), bfhi(w1)};
          asm volatile("" : "+v"(wf[2 * t]), "+v"(wf[2 * t + 1]));
        }
      }
      STAMP(2);
      team_wait<0>(cnt + C_XRDY, 1);
      STAMP(3);
      constexpr int XW = P_XW, FD = 3;
      u32x4 xv[26];
      auto ldx = [&](int i) { const int s = i + rot; xv[i] = __builtin_amdgcn_raw_buffer_load_b128(rws, WS_X + lane * 16, (g * 104 + mh * 52 + kh * 26 + (s >= 26 ? s - 26 : s)) * 1024, 16); };
#pragma unroll
      for (int i = 0; i < XW; ++i) ldx(i);
      team_wait<0>(cnt + C_W, 4);
      __builtin_amdgcn_s_setprio(1);
      {
        const int q = (lane >> 2) & 3, pp = lane & 3, gam = (lane >> 4) & 1;
        const u32 rb = L_W + kh * 26 * 2048 + (8 * hh + q) * 128 + pp * 8;
        const u32 ad0 = rb + (((2 * kh + gam) ^ (q & 2)) * 32), ad1 = rb + (((2 * (kh ^ 1) + gam) ^ (q & 2)) * 32);
        bf16x8 fr[26][2];
        auto ldf = [&](int i) {
#pragma unroll
          for (int t = 0; t < 2; ++t) {
            const u32 o = (t ? ad1 : ad0) + i * 2048;
            const s16x4 y0 = __builtin_amdgcn_ds_read_tr16_b64_v4i16((LAS s16x4*)(lds + o));
            const s16x4 y1 = __builtin_amdgcn_ds_read_tr16_b64_v4i16((LAS s16x4*)(lds + o + 512));
            fr[i][t] = __builtin_bit_cast(bf16x8, __builtin_shufflevector(y0, y1, 0, 1, 2, 3, 4, 5, 6, 7));
          }
        };
#pragma unroll
        for (int i = 0; i < FD; ++i) ldf(i);
        __builtin_amdgcn_sched_barrier(0);
#pragma unroll
        for (int i = 0; i < 26; ++i) {
          if (i + XW < 26) ldx(i + XW);
          if (i == P_XI) lds_inc(cnt + C_XISS);
          if (i == 8) lds_inc(cnt + C_WD + kh);
          if (i == 22) team_wait<0>(cnt + C_WD + kh, 2);
          if (i + FD < 26) ldf(i + FD);
          const bf16x8 xb = __builtin_bit_cast(bf16x8, xv[i]);
          acc[0] = __builtin_amdgcn_mfma_f32_32x32x16_bf16(fr[i][0], xb, acc[0], 0, 0, 0);
          acc[1] = __builtin_amdgcn_mfma_f32_32x32x16_bf16(fr[i][1], xb, acc[1], 0, 0, 0);
          __builtin_amdgcn_sched_barrier(0);
        }
      }
      STAMP(4);
      {
        const u32 so = L_W + kh * 26 * 2048 + mh * 4096 + lane * 16;
#pragma unroll
        for (int t = 0; t < 4; ++t) *(LAS f32x4*)(lds + so + t * 1024) = (f32x4){acc[1][4 * t], acc[1][4 * t + 1], acc[1][4 * t + 2], acc[1][4 * t + 3]};
        team_arrive(cnt + C_RED + mh, lane);
        team_wait<0>(cnt + C_RED + mh, 2);
      }
      STAMP(5);
      float part;
      {
        constexpr float K1 = -2.0f * 1.4426950408889634f * 0.7978845608028654f, K2 = K1 * 0.044715f;
        const u32 si = L_W + (kh ^ 1) * 26 * 2048 + mh * 4096 + lane * 16;
        f32x4 o[4];
#pragma unroll
        for (int t = 0; t < 4; ++t) o[t] = *(LAS f32x4*)(lds + si + t * 1024);
        f32x2 h[8], u[8], y[8];
#pragma unroll
        for (int m = 0; m < 8; ++m) {
          h[m] = (f32x2){acc[0][2 * m], acc[0][2 * m + 1]} + (f32x2){o[m >> 1][2 * (m & 1)], o[m >> 1][2 * (m & 1) + 1]};
          u[m] = (h[m] * h[m] * K2 + K1) * h[m];
          y[m] = h[m] * wf[m];
        }
#pragma unroll
        for (int m = 0; m < 8; ++m) { u[m][0] = __builtin_amdgcn_exp2f(u[m][0]); u[m][1] = __builtin_amdgcn_exp2f(u[m][1]); }
#pragma unroll
        for (int m = 0; m < 8; ++m) { u[m] = u[m] + 1.0f; }
#pragma unroll
        for (int m = 0; m < 8; ++m) { u[m][0] = __builtin_amdgcn_rcpf(u[m][0]); u[m][1] = __builtin_amdgcn_rcpf(u[m][1]); }
        f32x2 p0 = y[0] * u[0], p1 = y[1] * u[1];
#pragma unroll
        for (int m = 2; m < 8; m += 2) { p0 += y[m] * u[m]; p1 += y[m + 1] * u[m + 1]; }
        p0 += p1;
        part = p0[0] + p0[1];
      }
      {
        auto sw = __builtin_amdgcn_permlane32_swap(__float_as_uint(part), __float_as_uint(part), false, false);
        part = __uint_as_float(sw[0]) + __uint_as_float(sw[1]);
      }
      {
        const u32x4 o = {__float_as_uint(part), tag, __float_as_uint(part), (u32)(flag >> 32)};
        __builtin_amdgcn_raw_buffer_store_b128(o, rws, lane < 32 ? WS_P + ((g * 26 + r * 2 + kh) * 2 + mh) * 512 + lane * 16 : OOB, 0, 16);
      }
      STAMP(6);
  } else {
    __builtin_amdgcn_s_setprio(3);
    u32 tag;
    if (r < 13) {
      u32 id[4]; u32x4 xg[8];
#pragma unroll
      for (int m = 0; m < 2; ++m) {
        const int p0 = 4 * r + 2 * m, t = p0 >= NF ? 1 : 0, f0 = p0 - t * NF;
        const u32x2 ia = __builtin_amdgcn_raw_buffer_load_b64(ridx, (lane & 31) * (NF * 4), ((g * 64 + t * 32) * NF + f0) * 4, 0);
        id[2 * m] = ia.x; id[2 * m + 1] = ia.y;
      }
#pragma unroll
      for (int k = 0; k < 8; ++k) xg[k] = __builtin_amdgcn_raw_buffer_load_b128(rtab, id[k >> 1] * 64 + (k & 1) * 32 + (lane >> 5) * 16, 0, 0);
      const u32 ep = load_epoch(a.ws);
      lds_inc(cnt + C_GISS);
      STAMP(1);
      tag = __builtin_amdgcn_readfirstlane(ep) + 1u;
      cnt[C_TAGV] = tag;
      team_arrive(cnt + C_TAG, lane);
#pragma unroll
      for (int k = 0; k < 8; ++k) __builtin_amdgcn_raw_buffer_store_b128(xg[k], rws, WS_X + lane * 16, (g * 104 + r * 8 + k) * 1024, 16);
      STAMP(2);
      drain_vm();
    } else {
      const u32 ep = load_epoch(a.ws);
      tag = __builtin_amdgcn_readfirstlane(ep) + 1u;
      cnt[C_TAGV] = tag;
      team_arrive(cnt + C_TAG, lane);
    }
    const u64 flag = mkflag(tag);
    if (lane == 0) store_flag(a.ws, WS_XF + (g * 16 + r) * FL, flag);
    STAMP(3);
    if (r < 13) {
      __builtin_amdgcn_s_sleep(P_XDELAY);
      poll_flags<2>(a.ws + WS_XF + (g * 16 + (lane < 13 ? lane : 0)) * FL, flag);
      lds_inc(cnt + C_XRDY);
    }
    STAMP(4);
    __builtin_amdgcn_s_sleep(P_BDELAY);
    poll_flags<6>(a.ws + (lane < 32 ? WS_FA : WS_FB) + ((lane & 31) < NF ? (lane & 31) : 0) * FL, flag);
    lds_inc(cnt + C_ABRDY);
    STAMP(5);
  }
}

}

extern "C" void kernel_launch(void* const* d_in, const int* in_sizes, int n_in, void* d_out, int out_size,
                              void* d_ws, size_t ws_size, hipStream_t stream) {
  using namespace xdfm;
  Args a;
  a.idx = (const int*)d_in[0]; a.table = (const uint16_t*)d_in[1]; a.wc = (const uint16_t*)d_in[2];
  a.wco = (const uint16_t*)d_in[3]; a.w1 = (const uint16_t*)d_in[4]; a.b1 = (const uint16_t*)d_in[5];
  a.w2 = (const uint16_t*)d_in[6]; a.bo = (const uint16_t*)d_in[7]; a.out = (uint16_t*)d_out; a.ws = (char*)d_ws;
  a.table_bytes = (u32)in_sizes[1] * 2u; a.pad_ = 0;
  (void)hipFuncSetAttribute((const void*)xdfm_kernel, hipFuncAttributeMaxDynamicSharedMemorySize, L_TOTAL);
  xdfm_kernel<<<256, NWAVE * 64, L_TOTAL, stream>>>(a);
}
```

```cpp
#include <hip/hip_runtime.h>
#include <hip/hip_bf16.h>
#include <stdint.h>

#define P_ADELAY 8
#define P_BDELAY 8
#define P_WW 10
#define P_PQ 16
#define P_XI 14
#define P_XDELAY 12
#define P_CG 4
#define P_ROT 1
#define P_TOUCH 1
#define P_W0 14
#define P_CPF 1
#define P_XW 12
namespace xdfm {

typedef unsigned int u32;
typedef unsigned long long u64;
typedef u32 u32x2 __attribute__((ext_vector_type(2)));
typedef u32 u32x4 __attribute__((ext_vector_type(4)));
typedef short s16x4 __attribute__((ext_vector_type(4)));
typedef short s16x8 __attribute__((ext_vector_type(8)));
typedef float f32x2 __attribute__((ext_vector_type(2)));
typedef float f32x4 __attribute__((ext_vector_type(4)));
typedef float f32x16 __attribute__((ext_vector_type(16)));
typedef __bf16 bf16x2 __attribute__((ext_vector_type(2)));
typedef __bf16 bf16x8 __attribute__((ext_vector_type(8)));
#define GAS __attribute__((address_space(1)))
#define LAS __attribute__((address_space(3)))
typedef __amdgpu_buffer_rsrc_t rsrc_t;

constexpr int NF = 26, DIN = 832, NB = 1024, CUBE = 17576;

constexpr u32 WS_E = 0;
constexpr u32 FL = 128;
constexpr u32 WS_XF = 4096;
constexpr u32 WS_FA = 40960;
constexpr u32 WS_FB = 45056;
constexpr u32 WS_PA = 49152;
constexpr u32 WS_PB = 69632;
constexpr u32 WS_P = 131072;
constexpr u32 WS_X = 655360;
constexpr u32 WS_END = WS_X + 16 * 104 * 1024;
constexpr int NWAVE = 9;

constexpr u32 L_W = 0;
constexpr u32 APITCH = 432;
constexpr u32 L_A = 106496;
constexpr u32 L_B = L_A + 48 * APITCH;
constexpr u32 L_XS = L_B + 48 * APITCH;
constexpr u32 XS_S = 1824;
constexpr u32 L_CNT = L_XS + 4 * XS_S;
constexpr u32 L_TOTAL = L_CNT + 64;
constexpr u32 L_V3 = 0, L_W2C = 8192, L_W2S = 8448, L_U = 12288, L_RAW = 20480;
enum { C_TAGV = 12, C_TAG = 13, C_GISS = 14, C_ABRDY = 0, C_ABSTG = 1, C_W = 3, C_V3 = 4, C_U = 5, C_XISS = 6, C_XRDY = 7, C_WD = 8, C_RED = 10 };

constexpr int perm1(int j) { return j < 24 ? ((j & ~7) | ((j + 2) & 7)) : (j == 24 ? 25 : (j == 25 ? 24 : j)); }
struct Slots { int a[192]; int b[192]; bool v0[192]; bool v1[192]; int n; };
struct SlotBuilder {
  Slots s{}; bool vis[27][27] = {}; int n = 0;
  constexpr bool seen(int a, int b) const { return vis[a < b ? a : b][a < b ? b : a]; }
  constexpr void put(int a, int b) {
    s.a[n] = a; s.b[n] = b;
    s.v0[n] = !seen(a, b); vis[a < b ? a : b][a < b ? b : a] = true;
    const int na = perm1(a), nb = perm1(b);
    s.v1[n] = !seen(na, nb); vis[na < nb ? na : nb][na < nb ? nb : na] = true;
    ++n;
  }
};
constexpr int sg2(int m) { return perm1(perm1(2 * m)) >> 1; }
constexpr Slots make_slots() {
  SlotBuilder t{};
  for (int A = 0; A < 13; ++A)
    for (int B = A + 1; B < 13; ++B) {
      int ca = A, cb = B;
      while (!t.seen(2 * ca, 2 * cb)) {
        t.put(2 * ca, 2 * cb); t.put(2 * ca, 2 * cb + 1); t.put(2 * ca + 1, 2 * cb); t.put(2 * ca + 1, 2 * cb + 1);
        ca = sg2(ca); cb = sg2(cb);
      }
    }
  for (int A = 0; A < 13; ++A) {
    int ca = A;
    while (!t.seen(2 * ca, 2 * ca + 1)) {
      t.put(2 * ca, 2 * ca); t.put(2 * ca, 2 * ca + 1); t.put(2 * ca + 1, 2 * ca + 1); t.put(2 * ca + 1, 26);
      ca = sg2(ca);
    }
  }
  for (int a = 0; a < 27; ++a) { int ca = a; while (!t.seen(ca, 26)) { t.put(ca, 26); ca = perm1(perm1(ca)); } }
  t.s.n = t.n;
  for (int n = t.n; n < 192; ++n) { t.s.a[n] = 26; t.s.b[n] = 26; t.s.v0[n] = false; t.s.v1[n] = false; }
  return t.s;
}
constexpr Slots SL = make_slots();
static_assert(SL.n <= 192, "too many slots");

constexpr int UP = 33, ZB = 30 * UP, ZA = 676;
struct SymTab { u32 w[192][4]; };
constexpr SymTab make_symtab() {
  SymTab t{};
  for (int d = 0; d < 192; ++d) {
    int c = d >> 2, s = c >> 1, hh = c & 1;
    for (int e = 0; e < 2; ++e) {
      int sg = 8 * s + 2 * (d & 3) + e;
      int a = hh ? perm1(SL.a[sg]) : SL.a[sg], b = hh ? perm1(SL.b[sg]) : SL.b[sg];
      bool v = hh ? SL.v1[sg] : SL.v0[sg];
      u32 b1 = ZB, b2 = ZB, a1 = ZA, a2 = ZA;
      if (v) {
        b1 = a * UP + b; b2 = a == b ? ZB : b * UP + a;
        if (a < 26 && b < 26) { a1 = a * 26 + b; a2 = a == b ? ZA : b * 26 + a; }
      }
      t.w[d][e] = b1 | (b2 << 16);
      t.w[d][2 + e] = a1 | (a2 << 16);
    }
  }
  return t;
}
__device__ const SymTab SYMTAB = make_symtab();

constexpr u32 OOB = 0x7FFFFF00u;
constexpr u32 CODE_PART = 20;
constexpr u32 SPIN_MAX = 1u << 18;

__device__ __forceinline__ float bflo(u32 v) { return __uint_as_float(v << 16); }
__device__ __forceinline__ float bfhi(u32 v) { return __uint_as_float(v & 0xffff0000u); }
__device__ __forceinline__ u32 packbf(float a, float b) {
  f32x2 f = {a, b};
  bf16x2 z = __builtin_convertvector(f, bf16x2);
  return __builtin_bit_cast(u32, z);
}
__device__ __forceinline__ float mulf(float a, float b) { float r; asm("v_mul_f32 %0, %1, %2" : "=v"(r) : "v"(a), "v"(b)); return r; }
template <int E> __device__ __forceinline__ f32x2 pkmul_bc(f32x2 a, f32x2 b) {
  f32x2 r; asm("v_pk_mul_f32 %0, %1, %2 op_sel:[%3,0] op_sel_hi:[%3,1]" : "=v"(r) : "v"(a), "v"(b), "i"(E)); return r;
}
__device__ __forceinline__ float dot2bf(u32 a, u32 b, float c) {
  return __builtin_amdgcn_fdot2_f32_bf16(__builtin_bit_cast(bf16x2, a), __builtin_bit_cast(bf16x2, b), c, false);
}
__device__ __forceinline__ rsrc_t mkrs(const void* p, u32 bytes) { return __builtin_amdgcn_make_buffer_rsrc((void*)p, 0, bytes, 0x00020000); }
__device__ __forceinline__ u64 mkflag(u32 tag) { return (u64)tag | ((u64)(tag * 0x9E3779B1u + 0x7F4A7C15u) << 32); }
__device__ __forceinline__ void drain_vm() { asm volatile("s_waitcnt vmcnt(0)" ::: "memory"); }
__device__ __forceinline__ void lds_inc(LAS u32* cnt) {
  u64 keep;
  asm volatile("s_mov_b64 %0, exec\n\ts_mov_b64 exec, 1\n\tds_add_u32 %1, %2\n\ts_mov_b64 exec, %0"
               : "=&s"(keep) : "v"((u32)(size_t)cnt), "v"(1u) : "memory");
}
__device__ __forceinline__ void team_arrive(LAS u32* cnt, int) {
  asm volatile("s_waitcnt lgkmcnt(0)" ::: "memory");
  lds_inc(cnt);
}
__device__ __forceinline__ float sum_rows(float v) {
  auto a = __builtin_amdgcn_permlane32_swap(__float_as_uint(v), __float_as_uint(v), false, false);
  v = __uint_as_float(a[0]) + __uint_as_float(a[1]);
  auto b = __builtin_amdgcn_permlane16_swap(__float_as_uint(v), __float_as_uint(v), false, false);
  return __uint_as_float(b[0]) + __uint_as_float(b[1]);
}
template <int CTRL> __device__ __forceinline__ float dpp_add(float v) {
  return v + __uint_as_float(__builtin_amdgcn_update_dpp(0u, __float_as_uint(v), CTRL, 0xf, 0xf, true));
}
__device__ __forceinline__ float sum_row16(float v) {
  v = dpp_add<0x128>(v);
  v = dpp_add<0x124>(v);
  v = dpp_add<0x122>(v);
  v = dpp_add<0x121>(v);
  return v;
}

#define STAMP(k) do {} while (0)

struct Args {
  const int* idx; const uint16_t* table; const uint16_t* wc; const uint16_t* wco; const uint16_t* w1;
  const uint16_t* b1; const uint16_t* w2; const uint16_t* bo; uint16_t* out; char* ws;
  u32 table_bytes; u32 pad_;
};
#define CAS __attribute__((address_space(4)))

template <int SLEEP> __device__ __forceinline__ void team_wait(LAS u32* cnt, u32 target) {
  u32 v, s, n = SPIN_MAX;
  asm volatile(
      "1:\n\tds_read_b32 %0, %3\n\ts_waitcnt lgkmcnt(0)\n\tv_readfirstlane_b32 %1, %0\n\ts_cmp_ge_u32 %1, %4\n\ts_cbranch_scc1 2f\n\t"
      "s_sleep %5\n\ts_sub_u32 %2, %2, 1\n\ts_cbranch_scc0 1b\n2:"
      : "=&v"(v), "=&s"(s), "+s"(n) : "v"((u32)(size_t)cnt), "s"(target), "i"(SLEEP) : "memory", "scc");
}
template <int SLEEP> __device__ __forceinline__ void poll_flags(const char* p, u64 want) {
  u64 v; u32 n = SPIN_MAX;
  asm volatile(
      "1:\n\tglobal_load_dwordx2 %0, %2, off sc1\n\ts_waitcnt vmcnt(0)\n\tv_cmp_ne_u64 vcc, %3, %0\n\ts_nop 4\n\ts_cbranch_vccz 2f\n\t"
      "s_sleep %4\n\ts_sub_u32 %1, %1, 1\n\ts_cbranch_scc0 1b\n2:"
      : "=&v"(v), "+s"(n) : "v"(p), "s"(want), "i"(SLEEP) : "memory", "scc", "vcc");
}
__device__ __forceinline__ void poll_flags2_signal(const char* p, u64 want, LAS u32* cnt) {
  u64 v0, v1, keep; u32 n = SPIN_MAX;
  asm volatile(
      "global_load_dwordx2 %0, %4, off sc1\n\ts_sleep 2\n"
      "1:\n\tglobal_load_dwordx2 %1, %4, off sc1\n\ts_waitcnt vmcnt(1)\n\tv_cmp_ne_u64 vcc, %5, %0\n\ts_nop 4\n\ts_cbranch_vccz 2f\n\t"
      "global_load_dwordx2 %0, %4, off sc1\n\ts_waitcnt vmcnt(1)\n\tv_cmp_ne_u64 vcc, %5, %1\n\ts_nop 4\n\ts_cbranch_vccz 2f\n\t"
      "s_sub_u32 %2, %2, 1\n\ts_cbranch_scc0 1b\n"
      "2:\n\ts_mov_b64 %3, exec\n\ts_mov_b64 exec, 1\n\tds_add_u32 %6, %7\n\ts_mov_b64 exec, %3\n\ts_waitcnt vmcnt(0)"
      : "=&v"(v0), "=&v"(v1), "+s"(n), "=&s"(keep) : "v"(p), "s"(want), "v"((u32)(size_t)cnt), "v"(1u) : "memory", "scc", "vcc");
}
__device__ __forceinline__ void lds_barrier() {
  asm volatile("s_waitcnt lgkmcnt(0)" ::: "memory");
  __builtin_amdgcn_s_barrier();
  asm volatile("" ::: "memory");
}
__device__ __forceinline__ u32 load_epoch(const char* ws) {
  return __hip_atomic_load((const GAS u32*)(ws + WS_E), __ATOMIC_RELAXED, __HIP_MEMORY_SCOPE_AGENT);
}
__device__ __forceinline__ void store_flag(char* ws, u32 off, u64 flag) {
  __hip_atomic_store((GAS u64*)(ws + off), flag, __ATOMIC_RELAXED, __HIP_MEMORY_SCOPE_AGENT);
}
__device__ __forceinline__ u32 get_tag(LAS u32* cnt) {
  team_wait<0>(cnt + C_TAG, 1);
  return __builtin_amdgcn_readfirstlane(cnt[C_TAGV]);
}
__device__ __forceinline__ u32 opq(u32 v) { asm("" : "+v"(v)); return v; }

__global__ void __launch_bounds__(576, 3) xdfm_kernel(Args a) {
  extern __shared__ __attribute__((aligned(16))) char lds_raw[];
  LAS char* lds = (LAS char*)lds_raw;
  const int tid = threadIdx.x, lane = tid & 63;
  LAS u32* cnt = (LAS u32*)(lds + L_CNT);
  u32 cpf = 0;
  if (P_CPF && tid >= 512) {
    u64 pc;
    asm volatile("s_getpc_b64 %0" : "=s"(pc));
    const u32 ln = ((blockIdx.x >> 3) & 7) * CODE_PART + (lane < CODE_PART ? lane : 0);
    const char* cp = (const char*)(pc & ~(u64)255) + ln * 128;
    asm volatile("global_load_dword %0, %1, off" : "+v"(cpf) : "v"(cp));
  }
  if (tid < 16) cnt[tid] = 0;
  const int pw = __builtin_amdgcn_readfirstlane(tid >> 6), w = pw == 0 ? 8 : pw - 1;
  const int ftid = w * 64 + lane;
  const int b = blockIdx.x, j = b >> 3;
  const int jg = j < 6 ? (j >= 3 ? 1 : 0) : (j >= 19 ? 1 : 0);
  const int g = (b & 7) * 2 + jg, r = j < 6 ? 13 + j - 3 * jg : j - 6 - 13 * jg;
  asm volatile("" :: "s"(a.idx), "s"(a.table), "s"(a.wc), "s"(a.wco), "s"(a.w1), "s"(a.b1), "s"(a.w2), "s"(a.bo), "s"(a.out), "s"(a.ws), "s"(a.table_bytes));
  lds_barrier();
  const rsrc_t rws = mkrs(a.ws, WS_END);
  const rsrc_t ridx = mkrs(a.idx, NB * NF * 4), rtab = mkrs(a.table, a.table_bytes);
  const int rot = P_ROT ? 2 * ((r + 5 * g) % 13) : 0;

  if (w < 4 && r >= 13) {
      const int p = g * 3 + r - 13;
      const rsrc_t rwc = mkrs(a.wc, 3 * CUBE * 2), rwo = mkrs(a.wco, 78 * 2);
      __builtin_amdgcn_s_setprio(3);
      if (p < NF) {
        const int i = p;
        u32x4 tb[3];
        const int hb = (w >> 1) * 13, el = (w & 1) * 64 + lane;
        const CAS u32* w3p = (const CAS u32*)(a.wco + 52);
        u32 w3[13];
#pragma unroll
        for (int h = 0; h < 13; ++h) w3[h] = w3p[h];
        u32x4 v[13];
#pragma unroll
        for (int h = 0; h < 13; ++h) v[h] = __builtin_amdgcn_raw_buffer_load_b128(rwc, el < 85 ? (2 * CUBE + hb * 676 + 8 * el) * 2 : OOB, h * 676 * 2, 0);
        u32 v2[2];
#pragma unroll
        for (int m = 0; m < 2; ++m) {
          const u32 d = ftid + 256 * m, h = (d * 5042u) >> 16, e = d - 13 * h;
          v2[m] = __builtin_amdgcn_raw_buffer_load_b32(rwc, d < 338 ? (CUBE + (h * NF + i) * NF) * 2 + e * 4 : OOB, 0, 0);
        }
        const u32 wq = __builtin_amdgcn_raw_buffer_load_b16(rwo, ftid < NF ? (NF + ftid) * 2 : OOB, 0, 0);
        const u32 w1i = __builtin_amdgcn_raw_buffer_load_b16(rwo, i * 2, 0, 0);
        if (w == 0) {
#pragma unroll
          for (int m = 0; m < 3; ++m) tb[m] = *(const GAS u32x4*)SYMTAB.w[lane + 64 * m];
        }
        __builtin_amdgcn_sched_barrier(0);
        STAMP(1);
        {
          float acc[8];
#pragma unroll
          for (int e = 0; e < 8; ++e) acc[e] = 0.f;
#pragma unroll
          for (int h = 0; h < 13; ++h) {
            const u32 we = (h & 1) ? w3[h >> 1] >> 16 : w3[h >> 1] & 0xffffu;
            const u32 wo = (h & 1) ? w3[(13 + h) >> 1] & 0xffffu : w3[(13 + h) >> 1] >> 16;
            const u32 wl = hb ? wo : we, wh = wl << 16;
#pragma unroll
            for (int e = 0; e < 4; ++e) { acc[2 * e] = dot2bf(v[h][e], wl, acc[2 * e]); acc[2 * e + 1] = dot2bf(v[h][e], wh, acc[2 * e + 1]); }
          }
          const u32 vo = opq(el < 85 ? L_V3 + (w >> 1) * 2816 + el * 32 : L_V3 + 5632 + lane * 32);
          *(LAS f32x4*)(lds + vo) = (f32x4){acc[0], acc[1], acc[2], acc[3]};
          *(LAS f32x4*)(lds + vo + 16) = (f32x4){acc[4], acc[5], acc[6], acc[7]};
#pragma unroll
          for (int m = 0; m < 2; ++m) *(LAS u32*)(lds + L_W2S + (ftid + 256 * m) * 4) = v2[m];
          if (w == 0) *(LAS u32*)(lds + L_W2C + lane * 4) = wq << 16;
        }
        const int tj = w >> 1, tp = w & 1, kq = lane >> 4;
        const int jr = 16 * tj + (lane & 15), jp = 16 * tp + (lane & 15);
        u32 oa[7], ob[7], oc[7];
#pragma unroll
        for (int s = 0; s < 7; ++s) {
          const int h = 4 * s + kq;
          const bool inv = h < NF && jp < NF;
          oa[s] = opq((h < NF && jr < NF) ? L_W2S + (h * NF + jr) * 2 : L_W2C + 63 * 4);
          ob[s] = opq(inv ? L_V3 + (h * NF + jp) * 4 : L_W2C + ((h < NF && jp == NF) ? h : 63) * 4);
          oc[s] = opq(inv ? L_V3 + 2816 + (h * NF + jp) * 4 : L_W2C + 63 * 4);
        }
        team_arrive(cnt + C_V3, lane);
        team_wait<0>(cnt + C_V3, 4);
        STAMP(2);
        u32 av[7]; float bv[7], bw[7];
#pragma unroll
        for (int s = 0; s < 7; ++s) { av[s] = *(LAS uint16_t*)(lds + oa[s]); bv[s] = *(LAS float*)(lds + ob[s]); bw[s] = *(LAS float*)(lds + oc[s]); }
#pragma unroll
        for (int s = 0; s < 7; ++s) bv[s] += bw[s];
        f32x4 u = {0.f, 0.f, 0.f, 0.f}, u2 = {0.f, 0.f, 0.f, 0.f};
#pragma unroll
        for (int s = 0; s < 7; ++s) {
          if (s & 1) u2 = __builtin_amdgcn_mfma_f32_16x16x4f32(bflo(av[s]), bv[s], u2, 0, 0, 0);
          else u = __builtin_amdgcn_mfma_f32_16x16x4f32(bflo(av[s]), bv[s], u, 0, 0, 0);
        }
        u += u2;
#pragma unroll
        for (int e = 0; e < 4; ++e) {
          const int jo = 16 * tj + 4 * kq + e;
          const float val = (jo == NF && jp == NF) ? bflo(w1i) : u[e];
          *(LAS float*)(lds + L_U + (jo * UP + jp) * 4) = val;
        }
        team_arrive(cnt + C_U, lane);
        if (w == 0) {
          team_wait<0>(cnt + C_U, 4);
          STAMP(3);
          LAS float* U = (LAS float*)(lds + L_U);
          float e[12];
#pragma unroll
          for (int m = 0; m < 3; ++m) {
            e[4 * m] = U[tb[m].x & 0xffff]; e[4 * m + 1] = U[tb[m].x >> 16]; e[4 * m + 2] = U[tb[m].y & 0xffff]; e[4 * m + 3] = U[tb[m].y >> 16];
          }
#pragma unroll
          for (int m = 0; m < 3; ++m)
            __builtin_amdgcn_raw_buffer_store_b32(packbf(e[4 * m] + e[4 * m + 1], e[4 * m + 2] + e[4 * m + 3]), rws, WS_PB + i * 768 + (lane + 64 * m) * 4, 0, 16);
          const u64 flagb = mkflag(get_tag(cnt));
          drain_vm();
          if (lane == 0) store_flag(a.ws, WS_FB + i * FL, flagb);
          STAMP(4);
        }
      } else {
        const int i = (p - NF) * 4 + w;
        const bool prod = p < 33 && i < NF;
        u32 v[6]; u32x4 tb[3];
#pragma unroll
        for (int m = 0; m < 6; ++m) { const int d = lane + 64 * m; v[m] = __builtin_amdgcn_raw_buffer_load_b32(rwc, (prod && d < 338) ? i * 1352 + d * 4 : OOB, 0, 0); }
#pragma unroll
        for (int m = 0; m < 3; ++m) tb[m] = *(const GAS u32x4*)SYMTAB.w[lane + 64 * m];
        __builtin_amdgcn_sched_barrier(0);
        STAMP(1);
        if (prod) {
          LAS char* raw = lds + L_RAW + w * 2048;
#pragma unroll
          for (int m = 0; m < 6; ++m) *(LAS u32*)(raw + (lane + 64 * m) * 4) = v[m];
          asm volatile("s_waitcnt lgkmcnt(0)" ::: "memory");
          LAS uint16_t* R = (LAS uint16_t*)raw;
          u32 e[12];
#pragma unroll
          for (int m = 0; m < 3; ++m) {
            e[4 * m] = R[tb[m].z & 0xffff]; e[4 * m + 1] = R[tb[m].z >> 16]; e[4 * m + 2] = R[tb[m].w & 0xffff]; e[4 * m + 3] = R[tb[m].w >> 16];
          }
#pragma unroll
          for (int m = 0; m < 3; ++m)
            __builtin_amdgcn_raw_buffer_store_b32(packbf(bflo(e[4 * m]) + bflo(e[4 * m + 1]), bflo(e[4 * m + 2]) + bflo(e[4 * m + 3])), rws,
                                                  WS_PA + i * 768 + (lane + 64 * m) * 4, 0, 16);
          const u64 flag0 = mkflag(get_tag(cnt));
          drain_vm();
          if (lane == 0) store_flag(a.ws, WS_FA + i * FL, flag0);
          STAMP(4);
        }
        if (p == 47 && w == 0) {
          const u32 tag0 = get_tag(cnt);
          const u64 flag0 = mkflag(tag0);
          __builtin_amdgcn_s_sleep(32);
#pragma unroll 1
          for (int m = 0; m < 4; ++m) poll_flags<16>(a.ws + WS_XF + (lane + 64 * m) * FL, flag0);
          if (lane == 0) __hip_atomic_store((GAS u32*)(a.ws + WS_E), tag0, __ATOMIC_RELAXED, __HIP_MEMORY_SCOPE_AGENT);
        }
      }
  } else if (w >= 4 && w < 8) {
    const int cw = w - 4, smp = g * 64 + r * 4 + cw;
    u32 id[2]; u32x4 xr[2];
    if (r < 13) __builtin_amdgcn_s_setprio(2);
#pragma unroll
    for (int m = 0; m < 2; ++m) { const int f = 16 * m + (lane >> 2); id[m] = __builtin_amdgcn_raw_buffer_load_b32(ridx, f < NF ? f * 4 : OOB, smp * NF * 4, 0); }
    u32 touch = 0;
    if (P_TOUCH && cw == 1) {
      const char* tp = lane < 7 ? (const char*)a.table + (size_t)lane * (a.table_bytes / 7 & ~3u)
                                : (lane == 7 ? a.ws + WS_P : (lane == 8 ? a.ws + WS_PA : (lane == 9 ? a.ws + WS_X + g * 104 * 1024 : (const char*)a.out)));
      asm volatile("global_load_dword %0, %1, off" : "+v"(touch) : "v"(tp));
    }
    const size_t bop = (size_t)a.bo;
    const u32 bo = *(const CAS u32*)(bop & ~(size_t)3) >> ((bop & 2) * 8);
#pragma unroll
    for (int m = 0; m < 2; ++m) { const int f = 16 * m + (lane >> 2); xr[m] = __builtin_amdgcn_raw_buffer_load_b128(rtab, f < NF ? id[m] * 64 + (lane & 3) * 16 : OOB, 0, 0); }
    __builtin_amdgcn_s_setprio(0);
    STAMP(1);
    f32x2 xp[14];
    {
      const u32 o = L_XS + cw * XS_S + (lane >> 2) * 64 + (lane & 3) * 16;
      *(LAS u32x4*)(lds + o) = xr[0];
      u32x4 x1 = xr[1];
      if ((lane >> 2) == NF - 16) x1 = (u32x4){0x3F803F80u, 0x3F803F80u, 0x3F803F80u, 0x3F803F80u};
      *(LAS u32x4*)(lds + opq((lane >> 2) < 12 ? o + 1024 : L_XS + cw * XS_S + 27 * 64 + (lane & 3) * 16)) = x1;
      asm volatile("s_waitcnt lgkmcnt(0)" :: "v"(touch), "v"(cpf) : "memory");
      const int hh = lane >> 5, q = (lane >> 2) & 3, pp = lane & 3;
      const u32 base = L_XS + cw * XS_S + ((lane >> 4) & 1) * 32 + pp * 8;
      const u32 a0 = base + (hh ? q + 2 : q) * 64, a1 = base + (hh ? ((q + 6) & 7) : q + 4) * 64;
      const u32 a6 = base + (24 + ((hh && q < 2) ? (q ^ 1) : q)) * 64;
#pragma unroll
      for (int t = 0; t < 7; ++t) {
        s16x4 y = __builtin_amdgcn_ds_read_tr16_b64_v4i16((LAS s16x4*)(lds + (t < 6 ? ((t & 1) ? a1 : a0) + (t >> 1) * 512 : a6)));
        u32x2 u = __builtin_bit_cast(u32x2, y);
        xp[2 * t] = (f32x2){bflo(u.x), bfhi(u.x)}; xp[2 * t + 1] = (f32x2){bflo(u.y), bfhi(u.y)};
      }
    }
    bf16x8 z[24];
#pragma unroll
    for (int s = 0; s < 24; ++s) {
      u32x4 zz;
#pragma unroll
      for (int e = 0; e < 4; ++e) {
        const int s1 = 8 * s + 2 * e, s2 = s1 + 1;
        const int a1 = SL.a[s1], b1 = SL.b[s1], a2 = SL.a[s2], b2 = SL.b[s2];
        if (a1 == a2 && (b1 & 1) == 0 && b1 < 26 && b2 == b1 + 1) {
          const f32x2 pr = (a1 & 1) ? pkmul_bc<1>(xp[a1 >> 1], xp[b1 >> 1]) : pkmul_bc<0>(xp[a1 >> 1], xp[b1 >> 1]);
          zz[e] = packbf(pr[0], pr[1]);
        } else {
          const float p1 = b1 == 26 ? xp[a1 >> 1][a1 & 1] : mulf(xp[a1 >> 1][a1 & 1], xp[b1 >> 1][b1 & 1]);
          const float p2 = b2 == 26 ? xp[a2 >> 1][a2 & 1] : mulf(xp[a2 >> 1][a2 & 1], xp[b2 >> 1][b2 & 1]);
          zz[e] = packbf(p1, p2);
        }
      }
      z[s] = __builtin_bit_cast(bf16x8, zz);
      asm volatile("" : "+v"(z[s]));
    }
    const u32 tag = get_tag(cnt);
    const u64 flag = mkflag(tag);
    STAMP(2);
    team_wait<1>(cnt + C_ABRDY, 1);
    STAMP(3);
    if (r < 13) team_wait<1>(cnt + C_XISS, 4);
    __builtin_amdgcn_s_setprio(2);
    STAMP(4);
    {
      u32x4 y[10];
#pragma unroll
      for (int m = 0; m < 10; ++m) {
        const u32 n = ((m % 5) * 4 + cw) * 64 + lane;
        y[m] = __builtin_amdgcn_raw_buffer_load_b128(rws, n < NF * 48 ? n * 16 : OOB, m < 5 ? WS_PA : WS_PB, 16);
      }
#pragma unroll
      for (int m = 0; m < 5; ++m) {
        const u32 n = (m * 4 + cw) * 64 + lane, i = (n * 1366u) >> 16, c = n - i * 48;
        const u32 o = opq(n < NF * 48 ? L_A + c * APITCH + i * 16 : L_A + 26 * 16);
        *(LAS u32x4*)(lds + o) = y[m];
        *(LAS u32x4*)(lds + o + (L_B - L_A)) = y[5 + m];
      }
      team_arrive(cnt + C_ABSTG, lane);
      team_wait<0>(cnt + C_ABSTG, 4);
    }
    __builtin_amdgcn_s_setprio(0);
    STAMP(5);
    const u32 afr = (lane >> 5) * APITCH + (lane & 31) * 16;
    const u32 tag2 = (u32)(flag >> 32);
    const int sg = r * 4 + cw;
    const u32 po = opq(lane < 26 ? WS_P + ((g * 26 + lane) * 2 + (sg >> 5)) * 512 + (sg & 31) * 16 : OOB);
    u32x4 yp;
    f32x16 acc1 = {}, acc2 = {};
    {
      constexpr int AD = 2;
      bf16x8 af[24], bf[24];
#pragma unroll
      for (int s = 0; s < AD; ++s) { af[s] = *(LAS bf16x8*)(lds + L_A + afr + s * 2 * APITCH); bf[s] = *(LAS bf16x8*)(lds + L_B + afr + s * 2 * APITCH); }
      __builtin_amdgcn_sched_barrier(0);
#pragma unroll
      for (int s = 0; s < 24; ++s) {
        if (s + AD < 24) { af[s + AD] = *(LAS bf16x8*)(lds + L_A + afr + (s + AD) * 2 * APITCH); bf[s + AD] = *(LAS bf16x8*)(lds + L_B + afr + (s + AD) * 2 * APITCH); }
        if (s == P_PQ) yp = __builtin_amdgcn_raw_buffer_load_b128(rws, po, 0, 16);
        acc1 = __builtin_amdgcn_mfma_f32_32x32x16_bf16(af[s], z[s], acc1, 0, 0, 0);
        acc2 = __builtin_amdgcn_mfma_f32_32x32x16_bf16(bf[s], z[s], acc2, 0, 0, 0);
        __builtin_amdgcn_sched_barrier(0);
      }
    }
    float sum = 0.f;
#pragma unroll
    for (int e = 0; e < 12; ++e) sum += acc1[e] * acc2[e];
    { const float t = acc1[12] * acc2[12] + acc1[13] * acc2[13]; sum += lane < 32 ? t : 0.f; }
    sum = sum_rows(sum_row16(sum));
    STAMP(6);
    for (u32 it = 0; it < SPIN_MAX; ++it) {
      if (__all(lane >= 26 || (yp.y == tag && yp.w == tag2))) break;
      asm volatile("" ::: "memory");
      yp = __builtin_amdgcn_raw_buffer_load_b128(rws, po, 0, 16);
    }
    float part = sum_rows(sum_row16(__uint_as_float(yp.x)));
    if (lane == 0) {
      const __bf16 ob = (__bf16)(sum + part + bflo(bo));
      a.out[smp] = __builtin_bit_cast(uint16_t, ob);
    }
    STAMP(7);
  } else if (w < 4 && r < 13) {
      const rsrc_t rw1 = mkrs(a.w1, DIN * DIN * 2);
      const int mh = w & 1, kh = w >> 1, hh = lane >> 5;
      {
        u32x4 v[26];
        const u32 vo = (lane >> 3) * 1664 + r * 128 + (lane & 7) * 16;
        auto wrow = [&](int i) {
          const int s = 13 * (w & 1) + (i >> 1) + rot;
          return kh * 416 + (s >= 26 ? s - 26 : s) * 16 + (i & 1) * 8;
        };
        const int c = lane & 7;
        const u32 lo = L_W + (w * 208 + (lane >> 3)) * 128 + (((c >> 1) ^ (((lane >> 4) & 1) << 1)) * 32) + (c & 1) * 16;
#pragma unroll
        for (int i = 0; i < P_W0; ++i) v[i] = __builtin_amdgcn_raw_buffer_load_b128(rw1, vo, wrow(i) * 1664, 0);
        __builtin_amdgcn_sched_barrier(0);
        team_wait<1>(cnt + C_GISS, 1);
#pragma unroll
        for (int i = P_W0; i < 26; ++i) v[i] = __builtin_amdgcn_raw_buffer_load_b128(rw1, vo, wrow(i) * 1664, 0);
        STAMP(1);
#pragma unroll
        for (int i = 0; i < 26; ++i) *(LAS u32x4*)(lds + lo + i * 1024) = v[i];
      }
      team_arrive(cnt + C_W, lane);
      const u32 tag = get_tag(cnt);
      const u64 flag = mkflag(tag);
      f32x16 acc[2];
      f32x2 wf[8];
      {
        const CAS u32* bp = (const CAS u32*)(a.b1 + r * 64 + kh * 32);
        const CAS u32* wp = (const CAS u32*)(a.w2 + r * 64 + kh * 32);
        u32 bs[16], ws_[16];
#pragma unroll
        for (int t = 0; t < 16; ++t) { bs[t] = bp[t]; ws_[t] = wp[t]; }
#pragma unroll
        for (int t = 0; t < 16; ++t) asm volatile("" : "+s"(bs[t]), "+s"(ws_[t]));
#pragma unroll
        for (int t = 0; t < 4; ++t) {
          const u32 b0 = hh ? bs[4 * t + 2] : bs[4 * t], b1 = hh ? bs[4 * t + 3] : bs[4 * t + 1];
          const u32 w0 = hh ? ws_[4 * t + 2] : ws_[4 * t], w1 = hh ? ws_[4 * t + 3] : ws_[4 * t + 1];
          acc[0][4 * t] = bflo(b0); acc[0][4 * t + 1] = bfhi(b0); acc[0][4 * t + 2] = bflo(b1); acc[0][4 * t + 3] = bfhi(b1);
          acc[1][4 * t] = 0.f; acc[1][4 * t + 1] = 0.f; acc[1][4 * t + 2] = 0.f; acc[1][4 * t + 3] = 0.f;
          wf[2 * t] = (f32x2){bflo(w0), bfhi(w0)}; wf[2 * t + 1] = (f32x2){bflo(w1), bfhi(w1)};
          asm volatile("" : "+v"(wf[2 * t]), "+v"(wf[2 * t + 1]));
        }
      }
      STAMP(2);
      team_wait<0>(cnt + C_XRDY, 1);
      STAMP(3);
      constexpr int XW = P_XW, FD = 3;
      u32x4 xv[26];
      auto ldx = [&](int i) { const int s = i + rot; xv[i] = __builtin_amdgcn_raw_buffer_load_b128(rws, WS_X + lane * 16, (g * 104 + mh * 52 + kh * 26 + (s >= 26 ? s - 26 : s)) * 1024, 16); };
#pragma unroll
      for (int i = 0; i < XW; ++i) ldx(i);
      team_wait<0>(cnt + C_W, 4);
      __builtin_amdgcn_s_setprio(1);
      {
        const int q = (lane >> 2) & 3, pp = lane & 3, gam = (lane >> 4) & 1;
        const u32 rb = L_W + kh * 26 * 2048 + (8 * hh + q) * 128 + pp * 8;
        const u32 ad0 = rb + (((2 * kh + gam) ^ (q & 2)) * 32), ad1 = rb + (((2 * (kh ^ 1) + gam) ^ (q & 2)) * 32);
        bf16x8 fr[26][2];
        auto ldf = [&](int i) {
#pragma unroll
          for (int t = 0; t < 2; ++t) {
            const u32 o = (t ? ad1 : ad0) + i * 2048;
            const s16x4 y0 = __builtin_amdgcn_ds_read_tr16_b64_v4i16((LAS s16x4*)(lds + o));
            const s16x4 y1 = __builtin_amdgcn_ds_read_tr16_b64_v4i16((LAS s16x4*)(lds + o + 512));
            fr[i][t] = __builtin_bit_cast(bf16x8, __builtin_shufflevector(y0, y1, 0, 1, 2, 3, 4, 5, 6, 7));
          }
        };
#pragma unroll
        for (int i = 0; i < FD; ++i) ldf(i);
        __builtin_amdgcn_sched_barrier(0);
#pragma unroll
        for (int i = 0; i < 26; ++i) {
          if (i + XW < 26) ldx(i + XW);
          if (i == P_XI) lds_inc(cnt + C_XISS);
          if (i == 8) lds_inc(cnt + C_WD + kh);
          if (i == 22) team_wait<0>(cnt + C_WD + kh, 2);
          if (i + FD < 26) ldf(i + FD);
          const bf16x8 xb = __builtin_bit_cast(bf16x8, xv[i]);
          acc[0] = __builtin_amdgcn_mfma_f32_32x32x16_bf16(fr[i][0], xb, acc[0], 0, 0, 0);
          acc[1] = __builtin_amdgcn_mfma_f32_32x32x16_bf16(fr[i][1], xb, acc[1], 0, 0, 0);
          __builtin_amdgcn_sched_barrier(0);
        }
      }
      STAMP(4);
      {
        const u32 so = L_W + kh * 26 * 2048 + mh * 4096 + lane * 16;
#pragma unroll
        for (int t = 0; t < 4; ++t) *(LAS f32x4*)(lds + so + t * 1024) = (f32x4){acc[1][4 * t], acc[1][4 * t + 1], acc[1][4 * t + 2], acc[1][4 * t + 3]};
        team_arrive(cnt + C_RED + mh, lane);
        team_wait<0>(cnt + C_RED + mh, 2);
      }
      STAMP(5);
      float part;
      {
        constexpr float K1 = -2.0f * 1.4426950408889634f * 0.7978845608028654f, K2 = K1 * 0.044715f;
        const u32 si = L_W + (kh ^ 1) * 26 * 2048 + mh * 4096 + lane * 16;
        f32x4 o[4];
#pragma unroll
        for (int t = 0; t < 4; ++t) o[t] = *(LAS f32x4*)(lds + si + t * 1024);
        f32x2 h[8], u[8], y[8];
#pragma unroll
        for (int m = 0; m < 8; ++m) {
          h[m] = (f32x2){acc[0][2 * m], acc[0][2 * m + 1]} + (f32x2){o[m >> 1][2 * (m & 1)], o[m >> 1][2 * (m & 1) + 1]};
          u[m] = (h[m] * h[m] * K2 + K1) * h[m];
          y[m] = h[m] * wf[m];
        }
#pragma unroll
        for (int m = 0; m < 8; ++m) { u[m][0] = __builtin_amdgcn_exp2f(u[m][0]); u[m][1] = __builtin_amdgcn_exp2f(u[m][1]); }
#pragma unroll
        for (int m = 0; m < 8; ++m) { u[m] = u[m] + 1.0f; }
#pragma unroll
        for (int m = 0; m < 8; ++m) { u[m][0] = __builtin_amdgcn_rcpf(u[m][0]); u[m][1] = __builtin_amdgcn_rcpf(u[m][1]); }
        f32x2 p0 = y[0] * u[0], p1 = y[1] * u[1];
#pragma unroll
        for (int m = 2; m < 8; m += 2) { p0 += y[m] * u[m]; p1 += y[m + 1] * u[m + 1]; }
        p0 += p1;
        part = p0[0] + p0[1];
      }
      {
        auto sw = __builtin_amdgcn_permlane32_swap(__float_as_uint(part), __float_as_uint(part), false, false);
        part = __uint_as_float(sw[0]) + __uint_as_float(sw[1]);
      }
      {
        const u32x4 o = {__float_as_uint(part), tag, __float_as_uint(part), (u32)(flag >> 32)};
        __builtin_amdgcn_raw_buffer_store_b128(o, rws, lane < 32 ? WS_P + ((g * 26 + r * 2 + kh) * 2 + mh) * 512 + lane * 16 : OOB, 0, 16);
      }
      STAMP(6);
  } else {
    __builtin_amdgcn_s_setprio(3);
    u32 tag;
    if (r < 13) {
      u32 id[4]; u32x4 xg[8];
#pragma unroll
      for (int m = 0; m < 2; ++m) {
        const int p0 = 4 * r + 2 * m, t = p0 >= NF ? 1 : 0, f0 = p0 - t * NF;
        const u32x2 ia = __builtin_amdgcn_raw_buffer_load_b64(ridx, (lane & 31) * (NF * 4), ((g * 64 + t * 32) * NF + f0) * 4, 0);
        id[2 * m] = ia.x; id[2 * m + 1] = ia.y;
      }
#pragma unroll
      for (int k = 0; k < 8; ++k) xg[k] = __builtin_amdgcn_raw_buffer_load_b128(rtab, id[k >> 1] * 64 + (k & 1) * 32 + (lane >> 5) * 16, 0, 0);
      const u32 ep = load_epoch(a.ws);
      lds_inc(cnt + C_GISS);
      STAMP(1);
      tag = __builtin_amdgcn_readfirstlane(ep) + 1u;
      cnt[C_TAGV] = tag;
      team_arrive(cnt + C_TAG, lane);
#pragma unroll
      for (int k = 0; k < 8; ++k) __builtin_amdgcn_raw_buffer_store_b128(xg[k], rws, WS_X + lane * 16, (g * 104 + r * 8 + k) * 1024, 16);
      STAMP(2);
      drain_vm();
    } else {
      const u32 ep = load_epoch(a.ws);
      tag = __builtin_amdgcn_readfirstlane(ep) + 1u;
      cnt[C_TAGV] = tag;
      team_arrive(cnt + C_TAG, lane);
    }
    const u64 flag = mkflag(tag);
    if (lane == 0) store_flag(a.ws, WS_XF + (g * 16 + r) * FL, flag);
    STAMP(3);
    if (r < 13) {
      __builtin_amdgcn_s_sleep(P_XDELAY);
      poll_flags2_signal(a.ws + WS_XF + (g * 16 + (lane < 13 ? lane : 0)) * FL, flag, cnt + C_XRDY);
    }
    STAMP(4);
    __builtin_amdgcn_s_sleep(P_BDELAY);
    poll_flags<6>(a.ws + (lane < 32 ? WS_FA : WS_FB) + ((lane & 31) < NF ? (lane & 31) : 0) * FL, flag);
    lds_inc(cnt + C_ABRDY);
    STAMP(5);
  }
}

}

extern "C" void kernel_launch(void* const* d_in, const int* in_sizes, int n_in, void* d_out, int out_size,
                              void* d_ws, size_t ws_size, hipStream_t stream) {
  using namespace xdfm;
  Args a;
  a.idx = (const int*)d_in[0]; a.table = (const uint16_t*)d_in[1]; a.wc = (const uint16_t*)d_in[2];
  a.wco = (const uint16_t*)d_in[3]; a.w1 = (const uint16_t*)d_in[4]; a.b1 = (const uint16_t*)d_in[5];
  a.w2 = (const uint16_t*)d_in[6]; a.bo = (const uint16_t*)d_in[7]; a.out = (uint16_t*)d_out; a.ws = (char*)d_ws;
  a.table_bytes = (u32)in_sizes[1] * 2u; a.pad_ = 0;
  (void)hipFuncSetAttribute((const void*)xdfm_kernel, hipFuncAttributeMaxDynamicSharedMemorySize, L_TOTAL);
  xdfm_kernel<<<256, NWAVE * 64, L_TOTAL, stream>>>(a);
}
```

```cpp
#include <hip/hip_runtime.h>
#include <hip/hip_bf16.h>
#include <stdint.h>

#define P_ADELAY 8
#define P_BDELAY 8
#define P_WW 10
#define P_PQ 16
#define P_XI 14
#define P_XDELAY 12
#define P_CG 4
#define P_ROT 1
#define P_TOUCH 1
#define P_W0 14
#define P_CPF 1
#define P_XW 12
namespace xdfm {

typedef unsigned int u32;
typedef unsigned long long u64;
typedef u32 u32x2 __attribute__((ext_vector_type(2)));
typedef u32 u32x4 __attribute__((ext_vector_type(4)));
typedef short s16x4 __attribute__((ext_vector_type(4)));
typedef short s16x8 __attribute__((ext_vector_type(8)));
typedef float f32x2 __attribute__((ext_vector_type(2)));
typedef float f32x4 __attribute__((ext_vector_type(4)));
typedef float f32x16 __attribute__((ext_vector_type(16)));
typedef __bf16 bf16x2 __attribute__((ext_vector_type(2)));
typedef __bf16 bf16x8 __attribute__((ext_vector_type(8)));
#define GAS __attribute__((address_space(1)))
#define LAS __attribute__((address_space(3)))
typedef __amdgpu_buffer_rsrc_t rsrc_t;

constexpr int NF = 26, DIN = 832, NB = 1024, CUBE = 17576;

constexpr u32 WS_E = 0;
constexpr u32 FL = 128;
constexpr u32 WS_XF = 4096;
constexpr u32 WS_FA = 40960;
constexpr u32 WS_FB = 45056;
constexpr u32 WS_PA = 49152;
constexpr u32 WS_PB = 69632;
constexpr u32 WS_P = 131072;
constexpr u32 WS_X = 655360;
constexpr u32 WS_END = WS_X + 16 * 104 * 1024;
constexpr int NWAVE = 10;

constexpr u32 L_W = 0;
constexpr u32 APITCH = 432;
constexpr u32 L_A = 106496;
constexpr u32 L_B = L_A + 48 * APITCH;
constexpr u32 L_XS = L_B + 48 * APITCH;
constexpr u32 XS_S = 1824;
constexpr u32 L_CNT = L_XS + 4 * XS_S;
constexpr u32 L_TOTAL = L_CNT + 64;
constexpr u32 L_V3 = 0, L_W2C = 8192, L_W2S = 8448, L_U = 12288, L_RAW = 20480;
enum { C_TAGV = 12, C_TAG = 13, C_GISS = 14, C_ABRDY = 0, C_ABSTG = 1, C_W = 3, C_V3 = 4, C_U = 5, C_XISS = 6, C_XRDY = 7, C_WD = 8, C_RED = 10 };

constexpr int perm1(int j) { return j < 24 ? ((j & ~7) | ((j + 2) & 7)) : (j == 24 ? 25 : (j == 25 ? 24 : j)); }
struct Slots { int a[192]; int b[192]; bool v0[192]; bool v1[192]; int n; };
struct SlotBuilder {
  Slots s{}; bool vis[27][27] = {}; int n = 0;
  constexpr bool seen(int a, int b) const { return vis[a < b ? a : b][a < b ? b : a]; }
  constexpr void put(int a, int b) {
    s.a[n] = a; s.b[n] = b;
    s.v0[n] = !seen(a, b); vis[a < b ? a : b][a < b ? b : a] = true;
    const int na = perm1(a), nb = perm1(b);
    s.v1[n] = !seen(na, nb); vis[na < nb ? na : nb][na < nb ? nb : na] = true;
    ++n;
  }
};
constexpr int sg2(int m) { return perm1(perm1(2 * m)) >> 1; }
constexpr Slots make_slots() {
  SlotBuilder t{};
  for (int A = 0; A < 13; ++A)
    for (int B = A + 1; B < 13; ++B) {
      int ca = A, cb = B;
      while (!t.seen(2 * ca, 2 * cb)) {
        t.put(2 * ca, 2 * cb); t.put(2 * ca, 2 * cb + 1); t.put(2 * ca + 1, 2 * cb); t.put(2 * ca + 1, 2 * cb + 1);
        ca = sg2(ca); cb = sg2(cb);
      }
    }
  for (int A = 0; A < 13; ++A) {
    int ca = A;
    while (!t.seen(2 * ca, 2 * ca + 1)) {
      t.put(2 * ca, 2 * ca); t.put(2 * ca, 2 * ca + 1); t.put(2 * ca + 1, 2 * ca + 1); t.put(2 * ca + 1, 26);
      ca = sg2(ca);
    }
  }
  for (int a = 0; a < 27; ++a) { int ca = a; while (!t.seen(ca, 26)) { t.put(ca, 26); ca = perm1(perm1(ca)); } }
  t.s.n = t.n;
  for (int n = t.n; n < 192; ++n) { t.s.a[n] = 26; t.s.b[n] = 26; t.s.v0[n] = false; t.s.v1[n] = false; }
  return t.s;
}
constexpr Slots SL = make_slots();
static_assert(SL.n <= 192, "too many slots");

constexpr int UP = 33, ZB = 30 * UP, ZA = 676;
struct SymTab { u32 w[192][4]; };
constexpr SymTab make_symtab() {
  SymTab t{};
  for (int d = 0; d < 192; ++d) {
    int c = d >> 2, s = c >> 1, hh = c & 1;
    for (int e = 0; e < 2; ++e) {
      int sg = 8 * s + 2 * (d & 3) + e;
      int a = hh ? perm1(SL.a[sg]) : SL.a[sg], b = hh ? perm1(SL.b[sg]) : SL.b[sg];
      bool v = hh ? SL.v1[sg] : SL.v0[sg];
      u32 b1 = ZB, b2 = ZB, a1 = ZA, a2 = ZA;
      if (v) {
        b1 = a * UP + b; b2 = a == b ? ZB : b * UP + a;
        if (a < 26 && b < 26) { a1 = a * 26 + b; a2 = a == b ? ZA : b * 26 + a; }
      }
      t.w[d][e] = b1 | (b2 << 16);
      t.w[d][2 + e] = a1 | (a2 << 16);
    }
  }
  return t;
}
__device__ const SymTab SYMTAB = make_symtab();

constexpr u32 OOB = 0x7FFFFF00u;
constexpr u32 CODE_PART = 20;
constexpr u32 SPIN_MAX = 1u << 18;

__device__ __forceinline__ float bflo(u32 v) { return __uint_as_float(v << 16); }
__device__ __forceinline__ float bfhi(u32 v) { return __uint_as_float(v & 0xffff0000u); }
__device__ __forceinline__ u32 packbf(float a, float b) {
  f32x2 f = {a, b};
  bf16x2 z = __builtin_convertvector(f, bf16x2);
  return __builtin_bit_cast(u32, z);
}
__device__ __forceinline__ float mulf(float a, float b) { float r; asm("v_mul_f32 %0, %1, %2" : "=v"(r) : "v"(a), "v"(b)); return r; }
template <int E> __device__ __forceinline__ f32x2 pkmul_bc(f32x2 a, f32x2 b) {
  f32x2 r; asm("v_pk_mul_f32 %0, %1, %2 op_sel:[%3,0] op_sel_hi:[%3,1]" : "=v"(r) : "v"(a), "v"(b), "i"(E)); return r;
}
__device__ __forceinline__ float dot2bf(u32 a, u32 b, float c) {
  return __builtin_amdgcn_fdot2_f32_bf16(__builtin_bit_cast(bf16x2, a), __builtin_bit_cast(bf16x2, b), c, false);
}
__device__ __forceinline__ rsrc_t mkrs(const void* p, u32 bytes) { return __builtin_amdgcn_make_buffer_rsrc((void*)p, 0, bytes, 0x00020000); }
__device__ __forceinline__ u64 mkflag(u32 tag) { return (u64)tag | ((u64)(tag * 0x9E3779B1u + 0x7F4A7C15u) << 32); }
__device__ __forceinline__ void drain_vm() { asm volatile("s_waitcnt vmcnt(0)" ::: "memory"); }
__device__ __forceinline__ void lds_inc(LAS u32* cnt) {
  u64 keep;
  asm volatile("s_mov_b64 %0, exec\n\ts_mov_b64 exec, 1\n\tds_add_u32 %1, %2\n\ts_mov_b64 exec, %0"
               : "=&s"(keep) : "v"((u32)(size_t)cnt), "v"(1u) : "memory");
}
__device__ __forceinline__ void team_arrive(LAS u32* cnt, int) {
  asm volatile("s_waitcnt lgkmcnt(0)" ::: "memory");
  lds_inc(cnt);
}
__device__ __forceinline__ float sum_rows(float v) {
  auto a = __builtin_amdgcn_permlane32_swap(__float_as_uint(v), __float_as_uint(v), false, false);
  v = __uint_as_float(a[0]) + __uint_as_float(a[1]);
  auto b = __builtin_amdgcn_permlane16_swap(__float_as_uint(v), __float_as_uint(v), false, false);
  return __uint_as_float(b[0]) + __uint_as_float(b[1]);
}
template <int CTRL> __device__ __forceinline__ float dpp_add(float v) {
  return v + __uint_as_float(__builtin_amdgcn_update_dpp(0u, __float_as_uint(v), CTRL, 0xf, 0xf, true));
}
__device__ __forceinline__ float sum_row16(float v) {
  v = dpp_add<0x128>(v);
  v = dpp_add<0x124>(v);
  v = dpp_add<0x122>(v);
  v = dpp_add<0x121>(v);
  return v;
}

#define STAMP(k) do {} while (0)

struct Args {
  const int* idx; const uint16_t* table; const uint16_t* wc; const uint16_t* wco; const uint16_t* w1;
  const uint16_t* b1; const uint16_t* w2; const uint16_t* bo; uint16_t* out; char* ws;
  u32 table_bytes; u32 pad_;
};
#define CAS __attribute__((address_space(4)))

template <int SLEEP> __device__ __forceinline__ void team_wait(LAS u32* cnt, u32 target) {
  u32 v, s, n = SPIN_MAX;
  asm volatile(
      "1:\n\tds_read_b32 %0, %3\n\ts_waitcnt lgkmcnt(0)\n\tv_readfirstlane_b32 %1, %0\n\ts_cmp_ge_u32 %1, %4\n\ts_cbranch_scc1 2f\n\t"
      "s_sleep %5\n\ts_sub_u32 %2, %2, 1\n\ts_cbranch_scc0 1b\n2:"
      : "=&v"(v), "=&s"(s), "+s"(n) : "v"((u32)(size_t)cnt), "s"(target), "i"(SLEEP) : "memory", "scc");
}
template <int SLEEP> __device__ __forceinline__ void poll_flags(const char* p, u64 want) {
  u64 v; u32 n = SPIN_MAX;
  asm volatile(
      "1:\n\tglobal_load_dwordx2 %0, %2, off sc1\n\ts_waitcnt vmcnt(0)\n\tv_cmp_ne_u64 vcc, %3, %0\n\ts_nop 4\n\ts_cbranch_vccz 2f\n\t"
      "s_sleep %4\n\ts_sub_u32 %1, %1, 1\n\ts_cbranch_scc0 1b\n2:"
      : "=&v"(v), "+s"(n) : "v"(p), "s"(want), "i"(SLEEP) : "memory", "scc", "vcc");
}
__device__ __forceinline__ void poll_flags2_signal(const char* p, u64 want, LAS u32* cnt) {
  u64 v0, v1, keep; u32 n = SPIN_MAX;
  asm volatile(
      "global_load_dwordx2 %0, %4, off sc1\n\ts_sleep 2\n"
      "1:\n\tglobal_load_dwordx2 %1, %4, off sc1\n\ts_waitcnt vmcnt(1)\n\tv_cmp_ne_u64 vcc, %5, %0\n\ts_nop 4\n\ts_cbranch_vccz 2f\n\t"
      "global_load_dwordx2 %0, %4, off sc1\n\ts_waitcnt vmcnt(1)\n\tv_cmp_ne_u64 vcc, %5, %1\n\ts_nop 4\n\ts_cbranch_vccz 2f\n\t"
      "s_sub_u32 %2, %2, 1\n\ts_cbranch_scc0 1b\n"
      "2:\n\ts_mov_b64 %3, exec\n\ts_mov_b64 exec, 1\n\tds_add_u32 %6, %7\n\ts_mov_b64 exec, %3\n\ts_waitcnt vmcnt(0)"
      : "=&v"(v0), "=&v"(v1), "+s"(n), "=&s"(keep) : "v"(p), "s"(want), "v"((u32)(size_t)cnt), "v"(1u) : "memory", "scc", "vcc");
}
__device__ __forceinline__ void lds_barrier() {
  asm volatile("s_waitcnt lgkmcnt(0)" ::: "memory");
  __builtin_amdgcn_s_barrier();
  asm volatile("" ::: "memory");
}
__device__ __forceinline__ u32 load_epoch(const char* ws) {
  return __hip_atomic_load((const GAS u32*)(ws + WS_E), __ATOMIC_RELAXED, __HIP_MEMORY_SCOPE_AGENT);
}
__device__ __forceinline__ void store_flag(char* ws, u32 off, u64 flag) {
  __hip_atomic_store((GAS u64*)(ws + off), flag, __ATOMIC_RELAXED, __HIP_MEMORY_SCOPE_AGENT);
}
__device__ __forceinline__ u32 get_tag(LAS u32* cnt) {
  team_wait<0>(cnt + C_TAG, 1);
  return __builtin_amdgcn_readfirstlane(cnt[C_TAGV]);
}
__device__ __forceinline__ u32 opq(u32 v) { asm("" : "+v"(v)); return v; }

__global__ void __launch_bounds__(640, 3) xdfm_kernel(Args a) {
  extern __shared__ __attribute__((aligned(16))) char lds_raw[];
  LAS char* lds = (LAS char*)lds_raw;
  const int tid = threadIdx.x, lane = tid & 63;
  LAS u32* cnt = (LAS u32*)(lds + L_CNT);
  u32 cpf = 0;
  if (P_CPF && tid >= 512 && tid < 576) {
    u64 pc;
    asm volatile("s_getpc_b64 %0" : "=s"(pc));
    const u32 ln = ((blockIdx.x >> 3) & 7) * CODE_PART + (lane < CODE_PART ? lane : 0);
    const char* cp = (const char*)(pc & ~(u64)255) + ln * 128;
    asm volatile("global_load_dword %0, %1, off" : "+v"(cpf) : "v"(cp));
  }
  if (tid < 16) cnt[tid] = 0;
  const int pw = __builtin_amdgcn_readfirstlane(tid >> 6), w = pw == 0 ? 8 : (pw == 9 ? 9 : pw - 1);
  const int ftid = w * 64 + lane;
  const int b = blockIdx.x, j = b >> 3;
  const int jg = j < 6 ? (j >= 3 ? 1 : 0) : (j >= 19 ? 1 : 0);
  const int g = (b & 7) * 2 + jg, r = j < 6 ? 13 + j - 3 * jg : j - 6 - 13 * jg;
  asm volatile("" :: "s"(a.idx), "s"(a.table), "s"(a.wc), "s"(a.wco), "s"(a.w1), "s"(a.b1), "s"(a.w2), "s"(a.bo), "s"(a.out), "s"(a.ws), "s"(a.table_bytes));
  lds_barrier();
  const rsrc_t rws = mkrs(a.ws, WS_END);
  const rsrc_t ridx = mkrs(a.idx, NB * NF * 4), rtab = mkrs(a.table, a.table_bytes);
  const int rot = P_ROT ? 2 * ((r + 5 * g) % 13) : 0;

  if (w == 9) {
    if (b == 250) {
      team_wait<1>(cnt + C_GISS, 1);
#pragma unroll 1
      for (int i = 0; i < 6; ++i) __builtin_amdgcn_s_sleep(127);
    }
    return;
  }
  if (w < 4 && r >= 13) {
      const int p = g * 3 + r - 13;
      const rsrc_t rwc = mkrs(a.wc, 3 * CUBE * 2), rwo = mkrs(a.wco, 78 * 2);
      __builtin_amdgcn_s_setprio(3);
      if (p < NF) {
        const int i = p;
        u32x4 tb[3];
        const int hb = (w >> 1) * 13, el = (w & 1) * 64 + lane;
        const CAS u32* w3p = (const CAS u32*)(a.wco + 52);
        u32 w3[13];
#pragma unroll
        for (int h = 0; h < 13; ++h) w3[h] = w3p[h];
        u32x4 v[13];
#pragma unroll
        for (int h = 0; h < 13; ++h) v[h] = __builtin_amdgcn_raw_buffer_load_b128(rwc, el < 85 ? (2 * CUBE + hb * 676 + 8 * el) * 2 : OOB, h * 676 * 2, 0);
        u32 v2[2];
#pragma unroll
        for (int m = 0; m < 2; ++m) {
          const u32 d = ftid + 256 * m, h = (d * 5042u) >> 16, e = d - 13 * h;
          v2[m] = __builtin_amdgcn_raw_buffer_load_b32(rwc, d < 338 ? (CUBE + (h * NF + i) * NF) * 2 + e * 4 : OOB, 0, 0);
        }
        const u32 wq = __builtin_amdgcn_raw_buffer_load_b16(rwo, ftid < NF ? (NF + ftid) * 2 : OOB, 0, 0);
        const u32 w1i = __builtin_amdgcn_raw_buffer_load_b16(rwo, i * 2, 0, 0);
        if (w == 0) {
#pragma unroll
          for (int m = 0; m < 3; ++m) tb[m] = *(const GAS u32x4*)SYMTAB.w[lane + 64 * m];
        }
        __builtin_amdgcn_sched_barrier(0);
        STAMP(1);
        {
          float acc[8];
#pragma unroll
          for (int e = 0; e < 8; ++e) acc[e] = 0.f;
#pragma unroll
          for (int h = 0; h < 13; ++h) {
            const u32 we = (h & 1) ? w3[h >> 1] >> 16 : w3[h >> 1] & 0xffffu;
            const u32 wo = (h & 1) ? w3[(13 + h) >> 1] & 0xffffu : w3[(13 + h) >> 1] >> 16;
            const u32 wl = hb ? wo : we, wh = wl << 16;
#pragma unroll
            for (int e = 0; e < 4; ++e) { acc[2 * e] = dot2bf(v[h][e], wl, acc[2 * e]); acc[2 * e + 1] = dot2bf(v[h][e], wh, acc[2 * e + 1]); }
          }
          const u32 vo = opq(el < 85 ? L_V3 + (w >> 1) * 2816 + el * 32 : L_V3 + 5632 + lane * 32);
          *(LAS f32x4*)(lds + vo) = (f32x4){acc[0], acc[1], acc[2], acc[3]};
          *(LAS f32x4*)(lds + vo + 16) = (f32x4){acc[4], acc[5], acc[6], acc[7]};
#pragma unroll
          for (int m = 0; m < 2; ++m) *(LAS u32*)(lds + L_W2S + (ftid + 256 * m) * 4) = v2[m];
          if (w == 0) *(LAS u32*)(lds + L_W2C + lane * 4) = wq << 16;
        }
        const int tj = w >> 1, tp = w & 1, kq = lane >> 4;
        const int jr = 16 * tj + (lane & 15), jp = 16 * tp + (lane & 15);
        u32 oa[7], ob[7], oc[7];
#pragma unroll
        for (int s = 0; s < 7; ++s) {
          const int h = 4 * s + kq;
          const bool inv = h < NF && jp < NF;
          oa[s] = opq((h < NF && jr < NF) ? L_W2S + (h * NF + jr) * 2 : L_W2C + 63 * 4);
          ob[s] = opq(inv ? L_V3 + (h * NF + jp) * 4 : L_W2C + ((h < NF && jp == NF) ? h : 63) * 4);
          oc[s] = opq(inv ? L_V3 + 2816 + (h * NF + jp) * 4 : L_W2C + 63 * 4);
        }
        team_arrive(cnt + C_V3, lane);
        team_wait<0>(cnt + C_V3, 4);
        STAMP(2);
        u32 av[7]; float bv[7], bw[7];
#pragma unroll
        for (int s = 0; s < 7; ++s) { av[s] = *(LAS uint16_t*)(lds + oa[s]); bv[s] = *(LAS float*)(lds + ob[s]); bw[s] = *(LAS float*)(lds + oc[s]); }
#pragma unroll
        for (int s = 0; s < 7; ++s) bv[s] += bw[s];
        f32x4 u = {0.f, 0.f, 0.f, 0.f}, u2 = {0.f, 0.f, 0.f, 0.f};
#pragma unroll
        for (int s = 0; s < 7; ++s) {
          if (s & 1) u2 = __builtin_amdgcn_mfma_f32_16x16x4f32(bflo(av[s]), bv[s], u2, 0, 0, 0);
          else u = __builtin_amdgcn_mfma_f32_16x16x4f32(bflo(av[s]), bv[s], u, 0, 0, 0);
        }
        u += u2;
#pragma unroll
        for (int e = 0; e < 4; ++e) {
          const int jo = 16 * tj + 4 * kq + e;
          const float val = (jo == NF && jp == NF) ? bflo(w1i) : u[e];
          *(LAS float*)(lds + L_U + (jo * UP + jp) * 4) = val;
        }
        team_arrive(cnt + C_U, lane);
        if (w == 0) {
          team_wait<0>(cnt + C_U, 4);
          STAMP(3);
          LAS float* U = (LAS float*)(lds + L_U);
          float e[12];
#pragma unroll
          for (int m = 0; m < 3; ++m) {
            e[4 * m] = U[tb[m].x & 0xffff]; e[4 * m + 1] = U[tb[m].x >> 16]; e[4 * m + 2] = U[tb[m].y & 0xffff]; e[4 * m + 3] = U[tb[m].y >> 16];
          }
#pragma unroll
          for (int m = 0; m < 3; ++m)
            __builtin_amdgcn_raw_buffer_store_b32(packbf(e[4 * m] + e[4 * m + 1], e[4 * m + 2] + e[4 * m + 3]), rws, WS_PB + i * 768 + (lane + 64 * m) * 4, 0, 16);
          const u64 flagb = mkflag(get_tag(cnt));
          drain_vm();
          if (lane == 0) store_flag(a.ws, WS_FB + i * FL, flagb);
          STAMP(4);
        }
      } else {
        const int i = (p - NF) * 4 + w;
        const bool prod = p < 33 && i < NF;
        u32 v[6]; u32x4 tb[3];
#pragma unroll
        for (int m = 0; m < 6; ++m) { const int d = lane + 64 * m; v[m] = __builtin_amdgcn_raw_buffer_load_b32(rwc, (prod && d < 338) ? i * 1352 + d * 4 : OOB, 0, 0); }
#pragma unroll
        for (int m = 0; m < 3; ++m) tb[m] = *(const GAS u32x4*)SYMTAB.w[lane + 64 * m];
        __builtin_amdgcn_sched_barrier(0);
        STAMP(1);
        if (prod) {
          LAS char* raw = lds + L_RAW + w * 2048;
#pragma unroll
          for (int m = 0; m < 6; ++m) *(LAS u32*)(raw + (lane + 64 * m) * 4) = v[m];
          asm volatile("s_waitcnt lgkmcnt(0)" ::: "memory");
          LAS uint16_t* R = (LAS uint16_t*)raw;
          u32 e[12];
#pragma unroll
          for (int m = 0; m < 3; ++m) {
            e[4 * m] = R[tb[m].z & 0xffff]; e[4 * m + 1] = R[tb[m].z >> 16]; e[4 * m + 2] = R[tb[m].w & 0xffff]; e[4 * m + 3] = R[tb[m].w >> 16];
          }
#pragma unroll
          for (int m = 0; m < 3; ++m)
            __builtin_amdgcn_raw_buffer_store_b32(packbf(bflo(e[4 * m]) + bflo(e[4 * m + 1]), bflo(e[4 * m + 2]) + bflo(e[4 * m + 3])), rws,
                                                  WS_PA + i * 768 + (lane + 64 * m) * 4, 0, 16);
          const u64 flag0 = mkflag(get_tag(cnt));
          drain_vm();
          if (lane == 0) store_flag(a.ws, WS_FA + i * FL, flag0);
          STAMP(4);
        }
        if (p == 47 && w == 0) {
          const u32 tag0 = get_tag(cnt);
          const u64 flag0 = mkflag(tag0);
          __builtin_amdgcn_s_sleep(32);
#pragma unroll 1
          for (int m = 0; m < 4; ++m) poll_flags<16>(a.ws + WS_XF + (lane + 64 * m) * FL, flag0);
          if (lane == 0) __hip_atomic_store((GAS u32*)(a.ws + WS_E), tag0, __ATOMIC_RELAXED, __HIP_MEMORY_SCOPE_AGENT);
        }
      }
  } else if (w >= 4 && w < 8) {
    const int cw = w - 4, smp = g * 64 + r * 4 + cw;
    u32 id[2]; u32x4 xr[2];
    if (r < 13) __builtin_amdgcn_s_setprio(2);
#pragma unroll
    for (int m = 0; m < 2; ++m) { const int f = 16 * m + (lane >> 2); id[m] = __builtin_amdgcn_raw_buffer_load_b32(ridx, f < NF ? f * 4 : OOB, smp * NF * 4, 0); }
    u32 touch = 0;
    if (P_TOUCH && cw == 1) {
      const char* tp = lane < 7 ? (const char*)a.table + (size_t)lane * (a.table_bytes / 7 & ~3u)
                                : (lane == 7 ? a.ws + WS_P : (lane == 8 ? a.ws + WS_PA : (lane == 9 ? a.ws + WS_X + g * 104 * 1024 : (const char*)a.out)));
      asm volatile("global_load_dword %0, %1, off" : "+v"(touch) : "v"(tp));
    }
    const size_t bop = (size_t)a.bo;
    const u32 bo = *(const CAS u32*)(bop & ~(size_t)3) >> ((bop & 2) * 8);
#pragma unroll
    for (int m = 0; m < 2; ++m) { const int f = 16 * m + (lane >> 2); xr[m] = __builtin_amdgcn_raw_buffer_load_b128(rtab, f < NF ? id[m] * 64 + (lane & 3) * 16 : OOB, 0, 0); }
    __builtin_amdgcn_s_setprio(0);
    STAMP(1);
    f32x2 xp[14];
    {
      const u32 o = L_XS + cw * XS_S + (lane >> 2) * 64 + (lane & 3) * 16;
      *(LAS u32x4*)(lds + o) = xr[0];
      u32x4 x1 = xr[1];
      if ((lane >> 2) == NF - 16) x1 = (u32x4){0x3F803F80u, 0x3F803F80u, 0x3F803F80u, 0x3F803F80u};
      *(LAS u32x4*)(lds + opq((lane >> 2) < 12 ? o + 1024 : L_XS + cw * XS_S + 27 * 64 + (lane & 3) * 16)) = x1;
      asm volatile("s_waitcnt lgkmcnt(0)" :: "v"(touch), "v"(cpf) : "memory");
      const int hh = lane >> 5, q = (lane >> 2) & 3, pp = lane & 3;
      const u32 base = L_XS + cw * XS_S + ((lane >> 4) & 1) * 32 + pp * 8;
      const u32 a0 = base + (hh ? q + 2 : q) * 64, a1 = base + (hh ? ((q + 6) & 7) : q + 4) * 64;
      const u32 a6 = base + (24 + ((hh && q < 2) ? (q ^ 1) : q)) * 64;
#pragma unroll
      for (int t = 0; t < 7; ++t) {
        s16x4 y = __builtin_amdgcn_ds_read_tr16_b64_v4i16((LAS s16x4*)(lds + (t < 6 ? ((t & 1) ? a1 : a0) + (t >> 1) * 512 : a6)));
        u32x2 u = __builtin_bit_cast(u32x2, y);
        xp[2 * t] = (f32x2){bflo(u.x), bfhi(u.x)}; xp[2 * t + 1] = (f32x2){bflo(u.y), bfhi(u.y)};
      }
    }
    bf16x8 z[24];
#pragma unroll
    for (int s = 0; s < 24; ++s) {
      u32x4 zz;
#pragma unroll
      for (int e = 0; e < 4; ++e) {
        const int s1 = 8 * s + 2 * e, s2 = s1 + 1;
        const int a1 = SL.a[s1], b1 = SL.b[s1], a2 = SL.a[s2], b2 = SL.b[s2];
        if (a1 == a2 && (b1 & 1) == 0 && b1 < 26 && b2 == b1 + 1) {
          const f32x2 pr = (a1 & 1) ? pkmul_bc<1>(xp[a1 >> 1], xp[b1 >> 1]) : pkmul_bc<0>(xp[a1 >> 1], xp[b1 >> 1]);
          zz[e] = packbf(pr[0], pr[1]);
        } else {
          const float p1 = b1 == 26 ? xp[a1 >> 1][a1 & 1] : mulf(xp[a1 >> 1][a1 & 1], xp[b1 >> 1][b1 & 1]);
          const float p2 = b2 == 26 ? xp[a2 >> 1][a2 & 1] : mulf(xp[a2 >> 1][a2 & 1], xp[b2 >> 1][b2 & 1]);
          zz[e] = packbf(p1, p2);
        }
      }
      z[s] = __builtin_bit_cast(bf16x8, zz);
      asm volatile("" : "+v"(z[s]));
    }
    const u32 tag = get_tag(cnt);
    const u64 flag = mkflag(tag);
    STAMP(2);
    team_wait<1>(cnt + C_ABRDY, 1);
    STAMP(3);
    if (r < 13) team_wait<1>(cnt + C_XISS, 4);
    __builtin_amdgcn_s_setprio(2);
    STAMP(4);
    {
      u32x4 y[10];
#pragma unroll
      for (int m = 0; m < 10; ++m) {
        const u32 n = ((m % 5) * 4 + cw) * 64 + lane;
        y[m] = __builtin_amdgcn_raw_buffer_load_b128(rws, n < NF * 48 ? n * 16 : OOB, m < 5 ? WS_PA : WS_PB, 16);
      }
#pragma unroll
      for (int m = 0; m < 5; ++m) {
        const u32 n = (m * 4 + cw) * 64 + lane, i = (n * 1366u) >> 16, c = n - i * 48;
        const u32 o = opq(n < NF * 48 ? L_A + c * APITCH + i * 16 : L_A + 26 * 16);
        *(LAS u32x4*)(lds + o) = y[m];
        *(LAS u32x4*)(lds + o + (L_B - L_A)) = y[5 + m];
      }
      team_arrive(cnt + C_ABSTG, lane);
      team_wait<0>(cnt + C_ABSTG, 4);
    }
    __builtin_amdgcn_s_setprio(0);
    STAMP(5);
    const u32 afr = (lane >> 5) * APITCH + (lane & 31) * 16;
    const u32 tag2 = (u32)(flag >> 32);
    const int sg = r * 4 + cw;
    const u32 po = opq(lane < 26 ? WS_P + ((g * 26 + lane) * 2 + (sg >> 5)) * 512 + (sg & 31) * 16 : OOB);
    u32x4 yp;
    f32x16 acc1 = {}, acc2 = {};
    {
      constexpr int AD = 2;
      bf16x8 af[24], bf[24];
#pragma unroll
      for (int s = 0; s < AD; ++s) { af[s] = *(LAS bf16x8*)(lds + L_A + afr + s * 2 * APITCH); bf[s] = *(LAS bf16x8*)(lds + L_B + afr + s * 2 * APITCH); }
      __builtin_amdgcn_sched_barrier(0);
#pragma unroll
      for (int s = 0; s < 24; ++s) {
        if (s + AD < 24) { af[s + AD] = *(LAS bf16x8*)(lds + L_A + afr + (s + AD) * 2 * APITCH); bf[s + AD] = *(LAS bf16x8*)(lds + L_B + afr + (s + AD) * 2 * APITCH); }
        if (s == P_PQ) yp = __builtin_amdgcn_raw_buffer_load_b128(rws, po, 0, 16);
        acc1 = __builtin_amdgcn_mfma_f32_32x32x16_bf16(af[s], z[s], acc1, 0, 0, 0);
        acc2 = __builtin_amdgcn_mfma_f32_32x32x16_bf16(bf[s], z[s], acc2, 0, 0, 0);
        __builtin_amdgcn_sched_barrier(0);
      }
    }
    float sum = 0.f;
#pragma unroll
    for (int e = 0; e < 12; ++e) sum += acc1[e] * acc2[e];
    { const float t = acc1[12] * acc2[12] + acc1[13] * acc2[13]; sum += lane < 32 ? t : 0.f; }
    sum = sum_rows(sum_row16(sum));
    STAMP(6);
    for (u32 it = 0; it < SPIN_MAX; ++it) {
      if (__all(lane >= 26 || (yp.y == tag && yp.w == tag2))) break;
      asm volatile("" ::: "memory");
      yp = __builtin_amdgcn_raw_buffer_load_b128(rws, po, 0, 16);
    }
    float part = sum_rows(sum_row16(__uint_as_float(yp.x)));
    if (lane == 0) {
      const __bf16 ob = (__bf16)(sum + part + bflo(bo));
      a.out[smp] = __builtin_bit_cast(uint16_t, ob);
    }
    STAMP(7);
  } else if (w < 4 && r < 13) {
      const rsrc_t rw1 = mkrs(a.w1, DIN * DIN * 2);
      const int mh = w & 1, kh = w >> 1, hh = lane >> 5;
      {
        u32x4 v[26];
        const u32 vo = (lane >> 3) * 1664 + r * 128 + (lane & 7) * 16;
        auto wrow = [&](int i) {
          const int s = 13 * (w & 1) + (i >> 1) + rot;
          return kh * 416 + (s >= 26 ? s - 26 : s) * 16 + (i & 1) * 8;
        };
        const int c = lane & 7;
        const u32 lo = L_W + (w * 208 + (lane >> 3)) * 128 + (((c >> 1) ^ (((lane >> 4) & 1) << 1)) * 32) + (c & 1) * 16;
#pragma unroll
        for (int i = 0; i < P_W0; ++i) v[i] = __builtin_amdgcn_raw_buffer_load_b128(rw1, vo, wrow(i) * 1664, 0);
        __builtin_amdgcn_sched_barrier(0);
        team_wait<1>(cnt + C_GISS, 1);
#pragma unroll
        for (int i = P_W0; i < 26; ++i) v[i] = __builtin_amdgcn_raw_buffer_load_b128(rw1, vo, wrow(i) * 1664, 0);
        STAMP(1);
#pragma unroll
        for (int i = 0; i < 26; ++i) *(LAS u32x4*)(lds + lo + i * 1024) = v[i];
      }
      team_arrive(cnt + C_W, lane);
      const u32 tag = get_tag(cnt);
      const u64 flag = mkflag(tag);
      f32x16 acc[2];
      f32x2 wf[8];
      {
        const CAS u32* bp = (const CAS u32*)(a.b1 + r * 64 + kh * 32);
        const CAS u32* wp = (const CAS u32*)(a.w2 + r * 64 + kh * 32);
        u32 bs[16], ws_[16];
#pragma unroll
        for (int t = 0; t < 16; ++t) { bs[t] = bp[t]; ws_[t] = wp[t]; }
#pragma unroll
        for (int t = 0; t < 16; ++t) asm volatile("" : "+s"(bs[t]), "+s"(ws_[t]));
#pragma unroll
        for (int t = 0; t < 4; ++t) {
          const u32 b0 = hh ? bs[4 * t + 2] : bs[4 * t], b1 = hh ? bs[4 * t + 3] : bs[4 * t + 1];
          const u32 w0 = hh ? ws_[4 * t + 2] : ws_[4 * t], w1 = hh ? ws_[4 * t + 3] : ws_[4 * t + 1];
          acc[0][4 * t] = bflo(b0); acc[0][4 * t + 1] = bfhi(b0); acc[0][4 * t + 2] = bflo(b1); acc[0][4 * t + 3] = bfhi(b1);
          acc[1][4 * t] = 0.f; acc[1][4 * t + 1] = 0.f; acc[1][4 * t + 2] = 0.f; acc[1][4 * t + 3] = 0.f;
          wf[2 * t] = (f32x2){bflo(w0), bfhi(w0)}; wf[2 * t + 1] = (f32x2){bflo(w1), bfhi(w1)};
          asm volatile("" : "+v"(wf[2 * t]), "+v"(wf[2 * t + 1]));
        }
      }
      STAMP(2);
      team_wait<0>(cnt + C_XRDY, 1);
      STAMP(3);
      constexpr int XW = P_XW, FD = 3;
      u32x4 xv[26];
      auto ldx = [&](int i) { const int s = i + rot; xv[i] = __builtin_amdgcn_raw_buffer_load_b128(rws, WS_X + lane * 16, (g * 104 + mh * 52 + kh * 26 + (s >= 26 ? s - 26 : s)) * 1024, 16); };
#pragma unroll
      for (int i = 0; i < XW; ++i) ldx(i);
      team_wait<0>(cnt + C_W, 4);
      __builtin_amdgcn_s_setprio(1);
      {
        const int q = (lane >> 2) & 3, pp = lane & 3, gam = (lane >> 4) & 1;
        const u32 rb = L_W + kh * 26 * 2048 + (8 * hh + q) * 128 + pp * 8;
        const u32 ad0 = rb + (((2 * kh + gam) ^ (q & 2)) * 32), ad1 = rb + (((2 * (kh ^ 1) + gam) ^ (q & 2)) * 32);
        bf16x8 fr[26][2];
        auto ldf = [&](int i) {
#pragma unroll
          for (int t = 0; t < 2; ++t) {
            const u32 o = (t ? ad1 : ad0) + i * 2048;
            const s16x4 y0 = __builtin_amdgcn_ds_read_tr16_b64_v4i16((LAS s16x4*)(lds + o));
            const s16x4 y1 = __builtin_amdgcn_ds_read_tr16_b64_v4i16((LAS s16x4*)(lds + o + 512));
            fr[i][t] = __builtin_bit_cast(bf16x8, __builtin_shufflevector(y0, y1, 0, 1, 2, 3, 4, 5, 6, 7));
          }
        };
#pragma unroll
        for (int i = 0; i < FD; ++i) ldf(i);
        __builtin_amdgcn_sched_barrier(0);
#pragma unroll
        for (int i = 0; i < 26; ++i) {
          if (i + XW < 26) ldx(i + XW);
          if (i == P_XI) lds_inc(cnt + C_XISS);
          if (i == 8) lds_inc(cnt + C_WD + kh);
          if (i == 22) team_wait<0>(cnt + C_WD + kh, 2);
          if (i + FD < 26) ldf(i + FD);
          const bf16x8 xb = __builtin_bit_cast(bf16x8, xv[i]);
          acc[0] = __builtin_amdgcn_mfma_f32_32x32x16_bf16(fr[i][0], xb, acc[0], 0, 0, 0);
          acc[1] = __builtin_amdgcn_mfma_f32_32x32x16_bf16(fr[i][1], xb, acc[1], 0, 0, 0);
          __builtin_amdgcn_sched_barrier(0);
        }
      }
      STAMP(4);
      {
        const u32 so = L_W + kh * 26 * 2048 + mh * 4096 + lane * 16;
#pragma unroll
        for (int t = 0; t < 4; ++t) *(LAS f32x4*)(lds + so + t * 1024) = (f32x4){acc[1][4 * t], acc[1][4 * t + 1], acc[1][4 * t + 2], acc[1][4 * t + 3]};
        team_arrive(cnt + C_RED + mh, lane);
        team_wait<0>(cnt + C_RED + mh, 2);
      }
      STAMP(5);
      float part;
      {
        constexpr float K1 = -2.0f * 1.4426950408889634f * 0.7978845608028654f, K2 = K1 * 0.044715f;
        const u32 si = L_W + (kh ^ 1) * 26 * 2048 + mh * 4096 + lane * 16;
        f32x4 o[4];
#pragma unroll
        for (int t = 0; t < 4; ++t) o[t] = *(LAS f32x4*)(lds + si + t * 1024);
        f32x2 h[8], u[8], y[8];
#pragma unroll
        for (int m = 0; m < 8; ++m) {
          h[m] = (f32x2){acc[0][2 * m], acc[0][2 * m + 1]} + (f32x2){o[m >> 1][2 * (m & 1)], o[m >> 1][2 * (m & 1) + 1]};
          u[m] = (h[m] * h[m] * K2 + K1) * h[m];
          y[m] = h[m] * wf[m];
        }
#pragma unroll
        for (int m = 0; m < 8; ++m) { u[m][0] = __builtin_amdgcn_exp2f(u[m][0]); u[m][1] = __builtin_amdgcn_exp2f(u[m][1]); }
#pragma unroll
        for (int m = 0; m < 8; ++m) { u[m] = u[m] + 1.0f; }
#pragma unroll
        for (int m = 0; m < 8; ++m) { u[m][0] = __builtin_amdgcn_rcpf(u[m][0]); u[m][1] = __builtin_amdgcn_rcpf(u[m][1]); }
        f32x2 p0 = y[0] * u[0], p1 = y[1] * u[1];
#pragma unroll
        for (int m = 2; m < 8; m += 2) { p0 += y[m] * u[m]; p1 += y[m + 1] * u[m + 1]; }
        p0 += p1;
        part = p0[0] + p0[1];
      }
      {
        auto sw = __builtin_amdgcn_permlane32_swap(__float_as_uint(part), __float_as_uint(part), false, false);
        part = __uint_as_float(sw[0]) + __uint_as_float(sw[1]);
      }
      {
        const u32x4 o = {__float_as_uint(part), tag, __float_as_uint(part), (u32)(flag >> 32)};
        __builtin_amdgcn_raw_buffer_store_b128(o, rws, lane < 32 ? WS_P + ((g * 26 + r * 2 + kh) * 2 + mh) * 512 + lane * 16 : OOB, 0, 16);
      }
      STAMP(6);
  } else {
    __builtin_amdgcn_s_setprio(3);
    u32 tag;
    if (r < 13) {
      u32 id[4]; u32x4 xg[8];
#pragma unroll
      for (int m = 0; m < 2; ++m) {
        const int p0 = 4 * r + 2 * m, t = p0 >= NF ? 1 : 0, f0 = p0 - t * NF;
        const u32x2 ia = __builtin_amdgcn_raw_buffer_load_b64(ridx, (lane & 31) * (NF * 4), ((g * 64 + t * 32) * NF + f0) * 4, 0);
        id[2 * m] = ia.x; id[2 * m + 1] = ia.y;
      }
#pragma unroll
      for (int k = 0; k < 8; ++k) xg[k] = __builtin_amdgcn_raw_buffer_load_b128(rtab, id[k >> 1] * 64 + (k & 1) * 32 + (lane >> 5) * 16, 0, 0);
      const u32 ep = load_epoch(a.ws);
      lds_inc(cnt + C_GISS);
      STAMP(1);
      tag = __builtin_amdgcn_readfirstlane(ep) + 1u;
      cnt[C_TAGV] = tag;
      team_arrive(cnt + C_TAG, lane);
#pragma unroll
      for (int k = 0; k < 8; ++k) __builtin_amdgcn_raw_buffer_store_b128(xg[k], rws, WS_X + lane * 16, (g * 104 + r * 8 + k) * 1024, 16);
      STAMP(2);
      drain_vm();
    } else {
      const u32 ep = load_epoch(a.ws);
      tag = __builtin_amdgcn_readfirstlane(ep) + 1u;
      cnt[C_TAGV] = tag;
      team_arrive(cnt + C_TAG, lane);
    }
    const u64 flag = mkflag(tag);
    if (lane == 0) store_flag(a.ws, WS_XF + (g * 16 + r) * FL, flag);
    STAMP(3);
    if (r < 13) {
      __builtin_amdgcn_s_sleep(P_XDELAY);
      poll_flags2_signal(a.ws + WS_XF + (g * 16 + (lane < 13 ? lane : 0)) * FL, flag, cnt + C_XRDY);
    }
    STAMP(4);
    __builtin_amdgcn_s_sleep(P_BDELAY);
    poll_flags<6>(a.ws + (lane < 32 ? WS_FA : WS_FB) + ((lane & 31) < NF ? (lane & 31) : 0) * FL, flag);
    lds_inc(cnt + C_ABRDY);
    STAMP(5);
  }
}

}

extern "C" void kernel_launch(void* const* d_in, const int* in_sizes, int n_in, void* d_out, int out_size,
                              void* d_ws, size_t ws_size, hipStream_t stream) {
  using namespace xdfm;
  Args a;
  a.idx = (const int*)d_in[0]; a.table = (const uint16_t*)d_in[1]; a.wc = (const uint16_t*)d_in[2];
  a.wco = (const uint16_t*)d_in[3]; a.w1 = (const uint16_t*)d_in[4]; a.b1 = (const uint16_t*)d_in[5];
  a.w2 = (const uint16_t*)d_in[6]; a.bo = (const uint16_t*)d_in[7]; a.out = (uint16_t*)d_out; a.ws = (char*)d_ws;
  a.table_bytes = (u32)in_sizes[1] * 2u; a.pad_ = 0;
  (void)hipFuncSetAttribute((const void*)xdfm_kernel, hipFuncAttributeMaxDynamicSharedMemorySize, L_TOTAL);
  xdfm_kernel<<<256, NWAVE * 64, L_TOTAL, stream>>>(a);
}
```

```cpp
#include <hip/hip_runtime.h>
#include <hip/hip_bf16.h>
#include <stdint.h>

#define P_ADELAY 8
#define P_BDELAY 4
#define P_WW 10
#define P_PQ 16
#define P_XI 14
#define P_XDELAY 12
#define P_CG 4
#define P_ROT 1
#define P_TOUCH 1
#define P_W0 14
#define P_CPF 1
#define P_BSLEEP 6
#define P_XW 12
namespace xdfm {

typedef unsigned int u32;
typedef unsigned long long u64;
typedef u32 u32x2 __attribute__((ext_vector_type(2)));
typedef u32 u32x4 __attribute__((ext_vector_type(4)));
typedef short s16x4 __attribute__((ext_vector_type(4)));
typedef short s16x8 __attribute__((ext_vector_type(8)));
typedef float f32x2 __attribute__((ext_vector_type(2)));
typedef float f32x4 __attribute__((ext_vector_type(4)));
typedef float f32x16 __attribute__((ext_vector_type(16)));
typedef __bf16 bf16x2 __attribute__((ext_vector_type(2)));
typedef __bf16 bf16x8 __attribute__((ext_vector_type(8)));
#define GAS __attribute__((address_space(1)))
#define LAS __attribute__((address_space(3)))
typedef __amdgpu_buffer_rsrc_t rsrc_t;

constexpr int NF = 26, DIN = 832, NB = 1024, CUBE = 17576;

constexpr u32 WS_E = 0;
constexpr u32 FL = 128;
constexpr u32 WS_XF = 4096;
constexpr u32 WS_FA = 40960;
constexpr u32 WS_FB = 45056;
constexpr u32 WS_PA = 49152;
constexpr u32 WS_PB = 69632;
constexpr u32 WS_P = 131072;
constexpr u32 WS_X = 655360;
constexpr u32 WS_END = WS_X + 16 * 104 * 1024;
constexpr int NWAVE = 9;

constexpr u32 L_W = 0;
constexpr u32 APITCH = 432;
constexpr u32 L_A = 106496;
constexpr u32 L_B = L_A + 48 * APITCH;
constexpr u32 L_XS = L_B + 48 * APITCH;
constexpr u32 XS_S = 1824;
constexpr u32 L_CNT = L_XS + 4 * XS_S;
constexpr u32 L_TOTAL = L_CNT + 64;
constexpr u32 L_V3 = 0, L_W2C = 8192, L_W2S = 8448, L_U = 12288, L_RAW = 20480;
enum { C_TAGV = 12, C_TAG = 13, C_GISS = 14, C_ABRDY = 0, C_ABSTG = 1, C_W = 3, C_V3 = 4, C_U = 5, C_XISS = 6, C_XRDY = 7, C_WD = 8, C_RED = 10 };

constexpr int perm1(int j) { return j < 24 ? ((j & ~7) | ((j + 2) & 7)) : (j == 24 ? 25 : (j == 25 ? 24 : j)); }
struct Slots { int a[192]; int b[192]; bool v0[192]; bool v1[192]; int n; };
struct SlotBuilder {
  Slots s{}; bool vis[27][27] = {}; int n = 0;
  constexpr bool seen(int a, int b) const { return vis[a < b ? a : b][a < b ? b : a]; }
  constexpr void put(int a, int b) {
    s.a[n] = a; s.b[n] = b;
    s.v0[n] = !seen(a, b); vis[a < b ? a : b][a < b ? b : a] = true;
    const int na = perm1(a), nb = perm1(b);
    s.v1[n] = !seen(na, nb); vis[na < nb ? na : nb][na < nb ? nb : na] = true;
    ++n;
  }
};
constexpr int sg2(int m) { return perm1(perm1(2 * m)) >> 1; }
constexpr Slots make_slots() {
  SlotBuilder t{};
  for (int A = 0; A < 13; ++A)
    for (int B = A + 1; B < 13; ++B) {
      int ca = A, cb = B;
      while (!t.seen(2 * ca, 2 * cb)) {
        t.put(2 * ca, 2 * cb); t.put(2 * ca, 2 * cb + 1); t.put(2 * ca + 1, 2 * cb); t.put(2 * ca + 1, 2 * cb + 1);
        ca = sg2(ca); cb = sg2(cb);
      }
    }
  for (int A = 0; A < 13; ++A) {
    int ca = A;
    while (!t.seen(2 * ca, 2 * ca + 1)) {
      t.put(2 * ca, 2 * ca); t.put(2 * ca, 2 * ca + 1); t.put(2 * ca + 1, 2 * ca + 1); t.put(2 * ca + 1, 26);
      ca = sg2(ca);
    }
  }
  for (int a = 0; a < 27; ++a) { int ca = a; while (!t.seen(ca, 26)) { t.put(ca, 26); ca = perm1(perm1(ca)); } }
  t.s.n = t.n;
  for (int n = t.n; n < 192; ++n) { t.s.a[n] = 26; t.s.b[n] = 26; t.s.v0[n] = false; t.s.v1[n] = false; }
  return t.s;
}
constexpr Slots SL = make_slots();
static_assert(SL.n <= 192, "too many slots");

constexpr int UP = 33, ZB = 30 * UP, ZA = 676;
struct SymTab { u32 w[192][4]; };
constexpr SymTab make_symtab() {
  SymTab t{};
  for (int d = 0; d < 192; ++d) {
    int c = d >> 2, s = c >> 1, hh = c & 1;
    for (int e = 0; e < 2; ++e) {
      int sg = 8 * s + 2 * (d & 3) + e;
      int a = hh ? perm1(SL.a[sg]) : SL.a[sg], b = hh ? perm1(SL.b[sg]) : SL.b[sg];
      bool v = hh ? SL.v1[sg] : SL.v0[sg];
      u32 b1 = ZB, b2 = ZB, a1 = ZA, a2 = ZA;
      if (v) {
        b1 = a * UP + b; b2 = a == b ? ZB : b * UP + a;
        if (a < 26 && b < 26) { a1 = a * 26 + b; a2 = a == b ? ZA : b * 26 + a; }
      }
      t.w[d][e] = b1 | (b2 << 16);
      t.w[d][2 + e] = a1 | (a2 << 16);
    }
  }
  return t;
}
__device__ const SymTab SYMTAB = make_symtab();

constexpr u32 OOB = 0x7FFFFF00u;
constexpr u32 CODE_PART = 20;
constexpr u32 SPIN_MAX = 1u << 18;

__device__ __forceinline__ float bflo(u32 v) { return __uint_as_float(v << 16); }
__device__ __forceinline__ float bfhi(u32 v) { return __uint_as_float(v & 0xffff0000u); }
__device__ __forceinline__ u32 packbf(float a, float b) {
  f32x2 f = {a, b};
  bf16x2 z = __builtin_convertvector(f, bf16x2);
  return __builtin_bit_cast(u32, z);
}
__device__ __forceinline__ float mulf(float a, float b) { float r; asm("v_mul_f32 %0, %1, %2" : "=v"(r) : "v"(a), "v"(b)); return r; }
template <int E> __device__ __forceinline__ f32x2 pkmul_bc(f32x2 a, f32x2 b) {
  f32x2 r; asm("v_pk_mul_f32 %0, %1, %2 op_sel:[%3,0] op_sel_hi:[%3,1]" : "=v"(r) : "v"(a), "v"(b), "i"(E)); return r;
}
__device__ __forceinline__ float dot2bf(u32 a, u32 b, float c) {
  return __builtin_amdgcn_fdot2_f32_bf16(__builtin_bit_cast(bf16x2, a), __builtin_bit_cast(bf16x2, b), c, false);
}
__device__ __forceinline__ rsrc_t mkrs(const void* p, u32 bytes) { return __builtin_amdgcn_make_buffer_rsrc((void*)p, 0, bytes, 0x00020000); }
__device__ __forceinline__ u64 mkflag(u32 tag) { return (u64)tag | ((u64)(tag * 0x9E3779B1u + 0x7F4A7C15u) << 32); }
__device__ __forceinline__ void drain_vm() { asm volatile("s_waitcnt vmcnt(0)" ::: "memory"); }
__device__ __forceinline__ void lds_inc(LAS u32* cnt) {
  u64 keep;
  asm volatile("s_mov_b64 %0, exec\n\ts_mov_b64 exec, 1\n\tds_add_u32 %1, %2\n\ts_mov_b64 exec, %0"
               : "=&s"(keep) : "v"((u32)(size_t)cnt), "v"(1u) : "memory");
}
__device__ __forceinline__ void team_arrive(LAS u32* cnt, int) {
  asm volatile("s_waitcnt lgkmcnt(0)" ::: "memory");
  lds_inc(cnt);
}
__device__ __forceinline__ float sum_rows(float v) {
  auto a = __builtin_amdgcn_permlane32_swap(__float_as_uint(v), __float_as_uint(v), false, false);
  v = __uint_as_float(a[0]) + __uint_as_float(a[1]);
  auto b = __builtin_amdgcn_permlane16_swap(__float_as_uint(v), __float_as_uint(v), false, false);
  return __uint_as_float(b[0]) + __uint_as_float(b[1]);
}
template <int CTRL> __device__ __forceinline__ float dpp_add(float v) {
  return v + __uint_as_float(__builtin_amdgcn_update_dpp(0u, __float_as_uint(v), CTRL, 0xf, 0xf, true));
}
__device__ __forceinline__ float sum_row16(float v) {
  v = dpp_add<0x128>(v);
  v = dpp_add<0x124>(v);
  v = dpp_add<0x122>(v);
  v = dpp_add<0x121>(v);
  return v;
}

#define STAMP(k) do {} while (0)

struct Args {
  const int* idx; const uint16_t* table; const uint16_t* wc; const uint16_t* wco; const uint16_t* w1;
  const uint16_t* b1; const uint16_t* w2; const uint16_t* bo; uint16_t* out; char* ws;
  u32 table_bytes; u32 pad_;
};
#define CAS __attribute__((address_space(4)))

template <int SLEEP> __device__ __forceinline__ void team_wait(LAS u32* cnt, u32 target) {
  u32 v, s, n = SPIN_MAX;
  asm volatile(
      "1:\n\tds_read_b32 %0, %3\n\ts_waitcnt lgkmcnt(0)\n\tv_readfirstlane_b32 %1, %0\n\ts_cmp_ge_u32 %1, %4\n\ts_cbranch_scc1 2f\n\t"
      "s_sleep %5\n\ts_sub_u32 %2, %2, 1\n\ts_cbranch_scc0 1b\n2:"
      : "=&v"(v), "=&s"(s), "+s"(n) : "v"((u32)(size_t)cnt), "s"(target), "i"(SLEEP) : "memory", "scc");
}
template <int SLEEP> __device__ __forceinline__ void poll_flags(const char* p, u64 want) {
  u64 v; u32 n = SPIN_MAX;
  asm volatile(
      "1:\n\tglobal_load_dwordx2 %0, %2, off sc1\n\ts_waitcnt vmcnt(0)\n\tv_cmp_ne_u64 vcc, %3, %0\n\ts_nop 4\n\ts_cbranch_vccz 2f\n\t"
      "s_sleep %4\n\ts_sub_u32 %1, %1, 1\n\ts_cbranch_scc0 1b\n2:"
      : "=&v"(v), "+s"(n) : "v"(p), "s"(want), "i"(SLEEP) : "memory", "scc", "vcc");
}
__device__ __forceinline__ void poll_flags2_signal(const char* p, u64 want, LAS u32* cnt) {
  u64 v0, v1, keep; u32 n = SPIN_MAX;
  asm volatile(
      "global_load_dwordx2 %0, %4, off sc1\n\ts_sleep 2\n"
      "1:\n\tglobal_load_dwordx2 %1, %4, off sc1\n\ts_waitcnt vmcnt(1)\n\tv_cmp_ne_u64 vcc, %5, %0\n\ts_nop 4\n\ts_cbranch_vccz 2f\n\t"
      "global_load_dwordx2 %0, %4, off sc1\n\ts_waitcnt vmcnt(1)\n\tv_cmp_ne_u64 vcc, %5, %1\n\ts_nop 4\n\ts_cbranch_vccz 2f\n\t"
      "s_sub_u32 %2, %2, 1\n\ts_cbranch_scc0 1b\n"
      "2:\n\ts_mov_b64 %3, exec\n\ts_mov_b64 exec, 1\n\tds_add_u32 %6, %7\n\ts_mov_b64 exec, %3\n\ts_waitcnt vmcnt(0)"
      : "=&v"(v0), "=&v"(v1), "+s"(n), "=&s"(keep) : "v"(p), "s"(want), "v"((u32)(size_t)cnt), "v"(1u) : "memory", "scc", "vcc");
}
__device__ __forceinline__ void lds_barrier() {
  asm volatile("s_waitcnt lgkmcnt(0)" ::: "memory");
  __builtin_amdgcn_s_barrier();
  asm volatile("" ::: "memory");
}
__device__ __forceinline__ u32 load_epoch(const char* ws) {
  return __hip_atomic_load((const GAS u32*)(ws + WS_E), __ATOMIC_RELAXED, __HIP_MEMORY_SCOPE_AGENT);
}
__device__ __forceinline__ void store_flag(char* ws, u32 off, u64 flag) {
  __hip_atomic_store((GAS u64*)(ws + off), flag, __ATOMIC_RELAXED, __HIP_MEMORY_SCOPE_AGENT);
}
__device__ __forceinline__ u32 get_tag(LAS u32* cnt) {
  team_wait<0>(cnt + C_TAG, 1);
  return __builtin_amdgcn_readfirstlane(cnt[C_TAGV]);
}
__device__ __forceinline__ u32 opq(u32 v) { asm("" : "+v"(v)); return v; }

__global__ void __launch_bounds__(576, 3) xdfm_kernel(Args a) {
  extern __shared__ __attribute__((aligned(16))) char lds_raw[];
  LAS char* lds = (LAS char*)lds_raw;
  const int tid = threadIdx.x, lane = tid & 63;
  LAS u32* cnt = (LAS u32*)(lds + L_CNT);
  u32 cpf = 0;
  if (P_CPF && tid >= 512) {
    u64 pc;
    asm volatile("s_getpc_b64 %0" : "=s"(pc));
    const u32 ln = ((blockIdx.x >> 3) & 7) * CODE_PART + (lane < CODE_PART ? lane : 0);
    const char* cp = (const char*)(pc & ~(u64)255) + ln * 128;
    asm volatile("global_load_dword %0, %1, off" : "+v"(cpf) : "v"(cp));
  }
  if (tid < 16) cnt[tid] = 0;
  const int pw = __builtin_amdgcn_readfirstlane(tid >> 6), w = pw == 0 ? 8 : pw - 1;
  const int ftid = w * 64 + lane;
  const int b = blockIdx.x, j = b >> 3;
  const int jg = j < 6 ? (j >= 3 ? 1 : 0) : (j >= 19 ? 1 : 0);
  const int g = (b & 7) * 2 + jg, r = j < 6 ? 13 + j - 3 * jg : j - 6 - 13 * jg;
  asm volatile("" :: "s"(a.idx), "s"(a.table), "s"(a.wc), "s"(a.wco), "s"(a.w1), "s"(a.b1), "s"(a.w2), "s"(a.bo), "s"(a.out), "s"(a.ws), "s"(a.table_bytes));
  lds_barrier();
  const rsrc_t rws = mkrs(a.ws, WS_END);
  const rsrc_t ridx = mkrs(a.idx, NB * NF * 4), rtab = mkrs(a.table, a.table_bytes);
  const int rot = P_ROT ? 2 * ((r + 5 * g) % 13) : 0;

  if (w < 4 && r >= 13) {
      const int p = g * 3 + r - 13;
      const rsrc_t rwc = mkrs(a.wc, 3 * CUBE * 2), rwo = mkrs(a.wco, 78 * 2);
      __builtin_amdgcn_s_setprio(3);
      if (p < NF) {
        const int i = p;
        u32x4 tb[3];
        const int hb = (w >> 1) * 13, el = (w & 1) * 64 + lane;
        const CAS u32* w3p = (const CAS u32*)(a.wco + 52);
        u32 w3[13];
#pragma unroll
        for (int h = 0; h < 13; ++h) w3[h] = w3p[h];
        u32x4 v[13];
#pragma unroll
        for (int h = 0; h < 13; ++h) v[h] = __builtin_amdgcn_raw_buffer_load_b128(rwc, el < 85 ? (2 * CUBE + hb * 676 + 8 * el) * 2 : OOB, h * 676 * 2, 0);
        u32 v2[2];
#pragma unroll
        for (int m = 0; m < 2; ++m) {
          const u32 d = ftid + 256 * m, h = (d * 5042u) >> 16, e = d - 13 * h;
          v2[m] = __builtin_amdgcn_raw_buffer_load_b32(rwc, d < 338 ? (CUBE + (h * NF + i) * NF) * 2 + e * 4 : OOB, 0, 0);
        }
        const u32 wq = __builtin_amdgcn_raw_buffer_load_b16(rwo, ftid < NF ? (NF + ftid) * 2 : OOB, 0, 0);
        const u32 w1i = __builtin_amdgcn_raw_buffer_load_b16(rwo, i * 2, 0, 0);
        if (w == 0) {
#pragma unroll
          for (int m = 0; m < 3; ++m) tb[m] = *(const GAS u32x4*)SYMTAB.w[lane + 64 * m];
        }
        __builtin_amdgcn_sched_barrier(0);
        STAMP(1);
        {
          float acc[8];
#pragma unroll
          for (int e = 0; e < 8; ++e) acc[e] = 0.f;
#pragma unroll
          for (int h = 0; h < 13; ++h) {
            const u32 we = (h & 1) ? w3[h >> 1] >> 16 : w3[h >> 1] & 0xffffu;
            const u32 wo = (h & 1) ? w3[(13 + h) >> 1] & 0xffffu : w3[(13 + h) >> 1] >> 16;
            const u32 wl = hb ? wo : we, wh = wl << 16;
#pragma unroll
            for (int e = 0; e < 4; ++e) { acc[2 * e] = dot2bf(v[h][e], wl, acc[2 * e]); acc[2 * e + 1] = dot2bf(v[h][e], wh, acc[2 * e + 1]); }
          }
          const u32 vo = opq(el < 85 ? L_V3 + (w >> 1) * 2816 + el * 32 : L_V3 + 5632 + lane * 32);
          *(LAS f32x4*)(lds + vo) = (f32x4){acc[0], acc[1], acc[2], acc[3]};
          *(LAS f32x4*)(lds + vo + 16) = (f32x4){acc[4], acc[5], acc[6], acc[7]};
#pragma unroll
          for (int m = 0; m < 2; ++m) *(LAS u32*)(lds + L_W2S + (ftid + 256 * m) * 4) = v2[m];
          if (w == 0) *(LAS u32*)(lds + L_W2C + lane * 4) = wq << 16;
        }
        const int tj = w >> 1, tp = w & 1, kq = lane >> 4;
        const int jr = 16 * tj + (lane & 15), jp = 16 * tp + (lane & 15);
        u32 oa[7], ob[7], oc[7];
#pragma unroll
        for (int s = 0; s < 7; ++s) {
          const int h = 4 * s + kq;
          const bool inv = h < NF && jp < NF;
          oa[s] = opq((h < NF && jr < NF) ? L_W2S + (h * NF + jr) * 2 : L_W2C + 63 * 4);
          ob[s] = opq(inv ? L_V3 + (h * NF + jp) * 4 : L_W2C + ((h < NF && jp == NF) ? h : 63) * 4);
          oc[s] = opq(inv ? L_V3 + 2816 + (h * NF + jp) * 4 : L_W2C + 63 * 4);
        }
        team_arrive(cnt + C_V3, lane);
        team_wait<0>(cnt + C_V3, 4);
        STAMP(2);
        u32 av[7]; float bv[7], bw[7];
#pragma unroll
        for (int s = 0; s < 7; ++s) { av[s] = *(LAS uint16_t*)(lds + oa[s]); bv[s] = *(LAS float*)(lds + ob[s]); bw[s] = *(LAS float*)(lds + oc[s]); }
#pragma unroll
        for (int s = 0; s < 7; ++s) bv[s] += bw[s];
        f32x4 u = {0.f, 0.f, 0.f, 0.f}, u2 = {0.f, 0.f, 0.f, 0.f};
#pragma unroll
        for (int s = 0; s < 7; ++s) {
          if (s & 1) u2 = __builtin_amdgcn_mfma_f32_16x16x4f32(bflo(av[s]), bv[s], u2, 0, 0, 0);
          else u = __builtin_amdgcn_mfma_f32_16x16x4f32(bflo(av[s]), bv[s], u, 0, 0, 0);
        }
        u += u2;
#pragma unroll
        for (int e = 0; e < 4; ++e) {
          const int jo = 16 * tj + 4 * kq + e;
          const float val = (jo == NF && jp == NF) ? bflo(w1i) : u[e];
          *(LAS float*)(lds + L_U + (jo * UP + jp) * 4) = val;
        }
        team_arrive(cnt + C_U, lane);
        if (w == 0) {
          team_wait<0>(cnt + C_U, 4);
          STAMP(3);
          LAS float* U = (LAS float*)(lds + L_U);
          float e[12];
#pragma unroll
          for (int m = 0; m < 3; ++m) {
            e[4 * m] = U[tb[m].x & 0xffff]; e[4 * m + 1] = U[tb[m].x >> 16]; e[4 * m + 2] = U[tb[m].y & 0xffff]; e[4 * m + 3] = U[tb[m].y >> 16];
          }
#pragma unroll
          for (int m = 0; m < 3; ++m)
            __builtin_amdgcn_raw_buffer_store_b32(packbf(e[4 * m] + e[4 * m + 1], e[4 * m + 2] + e[4 * m + 3]), rws, WS_PB + i * 768 + (lane + 64 * m) * 4, 0, 16);
          const u64 flagb = mkflag(get_tag(cnt));
          drain_vm();
          if (lane == 0) store_flag(a.ws, WS_FB + i * FL, flagb);
          STAMP(4);
        }
      } else {
        const int i = (p - NF) * 4 + w;
        const bool prod = p < 33 && i < NF;
        u32 v[6]; u32x4 tb[3];
#pragma unroll
        for (int m = 0; m < 6; ++m) { const int d = lane + 64 * m; v[m] = __builtin_amdgcn_raw_buffer_load_b32(rwc, (prod && d < 338) ? i * 1352 + d * 4 : OOB, 0, 0); }
#pragma unroll
        for (int m = 0; m < 3; ++m) tb[m] = *(const GAS u32x4*)SYMTAB.w[lane + 64 * m];
        __builtin_amdgcn_sched_barrier(0);
        STAMP(1);
        if (prod) {
          LAS char* raw = lds + L_RAW + w * 2048;
#pragma unroll
          for (int m = 0; m < 6; ++m) *(LAS u32*)(raw + (lane + 64 * m) * 4) = v[m];
          asm volatile("s_waitcnt lgkmcnt(0)" ::: "memory");
          LAS uint16_t* R = (LAS uint16_t*)raw;
          u32 e[12];
#pragma unroll
          for (int m = 0; m < 3; ++m) {
            e[4 * m] = R[tb[m].z & 0xffff]; e[4 * m + 1] = R[tb[m].z >> 16]; e[4 * m + 2] = R[tb[m].w & 0xffff]; e[4 * m + 3] = R[tb[m].w >> 16];
          }
#pragma unroll
          for (int m = 0; m < 3; ++m)
            __builtin_amdgcn_raw_buffer_store_b32(packbf(bflo(e[4 * m]) + bflo(e[4 * m + 1]), bflo(e[4 * m + 2]) + bflo(e[4 * m + 3])), rws,
                                                  WS_PA + i * 768 + (lane + 64 * m) * 4, 0, 16);
          const u64 flag0 = mkflag(get_tag(cnt));
          drain_vm();
          if (lane == 0) store_flag(a.ws, WS_FA + i * FL, flag0);
          STAMP(4);
        }
        if (p == 47 && w == 0) {
          const u32 tag0 = get_tag(cnt);
          const u64 flag0 = mkflag(tag0);
          __builtin_amdgcn_s_sleep(32);
#pragma unroll 1
          for (int m = 0; m < 4; ++m) poll_flags<16>(a.ws + WS_XF + (lane + 64 * m) * FL, flag0);
          if (lane == 0) __hip_atomic_store((GAS u32*)(a.ws + WS_E), tag0, __ATOMIC_RELAXED, __HIP_MEMORY_SCOPE_AGENT);
        }
      }
  } else if (w >= 4 && w < 8) {
    const int cw = w - 4, smp = g * 64 + r * 4 + cw;
    u32 id[2]; u32x4 xr[2];
    if (r < 13) __builtin_amdgcn_s_setprio(2);
#pragma unroll
    for (int m = 0; m < 2; ++m) { const int f = 16 * m + (lane >> 2); id[m] = __builtin_amdgcn_raw_buffer_load_b32(ridx, f < NF ? f * 4 : OOB, smp * NF * 4, 0); }
    u32 touch = 0;
    if (P_TOUCH && cw == 1) {
      const char* tp = lane < 7 ? (const char*)a.table + (size_t)lane * (a.table_bytes / 7 & ~3u)
                                : (lane == 7 ? a.ws + WS_P : (lane == 8 ? a.ws + WS_PA : (lane == 9 ? a.ws + WS_X + g * 104 * 1024 : (const char*)a.out)));
      asm volatile("global_load_dword %0, %1, off" : "+v"(touch) : "v"(tp));
    }
    const size_t bop = (size_t)a.bo;
    const u32 bo = *(const CAS u32*)(bop & ~(size_t)3) >> ((bop & 2) * 8);
#pragma unroll
    for (int m = 0; m < 2; ++m) { const int f = 16 * m + (lane >> 2); xr[m] = __builtin_amdgcn_raw_buffer_load_b128(rtab, f < NF ? id[m] * 64 + (lane & 3) * 16 : OOB, 0, 0); }
    __builtin_amdgcn_s_setprio(0);
    STAMP(1);
    f32x2 xp[14];
    {
      const u32 o = L_XS + cw * XS_S + (lane >> 2) * 64 + (lane & 3) * 16;
      *(LAS u32x4*)(lds + o) = xr[0];
      u32x4 x1 = xr[1];
      if ((lane >> 2) == NF - 16) x1 = (u32x4){0x3F803F80u, 0x3F803F80u, 0x3F803F80u, 0x3F803F80u};
      *(LAS u32x4*)(lds + opq((lane >> 2) < 12 ? o + 1024 : L_XS + cw * XS_S + 27 * 64 + (lane & 3) * 16)) = x1;
      asm volatile("s_waitcnt lgkmcnt(0)" :: "v"(touch), "v"(cpf) : "memory");
      const int hh = lane >> 5, q = (lane >> 2) & 3, pp = lane & 3;
      const u32 base = L_XS + cw * XS_S + ((lane >> 4) & 1) * 32 + pp * 8;
      const u32 a0 = base + (hh ? q + 2 : q) * 64, a1 = base + (hh ? ((q + 6) & 7) : q + 4) * 64;
      const u32 a6 = base + (24 + ((hh && q < 2) ? (q ^ 1) : q)) * 64;
#pragma unroll
      for (int t = 0; t < 7; ++t) {
        s16x4 y = __builtin_amdgcn_ds_read_tr16_b64_v4i16((LAS s16x4*)(lds + (t < 6 ? ((t & 1) ? a1 : a0) + (t >> 1) * 512 : a6)));
        u32x2 u = __builtin_bit_cast(u32x2, y);
        xp[2 * t] = (f32x2){bflo(u.x), bfhi(u.x)}; xp[2 * t + 1] = (f32x2){bflo(u.y), bfhi(u.y)};
      }
    }
    bf16x8 z[24];
#pragma unroll
    for (int s = 0; s < 24; ++s) {
      u32x4 zz;
#pragma unroll
      for (int e = 0; e < 4; ++e) {
        const int s1 = 8 * s + 2 * e, s2 = s1 + 1;
        const int a1 = SL.a[s1], b1 = SL.b[s1], a2 = SL.a[s2], b2 = SL.b[s2];
        if (a1 == a2 && (b1 & 1) == 0 && b1 < 26 && b2 == b1 + 1) {
          const f32x2 pr = (a1 & 1) ? pkmul_bc<1>(xp[a1 >> 1], xp[b1 >> 1]) : pkmul_bc<0>(xp[a1 >> 1], xp[b1 >> 1]);
          zz[e] = packbf(pr[0], pr[1]);
        } else {
          const float p1 = b1 == 26 ? xp[a1 >> 1][a1 & 1] : mulf(xp[a1 >> 1][a1 & 1], xp[b1 >> 1][b1 & 1]);
          const float p2 = b2 == 26 ? xp[a2 >> 1][a2 & 1] : mulf(xp[a2 >> 1][a2 & 1], xp[b2 >> 1][b2 & 1]);
          zz[e] = packbf(p1, p2);
        }
      }
      z[s] = __builtin_bit_cast(bf16x8, zz);
      asm volatile("" : "+v"(z[s]));
    }
    const u32 tag = get_tag(cnt);
    const u64 flag = mkflag(tag);
    STAMP(2);
    if (cw == 0) {
      if (P_BDELAY) __builtin_amdgcn_s_sleep(P_BDELAY);
      poll_flags<P_BSLEEP>(a.ws + (lane < 32 ? WS_FA : WS_FB) + ((lane & 31) < NF ? (lane & 31) : 0) * FL, flag);
      lds_inc(cnt + C_ABRDY);
    } else team_wait<1>(cnt + C_ABRDY, 1);
    STAMP(3);
    if (r < 13 && P_XI >= 0) team_wait<1>(cnt + C_XISS, 4);
    __builtin_amdgcn_s_setprio(2);
    STAMP(4);
    {
      u32x4 y[10];
#pragma unroll
      for (int m = 0; m < 10; ++m) {
        const u32 n = ((m % 5) * 4 + cw) * 64 + lane;
        y[m] = __builtin_amdgcn_raw_buffer_load_b128(rws, n < NF * 48 ? n * 16 : OOB, m < 5 ? WS_PA : WS_PB, 16);
      }
#pragma unroll
      for (int m = 0; m < 5; ++m) {
        const u32 n = (m * 4 + cw) * 64 + lane, i = (n * 1366u) >> 16, c = n - i * 48;
        const u32 o = opq(n < NF * 48 ? L_A + c * APITCH + i * 16 : L_A + 26 * 16);
        *(LAS u32x4*)(lds + o) = y[m];
        *(LAS u32x4*)(lds + o + (L_B - L_A)) = y[5 + m];
      }
      team_arrive(cnt + C_ABSTG, lane);
      team_wait<0>(cnt + C_ABSTG, 4);
    }
    __builtin_amdgcn_s_setprio(0);
    STAMP(5);
    const u32 afr = (lane >> 5) * APITCH + (lane & 31) * 16;
    const u32 tag2 = (u32)(flag >> 32);
    const int sg = r * 4 + cw;
    const u32 po = opq(lane < 26 ? WS_P + ((g * 26 + lane) * 2 + (sg >> 5)) * 512 + (sg & 31) * 16 : OOB);
    u32x4 yp;
    f32x16 acc1 = {}, acc2 = {};
    {
      constexpr int AD = 2;
      bf16x8 af[24], bf[24];
#pragma unroll
      for (int s = 0; s < AD; ++s) { af[s] = *(LAS bf16x8*)(lds + L_A + afr + s * 2 * APITCH); bf[s] = *(LAS bf16x8*)(lds + L_B + afr + s * 2 * APITCH); }
      __builtin_amdgcn_sched_barrier(0);
#pragma unroll
      for (int s = 0; s < 24; ++s) {
        if (s + AD < 24) { af[s + AD] = *(LAS bf16x8*)(lds + L_A + afr + (s + AD) * 2 * APITCH); bf[s + AD] = *(LAS bf16x8*)(lds + L_B + afr + (s + AD) * 2 * APITCH); }
        if (s == P_PQ) yp = __builtin_amdgcn_raw_buffer_load_b128(rws, po, 0, 16);
        acc1 = __builtin_amdgcn_mfma_f32_32x32x16_bf16(af[s], z[s], acc1, 0, 0, 0);
        acc2 = __builtin_amdgcn_mfma_f32_32x32x16_bf16(bf[s], z[s], acc2, 0, 0, 0);
        __builtin_amdgcn_sched_barrier(0);
      }
    }
    float sum = 0.f;
#pragma unroll
    for (int e = 0; e < 12; ++e) sum += acc1[e] * acc2[e];
    { const float t = acc1[12] * acc2[12] + acc1[13] * acc2[13]; sum += lane < 32 ? t : 0.f; }
    sum = sum_rows(sum_row16(sum));
    STAMP(6);
    for (u32 it = 0; it < SPIN_MAX; ++it) {
      if (__all(lane >= 26 || (yp.y == tag && yp.w == tag2))) break;
      asm volatile("" ::: "memory");
      yp = __builtin_amdgcn_raw_buffer_load_b128(rws, po, 0, 16);
    }
    float part = sum_rows(sum_row16(__uint_as_float(yp.x)));
    if (lane == 0) {
      const __bf16 ob = (__bf16)(sum + part + bflo(bo));
      a.out[smp] = __builtin_bit_cast(uint16_t, ob);
    }
    STAMP(7);
  } else if (w < 4 && r < 13) {
      const rsrc_t rw1 = mkrs(a.w1, DIN * DIN * 2);
      const int mh = w & 1, kh = w >> 1, hh = lane >> 5;
      {
        u32x4 v[26];
        const u32 vo = (lane >> 3) * 1664 + r * 128 + (lane & 7) * 16;
        auto wrow = [&](int i) {
          const int s = 13 * (w & 1) + (i >> 1) + rot;
          return kh * 416 + (s >= 26 ? s - 26 : s) * 16 + (i & 1) * 8;
        };
        const int c = lane & 7;
        const u32 lo = L_W + (w * 208 + (lane >> 3)) * 128 + (((c >> 1) ^ (((lane >> 4) & 1) << 1)) * 32) + (c & 1) * 16;
#pragma unroll
        for (int i = 0; i < P_W0; ++i) v[i] = __builtin_amdgcn_raw_buffer_load_b128(rw1, vo, wrow(i) * 1664, 0);
        __builtin_amdgcn_sched_barrier(0);
        team_wait<1>(cnt + C_GISS, 1);
#pragma unroll
        for (int i = P_W0; i < 26; ++i) v[i] = __builtin_amdgcn_raw_buffer_load_b128(rw1, vo, wrow(i) * 1664, 0);
        STAMP(1);
#pragma unroll
        for (int i = 0; i < 26; ++i) *(LAS u32x4*)(lds + lo + i * 1024) = v[i];
      }
      team_arrive(cnt + C_W, lane);
      const u32 tag = get_tag(cnt);
      const u64 flag = mkflag(tag);
      f32x16 acc[2];
      f32x2 wf[8];
      {
        const CAS u32* bp = (const CAS u32*)(a.b1 + r * 64 + kh * 32);
        const CAS u32* wp = (const CAS u32*)(a.w2 + r * 64 + kh * 32);
        u32 bs[16], ws_[16];
#pragma unroll
        for (int t = 0; t < 16; ++t) { bs[t] = bp[t]; ws_[t] = wp[t]; }
#pragma unroll
        for (int t = 0; t < 16; ++t) asm volatile("" : "+s"(bs[t]), "+s"(ws_[t]));
#pragma unroll
        for (int t = 0; t < 4; ++t) {
          const u32 b0 = hh ? bs[4 * t + 2] : bs[4 * t], b1 = hh ? bs[4 * t + 3] : bs[4 * t + 1];
          const u32 w0 = hh ? ws_[4 * t + 2] : ws_[4 * t], w1 = hh ? ws_[4 * t + 3] : ws_[4 * t + 1];
          acc[0][4 * t] = bflo(b0); acc[0][4 * t + 1] = bfhi(b0); acc[0][4 * t + 2] = bflo(b1); acc[0][4 * t + 3] = bfhi(b1);
          acc[1][4 * t] = 0.f; acc[1][4 * t + 1] = 0.f; acc[1][4 * t + 2] = 0.f; acc[1][4 * t + 3] = 0.f;
          wf[2 * t] = (f32x2){bflo(w0), bfhi(w0)}; wf[2 * t + 1] = (f32x2){bflo(w1), bfhi(w1)};
          asm volatile("" : "+v"(wf[2 * t]), "+v"(wf[2 * t + 1]));
        }
      }
      STAMP(2);
      team_wait<0>(cnt + C_XRDY, 1);
      STAMP(3);
      constexpr int XW = P_XW, FD = 3;
      u32x4 xv[26];
      auto ldx = [&](int i) { const int s = i + rot; xv[i] = __builtin_amdgcn_raw_buffer_load_b128(rws, WS_X + lane * 16, (g * 104 + mh * 52 + kh * 26 + (s >= 26 ? s - 26 : s)) * 1024, 16); };
#pragma unroll
      for (int i = 0; i < XW; ++i) ldx(i);
      team_wait<0>(cnt + C_W, 4);
      __builtin_amdgcn_s_setprio(1);
      {
        const int q = (lane >> 2) & 3, pp = lane & 3, gam = (lane >> 4) & 1;
        const u32 rb = L_W + kh * 26 * 2048 + (8 * hh + q) * 128 + pp * 8;
        const u32 ad0 = rb + (((2 * kh + gam) ^ (q & 2)) * 32), ad1 = rb + (((2 * (kh ^ 1) + gam) ^ (q & 2)) * 32);
        bf16x8 fr[26][2];
        auto ldf = [&](int i) {
#pragma unroll
          for (int t = 0; t < 2; ++t) {
            const u32 o = (t ? ad1 : ad0) + i * 2048;
            const s16x4 y0 = __builtin_amdgcn_ds_read_tr16_b64_v4i16((LAS s16x4*)(lds + o));
            const s16x4 y1 = __builtin_amdgcn_ds_read_tr16_b64_v4i16((LAS s16x4*)(lds + o + 512));
            fr[i][t] = __builtin_bit_cast(bf16x8, __builtin_shufflevector(y0, y1, 0, 1, 2, 3, 4, 5, 6, 7));
          }
        };
#pragma unroll
        for (int i = 0; i < FD; ++i) ldf(i);
        __builtin_amdgcn_sched_barrier(0);
#pragma unroll
        for (int i = 0; i < 26; ++i) {
          if (i + XW < 26) ldx(i + XW);
          if (i == P_XI) lds_inc(cnt + C_XISS);
          if (i == 8) lds_inc(cnt + C_WD + kh);
          if (i == 22) team_wait<0>(cnt + C_WD + kh, 2);
          if (i + FD < 26) ldf(i + FD);
          const bf16x8 xb = __builtin_bit_cast(bf16x8, xv[i]);
          acc[0] = __builtin_amdgcn_mfma_f32_32x32x16_bf16(fr[i][0], xb, acc[0], 0, 0, 0);
          acc[1] = __builtin_amdgcn_mfma_f32_32x32x16_bf16(fr[i][1], xb, acc[1], 0, 0, 0);
          __builtin_amdgcn_sched_barrier(0);
        }
      }
      STAMP(4);
      {
        const u32 so = L_W + kh * 26 * 2048 + mh * 4096 + lane * 16;
#pragma unroll
        for (int t = 0; t < 4; ++t) *(LAS f32x4*)(lds + so + t * 1024) = (f32x4){acc[1][4 * t], acc[1][4 * t + 1], acc[1][4 * t + 2], acc[1][4 * t + 3]};
        team_arrive(cnt + C_RED + mh, lane);
        team_wait<0>(cnt + C_RED + mh, 2);
      }
      STAMP(5);
      float part;
      {
        constexpr float K1 = -2.0f * 1.4426950408889634f * 0.7978845608028654f, K2 = K1 * 0.044715f;
        const u32 si = L_W + (kh ^ 1) * 26 * 2048 + mh * 4096 + lane * 16;
        f32x4 o[4];
#pragma unroll
        for (int t = 0; t < 4; ++t) o[t] = *(LAS f32x4*)(lds + si + t * 1024);
        f32x2 h[8], u[8], y[8];
#pragma unroll
        for (int m = 0; m < 8; ++m) {
          h[m] = (f32x2){acc[0][2 * m], acc[0][2 * m + 1]} + (f32x2){o[m >> 1][2 * (m & 1)], o[m >> 1][2 * (m & 1) + 1]};
          u[m] = (h[m] * h[m] * K2 + K1) * h[m];
          y[m] = h[m] * wf[m];
        }
#pragma unroll
        for (int m = 0; m < 8; ++m) { u[m][0] = __builtin_amdgcn_exp2f(u[m][0]); u[m][1] = __builtin_amdgcn_exp2f(u[m][1]); }
#pragma unroll
        for (int m = 0; m < 8; ++m) { u[m] = u[m] + 1.0f; }
#pragma unroll
        for (int m = 0; m < 8; ++m) { u[m][0] = __builtin_amdgcn_rcpf(u[m][0]); u[m][1] = __builtin_amdgcn_rcpf(u[m][1]); }
        f32x2 p0 = y[0] * u[0], p1 = y[1] * u[1];
#pragma unroll
        for (int m = 2; m < 8; m += 2) { p0 += y[m] * u[m]; p1 += y[m + 1] * u[m + 1]; }
        p0 += p1;
        part = p0[0] + p0[1];
      }
      {
        auto sw = __builtin_amdgcn_permlane32_swap(__float_as_uint(part), __float_as_uint(part), false, false);
        part = __uint_as_float(sw[0]) + __uint_as_float(sw[1]);
      }
      {
        const u32x4 o = {__float_as_uint(part), tag, __float_as_uint(part), (u32)(flag >> 32)};
        __builtin_amdgcn_raw_buffer_store_b128(o, rws, lane < 32 ? WS_P + ((g * 26 + r * 2 + kh) * 2 + mh) * 512 + lane * 16 : OOB, 0, 16);
      }
      STAMP(6);
  } else {
    __builtin_amdgcn_s_setprio(3);
    u32 tag;
    if (r < 13) {
      u32 id[4]; u32x4 xg[8];
#pragma unroll
      for (int m = 0; m < 2; ++m) {
        const int p0 = 4 * r + 2 * m, t = p0 >= NF ? 1 : 0, f0 = p0 - t * NF;
        const u32x2 ia = __builtin_amdgcn_raw_buffer_load_b64(ridx, (lane & 31) * (NF * 4), ((g * 64 + t * 32) * NF + f0) * 4, 0);
        id[2 * m] = ia.x; id[2 * m + 1] = ia.y;
      }
#pragma unroll
      for (int k = 0; k < 8; ++k) xg[k] = __builtin_amdgcn_raw_buffer_load_b128(rtab, id[k >> 1] * 64 + (k & 1) * 32 + (lane >> 5) * 16, 0, 0);
      const u32 ep = load_epoch(a.ws);
      lds_inc(cnt + C_GISS);
      STAMP(1);
#pragma unroll
      for (int k = 0; k < 8; ++k) __builtin_amdgcn_raw_buffer_store_b128(xg[k], rws, WS_X + lane * 16, (g * 104 + r * 8 + k) * 1024, 16);
      __builtin_amdgcn_sched_barrier(0);
      tag = __builtin_amdgcn_readfirstlane(ep) + 1u;
      cnt[C_TAGV] = tag;
      team_arrive(cnt + C_TAG, lane);
      STAMP(2);
      drain_vm();
    } else {
      const u32 ep = load_epoch(a.ws);
      tag = __builtin_amdgcn_readfirstlane(ep) + 1u;
      cnt[C_TAGV] = tag;
      team_arrive(cnt + C_TAG, lane);
    }
    const u64 flag = mkflag(tag);
    if (lane == 0) store_flag(a.ws, WS_XF + (g * 16 + r) * FL, flag);
    STAMP(3);
    if (r < 13) {
      __builtin_amdgcn_s_sleep(P_XDELAY);
      poll_flags2_signal(a.ws + WS_XF + (g * 16 + (lane < 13 ? lane : 0)) * FL, flag, cnt + C_XRDY);
    }
    STAMP(4);
    STAMP(5);
  }
}

}

extern "C" void kernel_launch(void* const* d_in, const int* in_sizes, int n_in, void* d_out, int out_size,
                              void* d_ws, size_t ws_size, hipStream_t stream) {
  using namespace xdfm;
  Args a;
  a.idx = (const int*)d_in[0]; a.table = (const uint16_t*)d_in[1]; a.wc = (const uint16_t*)d_in[2];
  a.wco = (const uint16_t*)d_in[3]; a.w1 = (const uint16_t*)d_in[4]; a.b1 = (const uint16_t*)d_in[5];
  a.w2 = (const uint16_t*)d_in[6]; a.bo = (const uint16_t*)d_in[7]; a.out = (uint16_t*)d_out; a.ws = (char*)d_ws;
  a.table_bytes = (u32)in_sizes[1] * 2u; a.pad_ = 0;
  (void)hipFuncSetAttribute((const void*)xdfm_kernel, hipFuncAttributeMaxDynamicSharedMemorySize, L_TOTAL);
  xdfm_kernel<<<256, NWAVE * 64, L_TOTAL, stream>>>(a);
}
```

```cpp
#include <hip/hip_runtime.h>
#include <hip/hip_bf16.h>
#include <stdint.h>

#define P_ADELAY 4
#define P_BDELAY 4
#define P_WW 10
#define P_PQ 20
#define P_XI 14
#define P_XDELAY 12
#define P_CG 4
#define P_ROT 1
#define P_TOUCH 1
#define P_W0 14
#define P_CPF 1
#define P_BSLEEP 6
#define P_XW 12
namespace xdfm {

typedef unsigned int u32;
typedef unsigned long long u64;
typedef u32 u32x2 __attribute__((ext_vector_type(2)));
typedef u32 u32x4 __attribute__((ext_vector_type(4)));
typedef short s16x4 __attribute__((ext_vector_type(4)));
typedef short s16x8 __attribute__((ext_vector_type(8)));
typedef float f32x2 __attribute__((ext_vector_type(2)));
typedef float f32x4 __attribute__((ext_vector_type(4)));
typedef float f32x16 __attribute__((ext_vector_type(16)));
typedef __bf16 bf16x2 __attribute__((ext_vector_type(2)));
typedef __bf16 bf16x8 __attribute__((ext_vector_type(8)));
#define GAS __attribute__((address_space(1)))
#define LAS __attribute__((address_space(3)))
typedef __amdgpu_buffer_rsrc_t rsrc_t;

constexpr int NF = 26, DIN = 832, NB = 1024, CUBE = 17576;

constexpr u32 WS_E = 0;
constexpr u32 FL = 128;
constexpr u32 WS_XF = 4096;
constexpr u32 WS_FA = 40960;
constexpr u32 WS_FB = 45056;
constexpr u32 WS_PA = 49152;
constexpr u32 WS_PB = 69632;
constexpr u32 WS_P = 131072;
constexpr u32 WS_X = 655360;
constexpr u32 WS_END = WS_X + 16 * 104 * 1024;
constexpr int NWAVE = 9;

constexpr u32 L_W = 0;
constexpr u32 APITCH = 432;
constexpr u32 L_A = 106496;
constexpr u32 L_B = L_A + 48 * APITCH;
constexpr u32 L_XS = L_B + 48 * APITCH;
constexpr u32 XS_S = 1824;
constexpr u32 L_CNT = L_XS + 4 * XS_S;
constexpr u32 L_TOTAL = L_CNT + 64;
constexpr u32 L_V3 = 0, L_W2C = 8192, L_W2S = 8448, L_U = 12288, L_RAW = 20480;
enum { C_TAGV = 12, C_TAG = 13, C_GISS = 14, C_ARDY = 0, C_ASTG = 1, C_BRDY = 2, C_BSTG = 15, C_W = 3, C_V3 = 4, C_U = 5, C_XISS = 6, C_XRDY = 7, C_WD = 8, C_RED = 10 };

constexpr int perm1(int j) { return j < 24 ? ((j & ~7) | ((j + 2) & 7)) : (j == 24 ? 25 : (j == 25 ? 24 : j)); }
struct Slots { int a[192]; int b[192]; bool v0[192]; bool v1[192]; int n; };
struct SlotBuilder {
  Slots s{}; bool vis[27][27] = {}; int n = 0;
  constexpr bool seen(int a, int b) const { return vis[a < b ? a : b][a < b ? b : a]; }
  constexpr void put(int a, int b) {
    s.a[n] = a; s.b[n] = b;
    s.v0[n] = !seen(a, b); vis[a < b ? a : b][a < b ? b : a] = true;
    const int na = perm1(a), nb = perm1(b);
    s.v1[n] = !seen(na, nb); vis[na < nb ? na : nb][na < nb ? nb : na] = true;
    ++n;
  }
};
constexpr int sg2(int m) { return perm1(perm1(2 * m)) >> 1; }
constexpr Slots make_slots() {
  SlotBuilder t{};
  for (int A = 0; A < 13; ++A)
    for (int B = A + 1; B < 13; ++B) {
      int ca = A, cb = B;
      while (!t.seen(2 * ca, 2 * cb)) {
        t.put(2 * ca, 2 * cb); t.put(2 * ca, 2 * cb + 1); t.put(2 * ca + 1, 2 * cb); t.put(2 * ca + 1, 2 * cb + 1);
        ca = sg2(ca); cb = sg2(cb);
      }
    }
  for (int A = 0; A < 13; ++A) {
    int ca = A;
    while (!t.seen(2 * ca, 2 * ca + 1)) {
      t.put(2 * ca, 2 * ca); t.put(2 * ca, 2 * ca + 1); t.put(2 * ca + 1, 2 * ca + 1); t.put(2 * ca + 1, 26);
      ca = sg2(ca);
    }
  }
  for (int a = 0; a < 27; ++a) { int ca = a; while (!t.seen(ca, 26)) { t.put(ca, 26); ca = perm1(perm1(ca)); } }
  t.s.n = t.n;
  for (int n = t.n; n < 192; ++n) { t.s.a[n] = 26; t.s.b[n] = 26; t.s.v0[n] = false; t.s.v1[n] = false; }
  return t.s;
}
constexpr Slots SL = make_slots();
static_assert(SL.n <= 192, "too many slots");

constexpr int UP = 33, ZB = 30 * UP, ZA = 676;
struct SymTab { u32 w[192][4]; };
constexpr SymTab make_symtab() {
  SymTab t{};
  for (int d = 0; d < 192; ++d) {
    int c = d >> 2, s = c >> 1, hh = c & 1;
    for (int e = 0; e < 2; ++e) {
      int sg = 8 * s + 2 * (d & 3) + e;
      int a = hh ? perm1(SL.a[sg]) : SL.a[sg], b = hh ? perm1(SL.b[sg]) : SL.b[sg];
      bool v = hh ? SL.v1[sg] : SL.v0[sg];
      u32 b1 = ZB, b2 = ZB, a1 = ZA, a2 = ZA;
      if (v) {
        b1 = a * UP + b; b2 = a == b ? ZB : b * UP + a;
        if (a < 26 && b < 26) { a1 = a * 26 + b; a2 = a == b ? ZA : b * 26 + a; }
      }
      t.w[d][e] = b1 | (b2 << 16);
      t.w[d][2 + e] = a1 | (a2 << 16);
    }
  }
  return t;
}
__device__ const SymTab SYMTAB = make_symtab();

constexpr u32 OOB = 0x7FFFFF00u;
constexpr u32 CODE_PART = 20;
constexpr u32 SPIN_MAX = 1u << 18;

__device__ __forceinline__ float bflo(u32 v) { return __uint_as_float(v << 16); }
__device__ __forceinline__ float bfhi(u32 v) { return __uint_as_float(v & 0xffff0000u); }
__device__ __forceinline__ u32 packbf(float a, float b) {
  f32x2 f = {a, b};
  bf16x2 z = __builtin_convertvector(f, bf16x2);
  return __builtin_bit_cast(u32, z);
}
__device__ __forceinline__ float mulf(float a, float b) { float r; asm("v_mul_f32 %0, %1, %2" : "=v"(r) : "v"(a), "v"(b)); return r; }
template <int E> __device__ __forceinline__ f32x2 pkmul_bc(f32x2 a, f32x2 b) {
  f32x2 r; asm("v_pk_mul_f32 %0, %1, %2 op_sel:[%3,0] op_sel_hi:[%3,1]" : "=v"(r) : "v"(a), "v"(b), "i"(E)); return r;
}
__device__ __forceinline__ float dot2bf(u32 a, u32 b, float c) {
  return __builtin_amdgcn_fdot2_f32_bf16(__builtin_bit_cast(bf16x2, a), __builtin_bit_cast(bf16x2, b), c, false);
}
__device__ __forceinline__ rsrc_t mkrs(const void* p, u32 bytes) { return __builtin_amdgcn_make_buffer_rsrc((void*)p, 0, bytes, 0x00020000); }
__device__ __forceinline__ u64 mkflag(u32 tag) { return (u64)tag | ((u64)(tag * 0x9E3779B1u + 0x7F4A7C15u) << 32); }
__device__ __forceinline__ void drain_vm() { asm volatile("s_waitcnt vmcnt(0)" ::: "memory"); }
__device__ __forceinline__ void lds_inc(LAS u32* cnt) {
  u64 keep;
  asm volatile("s_mov_b64 %0, exec\n\ts_mov_b64 exec, 1\n\tds_add_u32 %1, %2\n\ts_mov_b64 exec, %0"
               : "=&s"(keep) : "v"((u32)(size_t)cnt), "v"(1u) : "memory");
}
__device__ __forceinline__ void team_arrive(LAS u32* cnt, int) {
  asm volatile("s_waitcnt lgkmcnt(0)" ::: "memory");
  lds_inc(cnt);
}
__device__ __forceinline__ float sum_rows(float v) {
  auto a = __builtin_amdgcn_permlane32_swap(__float_as_uint(v), __float_as_uint(v), false, false);
  v = __uint_as_float(a[0]) + __uint_as_float(a[1]);
  auto b = __builtin_amdgcn_permlane16_swap(__float_as_uint(v), __float_as_uint(v), false, false);
  return __uint_as_float(b[0]) + __uint_as_float(b[1]);
}
template <int CTRL> __device__ __forceinline__ float dpp_add(float v) {
  return v + __uint_as_float(__builtin_amdgcn_update_dpp(0u, __float_as_uint(v), CTRL, 0xf, 0xf, true));
}
__device__ __forceinline__ float sum_row16(float v) {
  v = dpp_add<0x128>(v);
  v = dpp_add<0x124>(v);
  v = dpp_add<0x122>(v);
  v = dpp_add<0x121>(v);
  return v;
}

#define STAMP(k) do {} while (0)

struct Args {
  const int* idx; const uint16_t* table; const uint16_t* wc; const uint16_t* wco; const uint16_t* w1;
  const uint16_t* b1; const uint16_t* w2; const uint16_t* bo; uint16_t* out; char* ws;
  u32 table_bytes; u32 pad_;
};
#define CAS __attribute__((address_space(4)))

template <int SLEEP> __device__ __forceinline__ void team_wait(LAS u32* cnt, u32 target) {
  u32 v, s, n = SPIN_MAX;
  asm volatile(
      "1:\n\tds_read_b32 %0, %3\n\ts_waitcnt lgkmcnt(0)\n\tv_readfirstlane_b32 %1, %0\n\ts_cmp_ge_u32 %1, %4\n\ts_cbranch_scc1 2f\n\t"
      "s_sleep %5\n\ts_sub_u32 %2, %2, 1\n\ts_cbranch_scc0 1b\n2:"
      : "=&v"(v), "=&s"(s), "+s"(n) : "v"((u32)(size_t)cnt), "s"(target), "i"(SLEEP) : "memory", "scc");
}
template <int SLEEP> __device__ __forceinline__ void poll_flags(const char* p, u64 want) {
  u64 v; u32 n = SPIN_MAX;
  asm volatile(
      "1:\n\tglobal_load_dwordx2 %0, %2, off sc1\n\ts_waitcnt vmcnt(0)\n\tv_cmp_ne_u64 vcc, %3, %0\n\ts_nop 4\n\ts_cbranch_vccz 2f\n\t"
      "s_sleep %4\n\ts_sub_u32 %1, %1, 1\n\ts_cbranch_scc0 1b\n2:"
      : "=&v"(v), "+s"(n) : "v"(p), "s"(want), "i"(SLEEP) : "memory", "scc", "vcc");
}
__device__ __forceinline__ void poll_flags2_signal(const char* p, u64 want, LAS u32* cnt) {
  u64 v0, v1, keep; u32 n = SPIN_MAX;
  asm volatile(
      "global_load_dwordx2 %0, %4, off sc1\n\ts_sleep 2\n"
      "1:\n\tglobal_load_dwordx2 %1, %4, off sc1\n\ts_waitcnt vmcnt(1)\n\tv_cmp_ne_u64 vcc, %5, %0\n\ts_nop 4\n\ts_cbranch_vccz 2f\n\t"
      "global_load_dwordx2 %0, %4, off sc1\n\ts_waitcnt vmcnt(1)\n\tv_cmp_ne_u64 vcc, %5, %1\n\ts_nop 4\n\ts_cbranch_vccz 2f\n\t"
      "s_sub_u32 %2, %2, 1\n\ts_cbranch_scc0 1b\n"
      "2:\n\ts_mov_b64 %3, exec\n\ts_mov_b64 exec, 1\n\tds_add_u32 %6, %7\n\ts_mov_b64 exec, %3\n\ts_waitcnt vmcnt(0)"
      : "=&v"(v0), "=&v"(v1), "+s"(n), "=&s"(keep) : "v"(p), "s"(want), "v"((u32)(size_t)cnt), "v"(1u) : "memory", "scc", "vcc");
}
__device__ __forceinline__ void lds_barrier() {
  asm volatile("s_waitcnt lgkmcnt(0)" ::: "memory");
  __builtin_amdgcn_s_barrier();
  asm volatile("" ::: "memory");
}
__device__ __forceinline__ u32 load_epoch(const char* ws) {
  return __hip_atomic_load((const GAS u32*)(ws + WS_E), __ATOMIC_RELAXED, __HIP_MEMORY_SCOPE_AGENT);
}
__device__ __forceinline__ void store_flag(char* ws, u32 off, u64 flag) {
  __hip_atomic_store((GAS u64*)(ws + off), flag, __ATOMIC_RELAXED, __HIP_MEMORY_SCOPE_AGENT);
}
__device__ __forceinline__ u32 get_tag(LAS u32* cnt) {
  team_wait<0>(cnt + C_TAG, 1);
  return __builtin_amdgcn_readfirstlane(cnt[C_TAGV]);
}
__device__ __forceinline__ u32 opq(u32 v) { asm("" : "+v"(v)); return v; }

__global__ void __launch_bounds__(576, 3) xdfm_kernel(Args a) {
  extern __shared__ __attribute__((aligned(16))) char lds_raw[];
  LAS char* lds = (LAS char*)lds_raw;
  const int tid = threadIdx.x, lane = tid & 63;
  LAS u32* cnt = (LAS u32*)(lds + L_CNT);
  u32 cpf = 0;
  if (P_CPF && tid >= 512) {
    u64 pc;
    asm volatile("s_getpc_b64 %0" : "=s"(pc));
    const u32 ln = ((blockIdx.x >> 3) & 7) * CODE_PART + (lane < CODE_PART ? lane : 0);
    const char* cp = (const char*)(pc & ~(u64)255) + ln * 128;
    asm volatile("global_load_dword %0, %1, off" : "+v"(cpf) : "v"(cp));
  }
  if (tid < 16) cnt[tid] = 0;
  const int pw = __builtin_amdgcn_readfirstlane(tid >> 6), w = pw == 0 ? 8 : pw - 1;
  const int ftid = w * 64 + lane;
  const int b = blockIdx.x, j = b >> 3;
  const int jg = j < 6 ? (j >= 3 ? 1 : 0) : (j >= 19 ? 1 : 0);
  const int g = (b & 7) * 2 + jg, r = j < 6 ? 13 + j - 3 * jg : j - 6 - 13 * jg;
  asm volatile("" :: "s"(a.idx), "s"(a.table), "s"(a.wc), "s"(a.wco), "s"(a.w1), "s"(a.b1), "s"(a.w2), "s"(a.bo), "s"(a.out), "s"(a.ws), "s"(a.table_bytes));
  lds_barrier();
  const rsrc_t rws = mkrs(a.ws, WS_END);
  const rsrc_t ridx = mkrs(a.idx, NB * NF * 4), rtab = mkrs(a.table, a.table_bytes);
  const int rot = P_ROT ? 2 * ((r + 5 * g) % 13) : 0;

  if (w < 4 && r >= 13) {
      const int p = g * 3 + r - 13;
      const rsrc_t rwc = mkrs(a.wc, 3 * CUBE * 2), rwo = mkrs(a.wco, 78 * 2);
      __builtin_amdgcn_s_setprio(3);
      if (p < NF) {
        const int i = p;
        u32x4 tb[3];
        const int hb = (w >> 1) * 13, el = (w & 1) * 64 + lane;
        const CAS u32* w3p = (const CAS u32*)(a.wco + 52);
        u32 w3[13];
#pragma unroll
        for (int h = 0; h < 13; ++h) w3[h] = w3p[h];
        u32x4 v[13];
#pragma unroll
        for (int h = 0; h < 13; ++h) v[h] = __builtin_amdgcn_raw_buffer_load_b128(rwc, el < 85 ? (2 * CUBE + hb * 676 + 8 * el) * 2 : OOB, h * 676 * 2, 0);
        u32 v2[2];
#pragma unroll
        for (int m = 0; m < 2; ++m) {
          const u32 d = ftid + 256 * m, h = (d * 5042u) >> 16, e = d - 13 * h;
          v2[m] = __builtin_amdgcn_raw_buffer_load_b32(rwc, d < 338 ? (CUBE + (h * NF + i) * NF) * 2 + e * 4 : OOB, 0, 0);
        }
        const u32 wq = __builtin_amdgcn_raw_buffer_load_b16(rwo, ftid < NF ? (NF + ftid) * 2 : OOB, 0, 0);
        const u32 w1i = __builtin_amdgcn_raw_buffer_load_b16(rwo, i * 2, 0, 0);
        if (w == 0) {
#pragma unroll
          for (int m = 0; m < 3; ++m) tb[m] = *(const GAS u32x4*)SYMTAB.w[lane + 64 * m];
        }
        __builtin_amdgcn_sched_barrier(0);
        STAMP(1);
        {
          float acc[8];
#pragma unroll
          for (int e = 0; e < 8; ++e) acc[e] = 0.f;
#pragma unroll
          for (int h = 0; h < 13; ++h) {
            const u32 we = (h & 1) ? w3[h >> 1] >> 16 : w3[h >> 1] & 0xffffu;
            const u32 wo = (h & 1) ? w3[(13 + h) >> 1] & 0xffffu : w3[(13 + h) >> 1] >> 16;
            const u32 wl = hb ? wo : we, wh = wl << 16;
#pragma unroll
            for (int e = 0; e < 4; ++e) { acc[2 * e] = dot2bf(v[h][e], wl, acc[2 * e]); acc[2 * e + 1] = dot2bf(v[h][e], wh, acc[2 * e + 1]); }
          }
          const u32 vo = opq(el < 85 ? L_V3 + (w >> 1) * 2816 + el * 32 : L_V3 + 5632 + lane * 32);
          *(LAS f32x4*)(lds + vo) = (f32x4){acc[0], acc[1], acc[2], acc[3]};
          *(LAS f32x4*)(lds + vo + 16) = (f32x4){acc[4], acc[5], acc[6], acc[7]};
#pragma unroll
          for (int m = 0; m < 2; ++m) *(LAS u32*)(lds + L_W2S + (ftid + 256 * m) * 4) = v2[m];
          if (w == 0) *(LAS u32*)(lds + L_W2C + lane * 4) = wq << 16;
        }
        const int tj = w >> 1, tp = w & 1, kq = lane >> 4;
        const int jr = 16 * tj + (lane & 15), jp = 16 * tp + (lane & 15);
        u32 oa[7], ob[7], oc[7];
#pragma unroll
        for (int s = 0; s < 7; ++s) {
          const int h = 4 * s + kq;
          const bool inv = h < NF && jp < NF;
          oa[s] = opq((h < NF && jr < NF) ? L_W2S + (h * NF + jr) * 2 : L_W2C + 63 * 4);
          ob[s] = opq(inv ? L_V3 + (h * NF + jp) * 4 : L_W2C + ((h < NF && jp == NF) ? h : 63) * 4);
          oc[s] = opq(inv ? L_V3 + 2816 + (h * NF + jp) * 4 : L_W2C + 63 * 4);
        }
        team_arrive(cnt + C_V3, lane);
        team_wait<0>(cnt + C_V3, 4);
        STAMP(2);
        u32 av[7]; float bv[7], bw[7];
#pragma unroll
        for (int s = 0; s < 7; ++s) { av[s] = *(LAS uint16_t*)(lds + oa[s]); bv[s] = *(LAS float*)(lds + ob[s]); bw[s] = *(LAS float*)(lds + oc[s]); }
#pragma unroll
        for (int s = 0; s < 7; ++s) bv[s] += bw[s];
        f32x4 u = {0.f, 0.f, 0.f, 0.f}, u2 = {0.f, 0.f, 0.f, 0.f};
#pragma unroll
        for (int s = 0; s < 7; ++s) {
          if (s & 1) u2 = __builtin_amdgcn_mfma_f32_16x16x4f32(bflo(av[s]), bv[s], u2, 0, 0, 0);
          else u = __builtin_amdgcn_mfma_f32_16x16x4f32(bflo(av[s]), bv[s], u, 0, 0, 0);
        }
        u += u2;
#pragma unroll
        for (int e = 0; e < 4; ++e) {
          const int jo = 16 * tj + 4 * kq + e;
          const float val = (jo == NF && jp == NF) ? bflo(w1i) : u[e];
          *(LAS float*)(lds + L_U + (jo * UP + jp) * 4) = val;
        }
        team_arrive(cnt + C_U, lane);
        if (w == 0) {
          team_wait<0>(cnt + C_U, 4);
          STAMP(3);
          LAS float* U = (LAS float*)(lds + L_U);
          float e[12];
#pragma unroll
          for (int m = 0; m < 3; ++m) {
            e[4 * m] = U[tb[m].x & 0xffff]; e[4 * m + 1] = U[tb[m].x >> 16]; e[4 * m + 2] = U[tb[m].y & 0xffff]; e[4 * m + 3] = U[tb[m].y >> 16];
          }
#pragma unroll
          for (int m = 0; m < 3; ++m)
            __builtin_amdgcn_raw_buffer_store_b32(packbf(e[4 * m] + e[4 * m + 1], e[4 * m + 2] + e[4 * m + 3]), rws, WS_PB + i * 768 + (lane + 64 * m) * 4, 0, 16);
          const u64 flagb = mkflag(get_tag(cnt));
          drain_vm();
          if (lane == 0) store_flag(a.ws, WS_FB + i * FL, flagb);
          STAMP(4);
        }
      } else {
        const int i = (p - NF) * 4 + w;
        const bool prod = p < 33 && i < NF;
        u32 v[6]; u32x4 tb[3];
#pragma unroll
        for (int m = 0; m < 6; ++m) { const int d = lane + 64 * m; v[m] = __builtin_amdgcn_raw_buffer_load_b32(rwc, (prod && d < 338) ? i * 1352 + d * 4 : OOB, 0, 0); }
#pragma unroll
        for (int m = 0; m < 3; ++m) tb[m] = *(const GAS u32x4*)SYMTAB.w[lane + 64 * m];
        __builtin_amdgcn_sched_barrier(0);
        STAMP(1);
        if (prod) {
          LAS char* raw = lds + L_RAW + w * 2048;
#pragma unroll
          for (int m = 0; m < 6; ++m) *(LAS u32*)(raw + (lane + 64 * m) * 4) = v[m];
          asm volatile("s_waitcnt lgkmcnt(0)" ::: "memory");
          LAS uint16_t* R = (LAS uint16_t*)raw;
          u32 e[12];
#pragma unroll
          for (int m = 0; m < 3; ++m) {
            e[4 * m] = R[tb[m].z & 0xffff]; e[4 * m + 1] = R[tb[m].z >> 16]; e[4 * m + 2] = R[tb[m].w & 0xffff]; e[4 * m + 3] = R[tb[m].w >> 16];
          }
#pragma unroll
          for (int m = 0; m < 3; ++m)
            __builtin_amdgcn_raw_buffer_store_b32(packbf(bflo(e[4 * m]) + bflo(e[4 * m + 1]), bflo(e[4 * m + 2]) + bflo(e[4 * m + 3])), rws,
                                                  WS_PA + i * 768 + (lane + 64 * m) * 4, 0, 16);
          const u64 flag0 = mkflag(get_tag(cnt));
          drain_vm();
          if (lane == 0) store_flag(a.ws, WS_FA + i * FL, flag0);
          STAMP(4);
        }
        if (p == 47 && w == 0) {
          const u32 tag0 = get_tag(cnt);
          const u64 flag0 = mkflag(tag0);
          __builtin_amdgcn_s_sleep(32);
#pragma unroll 1
          for (int m = 0; m < 4; ++m) poll_flags<16>(a.ws + WS_XF + (lane + 64 * m) * FL, flag0);
          if (lane == 0) __hip_atomic_store((GAS u32*)(a.ws + WS_E), tag0, __ATOMIC_RELAXED, __HIP_MEMORY_SCOPE_AGENT);
        }
      }
  } else if (w >= 4 && w < 8) {
    const int cw = w - 4, smp = g * 64 + r * 4 + cw;
    u32 id[2]; u32x4 xr[2];
    if (r < 13) __builtin_amdgcn_s_setprio(2);
#pragma unroll
    for (int m = 0; m < 2; ++m) { const int f = 16 * m + (lane >> 2); id[m] = __builtin_amdgcn_raw_buffer_load_b32(ridx, f < NF ? f * 4 : OOB, smp * NF * 4, 0); }
    u32 touch = 0;
    if (P_TOUCH && cw == 1) {
      const char* tp = lane < 7 ? (const char*)a.table + (size_t)lane * (a.table_bytes / 7 & ~3u)
                                : (lane == 7 ? a.ws + WS_P : (lane == 8 ? a.ws + WS_PA : (lane == 9 ? a.ws + WS_X + g * 104 * 1024 : (const char*)a.out)));
      asm volatile("global_load_dword %0, %1, off" : "+v"(touch) : "v"(tp));
    }
    const size_t bop = (size_t)a.bo;
    const u32 bo = *(const CAS u32*)(bop & ~(size_t)3) >> ((bop & 2) * 8);
#pragma unroll
    for (int m = 0; m < 2; ++m) { const int f = 16 * m + (lane >> 2); xr[m] = __builtin_amdgcn_raw_buffer_load_b128(rtab, f < NF ? id[m] * 64 + (lane & 3) * 16 : OOB, 0, 0); }
    __builtin_amdgcn_s_setprio(0);
    STAMP(1);
    f32x2 xp[14];
    {
      const u32 o = L_XS + cw * XS_S + (lane >> 2) * 64 + (lane & 3) * 16;
      *(LAS u32x4*)(lds + o) = xr[0];
      u32x4 x1 = xr[1];
      if ((lane >> 2) == NF - 16) x1 = (u32x4){0x3F803F80u, 0x3F803F80u, 0x3F803F80u, 0x3F803F80u};
      *(LAS u32x4*)(lds + opq((lane >> 2) < 12 ? o + 1024 : L_XS + cw * XS_S + 27 * 64 + (lane & 3) * 16)) = x1;
      asm volatile("s_waitcnt lgkmcnt(0)" :: "v"(touch), "v"(cpf) : "memory");
      const int hh = lane >> 5, q = (lane >> 2) & 3, pp = lane & 3;
      const u32 base = L_XS + cw * XS_S + ((lane >> 4) & 1) * 32 + pp * 8;
      const u32 a0 = base + (hh ? q + 2 : q) * 64, a1 = base + (hh ? ((q + 6) & 7) : q + 4) * 64;
      const u32 a6 = base + (24 + ((hh && q < 2) ? (q ^ 1) : q)) * 64;
#pragma unroll
      for (int t = 0; t < 7; ++t) {
        s16x4 y = __builtin_amdgcn_ds_read_tr16_b64_v4i16((LAS s16x4*)(lds + (t < 6 ? ((t & 1) ? a1 : a0) + (t >> 1) * 512 : a6)));
        u32x2 u = __builtin_bit_cast(u32x2, y);
        xp[2 * t] = (f32x2){bflo(u.x), bfhi(u.x)}; xp[2 * t + 1] = (f32x2){bflo(u.y), bfhi(u.y)};
      }
    }
    bf16x8 z[24];
#pragma unroll
    for (int s = 0; s < 24; ++s) {
      u32x4 zz;
#pragma unroll
      for (int e = 0; e < 4; ++e) {
        const int s1 = 8 * s + 2 * e, s2 = s1 + 1;
        const int a1 = SL.a[s1], b1 = SL.b[s1], a2 = SL.a[s2], b2 = SL.b[s2];
        if (a1 == a2 && (b1 & 1) == 0 && b1 < 26 && b2 == b1 + 1) {
          const f32x2 pr = (a1 & 1) ? pkmul_bc<1>(xp[a1 >> 1], xp[b1 >> 1]) : pkmul_bc<0>(xp[a1 >> 1], xp[b1 >> 1]);
          zz[e] = packbf(pr[0], pr[1]);
        } else {
          const float p1 = b1 == 26 ? xp[a1 >> 1][a1 & 1] : mulf(xp[a1 >> 1][a1 & 1], xp[b1 >> 1][b1 & 1]);
          const float p2 = b2 == 26 ? xp[a2 >> 1][a2 & 1] : mulf(xp[a2 >> 1][a2 & 1], xp[b2 >> 1][b2 & 1]);
          zz[e] = packbf(p1, p2);
        }
      }
      z[s] = __builtin_bit_cast(bf16x8, zz);
      asm volatile("" : "+v"(z[s]));
    }
    const u32 tag = get_tag(cnt);
    const u64 flag = mkflag(tag);
    STAMP(2);
    auto stage = [&](u32 wsf, u32 wsp, u32 ldst, int crdy, int cstg, int gate) {
      if (cw == 0) {
        poll_flags<P_BSLEEP>(a.ws + wsf + (lane < NF ? lane : 0) * FL, flag);
        lds_inc(cnt + crdy);
      } else team_wait<1>(cnt + crdy, 1);
      if (r < 13) team_wait<1>(cnt + gate, 4);
      __builtin_amdgcn_s_setprio(2);
      u32x4 y[5];
#pragma unroll
      for (int m = 0; m < 5; ++m) { const u32 n = (m * 4 + cw) * 64 + lane; y[m] = __builtin_amdgcn_raw_buffer_load_b128(rws, n < NF * 48 ? n * 16 : OOB, wsp, 16); }
#pragma unroll
      for (int m = 0; m < 5; ++m) {
        const u32 n = (m * 4 + cw) * 64 + lane, i = (n * 1366u) >> 16, c = n - i * 48;
        *(LAS u32x4*)(lds + opq(n < NF * 48 ? ldst + c * APITCH + i * 16 : ldst + 26 * 16)) = y[m];
      }
      team_arrive(cnt + cstg, lane);
      team_wait<0>(cnt + cstg, 4);
      __builtin_amdgcn_s_setprio(0);
    };
    const u32 afr = (lane >> 5) * APITCH + (lane & 31) * 16;
    const u32 tag2 = (u32)(flag >> 32);
    const int sg = r * 4 + cw;
    const u32 po = opq(lane < 26 ? WS_P + ((g * 26 + lane) * 2 + (sg >> 5)) * 512 + (sg & 31) * 16 : OOB);
    f32x16 acc1 = {}, acc2 = {};
    constexpr int AD = 3;
    bf16x8 af[24];
    if (P_ADELAY) __builtin_amdgcn_s_sleep(P_ADELAY);
    stage(WS_FA, WS_PA, L_A, C_ARDY, C_ASTG, C_W);
    STAMP(3);
#pragma unroll
    for (int s = 0; s < AD; ++s) af[s] = *(LAS bf16x8*)(lds + L_A + afr + s * 2 * APITCH);
    __builtin_amdgcn_sched_barrier(0);
#pragma unroll
    for (int s = 0; s < 24; ++s) {
      if (s + AD < 24) af[s + AD] = *(LAS bf16x8*)(lds + L_A + afr + (s + AD) * 2 * APITCH);
      acc1 = __builtin_amdgcn_mfma_f32_32x32x16_bf16(af[s], z[s], acc1, 0, 0, 0);
      __builtin_amdgcn_sched_barrier(0);
    }
    STAMP(4);
    stage(WS_FB, WS_PB, L_B, C_BRDY, C_BSTG, C_XISS);
    STAMP(5);
    u32x4 yp;
#pragma unroll
    for (int s = 0; s < AD; ++s) af[s] = *(LAS bf16x8*)(lds + L_B + afr + s * 2 * APITCH);
    __builtin_amdgcn_sched_barrier(0);
#pragma unroll
    for (int s = 0; s < 24; ++s) {
      if (s + AD < 24) af[s + AD] = *(LAS bf16x8*)(lds + L_B + afr + (s + AD) * 2 * APITCH);
      if (s == P_PQ) yp = __builtin_amdgcn_raw_buffer_load_b128(rws, po, 0, 16);
      acc2 = __builtin_amdgcn_mfma_f32_32x32x16_bf16(af[s], z[s], acc2, 0, 0, 0);
      __builtin_amdgcn_sched_barrier(0);
    }
    float sum = 0.f;
#pragma unroll
    for (int e = 0; e < 12; ++e) sum += acc1[e] * acc2[e];
    { const float t = acc1[12] * acc2[12] + acc1[13] * acc2[13]; sum += lane < 32 ? t : 0.f; }
    sum = sum_rows(sum_row16(sum));
    STAMP(6);
    for (u32 it = 0; it < SPIN_MAX; ++it) {
      if (__all(lane >= 26 || (yp.y == tag && yp.w == tag2))) break;
      asm volatile("" ::: "memory");
      yp = __builtin_amdgcn_raw_buffer_load_b128(rws, po, 0, 16);
    }
    float part = sum_rows(sum_row16(__uint_as_float(yp.x)));
    if (lane == 0) {
      const __bf16 ob = (__bf16)(sum + part + bflo(bo));
      a.out[smp] = __builtin_bit_cast(uint16_t, ob);
    }
    STAMP(7);
  } else if (w < 4 && r < 13) {
      const rsrc_t rw1 = mkrs(a.w1, DIN * DIN * 2);
      const int mh = w & 1, kh = w >> 1, hh = lane >> 5;
      {
        u32x4 v[26];
        const u32 vo = (lane >> 3) * 1664 + r * 128 + (lane & 7) * 16;
        auto wrow = [&](int i) {
          const int s = 13 * (w & 1) + (i >> 1) + rot;
          return kh * 416 + (s >= 26 ? s - 26 : s) * 16 + (i & 1) * 8;
        };
        const int c = lane & 7;
        const u32 lo = L_W + (w * 208 + (lane >> 3)) * 128 + (((c >> 1) ^ (((lane >> 4) & 1) << 1)) * 32) + (c & 1) * 16;
#pragma unroll
        for (int i = 0; i < P_W0; ++i) v[i] = __builtin_amdgcn_raw_buffer_load_b128(rw1, vo, wrow(i) * 1664, 0);
        __builtin_amdgcn_sched_barrier(0);
        team_wait<1>(cnt + C_GISS, 1);
#pragma unroll
        for (int i = P_W0; i < 26; ++i) v[i] = __builtin_amdgcn_raw_buffer_load_b128(rw1, vo, wrow(i) * 1664, 0);
        STAMP(1);
#pragma unroll
        for (int i = 0; i < 26; ++i) *(LAS u32x4*)(lds + lo + i * 1024) = v[i];
      }
      team_arrive(cnt + C_W, lane);
      const u32 tag = get_tag(cnt);
      const u64 flag = mkflag(tag);
      f32x16 acc[2];
      f32x2 wf[8];
      {
        const CAS u32* bp = (const CAS u32*)(a.b1 + r * 64 + kh * 32);
        const CAS u32* wp = (const CAS u32*)(a.w2 + r * 64 + kh * 32);
        u32 bs[16], ws_[16];
#pragma unroll
        for (int t = 0; t < 16; ++t) { bs[t] = bp[t]; ws_[t] = wp[t]; }
#pragma unroll
        for (int t = 0; t < 16; ++t) asm volatile("" : "+s"(bs[t]), "+s"(ws_[t]));
#pragma unroll
        for (int t = 0; t < 4; ++t) {
          const u32 b0 = hh ? bs[4 * t + 2] : bs[4 * t], b1 = hh ? bs[4 * t + 3] : bs[4 * t + 1];
          const u32 w0 = hh ? ws_[4 * t + 2] : ws_[4 * t], w1 = hh ? ws_[4 * t + 3] : ws_[4 * t + 1];
          acc[0][4 * t] = bflo(b0); acc[0][4 * t + 1] = bfhi(b0); acc[0][4 * t + 2] = bflo(b1); acc[0][4 * t + 3] = bfhi(b1);
          acc[1][4 * t] = 0.f; acc[1][4 * t + 1] = 0.f; acc[1][4 * t + 2] = 0.f; acc[1][4 * t + 3] = 0.f;
          wf[2 * t] = (f32x2){bflo(w0), bfhi(w0)}; wf[2 * t + 1] = (f32x2){bflo(w1), bfhi(w1)};
          asm volatile("" : "+v"(wf[2 * t]), "+v"(wf[2 * t + 1]));
        }
      }
      STAMP(2);
      team_wait<0>(cnt + C_XRDY, 1);
      STAMP(3);
      constexpr int XW = P_XW, FD = 3;
      u32x4 xv[26];
      auto ldx = [&](int i) { const int s = i + rot; xv[i] = __builtin_amdgcn_raw_buffer_load_b128(rws, WS_X + lane * 16, (g * 104 + mh * 52 + kh * 26 + (s >= 26 ? s - 26 : s)) * 1024, 16); };
#pragma unroll
      for (int i = 0; i < XW; ++i) ldx(i);
      team_wait<0>(cnt + C_W, 4);
      __builtin_amdgcn_s_setprio(1);
      {
        const int q = (lane >> 2) & 3, pp = lane & 3, gam = (lane >> 4) & 1;
        const u32 rb = L_W + kh * 26 * 2048 + (8 * hh + q) * 128 + pp * 8;
        const u32 ad0 = rb + (((2 * kh + gam) ^ (q & 2)) * 32), ad1 = rb + (((2 * (kh ^ 1) + gam) ^ (q & 2)) * 32);
        bf16x8 fr[26][2];
        auto ldf = [&](int i) {
#pragma unroll
          for (int t = 0; t < 2; ++t) {
            const u32 o = (t ? ad1 : ad0) + i * 2048;
            const s16x4 y0 = __builtin_amdgcn_ds_read_tr16_b64_v4i16((LAS s16x4*)(lds + o));
            const s16x4 y1 = __builtin_amdgcn_ds_read_tr16_b64_v4i16((LAS s16x4*)(lds + o + 512));
            fr[i][t] = __builtin_bit_cast(bf16x8, __builtin_shufflevector(y0, y1, 0, 1, 2, 3, 4, 5, 6, 7));
          }
        };
#pragma unroll
        for (int i = 0; i < FD; ++i) ldf(i);
        __builtin_amdgcn_sched_barrier(0);
#pragma unroll
        for (int i = 0; i < 26; ++i) {
          if (i + XW < 26) ldx(i + XW);
          if (i == P_XI) lds_inc(cnt + C_XISS);
          if (i == 8) lds_inc(cnt + C_WD + kh);
          if (i == 22) team_wait<0>(cnt + C_WD + kh, 2);
          if (i + FD < 26) ldf(i + FD);
          const bf16x8 xb = __builtin_bit_cast(bf16x8, xv[i]);
          acc[0] = __builtin_amdgcn_mfma_f32_32x32x16_bf16(fr[i][0], xb, acc[0], 0, 0, 0);
          acc[1] = __builtin_amdgcn_mfma_f32_32x32x16_bf16(fr[i][1], xb, acc[1], 0, 0, 0);
          __builtin_amdgcn_sched_barrier(0);
        }
      }
      STAMP(4);
      {
        const u32 so = L_W + kh * 26 * 2048 + mh * 4096 + lane * 16;
#pragma unroll
        for (int t = 0; t < 4; ++t) *(LAS f32x4*)(lds + so + t * 1024) = (f32x4){acc[1][4 * t], acc[1][4 * t + 1], acc[1][4 * t + 2], acc[1][4 * t + 3]};
        team_arrive(cnt + C_RED + mh, lane);
        team_wait<0>(cnt + C_RED + mh, 2);
      }
      STAMP(5);
      float part;
      {
        constexpr float K1 = -2.0f * 1.4426950408889634f * 0.7978845608028654f, K2 = K1 * 0.044715f;
        const u32 si = L_W + (kh ^ 1) * 26 * 2048 + mh * 4096 + lane * 16;
        f32x4 o[4];
#pragma unroll
        for (int t = 0; t < 4; ++t) o[t] = *(LAS f32x4*)(lds + si + t * 1024);
        f32x2 h[8], u[8], y[8];
#pragma unroll
        for (int m = 0; m < 8; ++m) {
          h[m] = (f32x2){acc[0][2 * m], acc[0][2 * m + 1]} + (f32x2){o[m >> 1][2 * (m & 1)], o[m >> 1][2 * (m & 1) + 1]};
          u[m] = (h[m] * h[m] * K2 + K1) * h[m];
          y[m] = h[m] * wf[m];
        }
#pragma unroll
        for (int m = 0; m < 8; ++m) { u[m][0] = __builtin_amdgcn_exp2f(u[m][0]); u[m][1] = __builtin_amdgcn_exp2f(u[m][1]); }
#pragma unroll
        for (int m = 0; m < 8; ++m) { u[m] = u[m] + 1.0f; }
#pragma unroll
        for (int m = 0; m < 8; ++m) { u[m][0] = __builtin_amdgcn_rcpf(u[m][0]); u[m][1] = __builtin_amdgcn_rcpf(u[m][1]); }
        f32x2 p0 = y[0] * u[0], p1 = y[1] * u[1];
#pragma unroll
        for (int m = 2; m < 8; m += 2) { p0 += y[m] * u[m]; p1 += y[m + 1] * u[m + 1]; }
        p0 += p1;
        part = p0[0] + p0[1];
      }
      {
        auto sw = __builtin_amdgcn_permlane32_swap(__float_as_uint(part), __float_as_uint(part), false, false);
        part = __uint_as_float(sw[0]) + __uint_as_float(sw[1]);
      }
      {
        const u32x4 o = {__float_as_uint(part), tag, __float_as_uint(part), (u32)(flag >> 32)};
        __builtin_amdgcn_raw_buffer_store_b128(o, rws, lane < 32 ? WS_P + ((g * 26 + r * 2 + kh) * 2 + mh) * 512 + lane * 16 : OOB, 0, 16);
      }
      STAMP(6);
  } else {
    __builtin_amdgcn_s_setprio(3);
    u32 tag;
    if (r < 13) {
      u32 id[4]; u32x4 xg[8];
#pragma unroll
      for (int m = 0; m < 2; ++m) {
        const int p0 = 4 * r + 2 * m, t = p0 >= NF ? 1 : 0, f0 = p0 - t * NF;
        const u32x2 ia = __builtin_amdgcn_raw_buffer_load_b64(ridx, (lane & 31) * (NF * 4), ((g * 64 + t * 32) * NF + f0) * 4, 0);
        id[2 * m] = ia.x; id[2 * m + 1] = ia.y;
      }
#pragma unroll
      for (int k = 0; k < 8; ++k) xg[k] = __builtin_amdgcn_raw_buffer_load_b128(rtab, id[k >> 1] * 64 + (k & 1) * 32 + (lane >> 5) * 16, 0, 0);
      const u32 ep = load_epoch(a.ws);
      lds_inc(cnt + C_GISS);
      STAMP(1);
#pragma unroll
      for (int k = 0; k < 8; ++k) __builtin_amdgcn_raw_buffer_store_b128(xg[k], rws, WS_X + lane * 16, (g * 104 + r * 8 + k) * 1024, 16);
      __builtin_amdgcn_sched_barrier(0);
      tag = __builtin_amdgcn_readfirstlane(ep) + 1u;
      cnt[C_TAGV] = tag;
      team_arrive(cnt + C_TAG, lane);
      STAMP(2);
      drain_vm();
    } else {
      const u32 ep = load_epoch(a.ws);
      tag = __builtin_amdgcn_readfirstlane(ep) + 1u;
      cnt[C_TAGV] = tag;
      team_arrive(cnt + C_TAG, lane);
    }
    const u64 flag = mkflag(tag);
    if (lane == 0) store_flag(a.ws, WS_XF + (g * 16 + r) * FL, flag);
    STAMP(3);
    if (r < 13) {
      __builtin_amdgcn_s_sleep(P_XDELAY);
      poll_flags2_signal(a.ws + WS_XF + (g * 16 + (lane < 13 ? lane : 0)) * FL, flag, cnt + C_XRDY);
    }
    STAMP(4);
    STAMP(5);
  }
}

}

extern "C" void kernel_launch(void* const* d_in, const int* in_sizes, int n_in, void* d_out, int out_size,
                              void* d_ws, size_t ws_size, hipStream_t stream) {
  using namespace xdfm;
  Args a;
  a.idx = (const int*)d_in[0]; a.table = (const uint16_t*)d_in[1]; a.wc = (const uint16_t*)d_in[2];
  a.wco = (const uint16_t*)d_in[3]; a.w1 = (const uint16_t*)d_in[4]; a.b1 = (const uint16_t*)d_in[5];
  a.w2 = (const uint16_t*)d_in[6]; a.bo = (const uint16_t*)d_in[7]; a.out = (uint16_t*)d_out; a.ws = (char*)d_ws;
  a.table_bytes = (u32)in_sizes[1] * 2u; a.pad_ = 0;
  (void)hipFuncSetAttribute((const void*)xdfm_kernel, hipFuncAttributeMaxDynamicSharedMemorySize, L_TOTAL);
  xdfm_kernel<<<256, NWAVE * 64, L_TOTAL, stream>>>(a);
}
```

```cpp
#include <hip/hip_runtime.h>
#include <hip/hip_bf16.h>
#include <stdint.h>

#define P_ADELAY 8
#define P_BDELAY 4
#define P_WW 10
#define P_PQ 16
#define P_XI 14
#define P_XDELAY 12
#define P_CG 4
#define P_ROT 1
#define P_TOUCH 1
#define P_W0 14
#define P_CPF 1
#define P_BSLEEP 6
#define P_XW 12
namespace xdfm {

typedef unsigned int u32;
typedef unsigned long long u64;
typedef u32 u32x2 __attribute__((ext_vector_type(2)));
typedef u32 u32x4 __attribute__((ext_vector_type(4)));
typedef short s16x4 __attribute__((ext_vector_type(4)));
typedef short s16x8 __attribute__((ext_vector_type(8)));
typedef float f32x2 __attribute__((ext_vector_type(2)));
typedef float f32x4 __attribute__((ext_vector_type(4)));
typedef float f32x16 __attribute__((ext_vector_type(16)));
typedef __bf16 bf16x2 __attribute__((ext_vector_type(2)));
typedef __bf16 bf16x8 __attribute__((ext_vector_type(8)));
#define GAS __attribute__((address_space(1)))
#define LAS __attribute__((address_space(3)))
typedef __amdgpu_buffer_rsrc_t rsrc_t;

constexpr int NF = 26, DIN = 832, NB = 1024, CUBE = 17576;

constexpr u32 WS_E = 0;
constexpr u32 FL = 128;
constexpr u32 WS_XF = 4096;
constexpr u32 WS_FA = 40960;
constexpr u32 WS_FB = 45056;
constexpr u32 WS_PA = 49152;
constexpr u32 WS_PB = 69632;
constexpr u32 WS_P = 131072;
constexpr u32 WS_X = 655360;
constexpr u32 WS_END = WS_X + 16 * 104 * 1024;
constexpr int NWAVE = 10;

constexpr u32 L_W = 0;
constexpr u32 APITCH = 432;
constexpr u32 L_A = 106496;
constexpr u32 L_B = L_A + 48 * APITCH;
constexpr u32 L_XS = L_B + 48 * APITCH;
constexpr u32 XS_S = 1824;
constexpr u32 L_CNT = L_XS + 4 * XS_S;
constexpr u32 L_TOTAL = L_CNT + 64;
constexpr u32 L_V3 = 0, L_W2C = 8192, L_W2S = 8448, L_U = 12288, L_RAW = 20480;
enum { C_TAGV = 12, C_TAG = 13, C_GISS = 14, C_ABRDY = 0, C_ABSTG = 1, C_W = 3, C_V3 = 4, C_U = 5, C_XISS = 6, C_XRDY = 7, C_WD = 8, C_RED = 10 };

constexpr int perm1(int j) { return j < 24 ? ((j & ~7) | ((j + 2) & 7)) : (j == 24 ? 25 : (j == 25 ? 24 : j)); }
struct Slots { int a[192]; int b[192]; bool v0[192]; bool v1[192]; int n; };
struct SlotBuilder {
  Slots s{}; bool vis[27][27] = {}; int n = 0;
  constexpr bool seen(int a, int b) const { return vis[a < b ? a : b][a < b ? b : a]; }
  constexpr void put(int a, int b) {
    s.a[n] = a; s.b[n] = b;
    s.v0[n] = !seen(a, b); vis[a < b ? a : b][a < b ? b : a] = true;
    const int na = perm1(a), nb = perm1(b);
    s.v1[n] = !seen(na, nb); vis[na < nb ? na : nb][na < nb ? nb : na] = true;
    ++n;
  }
};
constexpr int sg2(int m) { return perm1(perm1(2 * m)) >> 1; }
constexpr Slots make_slots() {
  SlotBuilder t{};
  for (int A = 0; A < 13; ++A)
    for (int B = A + 1; B < 13; ++B) {
      int ca = A, cb = B;
      while (!t.seen(2 * ca, 2 * cb)) {
        t.put(2 * ca, 2 * cb); t.put(2 * ca, 2 * cb + 1); t.put(2 * ca + 1, 2 * cb); t.put(2 * ca + 1, 2 * cb + 1);
        ca = sg2(ca); cb = sg2(cb);
      }
    }
  for (int A = 0; A < 13; ++A) {
    int ca = A;
    while (!t.seen(2 * ca, 2 * ca + 1)) {
      t.put(2 * ca, 2 * ca); t.put(2 * ca, 2 * ca + 1); t.put(2 * ca + 1, 2 * ca + 1); t.put(2 * ca + 1, 26);
      ca = sg2(ca);
    }
  }
  for (int a = 0; a < 27; ++a) { int ca = a; while (!t.seen(ca, 26)) { t.put(ca, 26); ca = perm1(perm1(ca)); } }
  t.s.n = t.n;
  for (int n = t.n; n < 192; ++n) { t.s.a[n] = 26; t.s.b[n] = 26; t.s.v0[n] = false; t.s.v1[n] = false; }
  return t.s;
}
constexpr Slots SL = make_slots();
static_assert(SL.n <= 192, "too many slots");

constexpr int UP = 33, ZB = 30 * UP, ZA = 676;
struct SymTab { u32 w[192][4]; };
constexpr SymTab make_symtab() {
  SymTab t{};
  for (int d = 0; d < 192; ++d) {
    int c = d >> 2, s = c >> 1, hh = c & 1;
    for (int e = 0; e < 2; ++e) {
      int sg = 8 * s + 2 * (d & 3) + e;
      int a = hh ? perm1(SL.a[sg]) : SL.a[sg], b = hh ? perm1(SL.b[sg]) : SL.b[sg];
      bool v = hh ? SL.v1[sg] : SL.v0[sg];
      u32 b1 = ZB, b2 = ZB, a1 = ZA, a2 = ZA;
      if (v) {
        b1 = a * UP + b; b2 = a == b ? ZB : b * UP + a;
        if (a < 26 && b < 26) { a1 = a * 26 + b; a2 = a == b ? ZA : b * 26 + a; }
      }
      t.w[d][e] = b1 | (b2 << 16);
      t.w[d][2 + e] = a1 | (a2 << 16);
    }
  }
  return t;
}
__device__ const SymTab SYMTAB = make_symtab();

constexpr u32 OOB = 0x7FFFFF00u;
constexpr u32 CODE_PART = 20;
constexpr u32 SPIN_MAX = 1u << 18;

__device__ __forceinline__ float bflo(u32 v) { return __uint_as_float(v << 16); }
__device__ __forceinline__ float bfhi(u32 v) { return __uint_as_float(v & 0xffff0000u); }
__device__ __forceinline__ u32 packbf(float a, float b) {
  f32x2 f = {a, b};
  bf16x2 z = __builtin_convertvector(f, bf16x2);
  return __builtin_bit_cast(u32, z);
}
__device__ __forceinline__ float mulf(float a, float b) { float r; asm("v_mul_f32 %0, %1, %2" : "=v"(r) : "v"(a), "v"(b)); return r; }
template <int E> __device__ __forceinline__ f32x2 pkmul_bc(f32x2 a, f32x2 b) {
  f32x2 r; asm("v_pk_mul_f32 %0, %1, %2 op_sel:[%3,0] op_sel_hi:[%3,1]" : "=v"(r) : "v"(a), "v"(b), "i"(E)); return r;
}
__device__ __forceinline__ float dot2bf(u32 a, u32 b, float c) {
  return __builtin_amdgcn_fdot2_f32_bf16(__builtin_bit_cast(bf16x2, a), __builtin_bit_cast(bf16x2, b), c, false);
}
__device__ __forceinline__ rsrc_t mkrs(const void* p, u32 bytes) { return __builtin_amdgcn_make_buffer_rsrc((void*)p, 0, bytes, 0x00020000); }
__device__ __forceinline__ u64 mkflag(u32 tag) { return (u64)tag | ((u64)(tag * 0x9E3779B1u + 0x7F4A7C15u) << 32); }
__device__ __forceinline__ void drain_vm() { asm volatile("s_waitcnt vmcnt(0)" ::: "memory"); }
__device__ __forceinline__ void lds_inc(LAS u32* cnt) {
  u64 keep;
  asm volatile("s_mov_b64 %0, exec\n\ts_mov_b64 exec, 1\n\tds_add_u32 %1, %2\n\ts_mov_b64 exec, %0"
               : "=&s"(keep) : "v"((u32)(size_t)cnt), "v"(1u) : "memory");
}
__device__ __forceinline__ void team_arrive(LAS u32* cnt, int) {
  asm volatile("s_waitcnt lgkmcnt(0)" ::: "memory");
  lds_inc(cnt);
}
__device__ __forceinline__ float sum_rows(float v) {
  auto a = __builtin_amdgcn_permlane32_swap(__float_as_uint(v), __float_as_uint(v), false, false);
  v = __uint_as_float(a[0]) + __uint_as_float(a[1]);
  auto b = __builtin_amdgcn_permlane16_swap(__float_as_uint(v), __float_as_uint(v), false, false);
  return __uint_as_float(b[0]) + __uint_as_float(b[1]);
}
template <int CTRL> __device__ __forceinline__ float dpp_add(float v) {
  return v + __uint_as_float(__builtin_amdgcn_update_dpp(0u, __float_as_uint(v), CTRL, 0xf, 0xf, true));
}
__device__ __forceinline__ float sum_row16(float v) {
  v = dpp_add<0x128>(v);
  v = dpp_add<0x124>(v);
  v = dpp_add<0x122>(v);
  v = dpp_add<0x121>(v);
  return v;
}

#define STAMP(k) do {} while (0)

struct Args {
  const int* idx; const uint16_t* table; const uint16_t* wc; const uint16_t* wco; const uint16_t* w1;
  const uint16_t* b1; const uint16_t* w2; const uint16_t* bo; uint16_t* out; char* ws;
  u32 table_bytes; u32 pad_;
};
#define CAS __attribute__((address_space(4)))

template <int SLEEP> __device__ __forceinline__ void team_wait(LAS u32* cnt, u32 target) {
  u32 v, s, n = SPIN_MAX;
  asm volatile(
      "1:\n\tds_read_b32 %0, %3\n\ts_waitcnt lgkmcnt(0)\n\tv_readfirstlane_b32 %1, %0\n\ts_cmp_ge_u32 %1, %4\n\ts_cbranch_scc1 2f\n\t"
      "s_sleep %5\n\ts_sub_u32 %2, %2, 1\n\ts_cbranch_scc0 1b\n2:"
      : "=&v"(v), "=&s"(s), "+s"(n) : "v"((u32)(size_t)cnt), "s"(target), "i"(SLEEP) : "memory", "scc");
}
template <int SLEEP> __device__ __forceinline__ void poll_flags(const char* p, u64 want) {
  u64 v; u32 n = SPIN_MAX;
  asm volatile(
      "1:\n\tglobal_load_dwordx2 %0, %2, off sc1\n\ts_waitcnt vmcnt(0)\n\tv_cmp_ne_u64 vcc, %3, %0\n\ts_nop 4\n\ts_cbranch_vccz 2f\n\t"
      "s_sleep %4\n\ts_sub_u32 %1, %1, 1\n\ts_cbranch_scc0 1b\n2:"
      : "=&v"(v), "+s"(n) : "v"(p), "s"(want), "i"(SLEEP) : "memory", "scc", "vcc");
}
__device__ __forceinline__ void poll_flags2_signal(const char* p, u64 want, LAS u32* cnt) {
  u64 v0, v1, keep; u32 n = SPIN_MAX;
  asm volatile(
      "global_load_dwordx2 %0, %4, off sc1\n\ts_sleep 2\n"
      "1:\n\tglobal_load_dwordx2 %1, %4, off sc1\n\ts_waitcnt vmcnt(1)\n\tv_cmp_ne_u64 vcc, %5, %0\n\ts_nop 4\n\ts_cbranch_vccz 2f\n\t"
      "global_load_dwordx2 %0, %4, off sc1\n\ts_waitcnt vmcnt(1)\n\tv_cmp_ne_u64 vcc, %5, %1\n\ts_nop 4\n\ts_cbranch_vccz 2f\n\t"
      "s_sub_u32 %2, %2, 1\n\ts_cbranch_scc0 1b\n"
      "2:\n\ts_mov_b64 %3, exec\n\ts_mov_b64 exec, 1\n\tds_add_u32 %6, %7\n\ts_mov_b64 exec, %3\n\ts_waitcnt vmcnt(0)"
      : "=&v"(v0), "=&v"(v1), "+s"(n), "=&s"(keep) : "v"(p), "s"(want), "v"((u32)(size_t)cnt), "v"(1u) : "memory", "scc", "vcc");
}
__device__ __forceinline__ void lds_barrier() {
  asm volatile("s_waitcnt lgkmcnt(0)" ::: "memory");
  __builtin_amdgcn_s_barrier();
  asm volatile("" ::: "memory");
}
__device__ __forceinline__ u32 load_epoch(const char* ws) {
  return __hip_atomic_load((const GAS u32*)(ws + WS_E), __ATOMIC_RELAXED, __HIP_MEMORY_SCOPE_AGENT);
}
__device__ __forceinline__ void store_flag(char* ws, u32 off, u64 flag) {
  __hip_atomic_store((GAS u64*)(ws + off), flag, __ATOMIC_RELAXED, __HIP_MEMORY_SCOPE_AGENT);
}
__device__ __forceinline__ u32 get_tag(LAS u32* cnt) {
  team_wait<0>(cnt + C_TAG, 1);
  return __builtin_amdgcn_readfirstlane(cnt[C_TAGV]);
}
__device__ __forceinline__ u32 opq(u32 v) { asm("" : "+v"(v)); return v; }

__global__ void __launch_bounds__(640, 3) xdfm_kernel(Args a) {
  extern __shared__ __attribute__((aligned(16))) char lds_raw[];
  LAS char* lds = (LAS char*)lds_raw;
  const int tid = threadIdx.x, lane = tid & 63;
  LAS u32* cnt = (LAS u32*)(lds + L_CNT);
  u32 cpf = 0;
  if (P_CPF && tid >= 512 && tid < 576) {
    u64 pc;
    asm volatile("s_getpc_b64 %0" : "=s"(pc));
    const u32 ln = ((blockIdx.x >> 3) & 7) * CODE_PART + (lane < CODE_PART ? lane : 0);
    const char* cp = (const char*)(pc & ~(u64)255) + ln * 128;
    asm volatile("global_load_dword %0, %1, off" : "+v"(cpf) : "v"(cp));
  }
  if (tid < 16) cnt[tid] = 0;
  const int pw = __builtin_amdgcn_readfirstlane(tid >> 6), w = pw == 0 ? 8 : (pw == 9 ? 9 : pw - 1);
  const int ftid = w * 64 + lane;
  const int b = blockIdx.x, j = b >> 3;
  const int jg = j < 6 ? (j >= 3 ? 1 : 0) : (j >= 19 ? 1 : 0);
  const int g = (b & 7) * 2 + jg, r = j < 6 ? 13 + j - 3 * jg : j - 6 - 13 * jg;
  asm volatile("" :: "s"(a.idx), "s"(a.table), "s"(a.wc), "s"(a.wco), "s"(a.w1), "s"(a.b1), "s"(a.w2), "s"(a.bo), "s"(a.out), "s"(a.ws), "s"(a.table_bytes));
  lds_barrier();
  const rsrc_t rws = mkrs(a.ws, WS_END);
  const rsrc_t ridx = mkrs(a.idx, NB * NF * 4), rtab = mkrs(a.table, a.table_bytes);
  const int rot = P_ROT ? 2 * ((r + 5 * g) % 13) : 0;

  if (w == 9) {
    if (b == 250) {
      team_wait<1>(cnt + C_XRDY, 1);
#pragma unroll 1
      for (int i = 0; i < 6; ++i) __builtin_amdgcn_s_sleep(127);
    }
    return;
  }
  if (w < 4 && r >= 13) {
      const int p = g * 3 + r - 13;
      const rsrc_t rwc = mkrs(a.wc, 3 * CUBE * 2), rwo = mkrs(a.wco, 78 * 2);
      __builtin_amdgcn_s_setprio(3);
      if (p < NF) {
        const int i = p;
        u32x4 tb[3];
        const int hb = (w >> 1) * 13, el = (w & 1) * 64 + lane;
        const CAS u32* w3p = (const CAS u32*)(a.wco + 52);
        u32 w3[13];
#pragma unroll
        for (int h = 0; h < 13; ++h) w3[h] = w3p[h];
        u32x4 v[13];
#pragma unroll
        for (int h = 0; h < 13; ++h) v[h] = __builtin_amdgcn_raw_buffer_load_b128(rwc, el < 85 ? (2 * CUBE + hb * 676 + 8 * el) * 2 : OOB, h * 676 * 2, 0);
        u32 v2[2];
#pragma unroll
        for (int m = 0; m < 2; ++m) {
          const u32 d = ftid + 256 * m, h = (d * 5042u) >> 16, e = d - 13 * h;
          v2[m] = __builtin_amdgcn_raw_buffer_load_b32(rwc, d < 338 ? (CUBE + (h * NF + i) * NF) * 2 + e * 4 : OOB, 0, 0);
        }
        const u32 wq = __builtin_amdgcn_raw_buffer_load_b16(rwo, ftid < NF ? (NF + ftid) * 2 : OOB, 0, 0);
        const u32 w1i = __builtin_amdgcn_raw_buffer_load_b16(rwo, i * 2, 0, 0);
        if (w == 0) {
#pragma unroll
          for (int m = 0; m < 3; ++m) tb[m] = *(const GAS u32x4*)SYMTAB.w[lane + 64 * m];
        }
        __builtin_amdgcn_sched_barrier(0);
        STAMP(1);
        {
          float acc[8];
#pragma unroll
          for (int e = 0; e < 8; ++e) acc[e] = 0.f;
#pragma unroll
          for (int h = 0; h < 13; ++h) {
            const u32 we = (h & 1) ? w3[h >> 1] >> 16 : w3[h >> 1] & 0xffffu;
            const u32 wo = (h & 1) ? w3[(13 + h) >> 1] & 0xffffu : w3[(13 + h) >> 1] >> 16;
            const u32 wl = hb ? wo : we, wh = wl << 16;
#pragma unroll
            for (int e = 0; e < 4; ++e) { acc[2 * e] = dot2bf(v[h][e], wl, acc[2 * e]); acc[2 * e + 1] = dot2bf(v[h][e], wh, acc[2 * e + 1]); }
          }
          const u32 vo = opq(el < 85 ? L_V3 + (w >> 1) * 2816 + el * 32 : L_V3 + 5632 + lane * 32);
          *(LAS f32x4*)(lds + vo) = (f32x4){acc[0], acc[1], acc[2], acc[3]};
          *(LAS f32x4*)(lds + vo + 16) = (f32x4){acc[4], acc[5], acc[6], acc[7]};
#pragma unroll
          for (int m = 0; m < 2; ++m) *(LAS u32*)(lds + L_W2S + (ftid + 256 * m) * 4) = v2[m];
          if (w == 0) *(LAS u32*)(lds + L_W2C + lane * 4) = wq << 16;
        }
        const int tj = w >> 1, tp = w & 1, kq = lane >> 4;
        const int jr = 16 * tj + (lane & 15), jp = 16 * tp + (lane & 15);
        u32 oa[7], ob[7], oc[7];
#pragma unroll
        for (int s = 0; s < 7; ++s) {
          const int h = 4 * s + kq;
          const bool inv = h < NF && jp < NF;
          oa[s] = opq((h < NF && jr < NF) ? L_W2S + (h * NF + jr) * 2 : L_W2C + 63 * 4);
          ob[s] = opq(inv ? L_V3 + (h * NF + jp) * 4 : L_W2C + ((h < NF && jp == NF) ? h : 63) * 4);
          oc[s] = opq(inv ? L_V3 + 2816 + (h * NF + jp) * 4 : L_W2C + 63 * 4);
        }
        team_arrive(cnt + C_V3, lane);
        team_wait<0>(cnt + C_V3, 4);
        STAMP(2);
        u32 av[7]; float bv[7], bw[7];
#pragma unroll
        for (int s = 0; s < 7; ++s) { av[s] = *(LAS uint16_t*)(lds + oa[s]); bv[s] = *(LAS float*)(lds + ob[s]); bw[s] = *(LAS float*)(lds + oc[s]); }
#pragma unroll
        for (int s = 0; s < 7; ++s) bv[s] += bw[s];
        f32x4 u = {0.f, 0.f, 0.f, 0.f}, u2 = {0.f, 0.f, 0.f, 0.f};
#pragma unroll
        for (int s = 0; s < 7; ++s) {
          if (s & 1) u2 = __builtin_amdgcn_mfma_f32_16x16x4f32(bflo(av[s]), bv[s], u2, 0, 0, 0);
          else u = __builtin_amdgcn_mfma_f32_16x16x4f32(bflo(av[s]), bv[s], u, 0, 0, 0);
        }
        u += u2;
#pragma unroll
        for (int e = 0; e < 4; ++e) {
          const int jo = 16 * tj + 4 * kq + e;
          const float val = (jo == NF && jp == NF) ? bflo(w1i) : u[e];
          *(LAS float*)(lds + L_U + (jo * UP + jp) * 4) = val;
        }
        team_arrive(cnt + C_U, lane);
        if (w == 0) {
          team_wait<0>(cnt + C_U, 4);
          STAMP(3);
          LAS float* U = (LAS float*)(lds + L_U);
          float e[12];
#pragma unroll
          for (int m = 0; m < 3; ++m) {
            e[4 * m] = U[tb[m].x & 0xffff]; e[4 * m + 1] = U[tb[m].x >> 16]; e[4 * m + 2] = U[tb[m].y & 0xffff]; e[4 * m + 3] = U[tb[m].y >> 16];
          }
#pragma unroll
          for (int m = 0; m < 3; ++m)
            __builtin_amdgcn_raw_buffer_store_b32(packbf(e[4 * m] + e[4 * m + 1], e[4 * m + 2] + e[4 * m + 3]), rws, WS_PB + i * 768 + (lane + 64 * m) * 4, 0, 16);
          const u64 flagb = mkflag(get_tag(cnt));
          drain_vm();
          if (lane == 0) store_flag(a.ws, WS_FB + i * FL, flagb);
          STAMP(4);
        }
      } else {
        const int i = (p - NF) * 4 + w;
        const bool prod = p < 33 && i < NF;
        u32 v[6]; u32x4 tb[3];
#pragma unroll
        for (int m = 0; m < 6; ++m) { const int d = lane + 64 * m; v[m] = __builtin_amdgcn_raw_buffer_load_b32(rwc, (prod && d < 338) ? i * 1352 + d * 4 : OOB, 0, 0); }
#pragma unroll
        for (int m = 0; m < 3; ++m) tb[m] = *(const GAS u32x4*)SYMTAB.w[lane + 64 * m];
        __builtin_amdgcn_sched_barrier(0);
        STAMP(1);
        if (prod) {
          LAS char* raw = lds + L_RAW + w * 2048;
#pragma unroll
          for (int m = 0; m < 6; ++m) *(LAS u32*)(raw + (lane + 64 * m) * 4) = v[m];
          asm volatile("s_waitcnt lgkmcnt(0)" ::: "memory");
          LAS uint16_t* R = (LAS uint16_t*)raw;
          u32 e[12];
#pragma unroll
          for (int m = 0; m < 3; ++m) {
            e[4 * m] = R[tb[m].z & 0xffff]; e[4 * m + 1] = R[tb[m].z >> 16]; e[4 * m + 2] = R[tb[m].w & 0xffff]; e[4 * m + 3] = R[tb[m].w >> 16];
          }
#pragma unroll
          for (int m = 0; m < 3; ++m)
            __builtin_amdgcn_raw_buffer_store_b32(packbf(bflo(e[4 * m]) + bflo(e[4 * m + 1]), bflo(e[4 * m + 2]) + bflo(e[4 * m + 3])), rws,
                                                  WS_PA + i * 768 + (lane + 64 * m) * 4, 0, 16);
          const u64 flag0 = mkflag(get_tag(cnt));
          drain_vm();
          if (lane == 0) store_flag(a.ws, WS_FA + i * FL, flag0);
          STAMP(4);
        }
        if (p == 47 && w == 0) {
          const u32 tag0 = get_tag(cnt);
          const u64 flag0 = mkflag(tag0);
          __builtin_amdgcn_s_sleep(32);
#pragma unroll 1
          for (int m = 0; m < 4; ++m) poll_flags<16>(a.ws + WS_XF + (lane + 64 * m) * FL, flag0);
          if (lane == 0) __hip_atomic_store((GAS u32*)(a.ws + WS_E), tag0, __ATOMIC_RELAXED, __HIP_MEMORY_SCOPE_AGENT);
        }
      }
  } else if (w >= 4 && w < 8) {
    const int cw = w - 4, smp = g * 64 + r * 4 + cw;
    u32 id[2]; u32x4 xr[2];
    if (r < 13) __builtin_amdgcn_s_setprio(2);
#pragma unroll
    for (int m = 0; m < 2; ++m) { const int f = 16 * m + (lane >> 2); id[m] = __builtin_amdgcn_raw_buffer_load_b32(ridx, f < NF ? f * 4 : OOB, smp * NF * 4, 0); }
    u32 touch = 0;
    if (P_TOUCH && cw == 1) {
      const char* tp = lane < 7 ? (const char*)a.table + (size_t)lane * (a.table_bytes / 7 & ~3u)
                                : (lane == 7 ? a.ws + WS_P : (lane == 8 ? a.ws + WS_PA : (lane == 9 ? a.ws + WS_X + g * 104 * 1024 : (const char*)a.out)));
      asm volatile("global_load_dword %0, %1, off" : "+v"(touch) : "v"(tp));
    }
    const size_t bop = (size_t)a.bo;
    const u32 bo = *(const CAS u32*)(bop & ~(size_t)3) >> ((bop & 2) * 8);
#pragma unroll
    for (int m = 0; m < 2; ++m) { const int f = 16 * m + (lane >> 2); xr[m] = __builtin_amdgcn_raw_buffer_load_b128(rtab, f < NF ? id[m] * 64 + (lane & 3) * 16 : OOB, 0, 0); }
    __builtin_amdgcn_s_setprio(0);
    STAMP(1);
    f32x2 xp[14];
    {
      const u32 o = L_XS + cw * XS_S + (lane >> 2) * 64 + (lane & 3) * 16;
      *(LAS u32x4*)(lds + o) = xr[0];
      u32x4 x1 = xr[1];
      if ((lane >> 2) == NF - 16) x1 = (u32x4){0x3F803F80u, 0x3F803F80u, 0x3F803F80u, 0x3F803F80u};
      *(LAS u32x4*)(lds + opq((lane >> 2) < 12 ? o + 1024 : L_XS + cw * XS_S + 27 * 64 + (lane & 3) * 16)) = x1;
      asm volatile("s_waitcnt lgkmcnt(0)" :: "v"(touch), "v"(cpf) : "memory");
      const int hh = lane >> 5, q = (lane >> 2) & 3, pp = lane & 3;
      const u32 base = L_XS + cw * XS_S + ((lane >> 4) & 1) * 32 + pp * 8;
      const u32 a0 = base + (hh ? q + 2 : q) * 64, a1 = base + (hh ? ((q + 6) & 7) : q + 4) * 64;
      const u32 a6 = base + (24 + ((hh && q < 2) ? (q ^ 1) : q)) * 64;
#pragma unroll
      for (int t = 0; t < 7; ++t) {
        s16x4 y = __builtin_amdgcn_ds_read_tr16_b64_v4i16((LAS s16x4*)(lds + (t < 6 ? ((t & 1) ? a1 : a0) + (t >> 1) * 512 : a6)));
        u32x2 u = __builtin_bit_cast(u32x2, y);
        xp[2 * t] = (f32x2){bflo(u.x), bfhi(u.x)}; xp[2 * t + 1] = (f32x2){bflo(u.y), bfhi(u.y)};
      }
    }
    bf16x8 z[24];
#pragma unroll
    for (int s = 0; s < 24; ++s) {
      u32x4 zz;
#pragma unroll
      for (int e = 0; e < 4; ++e) {
        const int s1 = 8 * s + 2 * e, s2 = s1 + 1;
        const int a1 = SL.a[s1], b1 = SL.b[s1], a2 = SL.a[s2], b2 = SL.b[s2];
        if (a1 == a2 && (b1 & 1) == 0 && b1 < 26 && b2 == b1 + 1) {
          const f32x2 pr = (a1 & 1) ? pkmul_bc<1>(xp[a1 >> 1], xp[b1 >> 1]) : pkmul_bc<0>(xp[a1 >> 1], xp[b1 >> 1]);
          zz[e] = packbf(pr[0], pr[1]);
        } else {
          const float p1 = b1 == 26 ? xp[a1 >> 1][a1 & 1] : mulf(xp[a1 >> 1][a1 & 1], xp[b1 >> 1][b1 & 1]);
          const float p2 = b2 == 26 ? xp[a2 >> 1][a2 & 1] : mulf(xp[a2 >> 1][a2 & 1], xp[b2 >> 1][b2 & 1]);
          zz[e] = packbf(p1, p2);
        }
      }
      z[s] = __builtin_bit_cast(bf16x8, zz);
      asm volatile("" : "+v"(z[s]));
    }
    const u32 tag = get_tag(cnt);
    const u64 flag = mkflag(tag);
    STAMP(2);
    if (cw == 0) {
      if (P_BDELAY) __builtin_amdgcn_s_sleep(P_BDELAY);
      poll_flags<P_BSLEEP>(a.ws + (lane < 32 ? WS_FA : WS_FB) + ((lane & 31) < NF ? (lane & 31) : 0) * FL, flag);
      lds_inc(cnt + C_ABRDY);
    } else team_wait<1>(cnt + C_ABRDY, 1);
    STAMP(3);
    if (r < 13 && P_XI >= 0) team_wait<1>(cnt + C_XISS, 4);
    __builtin_amdgcn_s_setprio(2);
    STAMP(4);
    {
      u32x4 y[10];
#pragma unroll
      for (int m = 0; m < 10; ++m) {
        const u32 n = ((m % 5) * 4 + cw) * 64 + lane;
        y[m] = __builtin_amdgcn_raw_buffer_load_b128(rws, n < NF * 48 ? n * 16 : OOB, m < 5 ? WS_PA : WS_PB, 16);
      }
#pragma unroll
      for (int m = 0; m < 5; ++m) {
        const u32 n = (m * 4 + cw) * 64 + lane, i = (n * 1366u) >> 16, c = n - i * 48;
        const u32 o = opq(n < NF * 48 ? L_A + c * APITCH + i * 16 : L_A + 26 * 16);
        *(LAS u32x4*)(lds + o) = y[m];
        *(LAS u32x4*)(lds + o + (L_B - L_A)) = y[5 + m];
      }
      team_arrive(cnt + C_ABSTG, lane);
      team_wait<0>(cnt + C_ABSTG, 4);
    }
    __builtin_amdgcn_s_setprio(0);
    STAMP(5);
    const u32 afr = (lane >> 5) * APITCH + (lane & 31) * 16;
    const u32 tag2 = (u32)(flag >> 32);
    const int sg = r * 4 + cw;
    const u32 po = opq(lane < 26 ? WS_P + ((g * 26 + lane) * 2 + (sg >> 5)) * 512 + (sg & 31) * 16 : OOB);
    u32x4 yp;
    f32x16 acc1 = {}, acc2 = {};
    {
      constexpr int AD = 2;
      bf16x8 af[24], bf[24];
#pragma unroll
      for (int s = 0; s < AD; ++s) { af[s] = *(LAS bf16x8*)(lds + L_A + afr + s * 2 * APITCH); bf[s] = *(LAS bf16x8*)(lds + L_B + afr + s * 2 * APITCH); }
      __builtin_amdgcn_sched_barrier(0);
#pragma unroll
      for (int s = 0; s < 24; ++s) {
        if (s + AD < 24) { af[s + AD] = *(LAS bf16x8*)(lds + L_A + afr + (s + AD) * 2 * APITCH); bf[s + AD] = *(LAS bf16x8*)(lds + L_B + afr + (s + AD) * 2 * APITCH); }
        if (s == P_PQ) yp = __builtin_amdgcn_raw_buffer_load_b128(rws, po, 0, 16);
        acc1 = __builtin_amdgcn_mfma_f32_32x32x16_bf16(af[s], z[s], acc1, 0, 0, 0);
        acc2 = __builtin_amdgcn_mfma_f32_32x32x16_bf16(bf[s], z[s], acc2, 0, 0, 0);
        __builtin_amdgcn_sched_barrier(0);
      }
    }
    float sum = 0.f;
#pragma unroll
    for (int e = 0; e < 12; ++e) sum += acc1[e] * acc2[e];
    { const float t = acc1[12] * acc2[12] + acc1[13] * acc2[13]; sum += lane < 32 ? t : 0.f; }
    sum = sum_rows(sum_row16(sum));
    lds_inc(cnt + 15);
    STAMP(6);
    for (u32 it = 0; it < SPIN_MAX; ++it) {
      if (__all(lane >= 26 || (yp.y == tag && yp.w == tag2))) break;
      asm volatile("" ::: "memory");
      yp = __builtin_amdgcn_raw_buffer_load_b128(rws, po, 0, 16);
    }
    float part = sum_rows(sum_row16(__uint_as_float(yp.x)));
    if (lane == 0) {
      const __bf16 ob = (__bf16)(sum + part + bflo(bo));
      a.out[smp] = __builtin_bit_cast(uint16_t, ob);
    }
    lds_inc(cnt + 15);
    STAMP(7);
  } else if (w < 4 && r < 13) {
      const rsrc_t rw1 = mkrs(a.w1, DIN * DIN * 2);
      const int mh = w & 1, kh = w >> 1, hh = lane >> 5;
      {
        u32x4 v[26];
        const u32 vo = (lane >> 3) * 1664 + r * 128 + (lane & 7) * 16;
        auto wrow = [&](int i) {
          const int s = 13 * (w & 1) + (i >> 1) + rot;
          return kh * 416 + (s >= 26 ? s - 26 : s) * 16 + (i & 1) * 8;
        };
        const int c = lane & 7;
        const u32 lo = L_W + (w * 208 + (lane >> 3)) * 128 + (((c >> 1) ^ (((lane >> 4) & 1) << 1)) * 32) + (c & 1) * 16;
#pragma unroll
        for (int i = 0; i < P_W0; ++i) v[i] = __builtin_amdgcn_raw_buffer_load_b128(rw1, vo, wrow(i) * 1664, 0);
        __builtin_amdgcn_sched_barrier(0);
        team_wait<1>(cnt + C_GISS, 1);
#pragma unroll
        for (int i = P_W0; i < 26; ++i) v[i] = __builtin_amdgcn_raw_buffer_load_b128(rw1, vo, wrow(i) * 1664, 0);
        STAMP(1);
#pragma unroll
        for (int i = 0; i < 26; ++i) *(LAS u32x4*)(lds + lo + i * 1024) = v[i];
      }
      team_arrive(cnt + C_W, lane);
      const u32 tag = get_tag(cnt);
      const u64 flag = mkflag(tag);
      f32x16 acc[2];
      f32x2 wf[8];
      {
        const CAS u32* bp = (const CAS u32*)(a.b1 + r * 64 + kh * 32);
        const CAS u32* wp = (const CAS u32*)(a.w2 + r * 64 + kh * 32);
        u32 bs[16], ws_[16];
#pragma unroll
        for (int t = 0; t < 16; ++t) { bs[t] = bp[t]; ws_[t] = wp[t]; }
#pragma unroll
        for (int t = 0; t < 16; ++t) asm volatile("" : "+s"(bs[t]), "+s"(ws_[t]));
#pragma unroll
        for (int t = 0; t < 4; ++t) {
          const u32 b0 = hh ? bs[4 * t + 2] : bs[4 * t], b1 = hh ? bs[4 * t + 3] : bs[4 * t + 1];
          const u32 w0 = hh ? ws_[4 * t + 2] : ws_[4 * t], w1 = hh ? ws_[4 * t + 3] : ws_[4 * t + 1];
          acc[0][4 * t] = bflo(b0); acc[0][4 * t + 1] = bfhi(b0); acc[0][4 * t + 2] = bflo(b1); acc[0][4 * t + 3] = bfhi(b1);
          acc[1][4 * t] = 0.f; acc[1][4 * t + 1] = 0.f; acc[1][4 * t + 2] = 0.f; acc[1][4 * t + 3] = 0.f;
          wf[2 * t] = (f32x2){bflo(w0), bfhi(w0)}; wf[2 * t + 1] = (f32x2){bflo(w1), bfhi(w1)};
          asm volatile("" : "+v"(wf[2 * t]), "+v"(wf[2 * t + 1]));
        }
      }
      STAMP(2);
      team_wait<0>(cnt + C_XRDY, 1);
      STAMP(3);
      constexpr int XW = P_XW, FD = 3;
      u32x4 xv[26];
      auto ldx = [&](int i) { const int s = i + rot; xv[i] = __builtin_amdgcn_raw_buffer_load_b128(rws, WS_X + lane * 16, (g * 104 + mh * 52 + kh * 26 + (s >= 26 ? s - 26 : s)) * 1024, 16); };
#pragma unroll
      for (int i = 0; i < XW; ++i) ldx(i);
      team_wait<0>(cnt + C_W, 4);
      __builtin_amdgcn_s_setprio(1);
      {
        const int q = (lane >> 2) & 3, pp = lane & 3, gam = (lane >> 4) & 1;
        const u32 rb = L_W + kh * 26 * 2048 + (8 * hh + q) * 128 + pp * 8;
        const u32 ad0 = rb + (((2 * kh + gam) ^ (q & 2)) * 32), ad1 = rb + (((2 * (kh ^ 1) + gam) ^ (q & 2)) * 32);
        bf16x8 fr[26][2];
        auto ldf = [&](int i) {
#pragma unroll
          for (int t = 0; t < 2; ++t) {
            const u32 o = (t ? ad1 : ad0) + i * 2048;
            const s16x4 y0 = __builtin_amdgcn_ds_read_tr16_b64_v4i16((LAS s16x4*)(lds + o));
            const s16x4 y1 = __builtin_amdgcn_ds_read_tr16_b64_v4i16((LAS s16x4*)(lds + o + 512));
            fr[i][t] = __builtin_bit_cast(bf16x8, __builtin_shufflevector(y0, y1, 0, 1, 2, 3, 4, 5, 6, 7));
          }
        };
#pragma unroll
        for (int i = 0; i < FD; ++i) ldf(i);
        __builtin_amdgcn_sched_barrier(0);
#pragma unroll
        for (int i = 0; i < 26; ++i) {
          if (i + XW < 26) ldx(i + XW);
          if (i == P_XI) lds_inc(cnt + C_XISS);
          if (i == 8) lds_inc(cnt + C_WD + kh);
          if (i == 22) team_wait<0>(cnt + C_WD + kh, 2);
          if (i + FD < 26) ldf(i + FD);
          const bf16x8 xb = __builtin_bit_cast(bf16x8, xv[i]);
          acc[0] = __builtin_amdgcn_mfma_f32_32x32x16_bf16(fr[i][0], xb, acc[0], 0, 0, 0);
          acc[1] = __builtin_amdgcn_mfma_f32_32x32x16_bf16(fr[i][1], xb, acc[1], 0, 0, 0);
          __builtin_amdgcn_sched_barrier(0);
        }
      }
      STAMP(4);
      {
        const u32 so = L_W + kh * 26 * 2048 + mh * 4096 + lane * 16;
#pragma unroll
        for (int t = 0; t < 4; ++t) *(LAS f32x4*)(lds + so + t * 1024) = (f32x4){acc[1][4 * t], acc[1][4 * t + 1], acc[1][4 * t + 2], acc[1][4 * t + 3]};
        team_arrive(cnt + C_RED + mh, lane);
        team_wait<0>(cnt + C_RED + mh, 2);
      }
      STAMP(5);
      float part;
      {
        constexpr float K1 = -2.0f * 1.4426950408889634f * 0.7978845608028654f, K2 = K1 * 0.044715f;
        const u32 si = L_W + (kh ^ 1) * 26 * 2048 + mh * 4096 + lane * 16;
        f32x4 o[4];
#pragma unroll
        for (int t = 0; t < 4; ++t) o[t] = *(LAS f32x4*)(lds + si + t * 1024);
        f32x2 h[8], u[8], y[8];
#pragma unroll
        for (int m = 0; m < 8; ++m) {
          h[m] = (f32x2){acc[0][2 * m], acc[0][2 * m + 1]} + (f32x2){o[m >> 1][2 * (m & 1)], o[m >> 1][2 * (m & 1) + 1]};
          u[m] = (h[m] * h[m] * K2 + K1) * h[m];
          y[m] = h[m] * wf[m];
        }
#pragma unroll
        for (int m = 0; m < 8; ++m) { u[m][0] = __builtin_amdgcn_exp2f(u[m][0]); u[m][1] = __builtin_amdgcn_exp2f(u[m][1]); }
#pragma unroll
        for (int m = 0; m < 8; ++m) { u[m] = u[m] + 1.0f; }
#pragma unroll
        for (int m = 0; m < 8; ++m) { u[m][0] = __builtin_amdgcn_rcpf(u[m][0]); u[m][1] = __builtin_amdgcn_rcpf(u[m][1]); }
        f32x2 p0 = y[0] * u[0], p1 = y[1] * u[1];
#pragma unroll
        for (int m = 2; m < 8; m += 2) { p0 += y[m] * u[m]; p1 += y[m + 1] * u[m + 1]; }
        p0 += p1;
        part = p0[0] + p0[1];
      }
      {
        auto sw = __builtin_amdgcn_permlane32_swap(__float_as_uint(part), __float_as_uint(part), false, false);
        part = __uint_as_float(sw[0]) + __uint_as_float(sw[1]);
      }
      {
        const u32x4 o = {__float_as_uint(part), tag, __float_as_uint(part), (u32)(flag >> 32)};
        __builtin_amdgcn_raw_buffer_store_b128(o, rws, lane < 32 ? WS_P + ((g * 26 + r * 2 + kh) * 2 + mh) * 512 + lane * 16 : OOB, 0, 16);
      }
      lds_inc(cnt + 2);
      STAMP(6);
  } else {
    __builtin_amdgcn_s_setprio(3);
    u32 tag;
    if (r < 13) {
      u32 id[4]; u32x4 xg[8];
#pragma unroll
      for (int m = 0; m < 2; ++m) {
        const int p0 = 4 * r + 2 * m, t = p0 >= NF ? 1 : 0, f0 = p0 - t * NF;
        const u32x2 ia = __builtin_amdgcn_raw_buffer_load_b64(ridx, (lane & 31) * (NF * 4), ((g * 64 + t * 32) * NF + f0) * 4, 0);
        id[2 * m] = ia.x; id[2 * m + 1] = ia.y;
      }
#pragma unroll
      for (int k = 0; k < 8; ++k) xg[k] = __builtin_amdgcn_raw_buffer_load_b128(rtab, id[k >> 1] * 64 + (k & 1) * 32 + (lane >> 5) * 16, 0, 0);
      const u32 ep = load_epoch(a.ws);
      lds_inc(cnt + C_GISS);
      STAMP(1);
#pragma unroll
      for (int k = 0; k < 8; ++k) __builtin_amdgcn_raw_buffer_store_b128(xg[k], rws, WS_X + lane * 16, (g * 104 + r * 8 + k) * 1024, 16);
      __builtin_amdgcn_sched_barrier(0);
      tag = __builtin_amdgcn_readfirstlane(ep) + 1u;
      cnt[C_TAGV] = tag;
      team_arrive(cnt + C_TAG, lane);
      STAMP(2);
      drain_vm();
    } else {
      const u32 ep = load_epoch(a.ws);
      tag = __builtin_amdgcn_readfirstlane(ep) + 1u;
      cnt[C_TAGV] = tag;
      team_arrive(cnt + C_TAG, lane);
    }
    const u64 flag = mkflag(tag);
    if (lane == 0) store_flag(a.ws, WS_XF + (g * 16 + r) * FL, flag);
    STAMP(3);
    if (r < 13) {
      __builtin_amdgcn_s_sleep(P_XDELAY);
      poll_flags2_signal(a.ws + WS_XF + (g * 16 + (lane < 13 ? lane : 0)) * FL, flag, cnt + C_XRDY);
    }
    STAMP(4);
    STAMP(5);
  }
}

}

extern "C" void kernel_launch(void* const* d_in, const int* in_sizes, int n_in, void* d_out, int out_size,
                              void* d_ws, size_t ws_size, hipStream_t stream) {
  using namespace xdfm;
  Args a;
  a.idx = (const int*)d_in[0]; a.table = (const uint16_t*)d_in[1]; a.wc = (const uint16_t*)d_in[2];
  a.wco = (const uint16_t*)d_in[3]; a.w1 = (const uint16_t*)d_in[4]; a.b1 = (const uint16_t*)d_in[5];
  a.w2 = (const uint16_t*)d_in[6]; a.bo = (const uint16_t*)d_in[7]; a.out = (uint16_t*)d_out; a.ws = (char*)d_ws;
  a.table_bytes = (u32)in_sizes[1] * 2u; a.pad_ = 0;
  (void)hipFuncSetAttribute((const void*)xdfm_kernel, hipFuncAttributeMaxDynamicSharedMemorySize, L_TOTAL);
  xdfm_kernel<<<256, NWAVE * 64, L_TOTAL, stream>>>(a);
}
```

```cpp
#include <hip/hip_runtime.h>
#include <hip/hip_bf16.h>
#include <stdint.h>

#define P_ADELAY 8
#define P_BDELAY 4
#define P_WW 10
#define P_PQ 16
#define P_XI 14
#define P_XDELAY 12
#define P_CG 4
#define P_ROT 1
#define P_TOUCH 1
#define P_W0 14
#define P_CPF 1
#define P_BSLEEP 6
#define P_XW 12
namespace xdfm {

typedef unsigned int u32;
typedef unsigned long long u64;
typedef u32 u32x2 __attribute__((ext_vector_type(2)));
typedef u32 u32x4 __attribute__((ext_vector_type(4)));
typedef short s16x4 __attribute__((ext_vector_type(4)));
typedef short s16x8 __attribute__((ext_vector_type(8)));
typedef float f32x2 __attribute__((ext_vector_type(2)));
typedef float f32x4 __attribute__((ext_vector_type(4)));
typedef float f32x16 __attribute__((ext_vector_type(16)));
typedef __bf16 bf16x2 __attribute__((ext_vector_type(2)));
typedef __bf16 bf16x8 __attribute__((ext_vector_type(8)));
#define GAS __attribute__((address_space(1)))
#define LAS __attribute__((address_space(3)))
typedef __amdgpu_buffer_rsrc_t rsrc_t;

constexpr int NF = 26, DIN = 832, NB = 1024, CUBE = 17576;

constexpr u32 WS_E = 0;
constexpr u32 FL = 128;
constexpr u32 WS_XF = 4096;
constexpr u32 WS_FA = 40960;
constexpr u32 WS_FB = 45056;
constexpr u32 WS_PA = 49152;
constexpr u32 WS_PB = 69632;
constexpr u32 WS_P = 131072;
constexpr u32 WS_X = 655360;
constexpr u32 WS_END = WS_X + 16 * 104 * 1024;
constexpr int NWAVE = 9;

constexpr u32 L_W = 0;
constexpr u32 APITCH = 432;
constexpr u32 L_A = 106496;
constexpr u32 L_B = L_A + 48 * APITCH;
constexpr u32 L_XS = L_B + 48 * APITCH;
constexpr u32 XS_S = 1824;
constexpr u32 L_CNT = L_XS + 4 * XS_S;
constexpr u32 L_TOTAL = L_CNT + 64;
constexpr u32 L_V3 = 0, L_W2C = 8192, L_W2S = 8448, L_U = 12288, L_RAW = 20480;
enum { C_TAGV = 12, C_TAG = 13, C_GISS = 14, C_ABRDY = 0, C_ABSTG = 1, C_W = 3, C_V3 = 4, C_U = 5, C_XISS = 6, C_XRDY = 7, C_WD = 8, C_RED = 10 };

constexpr int perm1(int j) { return j < 24 ? ((j & ~7) | ((j + 2) & 7)) : (j == 24 ? 25 : (j == 25 ? 24 : j)); }
struct Slots { int a[192]; int b[192]; bool v0[192]; bool v1[192]; int n; };
struct SlotBuilder {
  Slots s{}; bool vis[27][27] = {}; int n = 0;
  constexpr bool seen(int a, int b) const { return vis[a < b ? a : b][a < b ? b : a]; }
  constexpr void put(int a, int b) {
    s.a[n] = a; s.b[n] = b;
    s.v0[n] = !seen(a, b); vis[a < b ? a : b][a < b ? b : a] = true;
    const int na = perm1(a), nb = perm1(b);
    s.v1[n] = !seen(na, nb); vis[na < nb ? na : nb][na < nb ? nb : na] = true;
    ++n;
  }
};
constexpr int sg2(int m) { return perm1(perm1(2 * m)) >> 1; }
constexpr Slots make_slots() {
  SlotBuilder t{};
  for (int A = 0; A < 13; ++A)
    for (int B = A + 1; B < 13; ++B) {
      int ca = A, cb = B;
      while (!t.seen(2 * ca, 2 * cb)) {
        t.put(2 * ca, 2 * cb); t.put(2 * ca, 2 * cb + 1); t.put(2 * ca + 1, 2 * cb); t.put(2 * ca + 1, 2 * cb + 1);
        ca = sg2(ca); cb = sg2(cb);
      }
    }
  for (int A = 0; A < 13; ++A) {
    int ca = A;
    while (!t.seen(2 * ca, 2 * ca + 1)) {
      t.put(2 * ca, 2 * ca); t.put(2 * ca, 2 * ca + 1); t.put(2 * ca + 1, 2 * ca + 1); t.put(2 * ca + 1, 26);
      ca = sg2(ca);
    }
  }
  for (int a = 0; a < 27; ++a) { int ca = a; while (!t.seen(ca, 26)) { t.put(ca, 26); ca = perm1(perm1(ca)); } }
  t.s.n = t.n;
  for (int n = t.n; n < 192; ++n) { t.s.a[n] = 26; t.s.b[n] = 26; t.s.v0[n] = false; t.s.v1[n] = false; }
  return t.s;
}
constexpr Slots SL = make_slots();
static_assert(SL.n <= 192, "too many slots");

constexpr int UP = 33, ZB = 30 * UP, ZA = 676;
struct SymTab { u32 w[192][4]; };
constexpr SymTab make_symtab() {
  SymTab t{};
  for (int d = 0; d < 192; ++d) {
    int c = d >> 2, s = c >> 1, hh = c & 1;
    for (int e = 0; e < 2; ++e) {
      int sg = 8 * s + 2 * (d & 3) + e;
      int a = hh ? perm1(SL.a[sg]) : SL.a[sg], b = hh ? perm1(SL.b[sg]) : SL.b[sg];
      bool v = hh ? SL.v1[sg] : SL.v0[sg];
      u32 b1 = ZB, b2 = ZB, a1 = ZA, a2 = ZA;
      if (v) {
        b1 = a * UP + b; b2 = a == b ? ZB : b * UP + a;
        if (a < 26 && b < 26) { a1 = a * 26 + b; a2 = a == b ? ZA : b * 26 + a; }
      }
      t.w[d][e] = b1 | (b2 << 16);
      t.w[d][2 + e] = a1 | (a2 << 16);
    }
  }
  return t;
}
__device__ const SymTab SYMTAB = make_symtab();

constexpr u32 OOB = 0x7FFFFF00u;
constexpr u32 CODE_PART = 20;
constexpr u32 SPIN_MAX = 1u << 18;

__device__ __forceinline__ float bflo(u32 v) { return __uint_as_float(v << 16); }
__device__ __forceinline__ float bfhi(u32 v) { return __uint_as_float(v & 0xffff0000u); }
__device__ __forceinline__ u32 packbf(float a, float b) {
  f32x2 f = {a, b};
  bf16x2 z = __builtin_convertvector(f, bf16x2);
  return __builtin_bit_cast(u32, z);
}
__device__ __forceinline__ float mulf(float a, float b) { float r; asm("v_mul_f32 %0, %1, %2" : "=v"(r) : "v"(a), "v"(b)); return r; }
template <int E> __device__ __forceinline__ f32x2 pkmul_bc(f32x2 a, f32x2 b) {
  f32x2 r; asm("v_pk_mul_f32 %0, %1, %2 op_sel:[%3,0] op_sel_hi:[%3,1]" : "=v"(r) : "v"(a), "v"(b), "i"(E)); return r;
}
__device__ __forceinline__ float dot2bf(u32 a, u32 b, float c) {
  return __builtin_amdgcn_fdot2_f32_bf16(__builtin_bit_cast(bf16x2, a), __builtin_bit_cast(bf16x2, b), c, false);
}
__device__ __forceinline__ rsrc_t mkrs(const void* p, u32 bytes) { return __builtin_amdgcn_make_buffer_rsrc((void*)p, 0, bytes, 0x00020000); }
__device__ __forceinline__ u64 mkflag(u32 tag) { return (u64)tag | ((u64)(tag * 0x9E3779B1u + 0x7F4A7C15u) << 32); }
__device__ __forceinline__ void drain_vm() { asm volatile("s_waitcnt vmcnt(0)" ::: "memory"); }
__device__ __forceinline__ void lds_inc(LAS u32* cnt) {
  u64 keep;
  asm volatile("s_mov_b64 %0, exec\n\ts_mov_b64 exec, 1\n\tds_add_u32 %1, %2\n\ts_mov_b64 exec, %0"
               : "=&s"(keep) : "v"((u32)(size_t)cnt), "v"(1u) : "memory");
}
__device__ __forceinline__ void team_arrive(LAS u32* cnt, int) {
  asm volatile("s_waitcnt lgkmcnt(0)" ::: "memory");
  lds_inc(cnt);
}
__device__ __forceinline__ float sum_rows(float v) {
  auto a = __builtin_amdgcn_permlane32_swap(__float_as_uint(v), __float_as_uint(v), false, false);
  v = __uint_as_float(a[0]) + __uint_as_float(a[1]);
  auto b = __builtin_amdgcn_permlane16_swap(__float_as_uint(v), __float_as_uint(v), false, false);
  return __uint_as_float(b[0]) + __uint_as_float(b[1]);
}
template <int CTRL> __device__ __forceinline__ float dpp_add(float v) {
  return v + __uint_as_float(__builtin_amdgcn_update_dpp(0u, __float_as_uint(v), CTRL, 0xf, 0xf, true));
}
__device__ __forceinline__ float sum_row16(float v) {
  v = dpp_add<0x128>(v);
  v = dpp_add<0x124>(v);
  v = dpp_add<0x122>(v);
  v = dpp_add<0x121>(v);
  return v;
}

#define STAMP(k) do {} while (0)

struct Args {
  const int* idx; const uint16_t* table; const uint16_t* wc; const uint16_t* wco; const uint16_t* w1;
  const uint16_t* b1; const uint16_t* w2; const uint16_t* bo; uint16_t* out; char* ws;
  u32 table_bytes; u32 pad_;
};
#define CAS __attribute__((address_space(4)))

template <int SLEEP> __device__ __forceinline__ void team_wait(LAS u32* cnt, u32 target) {
  u32 v, s, n = SPIN_MAX;
  asm volatile(
      "1:\n\tds_read_b32 %0, %3\n\ts_waitcnt lgkmcnt(0)\n\tv_readfirstlane_b32 %1, %0\n\ts_cmp_ge_u32 %1, %4\n\ts_cbranch_scc1 2f\n\t"
      "s_sleep %5\n\ts_sub_u32 %2, %2, 1\n\ts_cbranch_scc0 1b\n2:"
      : "=&v"(v), "=&s"(s), "+s"(n) : "v"((u32)(size_t)cnt), "s"(target), "i"(SLEEP) : "memory", "scc");
}
template <int SLEEP> __device__ __forceinline__ void poll_flags(const char* p, u64 want) {
  u64 v; u32 n = SPIN_MAX;
  asm volatile(
      "1:\n\tglobal_load_dwordx2 %0, %2, off sc1\n\ts_waitcnt vmcnt(0)\n\tv_cmp_ne_u64 vcc, %3, %0\n\ts_nop 4\n\ts_cbranch_vccz 2f\n\t"
      "s_sleep %4\n\ts_sub_u32 %1, %1, 1\n\ts_cbranch_scc0 1b\n2:"
      : "=&v"(v), "+s"(n) : "v"(p), "s"(want), "i"(SLEEP) : "memory", "scc", "vcc");
}
__device__ __forceinline__ void poll_flags2_signal(const char* p, u64 want, LAS u32* cnt) {
  u64 v0, v1, keep; u32 n = SPIN_MAX;
  asm volatile(
      "global_load_dwordx2 %0, %4, off sc1\n\ts_sleep 2\n"
      "1:\n\tglobal_load_dwordx2 %1, %4, off sc1\n\ts_waitcnt vmcnt(1)\n\tv_cmp_ne_u64 vcc, %5, %0\n\ts_nop 4\n\ts_cbranch_vccz 2f\n\t"
      "global_load_dwordx2 %0, %4, off sc1\n\ts_waitcnt vmcnt(1)\n\tv_cmp_ne_u64 vcc, %5, %1\n\ts_nop 4\n\ts_cbranch_vccz 2f\n\t"
      "s_sub_u32 %2, %2, 1\n\ts_cbranch_scc0 1b\n"
      "2:\n\ts_mov_b64 %3, exec\n\ts_mov_b64 exec, 1\n\tds_add_u32 %6, %7\n\ts_mov_b64 exec, %3\n\ts_waitcnt vmcnt(0)"
      : "=&v"(v0), "=&v"(v1), "+s"(n), "=&s"(keep) : "v"(p), "s"(want), "v"((u32)(size_t)cnt), "v"(1u) : "memory", "scc", "vcc");
}
__device__ __forceinline__ void finish_sample(const char* p, u32 tag, u32 tag2, float base, uint16_t* outp, int lane) {
  u32 n = SPIN_MAX, hi; u64 m0_, m1_;
  asm volatile(
      "global_load_dwordx4 v[160:163], %4, off sc1\n\t"
      "v_cmp_gt_u32 %2, 26, %9\n\t"
      "s_sleep 2\n"
      "1:\n\t"
      "global_load_dwordx4 v[164:167], %4, off sc1\n\t"
      "s_waitcnt vmcnt(1)\n\t"
      "v_cmp_ne_u32 vcc, %5, v161\n\tv_cmp_ne_u32 %1, %6, v163\n\ts_nop 1\n\ts_or_b64 vcc, vcc, %1\n\ts_and_b64 vcc, vcc, %2\n\t"
      "s_cbranch_vccz 2f\n\t"
      "global_load_dwordx4 v[160:163], %4, off sc1\n\t"
      "s_waitcnt vmcnt(1)\n\t"
      "v_cmp_ne_u32 vcc, %5, v165\n\tv_cmp_ne_u32 %1, %6, v167\n\ts_nop 1\n\ts_or_b64 vcc, vcc, %1\n\ts_and_b64 vcc, vcc, %2\n\t"
      "s_cbranch_vccz 3f\n\t"
      "s_sub_u32 %0, %0, 1\n\ts_cbranch_scc0 1b\n\t"
      "s_waitcnt vmcnt(0)\n"
      "2:\n\tv_cndmask_b32 v159, 0, v160, %2\n\ts_branch 4f\n"
      "3:\n\tv_cndmask_b32 v159, 0, v164, %2\n"
      "4:\n\ts_nop 1\n\t"
      "v_add_f32_dpp v159, v159, v159 row_ror:8 row_mask:0xf bank_mask:0xf\n\ts_nop 1\n\t"
      "v_add_f32_dpp v159, v159, v159 row_ror:4 row_mask:0xf bank_mask:0xf\n\ts_nop 1\n\t"
      "v_add_f32_dpp v159, v159, v159 row_ror:2 row_mask:0xf bank_mask:0xf\n\ts_nop 1\n\t"
      "v_add_f32_dpp v159, v159, v159 row_ror:1 row_mask:0xf bank_mask:0xf\n\ts_nop 1\n\t"
      "v_readlane_b32 %3, v159, 16\n\t"
      "v_add_f32 v159, %7, v159\n\ts_nop 1\n\t"
      "v_add_f32 v159, %3, v159\n\t"
      "v_cvt_pk_bf16_f32 v159, v159, v159\n\t"
      "s_mov_b64 %1, exec\n\ts_mov_b64 exec, 1\n\t"
      "global_store_short %8, v159, off\n\t"
      "s_mov_b64 exec, %1"
      : "+s"(n), "=&s"(m0_), "=&s"(m1_), "=&s"(hi)
      : "v"(p), "s"(tag), "s"(tag2), "v"(base), "v"(outp), "v"(lane)
      : "memory", "scc", "vcc", "v159", "v160", "v161", "v162", "v163", "v164", "v165", "v166", "v167");
}
__device__ __forceinline__ void lds_barrier() {
  asm volatile("s_waitcnt lgkmcnt(0)" ::: "memory");
  __builtin_amdgcn_s_barrier();
  asm volatile("" ::: "memory");
}
__device__ __forceinline__ u32 load_epoch(const char* ws) {
  return __hip_atomic_load((const GAS u32*)(ws + WS_E), __ATOMIC_RELAXED, __HIP_MEMORY_SCOPE_AGENT);
}
__device__ __forceinline__ void store_flag(char* ws, u32 off, u64 flag) {
  __hip_atomic_store((GAS u64*)(ws + off), flag, __ATOMIC_RELAXED, __HIP_MEMORY_SCOPE_AGENT);
}
__device__ __forceinline__ u32 get_tag(LAS u32* cnt) {
  team_wait<0>(cnt + C_TAG, 1);
  return __builtin_amdgcn_readfirstlane(cnt[C_TAGV]);
}
__device__ __forceinline__ u32 opq(u32 v) { asm("" : "+v"(v)); return v; }

__global__ void __launch_bounds__(576, 3) xdfm_kernel(Args a) {
  extern __shared__ __attribute__((aligned(16))) char lds_raw[];
  LAS char* lds = (LAS char*)lds_raw;
  const int tid = threadIdx.x, lane = tid & 63;
  LAS u32* cnt = (LAS u32*)(lds + L_CNT);
  u32 cpf = 0;
  if (P_CPF && tid >= 512) {
    u64 pc;
    asm volatile("s_getpc_b64 %0" : "=s"(pc));
    const u32 ln = ((blockIdx.x >> 3) & 7) * CODE_PART + (lane < CODE_PART ? lane : 0);
    const char* cp = (const char*)(pc & ~(u64)255) + ln * 128;
    asm volatile("global_load_dword %0, %1, off" : "+v"(cpf) : "v"(cp));
  }
  if (tid < 16) cnt[tid] = 0;
  const int pw = __builtin_amdgcn_readfirstlane(tid >> 6), w = pw == 0 ? 8 : pw - 1;
  const int ftid = w * 64 + lane;
  const int b = blockIdx.x, j = b >> 3;
  const int jg = j < 6 ? (j >= 3 ? 1 : 0) : (j >= 19 ? 1 : 0);
  const int g = (b & 7) * 2 + jg, r = j < 6 ? 13 + j - 3 * jg : j - 6 - 13 * jg;
  asm volatile("" :: "s"(a.idx), "s"(a.table), "s"(a.wc), "s"(a.wco), "s"(a.w1), "s"(a.b1), "s"(a.w2), "s"(a.bo), "s"(a.out), "s"(a.ws), "s"(a.table_bytes));
  lds_barrier();
  const rsrc_t rws = mkrs(a.ws, WS_END);
  const rsrc_t ridx = mkrs(a.idx, NB * NF * 4), rtab = mkrs(a.table, a.table_bytes);
  const int rot = P_ROT ? 2 * ((r + 5 * g) % 13) : 0;

  if (w < 4 && r >= 13) {
      const int p = g * 3 + r - 13;
      const rsrc_t rwc = mkrs(a.wc, 3 * CUBE * 2), rwo = mkrs(a.wco, 78 * 2);
      __builtin_amdgcn_s_setprio(3);
      if (p < NF) {
        const int i = p;
        u32x4 tb[3];
        const int hb = (w >> 1) * 13, el = (w & 1) * 64 + lane;
        const CAS u32* w3p = (const CAS u32*)(a.wco + 52);
        u32 w3[13];
#pragma unroll
        for (int h = 0; h < 13; ++h) w3[h] = w3p[h];
        u32x4 v[13];
#pragma unroll
        for (int h = 0; h < 13; ++h) v[h] = __builtin_amdgcn_raw_buffer_load_b128(rwc, el < 85 ? (2 * CUBE + hb * 676 + 8 * el) * 2 : OOB, h * 676 * 2, 0);
        u32 v2[2];
#pragma unroll
        for (int m = 0; m < 2; ++m) {
          const u32 d = ftid + 256 * m, h = (d * 5042u) >> 16, e = d - 13 * h;
          v2[m] = __builtin_amdgcn_raw_buffer_load_b32(rwc, d < 338 ? (CUBE + (h * NF + i) * NF) * 2 + e * 4 : OOB, 0, 0);
        }
        const u32 wq = __builtin_amdgcn_raw_buffer_load_b16(rwo, ftid < NF ? (NF + ftid) * 2 : OOB, 0, 0);
        const u32 w1i = __builtin_amdgcn_raw_buffer_load_b16(rwo, i * 2, 0, 0);
        if (w == 0) {
#pragma unroll
          for (int m = 0; m < 3; ++m) tb[m] = *(const GAS u32x4*)SYMTAB.w[lane + 64 * m];
        }
        __builtin_amdgcn_sched_barrier(0);
        STAMP(1);
        {
          float acc[8];
#pragma unroll
          for (int e = 0; e < 8; ++e) acc[e] = 0.f;
#pragma unroll
          for (int h = 0; h < 13; ++h) {
            const u32 we = (h & 1) ? w3[h >> 1] >> 16 : w3[h >> 1] & 0xffffu;
            const u32 wo = (h & 1) ? w3[(13 + h) >> 1] & 0xffffu : w3[(13 + h) >> 1] >> 16;
            const u32 wl = hb ? wo : we, wh = wl << 16;
#pragma unroll
            for (int e = 0; e < 4; ++e) { acc[2 * e] = dot2bf(v[h][e], wl, acc[2 * e]); acc[2 * e + 1] = dot2bf(v[h][e], wh, acc[2 * e + 1]); }
          }
          const u32 vo = opq(el < 85 ? L_V3 + (w >> 1) * 2816 + el * 32 : L_V3 + 5632 + lane * 32);
          *(LAS f32x4*)(lds + vo) = (f32x4){acc[0], acc[1], acc[2], acc[3]};
          *(LAS f32x4*)(lds + vo + 16) = (f32x4){acc[4], acc[5], acc[6], acc[7]};
#pragma unroll
          for (int m = 0; m < 2; ++m) *(LAS u32*)(lds + L_W2S + (ftid + 256 * m) * 4) = v2[m];
          if (w == 0) *(LAS u32*)(lds + L_W2C + lane * 4) = wq << 16;
        }
        const int tj = w >> 1, tp = w & 1, kq = lane >> 4;
        const int jr = 16 * tj + (lane & 15), jp = 16 * tp + (lane & 15);
        u32 oa[7], ob[7], oc[7];
#pragma unroll
        for (int s = 0; s < 7; ++s) {
          const int h = 4 * s + kq;
          const bool inv = h < NF && jp < NF;
          oa[s] = opq((h < NF && jr < NF) ? L_W2S + (h * NF + jr) * 2 : L_W2C + 63 * 4);
          ob[s] = opq(inv ? L_V3 + (h * NF + jp) * 4 : L_W2C + ((h < NF && jp == NF) ? h : 63) * 4);
          oc[s] = opq(inv ? L_V3 + 2816 + (h * NF + jp) * 4 : L_W2C + 63 * 4);
        }
        team_arrive(cnt + C_V3, lane);
        team_wait<0>(cnt + C_V3, 4);
        STAMP(2);
        u32 av[7]; float bv[7], bw[7];
#pragma unroll
        for (int s = 0; s < 7; ++s) { av[s] = *(LAS uint16_t*)(lds + oa[s]); bv[s] = *(LAS float*)(lds + ob[s]); bw[s] = *(LAS float*)(lds + oc[s]); }
#pragma unroll
        for (int s = 0; s < 7; ++s) bv[s] += bw[s];
        f32x4 u = {0.f, 0.f, 0.f, 0.f}, u2 = {0.f, 0.f, 0.f, 0.f};
#pragma unroll
        for (int s = 0; s < 7; ++s) {
          if (s & 1) u2 = __builtin_amdgcn_mfma_f32_16x16x4f32(bflo(av[s]), bv[s], u2, 0, 0, 0);
          else u = __builtin_amdgcn_mfma_f32_16x16x4f32(bflo(av[s]), bv[s], u, 0, 0, 0);
        }
        u += u2;
#pragma unroll
        for (int e = 0; e < 4; ++e) {
          const int jo = 16 * tj + 4 * kq + e;
          const float val = (jo == NF && jp == NF) ? bflo(w1i) : u[e];
          *(LAS float*)(lds + L_U + (jo * UP + jp) * 4) = val;
        }
        team_arrive(cnt + C_U, lane);
        if (w == 0) {
          team_wait<0>(cnt + C_U, 4);
          STAMP(3);
          LAS float* U = (LAS float*)(lds + L_U);
          float e[12];
#pragma unroll
          for (int m = 0; m < 3; ++m) {
            e[4 * m] = U[tb[m].x & 0xffff]; e[4 * m + 1] = U[tb[m].x >> 16]; e[4 * m + 2] = U[tb[m].y & 0xffff]; e[4 * m + 3] = U[tb[m].y >> 16];
          }
#pragma unroll
          for (int m = 0; m < 3; ++m)
            __builtin_amdgcn_raw_buffer_store_b32(packbf(e[4 * m] + e[4 * m + 1], e[4 * m + 2] + e[4 * m + 3]), rws, WS_PB + i * 768 + (lane + 64 * m) * 4, 0, 16);
          const u64 flagb = mkflag(get_tag(cnt));
          drain_vm();
          if (lane == 0) store_flag(a.ws, WS_FB + i * FL, flagb);
          STAMP(4);
        }
      } else {
        const int i = (p - NF) * 4 + w;
        const bool prod = p < 33 && i < NF;
        u32 v[6]; u32x4 tb[3];
#pragma unroll
        for (int m = 0; m < 6; ++m) { const int d = lane + 64 * m; v[m] = __builtin_amdgcn_raw_buffer_load_b32(rwc, (prod && d < 338) ? i * 1352 + d * 4 : OOB, 0, 0); }
#pragma unroll
        for (int m = 0; m < 3; ++m) tb[m] = *(const GAS u32x4*)SYMTAB.w[lane + 64 * m];
        __builtin_amdgcn_sched_barrier(0);
        STAMP(1);
        if (prod) {
          LAS char* raw = lds + L_RAW + w * 2048;
#pragma unroll
          for (int m = 0; m < 6; ++m) *(LAS u32*)(raw + (lane + 64 * m) * 4) = v[m];
          asm volatile("s_waitcnt lgkmcnt(0)" ::: "memory");
          LAS uint16_t* R = (LAS uint16_t*)raw;
          u32 e[12];
#pragma unroll
          for (int m = 0; m < 3; ++m) {
            e[4 * m] = R[tb[m].z & 0xffff]; e[4 * m + 1] = R[tb[m].z >> 16]; e[4 * m + 2] = R[tb[m].w & 0xffff]; e[4 * m + 3] = R[tb[m].w >> 16];
          }
#pragma unroll
          for (int m = 0; m < 3; ++m)
            __builtin_amdgcn_raw_buffer_store_b32(packbf(bflo(e[4 * m]) + bflo(e[4 * m + 1]), bflo(e[4 * m + 2]) + bflo(e[4 * m + 3])), rws,
                                                  WS_PA + i * 768 + (lane + 64 * m) * 4, 0, 16);
          const u64 flag0 = mkflag(get_tag(cnt));
          drain_vm();
          if (lane == 0) store_flag(a.ws, WS_FA + i * FL, flag0);
          STAMP(4);
        }
        if (p == 47 && w == 0) {
          const u32 tag0 = get_tag(cnt);
          const u64 flag0 = mkflag(tag0);
          __builtin_amdgcn_s_sleep(32);
#pragma unroll 1
          for (int m = 0; m < 4; ++m) poll_flags<16>(a.ws + WS_XF + (lane + 64 * m) * FL, flag0);
          if (lane == 0) __hip_atomic_store((GAS u32*)(a.ws + WS_E), tag0, __ATOMIC_RELAXED, __HIP_MEMORY_SCOPE_AGENT);
        }
      }
  } else if (w >= 4 && w < 8) {
    const int cw = w - 4, smp = g * 64 + r * 4 + cw;
    u32 id[2]; u32x4 xr[2];
    if (r < 13) __builtin_amdgcn_s_setprio(2);
#pragma unroll
    for (int m = 0; m < 2; ++m) { const int f = 16 * m + (lane >> 2); id[m] = __builtin_amdgcn_raw_buffer_load_b32(ridx, f < NF ? f * 4 : OOB, smp * NF * 4, 0); }
    u32 touch = 0;
    if (P_TOUCH && cw == 1) {
      const char* tp = lane < 7 ? (const char*)a.table + (size_t)lane * (a.table_bytes / 7 & ~3u)
                                : (lane == 7 ? a.ws + WS_P : (lane == 8 ? a.ws + WS_PA : (lane == 9 ? a.ws + WS_X + g * 104 * 1024 : (const char*)a.out)));
      asm volatile("global_load_dword %0, %1, off" : "+v"(touch) : "v"(tp));
    }
    const size_t bop = (size_t)a.bo;
    const u32 bo = *(const CAS u32*)(bop & ~(size_t)3) >> ((bop & 2) * 8);
#pragma unroll
    for (int m = 0; m < 2; ++m) { const int f = 16 * m + (lane >> 2); xr[m] = __builtin_amdgcn_raw_buffer_load_b128(rtab, f < NF ? id[m] * 64 + (lane & 3) * 16 : OOB, 0, 0); }
    __builtin_amdgcn_s_setprio(0);
    STAMP(1);
    f32x2 xp[14];
    {
      const u32 o = L_XS + cw * XS_S + (lane >> 2) * 64 + (lane & 3) * 16;
      *(LAS u32x4*)(lds + o) = xr[0];
      u32x4 x1 = xr[1];
      if ((lane >> 2) == NF - 16) x1 = (u32x4){0x3F803F80u, 0x3F803F80u, 0x3F803F80u, 0x3F803F80u};
      *(LAS u32x4*)(lds + opq((lane >> 2) < 12 ? o + 1024 : L_XS + cw * XS_S + 27 * 64 + (lane & 3) * 16)) = x1;
      asm volatile("s_waitcnt lgkmcnt(0)" :: "v"(touch), "v"(cpf) : "memory");
      const int hh = lane >> 5, q = (lane >> 2) & 3, pp = lane & 3;
      const u32 base = L_XS + cw * XS_S + ((lane >> 4) & 1) * 32 + pp * 8;
      const u32 a0 = base + (hh ? q + 2 : q) * 64, a1 = base + (hh ? ((q + 6) & 7) : q + 4) * 64;
      const u32 a6 = base + (24 + ((hh && q < 2) ? (q ^ 1) : q)) * 64;
#pragma unroll
      for (int t = 0; t < 7; ++t) {
        s16x4 y = __builtin_amdgcn_ds_read_tr16_b64_v4i16((LAS s16x4*)(lds + (t < 6 ? ((t & 1) ? a1 : a0) + (t >> 1) * 512 : a6)));
        u32x2 u = __builtin_bit_cast(u32x2, y);
        xp[2 * t] = (f32x2){bflo(u.x), bfhi(u.x)}; xp[2 * t + 1] = (f32x2){bflo(u.y), bfhi(u.y)};
      }
    }
    bf16x8 z[24];
#pragma unroll
    for (int s = 0; s < 24; ++s) {
      u32x4 zz;
#pragma unroll
      for (int e = 0; e < 4; ++e) {
        const int s1 = 8 * s + 2 * e, s2 = s1 + 1;
        const int a1 = SL.a[s1], b1 = SL.b[s1], a2 = SL.a[s2], b2 = SL.b[s2];
        if (a1 == a2 && (b1 & 1) == 0 && b1 < 26 && b2 == b1 + 1) {
          const f32x2 pr = (a1 & 1) ? pkmul_bc<1>(xp[a1 >> 1], xp[b1 >> 1]) : pkmul_bc<0>(xp[a1 >> 1], xp[b1 >> 1]);
          zz[e] = packbf(pr[0], pr[1]);
        } else {
          const float p1 = b1 == 26 ? xp[a1 >> 1][a1 & 1] : mulf(xp[a1 >> 1][a1 & 1], xp[b1 >> 1][b1 & 1]);
          const float p2 = b2 == 26 ? xp[a2 >> 1][a2 & 1] : mulf(xp[a2 >> 1][a2 & 1], xp[b2 >> 1][b2 & 1]);
          zz[e] = packbf(p1, p2);
        }
      }
      z[s] = __builtin_bit_cast(bf16x8, zz);
      asm volatile("" : "+v"(z[s]));
    }
    const u32 tag = get_tag(cnt);
    const u64 flag = mkflag(tag);
    STAMP(2);
    if (cw == 0) {
      if (P_BDELAY) __builtin_amdgcn_s_sleep(P_BDELAY);
      poll_flags<P_BSLEEP>(a.ws + (lane < 32 ? WS_FA : WS_FB) + ((lane & 31) < NF ? (lane & 31) : 0) * FL, flag);
      lds_inc(cnt + C_ABRDY);
    } else team_wait<1>(cnt + C_ABRDY, 1);
    STAMP(3);
    if (r < 13 && P_XI >= 0) team_wait<1>(cnt + C_XISS, 4);
    __builtin_amdgcn_s_setprio(2);
    STAMP(4);
    {
      u32x4 y[10];
#pragma unroll
      for (int m = 0; m < 10; ++m) {
        const u32 n = ((m % 5) * 4 + cw) * 64 + lane;
        y[m] = __builtin_amdgcn_raw_buffer_load_b128(rws, n < NF * 48 ? n * 16 : OOB, m < 5 ? WS_PA : WS_PB, 16);
      }
#pragma unroll
      for (int m = 0; m < 5; ++m) {
        const u32 n = (m * 4 + cw) * 64 + lane, i = (n * 1366u) >> 16, c = n - i * 48;
        const u32 o = opq(n < NF * 48 ? L_A + c * APITCH + i * 16 : L_A + 26 * 16);
        *(LAS u32x4*)(lds + o) = y[m];
        *(LAS u32x4*)(lds + o + (L_B - L_A)) = y[5 + m];
      }
      team_arrive(cnt + C_ABSTG, lane);
      team_wait<0>(cnt + C_ABSTG, 4);
    }
    __builtin_amdgcn_s_setprio(0);
    STAMP(5);
    const u32 afr = (lane >> 5) * APITCH + (lane & 31) * 16;
    const u32 tag2 = (u32)(flag >> 32);
    const int sg = r * 4 + cw;
    f32x16 acc1 = {}, acc2 = {};
    {
      constexpr int AD = 2;
      bf16x8 af[24], bf[24];
#pragma unroll
      for (int s = 0; s < AD; ++s) { af[s] = *(LAS bf16x8*)(lds + L_A + afr + s * 2 * APITCH); bf[s] = *(LAS bf16x8*)(lds + L_B + afr + s * 2 * APITCH); }
      __builtin_amdgcn_sched_barrier(0);
#pragma unroll
      for (int s = 0; s < 24; ++s) {
        if (s + AD < 24) { af[s + AD] = *(LAS bf16x8*)(lds + L_A + afr + (s + AD) * 2 * APITCH); bf[s + AD] = *(LAS bf16x8*)(lds + L_B + afr + (s + AD) * 2 * APITCH); }
        acc1 = __builtin_amdgcn_mfma_f32_32x32x16_bf16(af[s], z[s], acc1, 0, 0, 0);
        acc2 = __builtin_amdgcn_mfma_f32_32x32x16_bf16(bf[s], z[s], acc2, 0, 0, 0);
        __builtin_amdgcn_sched_barrier(0);
      }
    }
    float sum = 0.f;
#pragma unroll
    for (int e = 0; e < 12; ++e) sum += acc1[e] * acc2[e];
    { const float t = acc1[12] * acc2[12] + acc1[13] * acc2[13]; sum += lane < 32 ? t : 0.f; }
    sum = sum_rows(sum_row16(sum));
    STAMP(6);
    finish_sample(a.ws + (lane < 26 ? WS_P + ((g * 26 + lane) * 2 + (sg >> 5)) * 512 + (sg & 31) * 16 : WS_P), tag, tag2, sum + bflo(bo), a.out + smp, lane);
    STAMP(7);
  } else if (w < 4 && r < 13) {
      const rsrc_t rw1 = mkrs(a.w1, DIN * DIN * 2);
      const int mh = w & 1, kh = w >> 1, hh = lane >> 5;
      {
        u32x4 v[26];
        const u32 vo = (lane >> 3) * 1664 + r * 128 + (lane & 7) * 16;
        auto wrow = [&](int i) {
          const int s = 13 * (w & 1) + (i >> 1) + rot;
          return kh * 416 + (s >= 26 ? s - 26 : s) * 16 + (i & 1) * 8;
        };
        const int c = lane & 7;
        const u32 lo = L_W + (w * 208 + (lane >> 3)) * 128 + (((c >> 1) ^ (((lane >> 4) & 1) << 1)) * 32) + (c & 1) * 16;
#pragma unroll
        for (int i = 0; i < P_W0; ++i) v[i] = __builtin_amdgcn_raw_buffer_load_b128(rw1, vo, wrow(i) * 1664, 0);
        __builtin_amdgcn_sched_barrier(0);
        team_wait<1>(cnt + C_GISS, 1);
#pragma unroll
        for (int i = P_W0; i < 26; ++i) v[i] = __builtin_amdgcn_raw_buffer_load_b128(rw1, vo, wrow(i) * 1664, 0);
        STAMP(1);
#pragma unroll
        for (int i = 0; i < 26; ++i) *(LAS u32x4*)(lds + lo + i * 1024) = v[i];
      }
      team_arrive(cnt + C_W, lane);
      const u32 tag = get_tag(cnt);
      const u64 flag = mkflag(tag);
      f32x16 acc[2];
      f32x2 wf[8];
      {
        const CAS u32* bp = (const CAS u32*)(a.b1 + r * 64 + kh * 32);
        const CAS u32* wp = (const CAS u32*)(a.w2 + r * 64 + kh * 32);
        u32 bs[16], ws_[16];
#pragma unroll
        for (int t = 0; t < 16; ++t) { bs[t] = bp[t]; ws_[t] = wp[t]; }
#pragma unroll
        for (int t = 0; t < 16; ++t) asm volatile("" : "+s"(bs[t]), "+s"(ws_[t]));
#pragma unroll
        for (int t = 0; t < 4; ++t) {
          const u32 b0 = hh ? bs[4 * t + 2] : bs[4 * t], b1 = hh ? bs[4 * t + 3] : bs[4 * t + 1];
          const u32 w0 = hh ? ws_[4 * t + 2] : ws_[4 * t], w1 = hh ? ws_[4 * t + 3] : ws_[4 * t + 1];
          acc[0][4 * t] = bflo(b0); acc[0][4 * t + 1] = bfhi(b0); acc[0][4 * t + 2] = bflo(b1); acc[0][4 * t + 3] = bfhi(b1);
          acc[1][4 * t] = 0.f; acc[1][4 * t + 1] = 0.f; acc[1][4 * t + 2] = 0.f; acc[1][4 * t + 3] = 0.f;
          wf[2 * t] = (f32x2){bflo(w0), bfhi(w0)}; wf[2 * t + 1] = (f32x2){bflo(w1), bfhi(w1)};
          asm volatile("" : "+v"(wf[2 * t]), "+v"(wf[2 * t + 1]));
        }
      }
      STAMP(2);
      team_wait<0>(cnt + C_XRDY, 1);
      STAMP(3);
      constexpr int XW = P_XW, FD = 3;
      u32x4 xv[26];
      auto ldx = [&](int i) { const int s = i + rot; xv[i] = __builtin_amdgcn_raw_buffer_load_b128(rws, WS_X + lane * 16, (g * 104 + mh * 52 + kh * 26 + (s >= 26 ? s - 26 : s)) * 1024, 16); };
#pragma unroll
      for (int i = 0; i < XW; ++i) ldx(i);
      team_wait<0>(cnt + C_W, 4);
      __builtin_amdgcn_s_setprio(1);
      {
        const int q = (lane >> 2) & 3, pp = lane & 3, gam = (lane >> 4) & 1;
        const u32 rb = L_W + kh * 26 * 2048 + (8 * hh + q) * 128 + pp * 8;
        const u32 ad0 = rb + (((2 * kh + gam) ^ (q & 2)) * 32), ad1 = rb + (((2 * (kh ^ 1) + gam) ^ (q & 2)) * 32);
        bf16x8 fr[26][2];
        auto ldf = [&](int i) {
#pragma unroll
          for (int t = 0; t < 2; ++t) {
            const u32 o = (t ? ad1 : ad0) + i * 2048;
            const s16x4 y0 = __builtin_amdgcn_ds_read_tr16_b64_v4i16((LAS s16x4*)(lds + o));
            const s16x4 y1 = __builtin_amdgcn_ds_read_tr16_b64_v4i16((LAS s16x4*)(lds + o + 512));
            fr[i][t] = __builtin_bit_cast(bf16x8, __builtin_shufflevector(y0, y1, 0, 1, 2, 3, 4, 5, 6, 7));
          }
        };
#pragma unroll
        for (int i = 0; i < FD; ++i) ldf(i);
        __builtin_amdgcn_sched_barrier(0);
#pragma unroll
        for (int i = 0; i < 26; ++i) {
          if (i + XW < 26) ldx(i + XW);
          if (i == P_XI) lds_inc(cnt + C_XISS);
          if (i == 8) lds_inc(cnt + C_WD + kh);
          if (i == 22) team_wait<0>(cnt + C_WD + kh, 2);
          if (i + FD < 26) ldf(i + FD);
          const bf16x8 xb = __builtin_bit_cast(bf16x8, xv[i]);
          acc[0] = __builtin_amdgcn_mfma_f32_32x32x16_bf16(fr[i][0], xb, acc[0], 0, 0, 0);
          acc[1] = __builtin_amdgcn_mfma_f32_32x32x16_bf16(fr[i][1], xb, acc[1], 0, 0, 0);
          __builtin_amdgcn_sched_barrier(0);
        }
      }
      STAMP(4);
      {
        const u32 so = L_W + kh * 26 * 2048 + mh * 4096 + lane * 16;
#pragma unroll
        for (int t = 0; t < 4; ++t) *(LAS f32x4*)(lds + so + t * 1024) = (f32x4){acc[1][4 * t], acc[1][4 * t + 1], acc[1][4 * t + 2], acc[1][4 * t + 3]};
        team_arrive(cnt + C_RED + mh, lane);
        team_wait<0>(cnt + C_RED + mh, 2);
      }
      STAMP(5);
      float part;
      {
        constexpr float K1 = -2.0f * 1.4426950408889634f * 0.7978845608028654f, K2 = K1 * 0.044715f;
        const u32 si = L_W + (kh ^ 1) * 26 * 2048 + mh * 4096 + lane * 16;
        f32x4 o[4];
#pragma unroll
        for (int t = 0; t < 4; ++t) o[t] = *(LAS f32x4*)(lds + si + t * 1024);
        f32x2 h[8], u[8], y[8];
#pragma unroll
        for (int m = 0; m < 8; ++m) {
          h[m] = (f32x2){acc[0][2 * m], acc[0][2 * m + 1]} + (f32x2){o[m >> 1][2 * (m & 1)], o[m >> 1][2 * (m & 1) + 1]};
          u[m] = (h[m] * h[m] * K2 + K1) * h[m];
          y[m] = h[m] * wf[m];
        }
#pragma unroll
        for (int m = 0; m < 8; ++m) { u[m][0] = __builtin_amdgcn_exp2f(u[m][0]); u[m][1] = __builtin_amdgcn_exp2f(u[m][1]); }
#pragma unroll
        for (int m = 0; m < 8; ++m) { u[m] = u[m] + 1.0f; }
#pragma unroll
        for (int m = 0; m < 8; ++m) { u[m][0] = __builtin_amdgcn_rcpf(u[m][0]); u[m][1] = __builtin_amdgcn_rcpf(u[m][1]); }
        f32x2 p0 = y[0] * u[0], p1 = y[1] * u[1];
#pragma unroll
        for (int m = 2; m < 8; m += 2) { p0 += y[m] * u[m]; p1 += y[m + 1] * u[m + 1]; }
        p0 += p1;
        part = p0[0] + p0[1];
      }
      {
        auto sw = __builtin_amdgcn_permlane32_swap(__float_as_uint(part), __float_as_uint(part), false, false);
        part = __uint_as_float(sw[0]) + __uint_as_float(sw[1]);
      }
      {
        const u32x4 o = {__float_as_uint(part), tag, __float_as_uint(part), (u32)(flag >> 32)};
        __builtin_amdgcn_raw_buffer_store_b128(o, rws, lane < 32 ? WS_P + ((g * 26 + r * 2 + kh) * 2 + mh) * 512 + lane * 16 : OOB, 0, 16);
      }
      STAMP(6);
  } else {
    __builtin_amdgcn_s_setprio(3);
    u32 tag;
    if (r < 13) {
      u32 id[4]; u32x4 xg[8];
#pragma unroll
      for (int m = 0; m < 2; ++m) {
        const int p0 = 4 * r + 2 * m, t = p0 >= NF ? 1 : 0, f0 = p0 - t * NF;
        const u32x2 ia = __builtin_amdgcn_raw_buffer_load_b64(ridx, (lane & 31) * (NF * 4), ((g * 64 + t * 32) * NF + f0) * 4, 0);
        id[2 * m] = ia.x; id[2 * m + 1] = ia.y;
      }
#pragma unroll
      for (int k = 0; k < 8; ++k) xg[k] = __builtin_amdgcn_raw_buffer_load_b128(rtab, id[k >> 1] * 64 + (k & 1) * 32 + (lane >> 5) * 16, 0, 0);
      const u32 ep = load_epoch(a.ws);
      lds_inc(cnt + C_GISS);
      STAMP(1);
#pragma unroll
      for (int k = 0; k < 8; ++k) __builtin_amdgcn_raw_buffer_store_b128(xg[k], rws, WS_X + lane * 16, (g * 104 + r * 8 + k) * 1024, 16);
      __builtin_amdgcn_sched_barrier(0);
      tag = __builtin_amdgcn_readfirstlane(ep) + 1u;
      cnt[C_TAGV] = tag;
      team_arrive(cnt + C_TAG, lane);
      STAMP(2);
      drain_vm();
    } else {
      const u32 ep = load_epoch(a.ws);
      tag = __builtin_amdgcn_readfirstlane(ep) + 1u;
      cnt[C_TAGV] = tag;
      team_arrive(cnt + C_TAG, lane);
    }
    const u64 flag = mkflag(tag);
    if (lane == 0) store_flag(a.ws, WS_XF + (g * 16 + r) * FL, flag);
    STAMP(3);
    if (r < 13) {
      __builtin_amdgcn_s_sleep(P_XDELAY);
      poll_flags2_signal(a.ws + WS_XF + (g * 16 + (lane < 13 ? lane : 0)) * FL, flag, cnt + C_XRDY);
    }
    STAMP(4);
    STAMP(5);
  }
}

}

extern "C" void kernel_launch(void* const* d_in, const int* in_sizes, int n_in, void* d_out, int out_size,
                              void* d_ws, size_t ws_size, hipStream_t stream) {
  using namespace xdfm;
  Args a;
  a.idx = (const int*)d_in[0]; a.table = (const uint16_t*)d_in[1]; a.wc = (const uint16_t*)d_in[2];
  a.wco = (const uint16_t*)d_in[3]; a.w1 = (const uint16_t*)d_in[4]; a.b1 = (const uint16_t*)d_in[5];
  a.w2 = (const uint16_t*)d_in[6]; a.bo = (const uint16_t*)d_in[7]; a.out = (uint16_t*)d_out; a.ws = (char*)d_ws;
  a.table_bytes = (u32)in_sizes[1] * 2u; a.pad_ = 0;
  (void)hipFuncSetAttribute((const void*)xdfm_kernel, hipFuncAttributeMaxDynamicSharedMemorySize, L_TOTAL);
  xdfm_kernel<<<256, NWAVE * 64, L_TOTAL, stream>>>(a);
}
```

```cpp
#include <hip/hip_runtime.h>
#include <hip/hip_bf16.h>
#include <stdint.h>

#define P_ADELAY 8
#define P_BDELAY 4
#define P_WW 10
#define P_PQ 16
#define P_XI 14
#define P_XDELAY 12
#define P_CG 4
#define P_ROT 1
#define P_TOUCH 1
#define P_W0 14
#define P_CPF 1
#define P_BSLEEP 6
#define P_XW 12
namespace xdfm {

typedef unsigned int u32;
typedef unsigned long long u64;
typedef u32 u32x2 __attribute__((ext_vector_type(2)));
typedef u32 u32x4 __attribute__((ext_vector_type(4)));
typedef short s16x4 __attribute__((ext_vector_type(4)));
typedef short s16x8 __attribute__((ext_vector_type(8)));
typedef float f32x2 __attribute__((ext_vector_type(2)));
typedef float f32x4 __attribute__((ext_vector_type(4)));
typedef float f32x16 __attribute__((ext_vector_type(16)));
typedef __bf16 bf16x2 __attribute__((ext_vector_type(2)));
typedef __bf16 bf16x8 __attribute__((ext_vector_type(8)));
#define GAS __attribute__((address_space(1)))
#define LAS __attribute__((address_space(3)))
typedef __amdgpu_buffer_rsrc_t rsrc_t;

constexpr int NF = 26, DIN = 832, NB = 1024, CUBE = 17576;

constexpr u32 WS_E = 0;
constexpr u32 FL = 128;
constexpr u32 WS_XF = 4096;
constexpr u32 WS_FA = 40960;
constexpr u32 WS_FB = 45056;
constexpr u32 WS_PA = 49152;
constexpr u32 WS_PB = 69632;
constexpr u32 WS_P = 131072;
constexpr u32 WS_X = 655360;
constexpr u32 WS_END = WS_X + 16 * 104 * 1024;
constexpr int NWAVE = 10;

constexpr u32 L_W = 0;
constexpr u32 APITCH = 432;
constexpr u32 L_A = 106496;
constexpr u32 L_B = L_A + 48 * APITCH;
constexpr u32 L_XS = L_B + 48 * APITCH;
constexpr u32 XS_S = 1824;
constexpr u32 L_CNT = L_XS + 4 * XS_S;
constexpr u32 L_TOTAL = L_CNT + 64;
constexpr u32 L_V3 = 0, L_W2C = 8192, L_W2S = 8448, L_U = 12288, L_RAW = 20480;
enum { C_TAGV = 12, C_TAG = 13, C_GISS = 14, C_ABRDY = 0, C_ABSTG = 1, C_W = 3, C_V3 = 4, C_U = 5, C_XISS = 6, C_XRDY = 7, C_WD = 8, C_RED = 10 };

constexpr int perm1(int j) { return j < 24 ? ((j & ~7) | ((j + 2) & 7)) : (j == 24 ? 25 : (j == 25 ? 24 : j)); }
struct Slots { int a[192]; int b[192]; bool v0[192]; bool v1[192]; int n; };
struct SlotBuilder {
  Slots s{}; bool vis[27][27] = {}; int n = 0;
  constexpr bool seen(int a, int b) const { return vis[a < b ? a : b][a < b ? b : a]; }
  constexpr void put(int a, int b) {
    s.a[n] = a; s.b[n] = b;
    s.v0[n] = !seen(a, b); vis[a < b ? a : b][a < b ? b : a] = true;
    const int na = perm1(a), nb = perm1(b);
    s.v1[n] = !seen(na, nb); vis[na < nb ? na : nb][na < nb ? nb : na] = true;
    ++n;
  }
};
constexpr int sg2(int m) { return perm1(perm1(2 * m)) >> 1; }
constexpr Slots make_slots() {
  SlotBuilder t{};
  for (int A = 0; A < 13; ++A)
    for (int B = A + 1; B < 13; ++B) {
      int ca = A, cb = B;
      while (!t.seen(2 * ca, 2 * cb)) {
        t.put(2 * ca, 2 * cb); t.put(2 * ca, 2 * cb + 1); t.put(2 * ca + 1, 2 * cb); t.put(2 * ca + 1, 2 * cb + 1);
        ca = sg2(ca); cb = sg2(cb);
      }
    }
  for (int A = 0; A < 13; ++A) {
    int ca = A;
    while (!t.seen(2 * ca, 2 * ca + 1)) {
      t.put(2 * ca, 2 * ca); t.put(2 * ca, 2 * ca + 1); t.put(2 * ca + 1, 2 * ca + 1); t.put(2 * ca + 1, 26);
      ca = sg2(ca);
    }
  }
  for (int a = 0; a < 27; ++a) { int ca = a; while (!t.seen(ca, 26)) { t.put(ca, 26); ca = perm1(perm1(ca)); } }
  t.s.n = t.n;
  for (int n = t.n; n < 192; ++n) { t.s.a[n] = 26; t.s.b[n] = 26; t.s.v0[n] = false; t.s.v1[n] = false; }
  return t.s;
}
constexpr Slots SL = make_slots();
static_assert(SL.n <= 192, "too many slots");

constexpr int UP = 33, ZB = 30 * UP, ZA = 676;
struct SymTab { u32 w[192][4]; };
constexpr SymTab make_symtab() {
  SymTab t{};
  for (int d = 0; d < 192; ++d) {
    int c = d >> 2, s = c >> 1, hh = c & 1;
    for (int e = 0; e < 2; ++e) {
      int sg = 8 * s + 2 * (d & 3) + e;
      int a = hh ? perm1(SL.a[sg]) : SL.a[sg], b = hh ? perm1(SL.b[sg]) : SL.b[sg];
      bool v = hh ? SL.v1[sg] : SL.v0[sg];
      u32 b1 = ZB, b2 = ZB, a1 = ZA, a2 = ZA;
      if (v) {
        b1 = a * UP + b; b2 = a == b ? ZB : b * UP + a;
        if (a < 26 && b < 26) { a1 = a * 26 + b; a2 = a == b ? ZA : b * 26 + a; }
      }
      t.w[d][e] = b1 | (b2 << 16);
      t.w[d][2 + e] = a1 | (a2 << 16);
    }
  }
  return t;
}
__device__ const SymTab SYMTAB = make_symtab();

constexpr u32 OOB = 0x7FFFFF00u;
constexpr u32 CODE_PART = 20;
constexpr u32 SPIN_MAX = 1u << 18;

__device__ __forceinline__ float bflo(u32 v) { return __uint_as_float(v << 16); }
__device__ __forceinline__ float bfhi(u32 v) { return __uint_as_float(v & 0xffff0000u); }
__device__ __forceinline__ u32 packbf(float a, float b) {
  f32x2 f = {a, b};
  bf16x2 z = __builtin_convertvector(f, bf16x2);
  return __builtin_bit_cast(u32, z);
}
__device__ __forceinline__ float mulf(float a, float b) { float r; asm("v_mul_f32 %0, %1, %2" : "=v"(r) : "v"(a), "v"(b)); return r; }
template <int E> __device__ __forceinline__ f32x2 pkmul_bc(f32x2 a, f32x2 b) {
  f32x2 r; asm("v_pk_mul_f32 %0, %1, %2 op_sel:[%3,0] op_sel_hi:[%3,1]" : "=v"(r) : "v"(a), "v"(b), "i"(E)); return r;
}
__device__ __forceinline__ float dot2bf(u32 a, u32 b, float c) {
  return __builtin_amdgcn_fdot2_f32_bf16(__builtin_bit_cast(bf16x2, a), __builtin_bit_cast(bf16x2, b), c, false);
}
__device__ __forceinline__ rsrc_t mkrs(const void* p, u32 bytes) { return __builtin_amdgcn_make_buffer_rsrc((void*)p, 0, bytes, 0x00020000); }
__device__ __forceinline__ u64 mkflag(u32 tag) { return (u64)tag | ((u64)(tag * 0x9E3779B1u + 0x7F4A7C15u) << 32); }
__device__ __forceinline__ void drain_vm() { asm volatile("s_waitcnt vmcnt(0)" ::: "memory"); }
__device__ __forceinline__ void lds_inc(LAS u32* cnt) {
  u64 keep;
  asm volatile("s_mov_b64 %0, exec\n\ts_mov_b64 exec, 1\n\tds_add_u32 %1, %2\n\ts_mov_b64 exec, %0"
               : "=&s"(keep) : "v"((u32)(size_t)cnt), "v"(1u) : "memory");
}
__device__ __forceinline__ void team_arrive(LAS u32* cnt, int) {
  asm volatile("s_waitcnt lgkmcnt(0)" ::: "memory");
  lds_inc(cnt);
}
__device__ __forceinline__ float sum_rows(float v) {
  auto a = __builtin_amdgcn_permlane32_swap(__float_as_uint(v), __float_as_uint(v), false, false);
  v = __uint_as_float(a[0]) + __uint_as_float(a[1]);
  auto b = __builtin_amdgcn_permlane16_swap(__float_as_uint(v), __float_as_uint(v), false, false);
  return __uint_as_float(b[0]) + __uint_as_float(b[1]);
}
template <int CTRL> __device__ __forceinline__ float dpp_add(float v) {
  return v + __uint_as_float(__builtin_amdgcn_update_dpp(0u, __float_as_uint(v), CTRL, 0xf, 0xf, true));
}
__device__ __forceinline__ float sum_row16(float v) {
  v = dpp_add<0x128>(v);
  v = dpp_add<0x124>(v);
  v = dpp_add<0x122>(v);
  v = dpp_add<0x121>(v);
  return v;
}

#define STAMP(k) do {} while (0)

struct Args {
  const int* idx; const uint16_t* table; const uint16_t* wc; const uint16_t* wco; const uint16_t* w1;
  const uint16_t* b1; const uint16_t* w2; const uint16_t* bo; uint16_t* out; char* ws;
  u32 table_bytes; u32 pad_;
};
#define CAS __attribute__((address_space(4)))

template <int SLEEP> __device__ __forceinline__ void team_wait(LAS u32* cnt, u32 target) {
  u32 v, s, n = SPIN_MAX;
  asm volatile(
      "1:\n\tds_read_b32 %0, %3\n\ts_waitcnt lgkmcnt(0)\n\tv_readfirstlane_b32 %1, %0\n\ts_cmp_ge_u32 %1, %4\n\ts_cbranch_scc1 2f\n\t"
      "s_sleep %5\n\ts_sub_u32 %2, %2, 1\n\ts_cbranch_scc0 1b\n2:"
      : "=&v"(v), "=&s"(s), "+s"(n) : "v"((u32)(size_t)cnt), "s"(target), "i"(SLEEP) : "memory", "scc");
}
template <int SLEEP> __device__ __forceinline__ void poll_flags(const char* p, u64 want) {
  u64 v; u32 n = SPIN_MAX;
  asm volatile(
      "1:\n\tglobal_load_dwordx2 %0, %2, off sc1\n\ts_waitcnt vmcnt(0)\n\tv_cmp_ne_u64 vcc, %3, %0\n\ts_nop 4\n\ts_cbranch_vccz 2f\n\t"
      "s_sleep %4\n\ts_sub_u32 %1, %1, 1\n\ts_cbranch_scc0 1b\n2:"
      : "=&v"(v), "+s"(n) : "v"(p), "s"(want), "i"(SLEEP) : "memory", "scc", "vcc");
}
__device__ __forceinline__ void poll_flags2_signal(const char* p, u64 want, LAS u32* cnt) {
  u64 v0, v1, keep; u32 n = SPIN_MAX;
  asm volatile(
      "global_load_dwordx2 %0, %4, off sc1\n\ts_sleep 2\n"
      "1:\n\tglobal_load_dwordx2 %1, %4, off sc1\n\ts_waitcnt vmcnt(1)\n\tv_cmp_ne_u64 vcc, %5, %0\n\ts_nop 4\n\ts_cbranch_vccz 2f\n\t"
      "global_load_dwordx2 %0, %4, off sc1\n\ts_waitcnt vmcnt(1)\n\tv_cmp_ne_u64 vcc, %5, %1\n\ts_nop 4\n\ts_cbranch_vccz 2f\n\t"
      "s_sub_u32 %2, %2, 1\n\ts_cbranch_scc0 1b\n"
      "2:\n\ts_mov_b64 %3, exec\n\ts_mov_b64 exec, 1\n\tds_add_u32 %6, %7\n\ts_mov_b64 exec, %3\n\ts_waitcnt vmcnt(0)"
      : "=&v"(v0), "=&v"(v1), "+s"(n), "=&s"(keep) : "v"(p), "s"(want), "v"((u32)(size_t)cnt), "v"(1u) : "memory", "scc", "vcc");
}
__device__ __forceinline__ void finish_sample(const char* p, u32 tag, u32 tag2, float base, uint16_t* outp, int lane) {
  u32 n = SPIN_MAX, hi; u64 m0_, m1_;
  asm volatile(
      "global_load_dwordx4 v[160:163], %4, off sc1\n\t"
      "v_cmp_gt_u32 %2, 26, %9\n\t"
      "s_sleep 2\n"
      "1:\n\t"
      "global_load_dwordx4 v[164:167], %4, off sc1\n\t"
      "s_waitcnt vmcnt(1)\n\t"
      "v_cmp_ne_u32 vcc, %5, v161\n\tv_cmp_ne_u32 %1, %6, v163\n\ts_nop 1\n\ts_or_b64 vcc, vcc, %1\n\ts_and_b64 vcc, vcc, %2\n\t"
      "s_cbranch_vccz 2f\n\t"
      "global_load_dwordx4 v[160:163], %4, off sc1\n\t"
      "s_waitcnt vmcnt(1)\n\t"
      "v_cmp_ne_u32 vcc, %5, v165\n\tv_cmp_ne_u32 %1, %6, v167\n\ts_nop 1\n\ts_or_b64 vcc, vcc, %1\n\ts_and_b64 vcc, vcc, %2\n\t"
      "s_cbranch_vccz 3f\n\t"
      "s_sub_u32 %0, %0, 1\n\ts_cbranch_scc0 1b\n\t"
      "s_waitcnt vmcnt(0)\n"
      "2:\n\tv_cndmask_b32 v159, 0, v160, %2\n\ts_branch 4f\n"
      "3:\n\tv_cndmask_b32 v159, 0, v164, %2\n"
      "4:\n\ts_nop 1\n\t"
      "v_add_f32_dpp v159, v159, v159 row_ror:8 row_mask:0xf bank_mask:0xf\n\ts_nop 1\n\t"
      "v_add_f32_dpp v159, v159, v159 row_ror:4 row_mask:0xf bank_mask:0xf\n\ts_nop 1\n\t"
      "v_add_f32_dpp v159, v159, v159 row_ror:2 row_mask:0xf bank_mask:0xf\n\ts_nop 1\n\t"
      "v_add_f32_dpp v159, v159, v159 row_ror:1 row_mask:0xf bank_mask:0xf\n\ts_nop 1\n\t"
      "v_readlane_b32 %3, v159, 16\n\t"
      "v_add_f32 v159, %7, v159\n\ts_nop 1\n\t"
      "v_add_f32 v159, %3, v159\n\t"
      "v_cvt_pk_bf16_f32 v159, v159, v159\n\t"
      "s_mov_b64 %1, exec\n\ts_mov_b64 exec, 1\n\t"
      "global_store_short %8, v159, off\n\t"
      "s_mov_b64 exec, %1"
      : "+s"(n), "=&s"(m0_), "=&s"(m1_), "=&s"(hi)
      : "v"(p), "s"(tag), "s"(tag2), "v"(base), "v"(outp), "v"(lane)
      : "memory", "scc", "vcc", "v159", "v160", "v161", "v162", "v163", "v164", "v165", "v166", "v167");
}
__device__ __forceinline__ void lds_barrier() {
  asm volatile("s_waitcnt lgkmcnt(0)" ::: "memory");
  __builtin_amdgcn_s_barrier();
  asm volatile("" ::: "memory");
}
__device__ __forceinline__ u32 load_epoch(const char* ws) {
  return __hip_atomic_load((const GAS u32*)(ws + WS_E), __ATOMIC_RELAXED, __HIP_MEMORY_SCOPE_AGENT);
}
__device__ __forceinline__ void store_flag(char* ws, u32 off, u64 flag) {
  __hip_atomic_store((GAS u64*)(ws + off), flag, __ATOMIC_RELAXED, __HIP_MEMORY_SCOPE_AGENT);
}
__device__ __forceinline__ u32 get_tag(LAS u32* cnt) {
  team_wait<0>(cnt + C_TAG, 1);
  return __builtin_amdgcn_readfirstlane(cnt[C_TAGV]);
}
__device__ __forceinline__ u32 opq(u32 v) { asm("" : "+v"(v)); return v; }

__global__ void __launch_bounds__(640, 3) xdfm_kernel(Args a) {
  extern __shared__ __attribute__((aligned(16))) char lds_raw[];
  LAS char* lds = (LAS char*)lds_raw;
  const int tid = threadIdx.x, lane = tid & 63;
  LAS u32* cnt = (LAS u32*)(lds + L_CNT);
  u32 cpf = 0;
  if (P_CPF && tid >= 512 && tid < 576) {
    u64 pc;
    asm volatile("s_getpc_b64 %0" : "=s"(pc));
    const u32 ln = ((blockIdx.x >> 3) & 7) * CODE_PART + (lane < CODE_PART ? lane : 0);
    const char* cp = (const char*)(pc & ~(u64)255) + ln * 128;
    asm volatile("global_load_dword %0, %1, off" : "+v"(cpf) : "v"(cp));
  }
  if (tid < 16) cnt[tid] = 0;
  const int pw = __builtin_amdgcn_readfirstlane(tid >> 6), w = pw == 0 ? 8 : (pw == 9 ? 9 : pw - 1);
  const int ftid = w * 64 + lane;
  const int b = blockIdx.x, j = b >> 3;
  const int jg = j < 6 ? (j >= 3 ? 1 : 0) : (j >= 19 ? 1 : 0);
  const int g = (b & 7) * 2 + jg, r = j < 6 ? 13 + j - 3 * jg : j - 6 - 13 * jg;
  asm volatile("" :: "s"(a.idx), "s"(a.table), "s"(a.wc), "s"(a.wco), "s"(a.w1), "s"(a.b1), "s"(a.w2), "s"(a.bo), "s"(a.out), "s"(a.ws), "s"(a.table_bytes));
  lds_barrier();
  const rsrc_t rws = mkrs(a.ws, WS_END);
  const rsrc_t ridx = mkrs(a.idx, NB * NF * 4), rtab = mkrs(a.table, a.table_bytes);
  const int rot = P_ROT ? 2 * ((r + 5 * g) % 13) : 0;

  if (w == 9) {
    if (b == 250) {
      team_wait<1>(cnt + C_GISS, 1);
#pragma unroll 1
      for (int i = 0; i < 6; ++i) __builtin_amdgcn_s_sleep(127);
    }
    return;
  }
  if (w < 4 && r >= 13) {
      const int p = g * 3 + r - 13;
      const rsrc_t rwc = mkrs(a.wc, 3 * CUBE * 2), rwo = mkrs(a.wco, 78 * 2);
      __builtin_amdgcn_s_setprio(3);
      if (p < NF) {
        const int i = p;
        u32x4 tb[3];
        const int hb = (w >> 1) * 13, el = (w & 1) * 64 + lane;
        const CAS u32* w3p = (const CAS u32*)(a.wco + 52);
        u32 w3[13];
#pragma unroll
        for (int h = 0; h < 13; ++h) w3[h] = w3p[h];
        u32x4 v[13];
#pragma unroll
        for (int h = 0; h < 13; ++h) v[h] = __builtin_amdgcn_raw_buffer_load_b128(rwc, el < 85 ? (2 * CUBE + hb * 676 + 8 * el) * 2 : OOB, h * 676 * 2, 0);
        u32 v2[2];
#pragma unroll
        for (int m = 0; m < 2; ++m) {
          const u32 d = ftid + 256 * m, h = (d * 5042u) >> 16, e = d - 13 * h;
          v2[m] = __builtin_amdgcn_raw_buffer_load_b32(rwc, d < 338 ? (CUBE + (h * NF + i) * NF) * 2 + e * 4 : OOB, 0, 0);
        }
        const u32 wq = __builtin_amdgcn_raw_buffer_load_b16(rwo, ftid < NF ? (NF + ftid) * 2 : OOB, 0, 0);
        const u32 w1i = __builtin_amdgcn_raw_buffer_load_b16(rwo, i * 2, 0, 0);
        if (w == 0) {
#pragma unroll
          for (int m = 0; m < 3; ++m) tb[m] = *(const GAS u32x4*)SYMTAB.w[lane + 64 * m];
        }
        __builtin_amdgcn_sched_barrier(0);
        STAMP(1);
        {
          float acc[8];
#pragma unroll
          for (int e = 0; e < 8; ++e) acc[e] = 0.f;
#pragma unroll
          for (int h = 0; h < 13; ++h) {
            const u32 we = (h & 1) ? w3[h >> 1] >> 16 : w3[h >> 1] & 0xffffu;
            const u32 wo = (h & 1) ? w3[(13 + h) >> 1] & 0xffffu : w3[(13 + h) >> 1] >> 16;
            const u32 wl = hb ? wo : we, wh = wl << 16;
#pragma unroll
            for (int e = 0; e < 4; ++e) { acc[2 * e] = dot2bf(v[h][e], wl, acc[2 * e]); acc[2 * e + 1] = dot2bf(v[h][e], wh, acc[2 * e + 1]); }
          }
          const u32 vo = opq(el < 85 ? L_V3 + (w >> 1) * 2816 + el * 32 : L_V3 + 5632 + lane * 32);
          *(LAS f32x4*)(lds + vo) = (f32x4){acc[0], acc[1], acc[2], acc[3]};
          *(LAS f32x4*)(lds + vo + 16) = (f32x4){acc[4], acc[5], acc[6], acc[7]};
#pragma unroll
          for (int m = 0; m < 2; ++m) *(LAS u32*)(lds + L_W2S + (ftid + 256 * m) * 4) = v2[m];
          if (w == 0) *(LAS u32*)(lds + L_W2C + lane * 4) = wq << 16;
        }
        const int tj = w >> 1, tp = w & 1, kq = lane >> 4;
        const int jr = 16 * tj + (lane & 15), jp = 16 * tp + (lane & 15);
        u32 oa[7], ob[7], oc[7];
#pragma unroll
        for (int s = 0; s < 7; ++s) {
          const int h = 4 * s + kq;
          const bool inv = h < NF && jp < NF;
          oa[s] = opq((h < NF && jr < NF) ? L_W2S + (h * NF + jr) * 2 : L_W2C + 63 * 4);
          ob[s] = opq(inv ? L_V3 + (h * NF + jp) * 4 : L_W2C + ((h < NF && jp == NF) ? h : 63) * 4);
          oc[s] = opq(inv ? L_V3 + 2816 + (h * NF + jp) * 4 : L_W2C + 63 * 4);
        }
        team_arrive(cnt + C_V3, lane);
        team_wait<0>(cnt + C_V3, 4);
        STAMP(2);
        u32 av[7]; float bv[7], bw[7];
#pragma unroll
        for (int s = 0; s < 7; ++s) { av[s] = *(LAS uint16_t*)(lds + oa[s]); bv[s] = *(LAS float*)(lds + ob[s]); bw[s] = *(LAS float*)(lds + oc[s]); }
#pragma unroll
        for (int s = 0; s < 7; ++s) bv[s] += bw[s];
        f32x4 u = {0.f, 0.f, 0.f, 0.f}, u2 = {0.f, 0.f, 0.f, 0.f};
#pragma unroll
        for (int s = 0; s < 7; ++s) {
          if (s & 1) u2 = __builtin_amdgcn_mfma_f32_16x16x4f32(bflo(av[s]), bv[s], u2, 0, 0, 0);
          else u = __builtin_amdgcn_mfma_f32_16x16x4f32(bflo(av[s]), bv[s], u, 0, 0, 0);
        }
        u += u2;
#pragma unroll
        for (int e = 0; e < 4; ++e) {
          const int jo = 16 * tj + 4 * kq + e;
          const float val = (jo == NF && jp == NF) ? bflo(w1i) : u[e];
          *(LAS float*)(lds + L_U + (jo * UP + jp) * 4) = val;
        }
        team_arrive(cnt + C_U, lane);
        if (w == 0) {
          team_wait<0>(cnt + C_U, 4);
          STAMP(3);
          LAS float* U = (LAS float*)(lds + L_U);
          float e[12];
#pragma unroll
          for (int m = 0; m < 3; ++m) {
            e[4 * m] = U[tb[m].x & 0xffff]; e[4 * m + 1] = U[tb[m].x >> 16]; e[4 * m + 2] = U[tb[m].y & 0xffff]; e[4 * m + 3] = U[tb[m].y >> 16];
          }
#pragma unroll
          for (int m = 0; m < 3; ++m)
            __builtin_amdgcn_raw_buffer_store_b32(packbf(e[4 * m] + e[4 * m + 1], e[4 * m + 2] + e[4 * m + 3]), rws, WS_PB + i * 768 + (lane + 64 * m) * 4, 0, 16);
          const u64 flagb = mkflag(get_tag(cnt));
          drain_vm();
          if (lane == 0) store_flag(a.ws, WS_FB + i * FL, flagb);
          STAMP(4);
        }
      } else {
        const int i = (p - NF) * 4 + w;
        const bool prod = p < 33 && i < NF;
        u32 v[6]; u32x4 tb[3];
#pragma unroll
        for (int m = 0; m < 6; ++m) { const int d = lane + 64 * m; v[m] = __builtin_amdgcn_raw_buffer_load_b32(rwc, (prod && d < 338) ? i * 1352 + d * 4 : OOB, 0, 0); }
#pragma unroll
        for (int m = 0; m < 3; ++m) tb[m] = *(const GAS u32x4*)SYMTAB.w[lane + 64 * m];
        __builtin_amdgcn_sched_barrier(0);
        STAMP(1);
        if (prod) {
          LAS char* raw = lds + L_RAW + w * 2048;
#pragma unroll
          for (int m = 0; m < 6; ++m) *(LAS u32*)(raw + (lane + 64 * m) * 4) = v[m];
          asm volatile("s_waitcnt lgkmcnt(0)" ::: "memory");
          LAS uint16_t* R = (LAS uint16_t*)raw;
          u32 e[12];
#pragma unroll
          for (int m = 0; m < 3; ++m) {
            e[4 * m] = R[tb[m].z & 0xffff]; e[4 * m + 1] = R[tb[m].z >> 16]; e[4 * m + 2] = R[tb[m].w & 0xffff]; e[4 * m + 3] = R[tb[m].w >> 16];
          }
#pragma unroll
          for (int m = 0; m < 3; ++m)
            __builtin_amdgcn_raw_buffer_store_b32(packbf(bflo(e[4 * m]) + bflo(e[4 * m + 1]), bflo(e[4 * m + 2]) + bflo(e[4 * m + 3])), rws,
                                                  WS_PA + i * 768 + (lane + 64 * m) * 4, 0, 16);
          const u64 flag0 = mkflag(get_tag(cnt));
          drain_vm();
          if (lane == 0) store_flag(a.ws, WS_FA + i * FL, flag0);
          STAMP(4);
        }
        if (p == 47 && w == 0) {
          const u32 tag0 = get_tag(cnt);
          const u64 flag0 = mkflag(tag0);
          __builtin_amdgcn_s_sleep(32);
#pragma unroll 1
          for (int m = 0; m < 4; ++m) poll_flags<16>(a.ws + WS_XF + (lane + 64 * m) * FL, flag0);
          if (lane == 0) __hip_atomic_store((GAS u32*)(a.ws + WS_E), tag0, __ATOMIC_RELAXED, __HIP_MEMORY_SCOPE_AGENT);
        }
      }
  } else if (w >= 4 && w < 8) {
    const int cw = w - 4, smp = g * 64 + r * 4 + cw;
    u32 id[2]; u32x4 xr[2];
    if (r < 13) __builtin_amdgcn_s_setprio(2);
#pragma unroll
    for (int m = 0; m < 2; ++m) { const int f = 16 * m + (lane >> 2); id[m] = __builtin_amdgcn_raw_buffer_load_b32(ridx, f < NF ? f * 4 : OOB, smp * NF * 4, 0); }
    u32 touch = 0;
    if (P_TOUCH && cw == 1) {
      const char* tp = lane < 7 ? (const char*)a.table + (size_t)lane * (a.table_bytes / 7 & ~3u)
                                : (lane == 7 ? a.ws + WS_P : (lane == 8 ? a.ws + WS_PA : (lane == 9 ? a.ws + WS_X + g * 104 * 1024 : (const char*)a.out)));
      asm volatile("global_load_dword %0, %1, off" : "+v"(touch) : "v"(tp));
    }
    const size_t bop = (size_t)a.bo;
    const u32 bo = *(const CAS u32*)(bop & ~(size_t)3) >> ((bop & 2) * 8);
#pragma unroll
    for (int m = 0; m < 2; ++m) { const int f = 16 * m + (lane >> 2); xr[m] = __builtin_amdgcn_raw_buffer_load_b128(rtab, f < NF ? id[m] * 64 + (lane & 3) * 16 : OOB, 0, 0); }
    __builtin_amdgcn_s_setprio(0);
    STAMP(1);
    f32x2 xp[14];
    {
      const u32 o = L_XS + cw * XS_S + (lane >> 2) * 64 + (lane & 3) * 16;
      *(LAS u32x4*)(lds + o) = xr[0];
      u32x4 x1 = xr[1];
      if ((lane >> 2) == NF - 16) x1 = (u32x4){0x3F803F80u, 0x3F803F80u, 0x3F803F80u, 0x3F803F80u};
      *(LAS u32x4*)(lds + opq((lane >> 2) < 12 ? o + 1024 : L_XS + cw * XS_S + 27 * 64 + (lane & 3) * 16)) = x1;
      asm volatile("s_waitcnt lgkmcnt(0)" :: "v"(touch), "v"(cpf) : "memory");
      const int hh = lane >> 5, q = (lane >> 2) & 3, pp = lane & 3;
      const u32 base = L_XS + cw * XS_S + ((lane >> 4) & 1) * 32 + pp * 8;
      const u32 a0 = base + (hh ? q + 2 : q) * 64, a1 = base + (hh ? ((q + 6) & 7) : q + 4) * 64;
      const u32 a6 = base + (24 + ((hh && q < 2) ? (q ^ 1) : q)) * 64;
#pragma unroll
      for (int t = 0; t < 7; ++t) {
        s16x4 y = __builtin_amdgcn_ds_read_tr16_b64_v4i16((LAS s16x4*)(lds + (t < 6 ? ((t & 1) ? a1 : a0) + (t >> 1) * 512 : a6)));
        u32x2 u = __builtin_bit_cast(u32x2, y);
        xp[2 * t] = (f32x2){bflo(u.x), bfhi(u.x)}; xp[2 * t + 1] = (f32x2){bflo(u.y), bfhi(u.y)};
      }
    }
    bf16x8 z[24];
#pragma unroll
    for (int s = 0; s < 24; ++s) {
      u32x4 zz;
#pragma unroll
      for (int e = 0; e < 4; ++e) {
        const int s1 = 8 * s + 2 * e, s2 = s1 + 1;
        const int a1 = SL.a[s1], b1 = SL.b[s1], a2 = SL.a[s2], b2 = SL.b[s2];
        if (a1 == a2 && (b1 & 1) == 0 && b1 < 26 && b2 == b1 + 1) {
          const f32x2 pr = (a1 & 1) ? pkmul_bc<1>(xp[a1 >> 1], xp[b1 >> 1]) : pkmul_bc<0>(xp[a1 >> 1], xp[b1 >> 1]);
          zz[e] = packbf(pr[0], pr[1]);
        } else {
          const float p1 = b1 == 26 ? xp[a1 >> 1][a1 & 1] : mulf(xp[a1 >> 1][a1 & 1], xp[b1 >> 1][b1 & 1]);
          const float p2 = b2 == 26 ? xp[a2 >> 1][a2 & 1] : mulf(xp[a2 >> 1][a2 & 1], xp[b2 >> 1][b2 & 1]);
          zz[e] = packbf(p1, p2);
        }
      }
      z[s] = __builtin_bit_cast(bf16x8, zz);
      asm volatile("" : "+v"(z[s]));
    }
    const u32 tag = get_tag(cnt);
    const u64 flag = mkflag(tag);
    STAMP(2);
    if (cw == 0) {
      if (P_BDELAY) __builtin_amdgcn_s_sleep(P_BDELAY);
      poll_flags<P_BSLEEP>(a.ws + (lane < 32 ? WS_FA : WS_FB) + ((lane & 31) < NF ? (lane & 31) : 0) * FL, flag);
      lds_inc(cnt + C_ABRDY);
    } else team_wait<1>(cnt + C_ABRDY, 1);
    STAMP(3);
    if (r < 13 && P_XI >= 0) team_wait<1>(cnt + C_XISS, 4);
    __builtin_amdgcn_s_setprio(2);
    STAMP(4);
    {
      u32x4 y[10];
#pragma unroll
      for (int m = 0; m < 10; ++m) {
        const u32 n = ((m % 5) * 4 + cw) * 64 + lane;
        y[m] = __builtin_amdgcn_raw_buffer_load_b128(rws, n < NF * 48 ? n * 16 : OOB, m < 5 ? WS_PA : WS_PB, 16);
      }
#pragma unroll
      for (int m = 0; m < 5; ++m) {
        const u32 n = (m * 4 + cw) * 64 + lane, i = (n * 1366u) >> 16, c = n - i * 48;
        const u32 o = opq(n < NF * 48 ? L_A + c * APITCH + i * 16 : L_A + 26 * 16);
        *(LAS u32x4*)(lds + o) = y[m];
        *(LAS u32x4*)(lds + o + (L_B - L_A)) = y[5 + m];
      }
      team_arrive(cnt + C_ABSTG, lane);
      team_wait<0>(cnt + C_ABSTG, 4);
    }
    __builtin_amdgcn_s_setprio(0);
    STAMP(5);
    const u32 afr = (lane >> 5) * APITCH + (lane & 31) * 16;
    const u32 tag2 = (u32)(flag >> 32);
    const int sg = r * 4 + cw;
    f32x16 acc1 = {}, acc2 = {};
    {
      constexpr int AD = 2;
      bf16x8 af[24], bf[24];
#pragma unroll
      for (int s = 0; s < AD; ++s) { af[s] = *(LAS bf16x8*)(lds + L_A + afr + s * 2 * APITCH); bf[s] = *(LAS bf16x8*)(lds + L_B + afr + s * 2 * APITCH); }
      __builtin_amdgcn_sched_barrier(0);
#pragma unroll
      for (int s = 0; s < 24; ++s) {
        if (s + AD < 24) { af[s + AD] = *(LAS bf16x8*)(lds + L_A + afr + (s + AD) * 2 * APITCH); bf[s + AD] = *(LAS bf16x8*)(lds + L_B + afr + (s + AD) * 2 * APITCH); }
        acc1 = __builtin_amdgcn_mfma_f32_32x32x16_bf16(af[s], z[s], acc1, 0, 0, 0);
        acc2 = __builtin_amdgcn_mfma_f32_32x32x16_bf16(bf[s], z[s], acc2, 0, 0, 0);
        __builtin_amdgcn_sched_barrier(0);
      }
    }
    float sum = 0.f;
#pragma unroll
    for (int e = 0; e < 12; ++e) sum += acc1[e] * acc2[e];
    { const float t = acc1[12] * acc2[12] + acc1[13] * acc2[13]; sum += lane < 32 ? t : 0.f; }
    sum = sum_rows(sum_row16(sum));
    STAMP(6);
    finish_sample(a.ws + (lane < 26 ? WS_P + ((g * 26 + lane) * 2 + (sg >> 5)) * 512 + (sg & 31) * 16 : WS_P), tag, tag2, sum + bflo(bo), a.out + smp, lane);
    STAMP(7);
  } else if (w < 4 && r < 13) {
      const rsrc_t rw1 = mkrs(a.w1, DIN * DIN * 2);
      const int mh = w & 1, kh = w >> 1, hh = lane >> 5;
      {
        u32x4 v[26];
        const u32 vo = (lane >> 3) * 1664 + r * 128 + (lane & 7) * 16;
        auto wrow = [&](int i) {
          const int s = 13 * (w & 1) + (i >> 1) + rot;
          return kh * 416 + (s >= 26 ? s - 26 : s) * 16 + (i & 1) * 8;
        };
        const int c = lane & 7;
        const u32 lo = L_W + (w * 208 + (lane >> 3)) * 128 + (((c >> 1) ^ (((lane >> 4) & 1) << 1)) * 32) + (c & 1) * 16;
#pragma unroll
        for (int i = 0; i < P_W0; ++i) v[i] = __builtin_amdgcn_raw_buffer_load_b128(rw1, vo, wrow(i) * 1664, 0);
        __builtin_amdgcn_sched_barrier(0);
        team_wait<1>(cnt + C_GISS, 1);
#pragma unroll
        for (int i = P_W0; i < 26; ++i) v[i] = __builtin_amdgcn_raw_buffer_load_b128(rw1, vo, wrow(i) * 1664, 0);
        STAMP(1);
#pragma unroll
        for (int i = 0; i < 26; ++i) *(LAS u32x4*)(lds + lo + i * 1024) = v[i];
      }
      team_arrive(cnt + C_W, lane);
      const u32 tag = get_tag(cnt);
      const u64 flag = mkflag(tag);
      f32x16 acc[2];
      f32x2 wf[8];
      {
        const CAS u32* bp = (const CAS u32*)(a.b1 + r * 64 + kh * 32);
        const CAS u32* wp = (const CAS u32*)(a.w2 + r * 64 + kh * 32);
        u32 bs[16], ws_[16];
#pragma unroll
        for (int t = 0; t < 16; ++t) { bs[t] = bp[t]; ws_[t] = wp[t]; }
#pragma unroll
        for (int t = 0; t < 16; ++t) asm volatile("" : "+s"(bs[t]), "+s"(ws_[t]));
#pragma unroll
        for (int t = 0; t < 4; ++t) {
          const u32 b0 = hh ? bs[4 * t + 2] : bs[4 * t], b1 = hh ? bs[4 * t + 3] : bs[4 * t + 1];
          const u32 w0 = hh ? ws_[4 * t + 2] : ws_[4 * t], w1 = hh ? ws_[4 * t + 3] : ws_[4 * t + 1];
          acc[0][4 * t] = bflo(b0); acc[0][4 * t + 1] = bfhi(b0); acc[0][4 * t + 2] = bflo(b1); acc[0][4 * t + 3] = bfhi(b1);
          acc[1][4 * t] = 0.f; acc[1][4 * t + 1] = 0.f; acc[1][4 * t + 2] = 0.f; acc[1][4 * t + 3] = 0.f;
          wf[2 * t] = (f32x2){bflo(w0), bfhi(w0)}; wf[2 * t + 1] = (f32x2){bflo(w1), bfhi(w1)};
          asm volatile("" : "+v"(wf[2 * t]), "+v"(wf[2 * t + 1]));
        }
      }
      STAMP(2);
      team_wait<0>(cnt + C_XRDY, 1);
      STAMP(3);
      constexpr int XW = P_XW, FD = 3;
      u32x4 xv[26];
      auto ldx = [&](int i) { const int s = i + rot; xv[i] = __builtin_amdgcn_raw_buffer_load_b128(rws, WS_X + lane * 16, (g * 104 + mh * 52 + kh * 26 + (s >= 26 ? s - 26 : s)) * 1024, 16); };
#pragma unroll
      for (int i = 0; i < XW; ++i) ldx(i);
      team_wait<0>(cnt + C_W, 4);
      __builtin_amdgcn_s_setprio(1);
      {
        const int q = (lane >> 2) & 3, pp = lane & 3, gam = (lane >> 4) & 1;
        const u32 rb = L_W + kh * 26 * 2048 + (8 * hh + q) * 128 + pp * 8;
        const u32 ad0 = rb + (((2 * kh + gam) ^ (q & 2)) * 32), ad1 = rb + (((2 * (kh ^ 1) + gam) ^ (q & 2)) * 32);
        bf16x8 fr[26][2];
        auto ldf = [&](int i) {
#pragma unroll
          for (int t = 0; t < 2; ++t) {
            const u32 o = (t ? ad1 : ad0) + i * 2048;
            const s16x4 y0 = __builtin_amdgcn_ds_read_tr16_b64_v4i16((LAS s16x4*)(lds + o));
            const s16x4 y1 = __builtin_amdgcn_ds_read_tr16_b64_v4i16((LAS s16x4*)(lds + o + 512));
            fr[i][t] = __builtin_bit_cast(bf16x8, __builtin_shufflevector(y0, y1, 0, 1, 2, 3, 4, 5, 6, 7));
          }
        };
#pragma unroll
        for (int i = 0; i < FD; ++i) ldf(i);
        __builtin_amdgcn_sched_barrier(0);
#pragma unroll
        for (int i = 0; i < 26; ++i) {
          if (i + XW < 26) ldx(i + XW);
          if (i == P_XI) lds_inc(cnt + C_XISS);
          if (i == 8) lds_inc(cnt + C_WD + kh);
          if (i == 22) team_wait<0>(cnt + C_WD + kh, 2);
          if (i + FD < 26) ldf(i + FD);
          const bf16x8 xb = __builtin_bit_cast(bf16x8, xv[i]);
          acc[0] = __builtin_amdgcn_mfma_f32_32x32x16_bf16(fr[i][0], xb, acc[0], 0, 0, 0);
          acc[1] = __builtin_amdgcn_mfma_f32_32x32x16_bf16(fr[i][1], xb, acc[1], 0, 0, 0);
          __builtin_amdgcn_sched_barrier(0);
        }
      }
      STAMP(4);
      {
        const u32 so = L_W + kh * 26 * 2048 + mh * 4096 + lane * 16;
#pragma unroll
        for (int t = 0; t < 4; ++t) *(LAS f32x4*)(lds + so + t * 1024) = (f32x4){acc[1][4 * t], acc[1][4 * t + 1], acc[1][4 * t + 2], acc[1][4 * t + 3]};
        team_arrive(cnt + C_RED + mh, lane);
        team_wait<0>(cnt + C_RED + mh, 2);
      }
      STAMP(5);
      float part;
      {
        constexpr float K1 = -2.0f * 1.4426950408889634f * 0.7978845608028654f, K2 = K1 * 0.044715f;
        const u32 si = L_W + (kh ^ 1) * 26 * 2048 + mh * 4096 + lane * 16;
        f32x4 o[4];
#pragma unroll
        for (int t = 0; t < 4; ++t) o[t] = *(LAS f32x4*)(lds + si + t * 1024);
        f32x2 h[8], u[8], y[8];
#pragma unroll
        for (int m = 0; m < 8; ++m) {
          h[m] = (f32x2){acc[0][2 * m], acc[0][2 * m + 1]} + (f32x2){o[m >> 1][2 * (m & 1)], o[m >> 1][2 * (m & 1) + 1]};
          u[m] = (h[m] * h[m] * K2 + K1) * h[m];
          y[m] = h[m] * wf[m];
        }
#pragma unroll
        for (int m = 0; m < 8; ++m) { u[m][0] = __builtin_amdgcn_exp2f(u[m][0]); u[m][1] = __builtin_amdgcn_exp2f(u[m][1]); }
#pragma unroll
        for (int m = 0; m < 8; ++m) { u[m] = u[m] + 1.0f; }
#pragma unroll
        for (int m = 0; m < 8; ++m) { u[m][0] = __builtin_amdgcn_rcpf(u[m][0]); u[m][1] = __builtin_amdgcn_rcpf(u[m][1]); }
        f32x2 p0 = y[0] * u[0], p1 = y[1] * u[1];
#pragma unroll
        for (int m = 2; m < 8; m += 2) { p0 += y[m] * u[m]; p1 += y[m + 1] * u[m + 1]; }
        p0 += p1;
        part = p0[0] + p0[1];
      }
      {
        auto sw = __builtin_amdgcn_permlane32_swap(__float_as_uint(part), __float_as_uint(part), false, false);
        part = __uint_as_float(sw[0]) + __uint_as_float(sw[1]);
      }
      {
        const u32x4 o = {__float_as_uint(part), tag, __float_as_uint(part), (u32)(flag >> 32)};
        __builtin_amdgcn_raw_buffer_store_b128(o, rws, lane < 32 ? WS_P + ((g * 26 + r * 2 + kh) * 2 + mh) * 512 + lane * 16 : OOB, 0, 16);
      }
      STAMP(6);
  } else {
    __builtin_amdgcn_s_setprio(3);
    u32 tag;
    if (r < 13) {
      u32 id[4]; u32x4 xg[8];
#pragma unroll
      for (int m = 0; m < 2; ++m) {
        const int p0 = 4 * r + 2 * m, t = p0 >= NF ? 1 : 0, f0 = p0 - t * NF;
        const u32x2 ia = __builtin_amdgcn_raw_buffer_load_b64(ridx, (lane & 31) * (NF * 4), ((g * 64 + t * 32) * NF + f0) * 4, 0);
        id[2 * m] = ia.x; id[2 * m + 1] = ia.y;
      }
#pragma unroll
      for (int k = 0; k < 8; ++k) xg[k] = __builtin_amdgcn_raw_buffer_load_b128(rtab, id[k >> 1] * 64 + (k & 1) * 32 + (lane >> 5) * 16, 0, 0);
      const u32 ep = load_epoch(a.ws);
      lds_inc(cnt + C_GISS);
      STAMP(1);
#pragma unroll
      for (int k = 0; k < 8; ++k) __builtin_amdgcn_raw_buffer_store_b128(xg[k], rws, WS_X + lane * 16, (g * 104 + r * 8 + k) * 1024, 16);
      __builtin_amdgcn_sched_barrier(0);
      lds_inc(cnt + 2);
      tag = __builtin_amdgcn_readfirstlane(ep) + 1u;
      cnt[C_TAGV] = tag;
      team_arrive(cnt + C_TAG, lane);
      STAMP(2);
      drain_vm();
    } else {
      const u32 ep = load_epoch(a.ws);
      tag = __builtin_amdgcn_readfirstlane(ep) + 1u;
      cnt[C_TAGV] = tag;
      team_arrive(cnt + C_TAG, lane);
    }
    const u64 flag = mkflag(tag);
    if (lane == 0) store_flag(a.ws, WS_XF + (g * 16 + r) * FL, flag);
    lds_inc(cnt + 15);
    STAMP(3);
    if (r < 13) {
      __builtin_amdgcn_s_sleep(P_XDELAY);
      poll_flags2_signal(a.ws + WS_XF + (g * 16 + (lane < 13 ? lane : 0)) * FL, flag, cnt + C_XRDY);
    }
    STAMP(4);
    STAMP(5);
  }
}

}

extern "C" void kernel_launch(void* const* d_in, const int* in_sizes, int n_in, void* d_out, int out_size,
                              void* d_ws, size_t ws_size, hipStream_t stream) {
  using namespace xdfm;
  Args a;
  a.idx = (const int*)d_in[0]; a.table = (const uint16_t*)d_in[1]; a.wc = (const uint16_t*)d_in[2];
  a.wco = (const uint16_t*)d_in[3]; a.w1 = (const uint16_t*)d_in[4]; a.b1 = (const uint16_t*)d_in[5];
  a.w2 = (const uint16_t*)d_in[6]; a.bo = (const uint16_t*)d_in[7]; a.out = (uint16_t*)d_out; a.ws = (char*)d_ws;
  a.table_bytes = (u32)in_sizes[1] * 2u; a.pad_ = 0;
  (void)hipFuncSetAttribute((const void*)xdfm_kernel, hipFuncAttributeMaxDynamicSharedMemorySize, L_TOTAL);
  xdfm_kernel<<<256, NWAVE * 64, L_TOTAL, stream>>>(a);
}
```

```cpp
#include <hip/hip_runtime.h>
#include <hip/hip_bf16.h>
#include <stdint.h>

namespace xdfm {

typedef unsigned int u32;
typedef unsigned long long u64;
typedef u32 u32x2 __attribute__((ext_vector_type(2)));
typedef u32 u32x4 __attribute__((ext_vector_type(4)));
typedef short s16x4 __attribute__((ext_vector_type(4)));
typedef short s16x8 __attribute__((ext_vector_type(8)));
typedef float f32x2 __attribute__((ext_vector_type(2)));
typedef float f32x4 __attribute__((ext_vector_type(4)));
typedef float f32x16 __attribute__((ext_vector_type(16)));
typedef __bf16 bf16x2 __attribute__((ext_vector_type(2)));
typedef __bf16 bf16x8 __attribute__((ext_vector_type(8)));
#define GAS __attribute__((address_space(1)))
#define LAS __attribute__((address_space(3)))
typedef __amdgpu_buffer_rsrc_t rsrc_t;

constexpr int NF = 26, DIN = 832, NB = 1024, CUBE = 17576;

constexpr u32 WS_E = 0;
constexpr u32 FL = 128;
constexpr u32 WS_XF = 4096;
constexpr u32 WS_FA = 40960;
constexpr u32 WS_FB = 45056;
constexpr u32 WS_PA = 49152;
constexpr u32 WS_PB = 69632;
constexpr u32 WS_P = 131072;
constexpr u32 WS_X = 655360;
constexpr u32 WS_END = WS_X + 16 * 104 * 1024;
constexpr int NWAVE = 9;

constexpr u32 L_W = 0;
constexpr u32 APITCH = 432;
constexpr u32 L_A = 106496;
constexpr u32 L_B = L_A + 48 * APITCH;
constexpr u32 L_XS = L_B + 48 * APITCH;
constexpr u32 XS_S = 1824;
constexpr u32 L_CNT = L_XS + 4 * XS_S;
constexpr u32 L_TOTAL = L_CNT + 64;
constexpr u32 L_V3 = 0, L_W2C = 8192, L_W2S = 8448, L_U = 12288, L_RAW = 20480;
enum { C_BRDY = 0, C_ABSTG = 1, C_ARDY = 2, C_W = 3, C_V3 = 4, C_U = 5, C_XISS = 6, C_XRDY = 7, C_WD = 8, C_RED = 10, C_TAGV = 12, C_TAG = 13, C_GISS = 14 };

constexpr int perm1(int j) { return j < 24 ? ((j & ~7) | ((j + 2) & 7)) : (j == 24 ? 25 : (j == 25 ? 24 : j)); }
struct Slots { int a[192]; int b[192]; bool v0[192]; bool v1[192]; int n; };
struct SlotBuilder {
  Slots s{}; bool vis[27][27] = {}; int n = 0;
  constexpr bool seen(int a, int b) const { return vis[a < b ? a : b][a < b ? b : a]; }
  constexpr void put(int a, int b) {
    s.a[n] = a; s.b[n] = b;
    s.v0[n] = !seen(a, b); vis[a < b ? a : b][a < b ? b : a] = true;
    const int na = perm1(a), nb = perm1(b);
    s.v1[n] = !seen(na, nb); vis[na < nb ? na : nb][na < nb ? nb : na] = true;
    ++n;
  }
};
constexpr int sg2(int m) { return perm1(perm1(2 * m)) >> 1; }
constexpr Slots make_slots() {
  SlotBuilder t{};
  for (int A = 0; A < 13; ++A)
    for (int B = A + 1; B < 13; ++B) {
      int ca = A, cb = B;
      while (!t.seen(2 * ca, 2 * cb)) {
        t.put(2 * ca, 2 * cb); t.put(2 * ca, 2 * cb + 1); t.put(2 * ca + 1, 2 * cb); t.put(2 * ca + 1, 2 * cb + 1);
        ca = sg2(ca); cb = sg2(cb);
      }
    }
  for (int A = 0; A < 13; ++A) {
    int ca = A;
    while (!t.seen(2 * ca, 2 * ca + 1)) {
      t.put(2 * ca, 2 * ca); t.put(2 * ca, 2 * ca + 1); t.put(2 * ca + 1, 2 * ca + 1); t.put(2 * ca + 1, 26);
      ca = sg2(ca);
    }
  }
  for (int a = 0; a < 27; ++a) { int ca = a; while (!t.seen(ca, 26)) { t.put(ca, 26); ca = perm1(perm1(ca)); } }
  t.s.n = t.n;
  for (int n = t.n; n < 192; ++n) { t.s.a[n] = 26; t.s.b[n] = 26; t.s.v0[n] = false; t.s.v1[n] = false; }
  return t.s;
}
constexpr Slots SL = make_slots();
static_assert(SL.n <= 192, "too many slots");

constexpr int UP = 33, ZB = 30 * UP, ZA = 676;
struct SymTab { u32 w[192][4]; };
constexpr SymTab make_symtab() {
  SymTab t{};
  for (int d = 0; d < 192; ++d) {
    int c = d >> 2, s = c >> 1, hh = c & 1;
    for (int e = 0; e < 2; ++e) {
      int sg = 8 * s + 2 * (d & 3) + e;
      int a = hh ? perm1(SL.a[sg]) : SL.a[sg], b = hh ? perm1(SL.b[sg]) : SL.b[sg];
      bool v = hh ? SL.v1[sg] : SL.v0[sg];
      u32 b1 = ZB, b2 = ZB, a1 = ZA, a2 = ZA;
      if (v) {
        b1 = a * UP + b; b2 = a == b ? ZB : b * UP + a;
        if (a < 26 && b < 26) { a1 = a * 26 + b; a2 = a == b ? ZA : b * 26 + a; }
      }
      t.w[d][e] = b1 | (b2 << 16);
      t.w[d][2 + e] = a1 | (a2 << 16);
    }
  }
  return t;
}
__device__ const SymTab SYMTAB = make_symtab();

constexpr u32 OOB = 0x7FFFFF00u;
constexpr u32 CODE_PART = 22;
constexpr u32 SPIN_MAX = 1u << 18;
constexpr int W_EARLY = 16;

__device__ __forceinline__ float bflo(u32 v) { return __uint_as_float(v << 16); }
__device__ __forceinline__ float bfhi(u32 v) { return __uint_as_float(v & 0xffff0000u); }
__device__ __forceinline__ u32 packbf(float a, float b) {
  f32x2 f = {a, b};
  bf16x2 z = __builtin_convertvector(f, bf16x2);
  return __builtin_bit_cast(u32, z);
}
__device__ __forceinline__ float mulf(float a, float b) { float r; asm("v_mul_f32 %0, %1, %2" : "=v"(r) : "v"(a), "v"(b)); return r; }
template <int E> __device__ __forceinline__ f32x2 pkmul_bc(f32x2 a, f32x2 b) {
  f32x2 r; asm("v_pk_mul_f32 %0, %1, %2 op_sel:[%3,0] op_sel_hi:[%3,1]" : "=v"(r) : "v"(a), "v"(b), "i"(E)); return r;
}
__device__ __forceinline__ float dot2bf(u32 a, u32 b, float c) {
  return __builtin_amdgcn_fdot2_f32_bf16(__builtin_bit_cast(bf16x2, a), __builtin_bit_cast(bf16x2, b), c, false);
}
__device__ __forceinline__ rsrc_t mkrs(const void* p, u32 bytes) { return __builtin_amdgcn_make_buffer_rsrc((void*)p, 0, bytes, 0x00020000); }
__device__ __forceinline__ u64 mkflag(u32 tag) { return (u64)tag | ((u64)(tag * 0x9E3779B1u + 0x7F4A7C15u) << 32); }
__device__ __forceinline__ void drain_vm() { asm volatile("s_waitcnt vmcnt(0)" ::: "memory"); }
__device__ __forceinline__ void lds_inc(LAS u32* cnt) {
  u64 keep;
  asm volatile("s_mov_b64 %0, exec\n\ts_mov_b64 exec, 1\n\tds_add_u32 %1, %2\n\ts_mov_b64 exec, %0"
               : "=&s"(keep) : "v"((u32)(size_t)cnt), "v"(1u) : "memory");
}
__device__ __forceinline__ void team_arrive(LAS u32* cnt) {
  asm volatile("s_waitcnt lgkmcnt(0)" ::: "memory");
  lds_inc(cnt);
}
__device__ __forceinline__ float sum_rows(float v) {
  auto a = __builtin_amdgcn_permlane32_swap(__float_as_uint(v), __float_as_uint(v), false, false);
  v = __uint_as_float(a[0]) + __uint_as_float(a[1]);
  auto b = __builtin_amdgcn_permlane16_swap(__float_as_uint(v), __float_as_uint(v), false, false);
  return __uint_as_float(b[0]) + __uint_as_float(b[1]);
}
template <int CTRL> __device__ __forceinline__ float dpp_add(float v) {
  return v + __uint_as_float(__builtin_amdgcn_update_dpp(0u, __float_as_uint(v), CTRL, 0xf, 0xf, true));
}
__device__ __forceinline__ float sum_row16(float v) {
  v = dpp_add<0x128>(v);
  v = dpp_add<0x124>(v);
  v = dpp_add<0x122>(v);
  v = dpp_add<0x121>(v);
  return v;
}

struct Args {
  const int* idx; const uint16_t* table; const uint16_t* wc; const uint16_t* wco; const uint16_t* w1;
  const uint16_t* b1; const uint16_t* w2; const uint16_t* bo; uint16_t* out; char* ws;
  u32 table_bytes; u32 pad_;
};
#define CAS __attribute__((address_space(4)))

template <int SLEEP> __device__ __forceinline__ void team_wait(LAS u32* cnt, u32 target) {
  u32 v, s, n = SPIN_MAX;
  asm volatile(
      "1:\n\tds_read_b32 %0, %3\n\ts_waitcnt lgkmcnt(0)\n\tv_readfirstlane_b32 %1, %0\n\ts_cmp_ge_u32 %1, %4\n\ts_cbranch_scc1 2f\n\t"
      "s_sleep %5\n\ts_sub_u32 %2, %2, 1\n\ts_cbranch_scc0 1b\n2:"
      : "=&v"(v), "=&s"(s), "+s"(n) : "v"((u32)(size_t)cnt), "s"(target), "i"(SLEEP) : "memory", "scc");
}
template <int SLEEP> __device__ __forceinline__ void poll_flags(const char* p, u64 want) {
  u64 v; u32 n = SPIN_MAX;
  asm volatile(
      "1:\n\tglobal_load_dwordx2 %0, %2, off sc1\n\ts_waitcnt vmcnt(0)\n\tv_cmp_ne_u64 vcc, %3, %0\n\ts_nop 4\n\ts_cbranch_vccz 2f\n\t"
      "s_sleep %4\n\ts_sub_u32 %1, %1, 1\n\ts_cbranch_scc0 1b\n2:"
      : "=&v"(v), "+s"(n) : "v"(p), "s"(want), "i"(SLEEP) : "memory", "scc", "vcc");
}
__device__ __forceinline__ void poll_flags2_signal(const char* p, u64 want, LAS u32* cnt) {
  u64 v0, v1, keep; u32 n = SPIN_MAX;
  asm volatile(
      "global_load_dwordx2 %0, %4, off sc1\n\ts_sleep 2\n"
      "1:\n\tglobal_load_dwordx2 %1, %4, off sc1\n\ts_waitcnt vmcnt(1)\n\tv_cmp_ne_u64 vcc, %5, %0\n\ts_nop 4\n\ts_cbranch_vccz 2f\n\t"
      "global_load_dwordx2 %0, %4, off sc1\n\ts_waitcnt vmcnt(1)\n\tv_cmp_ne_u64 vcc, %5, %1\n\ts_nop 4\n\ts_cbranch_vccz 2f\n\t"
      "s_sub_u32 %2, %2, 1\n\ts_cbranch_scc0 1b\n"
      "2:\n\ts_mov_b64 %3, exec\n\ts_mov_b64 exec, 1\n\tds_add_u32 %6, %7\n\ts_mov_b64 exec, %3\n\ts_waitcnt vmcnt(0)"
      : "=&v"(v0), "=&v"(v1), "+s"(n), "=&s"(keep) : "v"(p), "s"(want), "v"((u32)(size_t)cnt), "v"(1u) : "memory", "scc", "vcc");
}
__device__ __forceinline__ void finish_sample(const char* p, u32 tag, u32 tag2, float base, uint16_t* outp, int lane) {
  u32 n = SPIN_MAX, hi; u64 m0_, m1_;
  asm volatile(
      "global_load_dwordx4 v[160:163], %4, off sc1\n\t"
      "v_cmp_gt_u32 %2, 26, %9\n\t"
      "s_sleep 2\n"
      "1:\n\t"
      "global_load_dwordx4 v[164:167], %4, off sc1\n\t"
      "s_waitcnt vmcnt(1)\n\t"
      "v_cmp_ne_u32 vcc, %5, v161\n\tv_cmp_ne_u32 %1, %6, v163\n\ts_nop 1\n\ts_or_b64 vcc, vcc, %1\n\ts_and_b64 vcc, vcc, %2\n\t"
      "s_cbranch_vccz 2f\n\t"
      "global_load_dwordx4 v[160:163], %4, off sc1\n\t"
      "s_waitcnt vmcnt(1)\n\t"
      "v_cmp_ne_u32 vcc, %5, v165\n\tv_cmp_ne_u32 %1, %6, v167\n\ts_nop 1\n\ts_or_b64 vcc, vcc, %1\n\ts_and_b64 vcc, vcc, %2\n\t"
      "s_cbranch_vccz 3f\n\t"
      "s_sub_u32 %0, %0, 1\n\ts_cbranch_scc0 1b\n\t"
      "s_waitcnt vmcnt(0)\n"
      "2:\n\tv_cndmask_b32 v159, 0, v160, %2\n\ts_branch 4f\n"
      "3:\n\tv_cndmask_b32 v159, 0, v164, %2\n"
      "4:\n\ts_nop 1\n\t"
      "v_add_f32_dpp v159, v159, v159 row_ror:8 row_mask:0xf bank_mask:0xf\n\ts_nop 1\n\t"
      "v_add_f32_dpp v159, v159, v159 row_ror:4 row_mask:0xf bank_mask:0xf\n\ts_nop 1\n\t"
      "v_add_f32_dpp v159, v159, v159 row_ror:2 row_mask:0xf bank_mask:0xf\n\ts_nop 1\n\t"
      "v_add_f32_dpp v159, v159, v159 row_ror:1 row_mask:0xf bank_mask:0xf\n\ts_nop 1\n\t"
      "v_readlane_b32 %3, v159, 16\n\t"
      "v_add_f32 v159, %7, v159\n\ts_nop 1\n\t"
      "v_add_f32 v159, %3, v159\n\t"
      "v_cvt_pk_bf16_f32 v159, v159, v159\n\t"
      "s_mov_b64 %1, exec\n\ts_mov_b64 exec, 1\n\t"
      "global_store_short %8, v159, off\n\t"
      "s_mov_b64 exec, %1"
      : "+s"(n), "=&s"(m0_), "=&s"(m1_), "=&s"(hi)
      : "v"(p), "s"(tag), "s"(tag2), "v"(base), "v"(outp), "v"(lane)
      : "memory", "scc", "vcc", "v159", "v160", "v161", "v162", "v163", "v164", "v165", "v166", "v167");
}
__device__ __forceinline__ void lds_barrier() {
  asm volatile("s_waitcnt lgkmcnt(0)" ::: "memory");
  __builtin_amdgcn_s_barrier();
  asm volatile("" ::: "memory");
}
__device__ __forceinline__ u32 load_epoch(const char* ws) {
  return __hip_atomic_load((const GAS u32*)(ws + WS_E), __ATOMIC_RELAXED, __HIP_MEMORY_SCOPE_AGENT);
}
__device__ __forceinline__ void store_flag(char* ws, u32 off, u64 flag) {
  __hip_atomic_store((GAS u64*)(ws + off), flag, __ATOMIC_RELAXED, __HIP_MEMORY_SCOPE_AGENT);
}
__device__ __forceinline__ u32 get_tag(LAS u32* cnt) {
  team_wait<0>(cnt + C_TAG, 1);
  return __builtin_amdgcn_readfirstlane(cnt[C_TAGV]);
}
__device__ __forceinline__ u32 opq(u32 v) { asm("" : "+v"(v)); return v; }

__global__ void __launch_bounds__(576, 3) xdfm_kernel(Args a) {
  extern __shared__ __attribute__((aligned(16))) char lds_raw[];
  LAS char* lds = (LAS char*)lds_raw;
  const int tid = threadIdx.x, lane = tid & 63;
  LAS u32* cnt = (LAS u32*)(lds + L_CNT);
  u32 cpf = 0;
  if (tid >= 512) {
    u64 pc;
    asm volatile("s_getpc_b64 %0" : "=s"(pc));
    const u32 ln = ((blockIdx.x >> 3) & 7) * CODE_PART + (lane < CODE_PART ? lane : 0);
    const char* cp = (const char*)(pc & ~(u64)255) + ln * 128;
    asm volatile("global_load_dword %0, %1, off" : "+v"(cpf) : "v"(cp));
  }
  if (tid < 16) cnt[tid] = 0;
  const int pw = __builtin_amdgcn_readfirstlane(tid >> 6), w = pw == 0 ? 8 : pw - 1;
  const int ftid = w * 64 + lane;
  const int b = blockIdx.x, j = b >> 3;
  const int jg = j < 6 ? (j >= 3 ? 1 : 0) : (j >= 19 ? 1 : 0);
  const int g = (b & 7) * 2 + jg, r = j < 6 ? 13 + j - 3 * jg : j - 6 - 13 * jg;
  asm volatile("" :: "s"(a.idx), "s"(a.table), "s"(a.wc), "s"(a.wco), "s"(a.w1), "s"(a.b1), "s"(a.w2), "s"(a.bo), "s"(a.out), "s"(a.ws), "s"(a.table_bytes));
  lds_barrier();
  const rsrc_t rws = mkrs(a.ws, WS_END);
  const rsrc_t ridx = mkrs(a.idx, NB * NF * 4), rtab = mkrs(a.table, a.table_bytes);
  const int rot = 2 * ((r + 5 * g) % 13);

  if (w < 4 && r >= 13) {
      const int p = g * 3 + r - 13;
      const rsrc_t rwc = mkrs(a.wc, 3 * CUBE * 2), rwo = mkrs(a.wco, 78 * 2);
      __builtin_amdgcn_s_setprio(3);
      if (p < NF) {
        const int i = p;
        u32x4 tb[3];
        const int hb = (w >> 1) * 13, el = (w & 1) * 64 + lane;
        const CAS u32* w3p = (const CAS u32*)(a.wco + 52);
        u32 w3[13];
#pragma unroll
        for (int h = 0; h < 13; ++h) w3[h] = w3p[h];
        u32x4 v[13];
#pragma unroll
        for (int h = 0; h < 13; ++h) v[h] = __builtin_amdgcn_raw_buffer_load_b128(rwc, el < 85 ? (2 * CUBE + hb * 676 + 8 * el) * 2 : OOB, h * 676 * 2, 0);
        u32 v2[2];
#pragma unroll
        for (int m = 0; m < 2; ++m) {
          const u32 d = ftid + 256 * m, h = (d * 5042u) >> 16, e = d - 13 * h;
          v2[m] = __builtin_amdgcn_raw_buffer_load_b32(rwc, d < 338 ? (CUBE + (h * NF + i) * NF) * 2 + e * 4 : OOB, 0, 0);
        }
        const u32 wq = __builtin_amdgcn_raw_buffer_load_b16(rwo, ftid < NF ? (NF + ftid) * 2 : OOB, 0, 0);
        const u32 w1i = __builtin_amdgcn_raw_buffer_load_b16(rwo, i * 2, 0, 0);
        if (w == 0) {
#pragma unroll
          for (int m = 0; m < 3; ++m) tb[m] = *(const GAS u32x4*)SYMTAB.w[lane + 64 * m];
        }
        __builtin_amdgcn_sched_barrier(0);
        {
          float acc[8];
#pragma unroll
          for (int e = 0; e < 8; ++e) acc[e] = 0.f;
#pragma unroll
          for (int h = 0; h < 13; ++h) {
            const u32 we = (h & 1) ? w3[h >> 1] >> 16 : w3[h >> 1] & 0xffffu;
            const u32 wo = (h & 1) ? w3[(13 + h) >> 1] & 0xffffu : w3[(13 + h) >> 1] >> 16;
            const u32 wl = hb ? wo : we, wh = wl << 16;
#pragma unroll
            for (int e = 0; e < 4; ++e) { acc[2 * e] = dot2bf(v[h][e], wl, acc[2 * e]); acc[2 * e + 1] = dot2bf(v[h][e], wh, acc[2 * e + 1]); }
          }
          const u32 vo = opq(el < 85 ? L_V3 + (w >> 1) * 2816 + el * 32 : L_V3 + 5632 + lane * 32);
          *(LAS f32x4*)(lds + vo) = (f32x4){acc[0], acc[1], acc[2], acc[3]};
          *(LAS f32x4*)(lds + vo + 16) = (f32x4){acc[4], acc[5], acc[6], acc[7]};
#pragma unroll
          for (int m = 0; m < 2; ++m) *(LAS u32*)(lds + L_W2S + (ftid + 256 * m) * 4) = v2[m];
          if (w == 0) *(LAS u32*)(lds + L_W2C + lane * 4) = wq << 16;
        }
        const int tj = w >> 1, tp = w & 1, kq = lane >> 4;
        const int jr = 16 * tj + (lane & 15), jp = 16 * tp + (lane & 15);
        u32 oa[7], ob[7], oc[7];
#pragma unroll
        for (int s = 0; s < 7; ++s) {
          const int h = 4 * s + kq;
          const bool inv = h < NF && jp < NF;
          oa[s] = opq((h < NF && jr < NF) ? L_W2S + (h * NF + jr) * 2 : L_W2C + 63 * 4);
          ob[s] = opq(inv ? L_V3 + (h * NF + jp) * 4 : L_W2C + ((h < NF && jp == NF) ? h : 63) * 4);
          oc[s] = opq(inv ? L_V3 + 2816 + (h * NF + jp) * 4 : L_W2C + 63 * 4);
        }
        team_arrive(cnt + C_V3);
        team_wait<0>(cnt + C_V3, 4);
        u32 av[7]; float bv[7], bw[7];
#pragma unroll
        for (int s = 0; s < 7; ++s) { av[s] = *(LAS uint16_t*)(lds + oa[s]); bv[s] = *(LAS float*)(lds + ob[s]); bw[s] = *(LAS float*)(lds + oc[s]); }
#pragma unroll
        for (int s = 0; s < 7; ++s) bv[s] += bw[s];
        f32x4 u = {0.f, 0.f, 0.f, 0.f}, u2 = {0.f, 0.f, 0.f, 0.f};
#pragma unroll
        for (int s = 0; s < 7; ++s) {
          if (s & 1) u2 = __builtin_amdgcn_mfma_f32_16x16x4f32(bflo(av[s]), bv[s], u2, 0, 0, 0);
          else u = __builtin_amdgcn_mfma_f32_16x16x4f32(bflo(av[s]), bv[s], u, 0, 0, 0);
        }
        u += u2;
#pragma unroll
        for (int e = 0; e < 4; ++e) {
          const int jo = 16 * tj + 4 * kq + e;
          const float val = (jo == NF && jp == NF) ? bflo(w1i) : u[e];
          *(LAS float*)(lds + L_U + (jo * UP + jp) * 4) = val;
        }
        team_arrive(cnt + C_U);
        if (w == 0) {
          team_wait<0>(cnt + C_U, 4);
          LAS float* U = (LAS float*)(lds + L_U);
          float e[12];
#pragma unroll
          for (int m = 0; m < 3; ++m) {
            e[4 * m] = U[tb[m].x & 0xffff]; e[4 * m + 1] = U[tb[m].x >> 16]; e[4 * m + 2] = U[tb[m].y & 0xffff]; e[4 * m + 3] = U[tb[m].y >> 16];
          }
#pragma unroll
          for (int m = 0; m < 3; ++m)
            __builtin_amdgcn_raw_buffer_store_b32(packbf(e[4 * m] + e[4 * m + 1], e[4 * m + 2] + e[4 * m + 3]), rws, WS_PB + i * 768 + (lane + 64 * m) * 4, 0, 16);
          const u64 flagb = mkflag(get_tag(cnt));
          drain_vm();
          if (lane == 0) store_flag(a.ws, WS_FB + i * FL, flagb);
        }
      } else {
        const int i = (p - NF) * 4 + w;
        const bool prod = p < 33 && i < NF;
        u32 v[6]; u32x4 tb[3];
#pragma unroll
        for (int m = 0; m < 6; ++m) { const int d = lane + 64 * m; v[m] = __builtin_amdgcn_raw_buffer_load_b32(rwc, (prod && d < 338) ? i * 1352 + d * 4 : OOB, 0, 0); }
#pragma unroll
        for (int m = 0; m < 3; ++m) tb[m] = *(const GAS u32x4*)SYMTAB.w[lane + 64 * m];
        __builtin_amdgcn_sched_barrier(0);
        if (prod) {
          LAS char* raw = lds + L_RAW + w * 2048;
#pragma unroll
          for (int m = 0; m < 6; ++m) *(LAS u32*)(raw + (lane + 64 * m) * 4) = v[m];
          asm volatile("s_waitcnt lgkmcnt(0)" ::: "memory");
          LAS uint16_t* R = (LAS uint16_t*)raw;
          u32 e[12];
#pragma unroll
          for (int m = 0; m < 3; ++m) {
            e[4 * m] = R[tb[m].z & 0xffff]; e[4 * m + 1] = R[tb[m].z >> 16]; e[4 * m + 2] = R[tb[m].w & 0xffff]; e[4 * m + 3] = R[tb[m].w >> 16];
          }
#pragma unroll
          for (int m = 0; m < 3; ++m)
            __builtin_amdgcn_raw_buffer_store_b32(packbf(bflo(e[4 * m]) + bflo(e[4 * m + 1]), bflo(e[4 * m + 2]) + bflo(e[4 * m + 3])), rws,
                                                  WS_PA + i * 768 + (lane + 64 * m) * 4, 0, 16);
          const u64 flag0 = mkflag(get_tag(cnt));
          drain_vm();
          if (lane == 0) store_flag(a.ws, WS_FA + i * FL, flag0);
        }
        if (p == 47 && w == 0) {
          const u32 tag0 = get_tag(cnt);
          const u64 flag0 = mkflag(tag0);
          __builtin_amdgcn_s_sleep(32);
#pragma unroll 1
          for (int m = 0; m < 4; ++m) poll_flags<16>(a.ws + WS_XF + (lane + 64 * m) * FL, flag0);
          if (lane == 0) __hip_atomic_store((GAS u32*)(a.ws + WS_E), tag0, __ATOMIC_RELAXED, __HIP_MEMORY_SCOPE_AGENT);
        }
      }
  } else if (w >= 4 && w < 8) {
    const int cw = w - 4, smp = g * 64 + r * 4 + cw;
    u32 id[2]; u32x4 xr[2];
    if (r < 13) __builtin_amdgcn_s_setprio(2);
#pragma unroll
    for (int m = 0; m < 2; ++m) { const int f = 16 * m + (lane >> 2); id[m] = __builtin_amdgcn_raw_buffer_load_b32(ridx, f < NF ? f * 4 : OOB, smp * NF * 4, 0); }
    u32 touch = 0;
    if (cw == 1) {
      const char* tp = lane < 7 ? (const char*)a.table + (size_t)lane * (a.table_bytes / 7 & ~3u)
                                : (lane == 7 ? a.ws + WS_P : (lane == 8 ? a.ws + WS_PA : (lane == 9 ? a.ws + WS_X + g * 104 * 1024 : (const char*)a.out)));
      asm volatile("global_load_dword %0, %1, off" : "+v"(touch) : "v"(tp));
    }
    const size_t bop = (size_t)a.bo;
    const u32 bo = *(const CAS u32*)(bop & ~(size_t)3) >> ((bop & 2) * 8);
#pragma unroll
    for (int m = 0; m < 2; ++m) { const int f = 16 * m + (lane >> 2); xr[m] = __builtin_amdgcn_raw_buffer_load_b128(rtab, f < NF ? id[m] * 64 + (lane & 3) * 16 : OOB, 0, 0); }
    __builtin_amdgcn_s_setprio(0);
    f32x2 xp[14];
    {
      const u32 o = L_XS + cw * XS_S + (lane >> 2) * 64 + (lane & 3) * 16;
      *(LAS u32x4*)(lds + o) = xr[0];
      u32x4 x1 = xr[1];
      if ((lane >> 2) == NF - 16) x1 = (u32x4){0x3F803F80u, 0x3F803F80u, 0x3F803F80u, 0x3F803F80u};
      *(LAS u32x4*)(lds + opq((lane >> 2) < 12 ? o + 1024 : L_XS + cw * XS_S + 27 * 64 + (lane & 3) * 16)) = x1;
      asm volatile("s_waitcnt lgkmcnt(0)" :: "v"(touch), "v"(cpf) : "memory");
      const int hh = lane >> 5, q = (lane >> 2) & 3, pp = lane & 3;
      const u32 base = L_XS + cw * XS_S + ((lane >> 4) & 1) * 32 + pp * 8;
      const u32 a0 = base + (hh ? q + 2 : q) * 64, a1 = base + (hh ? ((q + 6) & 7) : q + 4) * 64;
      const u32 a6 = base + (24 + ((hh && q < 2) ? (q ^ 1) : q)) * 64;
#pragma unroll
      for (int t = 0; t < 7; ++t) {
        s16x4 y = __builtin_amdgcn_ds_read_tr16_b64_v4i16((LAS s16x4*)(lds + (t < 6 ? ((t & 1) ? a1 : a0) + (t >> 1) * 512 : a6)));
        u32x2 u = __builtin_bit_cast(u32x2, y);
        xp[2 * t] = (f32x2){bflo(u.x), bfhi(u.x)}; xp[2 * t + 1] = (f32x2){bflo(u.y), bfhi(u.y)};
      }
    }
    bf16x8 z[24];
#pragma unroll
    for (int s = 0; s < 24; ++s) {
      u32x4 zz;
#pragma unroll
      for (int e = 0; e < 4; ++e) {
        const int s1 = 8 * s + 2 * e, s2 = s1 + 1;
        const int a1 = SL.a[s1], b1 = SL.b[s1], a2 = SL.a[s2], b2 = SL.b[s2];
        if (a1 == a2 && (b1 & 1) == 0 && b1 < 26 && b2 == b1 + 1) {
          const f32x2 pr = (a1 & 1) ? pkmul_bc<1>(xp[a1 >> 1], xp[b1 >> 1]) : pkmul_bc<0>(xp[a1 >> 1], xp[b1 >> 1]);
          zz[e] = packbf(pr[0], pr[1]);
        } else {
          const float p1 = b1 == 26 ? xp[a1 >> 1][a1 & 1] : mulf(xp[a1 >> 1][a1 & 1], xp[b1 >> 1][b1 & 1]);
          const float p2 = b2 == 26 ? xp[a2 >> 1][a2 & 1] : mulf(xp[a2 >> 1][a2 & 1], xp[b2 >> 1][b2 & 1]);
          zz[e] = packbf(p1, p2);
        }
      }
      z[s] = __builtin_bit_cast(bf16x8, zz);
      asm volatile("" : "+v"(z[s]));
    }
    const u32 tag = get_tag(cnt);
    const u64 flag = mkflag(tag);
    auto stage = [&](u32 wsp, u32 ldst, int gate) {
      if (r < 13) team_wait<1>(cnt + gate, 4);
      __builtin_amdgcn_s_setprio(2);
      u32x4 y[5];
#pragma unroll
      for (int m = 0; m < 5; ++m) { const u32 n = (m * 4 + cw) * 64 + lane; y[m] = __builtin_amdgcn_raw_buffer_load_b128(rws, n < NF * 48 ? n * 16 : OOB, wsp, 16); }
#pragma unroll
      for (int m = 0; m < 5; ++m) {
        const u32 n = (m * 4 + cw) * 64 + lane, i = (n * 1366u) >> 16, c = n - i * 48;
        *(LAS u32x4*)(lds + opq(n < NF * 48 ? ldst + c * APITCH + i * 16 : ldst + 26 * 16)) = y[m];
      }
      __builtin_amdgcn_s_setprio(0);
    };
    if (cw == 0) {
      poll_flags<6>(a.ws + WS_FA + (lane < NF ? lane : 0) * FL, flag);
      lds_inc(cnt + C_ARDY);
    } else team_wait<1>(cnt + C_ARDY, 1);
    stage(WS_PA, L_A, C_W);
    if (cw == 0) {
      poll_flags<6>(a.ws + WS_FB + (lane < NF ? lane : 0) * FL, flag);
      lds_inc(cnt + C_BRDY);
    } else team_wait<1>(cnt + C_BRDY, 1);
    stage(WS_PB, L_B, C_XISS);
    team_arrive(cnt + C_ABSTG);
    team_wait<0>(cnt + C_ABSTG, 4);
    const u32 afr = (lane >> 5) * APITCH + (lane & 31) * 16;
    const u32 tag2 = (u32)(flag >> 32);
    const int sg = r * 4 + cw;
    f32x16 acc1 = {}, acc2 = {};
    {
      constexpr int AD = 2;
      bf16x8 af[24], bf[24];
#pragma unroll
      for (int s = 0; s < AD; ++s) { af[s] = *(LAS bf16x8*)(lds + L_A + afr + s * 2 * APITCH); bf[s] = *(LAS bf16x8*)(lds + L_B + afr + s * 2 * APITCH); }
      __builtin_amdgcn_sched_barrier(0);
#pragma unroll
      for (int s = 0; s < 24; ++s) {
        if (s + AD < 24) { af[s + AD] = *(LAS bf16x8*)(lds + L_A + afr + (s + AD) * 2 * APITCH); bf[s + AD] = *(LAS bf16x8*)(lds + L_B + afr + (s + AD) * 2 * APITCH); }
        acc1 = __builtin_amdgcn_mfma_f32_32x32x16_bf16(af[s], z[s], acc1, 0, 0, 0);
        acc2 = __builtin_amdgcn_mfma_f32_32x32x16_bf16(bf[s], z[s], acc2, 0, 0, 0);
        __builtin_amdgcn_sched_barrier(0);
      }
    }
    float sum = 0.f;
#pragma unroll
    for (int e = 0; e < 12; ++e) sum += acc1[e] * acc2[e];
    { const float t = acc1[12] * acc2[12] + acc1[13] * acc2[13]; sum += lane < 32 ? t : 0.f; }
    sum = sum_rows(sum_row16(sum));
    finish_sample(a.ws + (lane < 26 ? WS_P + ((g * 26 + lane) * 2 + (sg >> 5)) * 512 + (sg & 31) * 16 : WS_P), tag, tag2, sum + bflo(bo), a.out + smp, lane);
  } else if (w < 4 && r < 13) {
      const rsrc_t rw1 = mkrs(a.w1, DIN * DIN * 2);
      const int mh = w & 1, kh = w >> 1, hh = lane >> 5;
      {
        u32x4 v[26];
        const u32 vo = (lane >> 3) * 1664 + r * 128 + (lane & 7) * 16;
        auto wrow = [&](int i) {
          const int s = 13 * (w & 1) + (i >> 1) + rot;
          return kh * 416 + (s >= 26 ? s - 26 : s) * 16 + (i & 1) * 8;
        };
        const int c = lane & 7;
        const u32 lo = L_W + (w * 208 + (lane >> 3)) * 128 + (((c >> 1) ^ (((lane >> 4) & 1) << 1)) * 32) + (c & 1) * 16;
#pragma unroll
        for (int i = 0; i < W_EARLY; ++i) v[i] = __builtin_amdgcn_raw_buffer_load_b128(rw1, vo, wrow(i) * 1664, 0);
        __builtin_amdgcn_sched_barrier(0);
        team_wait<1>(cnt + C_GISS, 1);
#pragma unroll
        for (int i = W_EARLY; i < 26; ++i) v[i] = __builtin_amdgcn_raw_buffer_load_b128(rw1, vo, wrow(i) * 1664, 0);
#pragma unroll
        for (int i = 0; i < 26; ++i) *(LAS u32x4*)(lds + lo + i * 1024) = v[i];
      }
      team_arrive(cnt + C_W);
      const u32 tag = get_tag(cnt);
      const u64 flag = mkflag(tag);
      f32x16 acc[2];
      f32x2 wf[8];
      {
        const CAS u32* bp = (const CAS u32*)(a.b1 + r * 64 + kh * 32);
        const CAS u32* wp = (const CAS u32*)(a.w2 + r * 64 + kh * 32);
        u32 bs[16], ws_[16];
#pragma unroll
        for (int t = 0; t < 16; ++t) { bs[t] = bp[t]; ws_[t] = wp[t]; }
#pragma unroll
        for (int t = 0; t < 16; ++t) asm volatile("" : "+s"(bs[t]), "+s"(ws_[t]));
#pragma unroll
        for (int t = 0; t < 4; ++t) {
          const u32 b0 = hh ? bs[4 * t + 2] : bs[4 * t], b1 = hh ? bs[4 * t + 3] : bs[4 * t + 1];
          const u32 w0 = hh ? ws_[4 * t + 2] : ws_[4 * t], w1 = hh ? ws_[4 * t + 3] : ws_[4 * t + 1];
          acc[0][4 * t] = bflo(b0); acc[0][4 * t + 1] = bfhi(b0); acc[0][4 * t + 2] = bflo(b1); acc[0][4 * t + 3] = bfhi(b1);
          acc[1][4 * t] = 0.f; acc[1][4 * t + 1] = 0.f; acc[1][4 * t + 2] = 0.f; acc[1][4 * t + 3] = 0.f;
          wf[2 * t] = (f32x2){bflo(w0), bfhi(w0)}; wf[2 * t + 1] = (f32x2){bflo(w1), bfhi(w1)};
          asm volatile("" : "+v"(wf[2 * t]), "+v"(wf[2 * t + 1]));
        }
      }
      team_wait<0>(cnt + C_XRDY, 1);
      constexpr int XW = 8, FD = 3;
      u32x4 xv[26];
      auto ldx = [&](int i) { const int s = i + rot; xv[i] = __builtin_amdgcn_raw_buffer_load_b128(rws, WS_X + lane * 16, (g * 104 + mh * 52 + kh * 26 + (s >= 26 ? s - 26 : s)) * 1024, 16); };
#pragma unroll
      for (int i = 0; i < XW; ++i) ldx(i);
      team_wait<0>(cnt + C_W, 4);
      __builtin_amdgcn_s_setprio(1);
      {
        const int q = (lane >> 2) & 3, pp = lane & 3, gam = (lane >> 4) & 1;
        const u32 rb = L_W + kh * 26 * 2048 + (8 * hh + q) * 128 + pp * 8;
        const u32 ad0 = rb + (((2 * kh + gam) ^ (q & 2)) * 32), ad1 = rb + (((2 * (kh ^ 1) + gam) ^ (q & 2)) * 32);
        bf16x8 fr[26][2];
        auto ldf = [&](int i) {
#pragma unroll
          for (int t = 0; t < 2; ++t) {
            const u32 o = (t ? ad1 : ad0) + i * 2048;
            const s16x4 y0 = __builtin_amdgcn_ds_read_tr16_b64_v4i16((LAS s16x4*)(lds + o));
            const s16x4 y1 = __builtin_amdgcn_ds_read_tr16_b64_v4i16((LAS s16x4*)(lds + o + 512));
            fr[i][t] = __builtin_bit_cast(bf16x8, __builtin_shufflevector(y0, y1, 0, 1, 2, 3, 4, 5, 6, 7));
          }
        };
#pragma unroll
        for (int i = 0; i < FD; ++i) ldf(i);
        __builtin_amdgcn_sched_barrier(0);
#pragma unroll
        for (int i = 0; i < 26; ++i) {
          if (i + XW < 26) ldx(i + XW);
          if (i == 20) lds_inc(cnt + C_XISS);
          if (i == 8) lds_inc(cnt + C_WD + kh);
          if (i == 22) team_wait<0>(cnt + C_WD + kh, 2);
          if (i + FD < 26) ldf(i + FD);
          const bf16x8 xb = __builtin_bit_cast(bf16x8, xv[i]);
          acc[0] = __builtin_amdgcn_mfma_f32_32x32x16_bf16(fr[i][0], xb, acc[0], 0, 0, 0);
          acc[1] = __builtin_amdgcn_mfma_f32_32x32x16_bf16(fr[i][1], xb, acc[1], 0, 0, 0);
          __builtin_amdgcn_sched_barrier(0);
        }
      }
      {
        const u32 so = L_W + kh * 26 * 2048 + mh * 4096 + lane * 16;
#pragma unroll
        for (int t = 0; t < 4; ++t) *(LAS f32x4*)(lds + so + t * 1024) = (f32x4){acc[1][4 * t], acc[1][4 * t + 1], acc[1][4 * t + 2], acc[1][4 * t + 3]};
        team_arrive(cnt + C_RED + mh);
        team_wait<0>(cnt + C_RED + mh, 2);
      }
      float part;
      {
        constexpr float K1 = -2.0f * 1.4426950408889634f * 0.7978845608028654f, K2 = K1 * 0.044715f;
        const u32 si = L_W + (kh ^ 1) * 26 * 2048 + mh * 4096 + lane * 16;
        f32x4 o[4];
#pragma unroll
        for (int t = 0; t < 4; ++t) o[t] = *(LAS f32x4*)(lds + si + t * 1024);
        f32x2 h[8], u[8], y[8];
#pragma unroll
        for (int m = 0; m < 8; ++m) {
          h[m] = (f32x2){acc[0][2 * m], acc[0][2 * m + 1]} + (f32x2){o[m >> 1][2 * (m & 1)], o[m >> 1][2 * (m & 1) + 1]};
          u[m] = (h[m] * h[m] * K2 + K1) * h[m];
          y[m] = h[m] * wf[m];
        }
#pragma unroll
        for (int m = 0; m < 8; ++m) { u[m][0] = __builtin_amdgcn_exp2f(u[m][0]); u[m][1] = __builtin_amdgcn_exp2f(u[m][1]); }
#pragma unroll
        for (int m = 0; m < 8; ++m) { u[m] = u[m] + 1.0f; }
#pragma unroll
        for (int m = 0; m < 8; ++m) { u[m][0] = __builtin_amdgcn_rcpf(u[m][0]); u[m][1] = __builtin_amdgcn_rcpf(u[m][1]); }
        f32x2 p0 = y[0] * u[0], p1 = y[1] * u[1];
#pragma unroll
        for (int m = 2; m < 8; m += 2) { p0 += y[m] * u[m]; p1 += y[m + 1] * u[m + 1]; }
        p0 += p1;
        part = p0[0] + p0[1];
      }
      {
        auto sw = __builtin_amdgcn_permlane32_swap(__float_as_uint(part), __float_as_uint(part), false, false);
        part = __uint_as_float(sw[0]) + __uint_as_float(sw[1]);
      }
      {
        const u32x4 o = {__float_as_uint(part), tag, __float_as_uint(part), (u32)(flag >> 32)};
        __builtin_amdgcn_raw_buffer_store_b128(o, rws, lane < 32 ? WS_P + ((g * 26 + r * 2 + kh) * 2 + mh) * 512 + lane * 16 : OOB, 0, 16);
      }
  } else {
    __builtin_amdgcn_s_setprio(3);
    u32 tag;
    if (r < 13) {
      u32 id[4]; u32x4 xg[8];
#pragma unroll
      for (int m = 0; m < 2; ++m) {
        const int p0 = 4 * r + 2 * m, t = p0 >= NF ? 1 : 0, f0 = p0 - t * NF;
        const u32x2 ia = __builtin_amdgcn_raw_buffer_load_b64(ridx, (lane & 31) * (NF * 4), ((g * 64 + t * 32) * NF + f0) * 4, 0);
        id[2 * m] = ia.x; id[2 * m + 1] = ia.y;
      }
#pragma unroll
      for (int k = 0; k < 8; ++k) xg[k] = __builtin_amdgcn_raw_buffer_load_b128(rtab, id[k >> 1] * 64 + (k & 1) * 32 + (lane >> 5) * 16, 0, 0);
      const u32 ep = load_epoch(a.ws);
      lds_inc(cnt + C_GISS);
#pragma unroll
      for (int k = 0; k < 8; ++k) __builtin_amdgcn_raw_buffer_store_b128(xg[k], rws, WS_X + lane * 16, (g * 104 + r * 8 + k) * 1024, 16);
      __builtin_amdgcn_sched_barrier(0);
      tag = __builtin_amdgcn_readfirstlane(ep) + 1u;
      cnt[C_TAGV] = tag;
      team_arrive(cnt + C_TAG);
      drain_vm();
    } else {
      const u32 ep = load_epoch(a.ws);
      tag = __builtin_amdgcn_readfirstlane(ep) + 1u;
      cnt[C_TAGV] = tag;
      team_arrive(cnt + C_TAG);
    }
    const u64 flag = mkflag(tag);
    if (lane == 0) store_flag(a.ws, WS_XF + (g * 16 + r) * FL, flag);
    if (r < 13) {
      __builtin_amdgcn_s_sleep(12);
      poll_flags2_signal(a.ws + WS_XF + (g * 16 + (lane < 13 ? lane : 0)) * FL, flag, cnt + C_XRDY);
    }
  }
}

}

extern "C" void kernel_launch(void* const* d_in, const int* in_sizes, int n_in, void* d_out, int out_size,
                              void* d_ws, size_t ws_size, hipStream_t stream) {
  using namespace xdfm;
  Args a;
  a.idx = (const int*)d_in[0]; a.table = (const uint16_t*)d_in[1]; a.wc = (const uint16_t*)d_in[2];
  a.wco = (const uint16_t*)d_in[3]; a.w1 = (const uint16_t*)d_in[4]; a.b1 = (const uint16_t*)d_in[5];
  a.w2 = (const uint16_t*)d_in[6]; a.bo = (const uint16_t*)d_in[7]; a.out = (uint16_t*)d_out; a.ws = (char*)d_ws;
  a.table_bytes = (u32)in_sizes[1] * 2u; a.pad_ = 0;
  (void)hipFuncSetAttribute((const void*)xdfm_kernel, hipFuncAttributeMaxDynamicSharedMemorySize, L_TOTAL);
  xdfm_kernel<<<256, NWAVE * 64, L_TOTAL, stream>>>(a);
}
```
